```python
import torch

def _softmax(x, axis=-1):
    return torch.softmax(x, dim=axis)
def _gelu(x):
    return torch.nn.functional.gelu(x, approximate="tanh")
def _silu(x):
    return torch.nn.functional.silu(x)
def _sigmoid(x):
    return torch.sigmoid(x)
def _layernorm(x, axis=-1, eps=1e-5):
    m = x.mean(dim=axis, keepdim=True)
    v = x.var(dim=axis, keepdim=True, unbiased=False)
    return (x - m) / torch.sqrt(v + eps)
def _scatter_add(tgt, idx, src, axis=0):
    return tgt.clone().index_add_(axis, idx, src)
def _segment_sum(src, seg, n):
    out = torch.zeros((n,) + src.shape[1:], dtype=src.dtype)
    return out.index_add_(0, seg, src)
def _pad(x, pw):
    flat = tuple(v for pair in reversed(pw) for v in pair)
    return torch.nn.functional.pad(x, flat)

kernel_name = 'parallel_sum__compressed_kv_cla__dense_activation_mish__prompt'

def setup_inputs():

    _w = (lambda a: (lambda t: t.to(torch.bfloat16) if t.is_floating_point() else t)(torch.as_tensor(a)))
    def _A_setup():
        B, H, Sq, Sk, D, M = 1, 32, 2048, 2048, 128, 512
        Dc = 4 * D
        return {
            "x": _w((lambda *s: torch.randn(*s, dtype=torch.bfloat16) * 0.05)(B, Sq, M)),
            "c_kv": _w((lambda *s: torch.randn(*s, dtype=torch.bfloat16) * 0.4)(B, Sk, Dc)),
            "w_q": _w((lambda *s: torch.randn(*s, dtype=torch.bfloat16) * 3.2)(M, H, D) * (M ** -0.5)),
            "w_uk": _w((lambda *s: torch.randn(*s, dtype=torch.bfloat16) * 3.2)(Dc, H, D) * (Dc ** -0.5)),
            "w_uv": _w((lambda *s: torch.randn(*s, dtype=torch.bfloat16) * 0.4)(Dc, H, D) * (Dc ** -0.5)),
            "w_o": _w((lambda *s: torch.randn(*s, dtype=torch.bfloat16) * 0.4)(H, D, M) * ((H * D) ** -0.5)),
        }
    _A_in = _A_setup()

    def _B_setup():
        B, S, D, F = 1, 2048, 512, 1376
        return {
            "x": _w((lambda *s: torch.randn(*s, dtype=torch.bfloat16) * 0.05)(B, S, D)),
            "w1": _w((lambda *s: torch.randn(*s, dtype=torch.bfloat16) * 0.8)(D, F) * (D ** -0.5)),
            "b1": _w((lambda *s: torch.randn(*s, dtype=torch.bfloat16) * 0.2)(F)),
            "w2": _w((lambda *s: torch.randn(*s, dtype=torch.bfloat16) * 0.1)(F, D) * (F ** -0.5)),
        }
    _B_in = _B_setup()

    return {
        "A_x": _A_in["x"],
        "A_c_kv": _A_in["c_kv"],
        "A_w_q": _A_in["w_q"],
        "A_w_uk": _A_in["w_uk"],
        "A_w_uv": _A_in["w_uv"],
        "A_w_o": _A_in["w_o"],
        "B_x": _B_in["x"],
        "B_w1": _B_in["w1"],
        "B_b1": _B_in["b1"],
        "B_w2": _B_in["w2"],
    }

def reference(A_x, A_c_kv, A_w_q, A_w_uk, A_w_uv, A_w_o, B_x, B_w1, B_b1, B_w2):
    def _A_ref(x, c_kv, w_q, w_uk, w_uv, w_o):
        B, Sq, M = x.shape[0], x.shape[1], x.shape[2]
        Sk = c_kv.shape[1]
        H, D = w_q.shape[1], w_q.shape[2]
        q = torch.einsum("bqm,mhd->bhqd", x, w_q)
        k = torch.einsum("bkc,chd->bhkd", c_kv, w_uk)
        v = torch.einsum("bkc,chd->bhkd", c_kv, w_uv)
        scores = torch.einsum("bhqd,bhkd->bhqk", q, k) * (D ** -0.5)
        off = Sk - Sq
        qi = torch.arange(Sq)[:, None]
        kj = torch.arange(Sk)[None, :]
        cmask = kj <= (qi + off)
        scores = torch.where(cmask[None, None], scores, -1e9)
        p = _softmax(scores, axis=-1)
        ctx = torch.einsum("bhqk,bhkd->bhqd", p, v)
        return torch.einsum("bhqd,hdm->bqm", ctx, w_o)

    def _B_ref(x, w1, b1, w2):
        h = torch.einsum("bsd,df->bsf", x, w1) + b1
        sp = torch.log1p(torch.exp(-torch.abs(h))) + torch.where(h > 0, h, torch.zeros_like(h))
        act = h * torch.tanh(sp)
        return torch.einsum("bsf,fd->bsd", act, w2)

    x = A_x
    xn = _layernorm(x, axis=-1)
    return x + _A_ref(xn, A_c_kv, A_w_q, A_w_uk, A_w_uv, A_w_o) + _B_ref(xn, B_w1, B_b1, B_w2)
```

```cpp
#include <hip/hip_runtime.h>
#include <hip/hip_bf16.h>
typedef unsigned short u16;
__device__ __forceinline__ float bf2f(u16 b){ return __uint_as_float(((unsigned)b)<<16); }
__device__ __forceinline__ u16 f2bf(float f){ unsigned u=__float_as_uint(f); u += 0x7fffu + ((u>>16)&1u); return (u16)(u>>16); }
constexpr int S=2048, M=512, H=32, D=128, F=1376;
__global__ void k_ln(const u16* x, u16* xn){
  int row=blockIdx.x; int t=threadIdx.x;
  float v[8]; float s=0;
  for(int i=0;i<8;i++){ v[i]=bf2f(x[row*M+t*8+i]); s+=v[i]; }
  for(int o=32;o;o>>=1) s+=__shfl_xor(s,o);
  float m=s/M; float q=0;
  for(int i=0;i<8;i++){ float d=v[i]-m; q+=d*d; }
  for(int o=32;o;o>>=1) q+=__shfl_xor(q,o);
  float r=rsqrtf(q/M+1e-5f);
  for(int i=0;i<8;i++) xn[row*M+t*8+i]=f2bf((v[i]-m)*r);
}
template<int MODE> __global__ void k_gemm(const u16* A,const u16* B,int N,int K,u16* C,float* Cf,const u16* aux){
  __shared__ float As[16][65], Bs[16][65];
  int tx=threadIdx.x&15, ty=threadIdx.x>>4; int m0=blockIdx.y*64, n0=blockIdx.x*64;
  float acc[4][4]={};
  for(int k0=0;k0<K;k0+=16){
    for(int i=threadIdx.x;i<1024;i+=256){ int r=i>>4,c=i&15; As[c][r]=bf2f(A[(size_t)(m0+r)*K+k0+c]); }
    for(int i=threadIdx.x;i<1024;i+=256){ int r=i>>6,c=i&63; Bs[r][c]=(n0+c<N)?bf2f(B[(size_t)(k0+r)*N+n0+c]):0.f; }
    __syncthreads();
    for(int k=0;k<16;k++){ float a[4],b[4]; for(int i=0;i<4;i++){a[i]=As[k][ty*4+i]; b[i]=Bs[k][tx*4+i];}
      for(int i=0;i<4;i++)for(int j=0;j<4;j++)acc[i][j]+=a[i]*b[j]; }
    __syncthreads();
  }
  for(int i=0;i<4;i++)for(int j=0;j<4;j++){ int r=m0+ty*4+i,c=n0+tx*4+j; if(c>=N)continue; float v=acc[i][j];
    if(MODE==0) C[(size_t)r*N+c]=f2bf(v);
    if(MODE==1){ v+=bf2f(aux[c]); float sp=log1pf(expf(-fabsf(v)))+fmaxf(v,0.f); C[(size_t)r*N+c]=f2bf(v*tanhf(sp)); }
    if(MODE==2) Cf[(size_t)r*N+c]=v;
    if(MODE==3) C[(size_t)r*N+c]=f2bf(v+Cf[(size_t)r*N+c]+bf2f(aux[(size_t)r*N+c]));
  }
}
__global__ void k_attn(const u16* q,const u16* k,const u16* v,u16* o){
  __shared__ float p[S]; __shared__ float qs[D]; __shared__ float red[4];
  int row=blockIdx.x, h=blockIdx.y, t=threadIdx.x;
  if(t<D) qs[t]=bf2f(q[(size_t)row*H*D+h*D+t]);
  __syncthreads();
  float mx=-1e30f;
  for(int j=t;j<=row;j+=256){ const u16* kr=k+(size_t)j*H*D+h*D; float s=0; for(int d=0;d<D;d++) s+=qs[d]*bf2f(kr[d]); s*=0.08838834764831845f; p[j]=s; mx=fmaxf(mx,s);}
  for(int o_=32;o_;o_>>=1) mx=fmaxf(mx,__shfl_xor(mx,o_));
  if((t&63)==0) red[t>>6]=mx; __syncthreads(); mx=fmaxf(fmaxf(red[0],red[1]),fmaxf(red[2],red[3])); __syncthreads();
  float l=0; for(int j=t;j<=row;j+=256){ float e=expf(p[j]-mx); p[j]=e; l+=e; }
  for(int o_=32;o_;o_>>=1) l+=__shfl_xor(l,o_);
  if((t&63)==0) red[t>>6]=l; __syncthreads(); l=red[0]+red[1]+red[2]+red[3];
  if(t<D){ float a=0; for(int j=0;j<=row;j++) a+=p[j]*bf2f(v[(size_t)j*H*D+h*D+t]); o[(size_t)row*H*D+h*D+t]=f2bf(a/l); }
}
extern "C" void kernel_launch(void* const* d_in, const int* in_sizes, int n_in, void* d_out, int out_size, void* d_ws, size_t ws_size, hipStream_t stream){
  const u16 *x=(const u16*)d_in[0],*ckv=(const u16*)d_in[1],*wq=(const u16*)d_in[2],*wuk=(const u16*)d_in[3],*wuv=(const u16*)d_in[4],*wo=(const u16*)d_in[5],*w1=(const u16*)d_in[7],*b1=(const u16*)d_in[8],*w2=(const u16*)d_in[9];
  char* ws=(char*)d_ws; u16* xn=(u16*)ws; u16* q=(u16*)(ws+(4<<20)); u16* k=(u16*)(ws+(20<<20)); u16* v=(u16*)(ws+(36<<20)); u16* o=(u16*)(ws+(52<<20)); u16* hh=(u16*)(ws+(68<<20)); float* pf=(float*)(ws+(80<<20));
  k_ln<<<S,64,0,stream>>>(x,xn);
  k_gemm<0><<<dim3(64,32),256,0,stream>>>(xn,wq,4096,512,q,nullptr,nullptr);
  k_gemm<0><<<dim3(64,32),256,0,stream>>>(ckv,wuk,4096,512,k,nullptr,nullptr);
  k_gemm<0><<<dim3(64,32),256,0,stream>>>(ckv,wuv,4096,512,v,nullptr,nullptr);
  k_gemm<1><<<dim3(22,32),256,0,stream>>>(xn,w1,F,512,hh,nullptr,b1);
  k_attn<<<dim3(S,H),256,0,stream>>>(q,k,v,o);
  k_gemm<2><<<dim3(8,32),256,0,stream>>>(o,wo,512,4096,nullptr,pf,nullptr);
  k_gemm<3><<<dim3(8,32),256,0,stream>>>(hh,w2,512,F,(u16*)d_out,pf,x);
}
```

```cpp
#include <hip/hip_runtime.h>
#include <hip/hip_bf16.h>
typedef unsigned short u16;
#define CLOB "v0", "v1", "v2", "v3", "v4", "v5", "v6", "v7", "v8", "v9", "v10", "v11", "v12", "v13", "v14", "v15", "v16", "v17", "v18", "v19", "v20", "v21", "v22", "v23", "v24", "v25", "v26", "v27", "v28", "v29", "v30", "v31", "v32", "v33", "v34", "v35", "v36", "v37", "v38", "v39", "v40", "v41", "v42", "v43", "v44", "v45", "v46", "v47", "v48", "v49", "v50", "v51", "v52", "v53", "v54", "v55", "v56", "v57", "v58", "v59", "v60", "v61", "v62", "v63", "v64", "v65", "v66", "v67", "v68", "v69", "v70", "v71", "v72", "v73", "v74", "v75", "v76", "v77", "v78", "v79", "v80", "v81", "v82", "v83", "v84", "v85", "v86", "v87", "v88", "v89", "v90", "v91", "v92", "v93", "v94", "v95", "v96", "v97", "v98", "v99", "v100", "v101", "v102", "v103", "v104", "v105", "v106", "v107", "v108", "v109", "v110", "v111", "v112", "v113", "v114", "v115", "v116", "v117", "v118", "v119", "v120", "v121", "v122", "v123", "v124", "v125", "v126", "v127", "v128", "v129", "v130", "v131", "v132", "v133", "v134", "v135", "v136", "v137", "v138", "v139", "v140", "v141", "v142", "v143", "v144", "v145", "v146", "v147", "v148", "v149", "v150", "v151", "v152", "v153", "v154", "v155", "v156", "v157", "v158", "v159", "v160", "v161", "v162", "v163", "v164", "v165", "v166", "v167", "v168", "v169", "v170", "v171", "v172", "v173", "v174", "v175", "v176", "v177", "v178", "v179", "v180", "v181", "v182", "v183", "v184", "v185", "v186", "v187", "v188", "v189", "v190", "v191", "v192", "v193", "v194", "v195", "v196", "v197", "v198", "v199", "v200", "v201", "v202", "v203", "v204", "v205", "v206", "v207", "v208", "v209", "v210", "v211", "v212", "v213", "v214", "v215", "v216", "v217", "v218", "v219", "v220", "v221", "v222", "v223", "v224", "v225", "v226", "v227", "v228", "v229", "v230", "v231", "v232", "v233", "v234", "v235", "v236", "v237", "v238", "v239", "v240", "v241", "v242", "v243", "v244", "v245", "v246", "v247", "v248", "v249", "v250", "v251", "v252", "v253", "v254", "v255", "a0", "a1", "a2", "a3", "a4", "a5", "a6", "a7", "a8", "a9", "a10", "a11", "a12", "a13", "a14", "a15", "a16", "a17", "a18", "a19", "a20", "a21", "a22", "a23", "a24", "a25", "a26", "a27", "a28", "a29", "a30", "a31", "a32", "a33", "a34", "a35", "a36", "a37", "a38", "a39", "a40", "a41", "a42", "a43", "a44", "a45", "a46", "a47", "a48", "a49", "a50", "a51", "a52", "a53", "a54", "a55", "a56", "a57", "a58", "a59", "a60", "a61", "a62", "a63", "a64", "a65", "a66", "a67", "a68", "a69", "a70", "a71", "a72", "a73", "a74", "a75", "a76", "a77", "a78", "a79", "a80", "a81", "a82", "a83", "a84", "a85", "a86", "a87", "a88", "a89", "a90", "a91", "a92", "a93", "a94", "a95", "a96", "a97", "a98", "a99", "a100", "a101", "a102", "a103", "a104", "a105", "a106", "a107", "a108", "a109", "a110", "a111", "a112", "a113", "a114", "a115", "a116", "a117", "a118", "a119", "a120", "a121", "a122", "a123", "a124", "a125", "a126", "a127", "a128", "a129", "a130", "a131", "a132", "a133", "a134", "a135", "a136", "a137", "a138", "a139", "a140", "a141", "a142", "a143", "a144", "a145", "a146", "a147", "a148", "a149", "a150", "a151", "a152", "a153", "a154", "a155", "a156", "a157", "a158", "a159", "a160", "a161", "a162", "a163", "a164", "a165", "a166", "a167", "a168", "a169", "a170", "a171", "a172", "a173", "a174", "a175", "a176", "a177", "a178", "a179", "a180", "a181", "a182", "a183", "a184", "a185", "a186", "a187", "a188", "a189", "a190", "a191", "a192", "a193", "a194", "a195", "a196", "a197", "a198", "a199", "a200", "a201", "a202", "a203", "a204", "a205", "a206", "a207", "a208", "a209", "a210", "a211", "a212", "a213", "a214", "a215", "a216", "a217", "a218", "a219", "a220", "a221", "a222", "a223", "a224", "a225", "a226", "a227", "a228", "a229", "a230", "a231", "a232", "a233", "a234", "a235", "a236", "a237", "a238", "a239", "a240", "a241", "a242", "a243", "a244", "a245", "a246", "a247", "a248", "a249", "a250", "a251", "a252", "a253", "a254", "a255", "s16", "s17", "s18", "s19", "s20", "s21", "s22", "s23", "s24", "s25", "s26", "s27", "s28", "s29", "s30", "s31", "s34", "s35", "s36", "s37", "s38", "s39", "s40", "s41", "s42", "s43", "s44", "s45", "s46", "s47", "s48", "s49", "s50", "s51", "s52", "s53", "s54", "s55", "s56", "s57", "s58", "s59", "s60", "s61", "s62", "s63", "s64", "s65", "s66", "s67", "s68", "s69", "s70", "s71", "s72", "s73", "s74", "s75", "s76", "s77", "s78", "s79", "s80", "s81", "s82", "s83", "s84", "s85", "s86", "s87", "s88", "s89", "s90", "s91", "s92", "s93", "s94", "s95", "s96", "s97", "s98", "s99", "vcc", "scc", "memory"
extern "C" __global__ __launch_bounds__(256, 1) void kf_block(const u16* x, const u16* ckv, const u16* wq, const u16* wuk, const u16* wuv, const u16* wo, const u16* w1, const u16* b1, const u16* w2, u16* out, char* ws) {
  extern __shared__ __attribute__((aligned(16))) char lds[];
  const unsigned wid = __builtin_amdgcn_readfirstlane(threadIdx.x >> 6);
  const unsigned ldsb = (unsigned)(size_t)lds;
  const unsigned bid = blockIdx.x;

  asm volatile(
  "L_start_%=:\n"
  "s_getpc_b64 s[60:61]\n"
  "L_pc_%=:\n"
  "s_lshr_b32 s36, %1, 3\n"
  "s_lshl_b32 s36, s36, 2\n"
  "s_add_u32 s36, s36, %2\n"
  "s_lshl_b32 s36, s36, 10\n"
  "s_min_u32 s36, s36, L_last_%=-L_pc_%=-1024\n"
  "v_mbcnt_lo_u32_b32 v235, -1, 0\n"
  "v_and_b32 v235, 7, v235\n"
  "v_lshlrev_b32 v235, 7, v235\n"
  "s_add_u32 s60, s60, s36\n"
  "s_addc_u32 s61, s61, 0\n"
  "global_load_dword v231, v235, s[60:61]\n"
  "s_load_dwordx16 s[64:79], %0, 0\n"
  "s_load_dwordx4 s[80:83], %0, 64\n"
  "s_load_dwordx2 s[84:85], %0, 80\n"
  "s_mov_b32 s86, %1\n"
  "s_mov_b32 s35, %2\n"
  "s_mov_b32 s38, %3\n"
  "s_and_b32 s39, %1, 7\n"
  "s_lshl_b32 s39, s39, 2\n"
  "s_lshr_b32 s36, %1, 3\n"
  "s_and_b32 s36, s36, 3\n"
  "s_add_u32 s39, s39, s36\n"
  "s_lshr_b32 s34, %1, 5\n"
  "v_mbcnt_lo_u32_b32 v233, -1, 0\n"
  "v_mbcnt_hi_u32_b32 v233, -1, v233\n"
  "s_lshl_b32 s36, s35, 12\n"
  "s_add_u32 s48, s38, s36\n"
  "v_lshrrev_b32 v234, 4, v233\n"
  "s_lshl_b32 s36, s35, 4\n"
  "v_add_u32 v235, s36, v234\n"
  "v_lshlrev_b32 v235, 13, v235\n"
  "v_lshlrev_b32 v234, 1, v234\n"
  "v_and_b32 v219, 15, v233\n"
  "v_xor_b32 v234, v234, v219\n"
  "v_lshl_add_u32 v220, v234, 4, v235\n"
  "v_lshl_add_u32 v221, v234, 4, v235\n"
  "v_add_u32 v221, 31744, v221\n"
  "v_xor_b32 v234, 8, v234\n"
  "v_lshl_add_u32 v222, v234, 4, v235\n"
  "v_add_u32 v222, 63488, v222\n"
  "v_lshl_add_u32 v223, v234, 4, v235\n"
  "v_add_u32 v223, 95232, v223\n"
  "s_lshl_b32 s36, s39, 8\n"
  "s_waitcnt lgkmcnt(0)\n"
  "s_add_u32 s22, s70, s36\n"
  "s_addc_u32 s23, s71, 0\n"
  "s_add_u32 s24, s72, s36\n"
  "s_addc_u32 s25, s73, 0\n"
  "s_add_u32 s28, s68, s36\n"
  "s_addc_u32 s29, s69, 0\n"
  "s_and_b32 s18, %1, 7\n"
  "s_lshr_b32 s19, s35, 1\n"
  "s_add_u32 s19, s19, s18\n"
  "s_add_u32 s98, s18, 0\n"
  "s_and_b32 s98, s98, 7\n"
  "s_mul_i32 s98, s98, 524288\n"
  "s_add_u32 s60, s22, s98\n"
  "s_addc_u32 s61, s23, 0\n"
  "s_add_u32 m0, s48, 0\n"
  "s_nop 0\n"
  "global_load_lds_dwordx4 v220, s[60:61] offset:0\n"
  "global_load_lds_dwordx4 v221, s[60:61] offset:1024\n"
  "global_load_lds_dwordx4 v222, s[60:61] offset:2048\n"
  "global_load_lds_dwordx4 v223, s[60:61] offset:3072\n"
  "v_lshlrev_b32 v232, 4, v233\n"
  "s_lshl_b32 s36, s34, 7\n"
  "s_sub_u32 s37, 1920, s36\n"
  "s_cmp_lt_u32 s39, 16\n"
  "s_cselect_b32 s59, s37, s36\n"
  "s_and_b32 s36, s39, 15\n"
  "s_lshl_b32 s36, s36, 3\n"
  "s_add_u32 s59, s59, s36\n"
  "v_and_b32 v234, 7, v233\n"
  "v_lshlrev_b32 v234, 10, v234\n"
  "v_lshrrev_b32 v235, 3, v233\n"
  "v_lshl_add_u32 v234, v235, 4, v234\n"
  "s_lshl_b32 s36, s59, 10\n"
  "s_lshl_b32 s37, s35, 8\n"
  "s_add_u32 s36, s36, s37\n"
  "s_add_u32 s30, s64, s36\n"
  "s_addc_u32 s31, s65, 0\n"
  "global_load_dwordx4 v[136:139], v234, s[30:31]\n"
  "global_load_dwordx4 v[140:143], v234, s[30:31] offset:128\n"
  "s_lshl_b32 s36, s34, 18\n"
  "s_add_u32 s52, s66, s36\n"
  "s_addc_u32 s53, s67, 0\n"
  "s_lshl_b32 s37, s35, 16\n"
  "s_add_u32 s20, s52, s37\n"
  "s_addc_u32 s21, s53, 0\n"
  "s_lshl_b32 s49, s35, 14\n"
  "s_add_u32 s49, s49, s38\n"
  "s_add_u32 s49, s49, 65536\n"
  "v_lshrrev_b32 v234, 4, v233\n"
  "v_and_b32 v235, 7, v233\n"
  "v_xor_b32 v234, v234, v235\n"
  "v_lshrrev_b32 v235, 3, v233\n"
  "v_lshlrev_b32 v235, 10, v235\n"
  "v_lshl_add_u32 v128, v234, 4, v235\n"
  "v_xor_b32 v129, 64, v128\n"
  "v_add_u32 v129, 7168, v129\n"
  "v_add_u32 v130, 14336, v128\n"
  "v_add_u32 v131, 14336, v129\n"
  "v_add_u32 v132, 32768, v128\n"
  "v_add_u32 v133, 32768, v129\n"
  "v_add_u32 v134, 47104, v128\n"
  "v_add_u32 v135, 47104, v129\n"
  "s_add_u32 s98, s18, 0\n"
  "s_and_b32 s98, s98, 7\n"
  "s_mul_i32 s98, s98, 128\n"
  "s_add_u32 s60, s20, s98\n"
  "s_addc_u32 s61, s21, 0\n"
  "s_add_u32 m0, s49, 0\n"
  "s_nop 0\n"
  "global_load_lds_dwordx4 v128, s[60:61] offset:0\n"
  "global_load_lds_dwordx4 v129, s[60:61] offset:1024\n"
  "global_load_lds_dwordx4 v130, s[60:61] offset:2048\n"
  "global_load_lds_dwordx4 v131, s[60:61] offset:3072\n"
  "s_add_u32 m0, s49, 4096\n"
  "s_nop 0\n"
  "global_load_lds_dwordx4 v132, s[60:61] offset:0\n"
  "global_load_lds_dwordx4 v133, s[60:61] offset:1024\n"
  "global_load_lds_dwordx4 v134, s[60:61] offset:2048\n"
  "global_load_lds_dwordx4 v135, s[60:61] offset:3072\n"
  "s_waitcnt vmcnt(9)\n"
  "v_lshlrev_b32 v144, 16, v136\n"
  "v_and_b32 v145, 0xffff0000, v136\n"
  "v_lshlrev_b32 v146, 16, v137\n"
  "v_and_b32 v147, 0xffff0000, v137\n"
  "v_lshlrev_b32 v148, 16, v138\n"
  "v_and_b32 v149, 0xffff0000, v138\n"
  "v_lshlrev_b32 v150, 16, v139\n"
  "v_and_b32 v151, 0xffff0000, v139\n"
  "s_waitcnt vmcnt(8)\n"
  "v_lshlrev_b32 v152, 16, v140\n"
  "v_and_b32 v153, 0xffff0000, v140\n"
  "v_lshlrev_b32 v154, 16, v141\n"
  "v_and_b32 v155, 0xffff0000, v141\n"
  "v_lshlrev_b32 v156, 16, v142\n"
  "v_and_b32 v157, 0xffff0000, v142\n"
  "v_lshlrev_b32 v158, 16, v143\n"
  "v_and_b32 v159, 0xffff0000, v143\n"
  "v_add_f32 v160, v144, v145\n"
  "v_add_f32 v161, v146, v147\n"
  "v_add_f32 v160, v160, v148\n"
  "v_add_f32 v161, v161, v149\n"
  "v_add_f32 v160, v160, v150\n"
  "v_add_f32 v161, v161, v151\n"
  "v_add_f32 v160, v160, v152\n"
  "v_add_f32 v161, v161, v153\n"
  "v_add_f32 v160, v160, v154\n"
  "v_add_f32 v161, v161, v155\n"
  "v_add_f32 v160, v160, v156\n"
  "v_add_f32 v161, v161, v157\n"
  "v_add_f32 v160, v160, v158\n"
  "v_add_f32 v161, v161, v159\n"
  "v_add_f32 v160, v160, v161\n"
  "v_and_b32 v172, 7, v233\n"
  "v_lshlrev_b32 v172, 5, v172\n"
  "v_mov_b32_dpp v161, v160 row_ror:8 row_mask:0xf bank_mask:0xf\n"
  "v_add_f32 v160, v160, v161\n"
  "v_mov_b32 v161, v160\n"
  "s_nop 1\n"
  "v_permlane16_swap_b32 v160, v161\n"
  "v_add_f32 v160, v160, v161\n"
  "v_mov_b32 v161, v160\n"
  "s_nop 1\n"
  "v_permlane32_swap_b32 v160, v161\n"
  "v_add_f32 v160, v160, v161\n"
  "v_mul_f32 v162, 0x3c000000, v160\n"
  "v_sub_f32 v144, v144, v162\n"
  "v_sub_f32 v145, v145, v162\n"
  "v_sub_f32 v146, v146, v162\n"
  "v_sub_f32 v147, v147, v162\n"
  "v_sub_f32 v148, v148, v162\n"
  "v_sub_f32 v149, v149, v162\n"
  "v_sub_f32 v150, v150, v162\n"
  "v_sub_f32 v151, v151, v162\n"
  "v_sub_f32 v152, v152, v162\n"
  "v_sub_f32 v153, v153, v162\n"
  "v_sub_f32 v154, v154, v162\n"
  "v_sub_f32 v155, v155, v162\n"
  "v_sub_f32 v156, v156, v162\n"
  "v_sub_f32 v157, v157, v162\n"
  "v_sub_f32 v158, v158, v162\n"
  "v_sub_f32 v159, v159, v162\n"
  "v_mul_f32 v163, v144, v144\n"
  "v_mul_f32 v161, v145, v145\n"
  "v_fmac_f32 v163, v146, v146\n"
  "v_fmac_f32 v161, v147, v147\n"
  "v_fmac_f32 v163, v148, v148\n"
  "v_fmac_f32 v161, v149, v149\n"
  "v_fmac_f32 v163, v150, v150\n"
  "v_fmac_f32 v161, v151, v151\n"
  "v_fmac_f32 v163, v152, v152\n"
  "v_fmac_f32 v161, v153, v153\n"
  "v_fmac_f32 v163, v154, v154\n"
  "v_fmac_f32 v161, v155, v155\n"
  "v_fmac_f32 v163, v156, v156\n"
  "v_fmac_f32 v161, v157, v157\n"
  "v_fmac_f32 v163, v158, v158\n"
  "v_fmac_f32 v161, v159, v159\n"
  "v_add_f32 v163, v163, v161\n"
  "v_add_u32 v172, s38, v172\n"
  "v_add_u32 v172, 0x20000, v172\n"
  "v_mov_b32_dpp v161, v163 row_ror:8 row_mask:0xf bank_mask:0xf\n"
  "v_add_f32 v163, v163, v161\n"
  "v_mov_b32 v161, v163\n"
  "s_nop 1\n"
  "v_permlane16_swap_b32 v163, v161\n"
  "v_add_f32 v163, v163, v161\n"
  "v_mov_b32 v161, v163\n"
  "s_nop 1\n"
  "v_permlane32_swap_b32 v163, v161\n"
  "v_add_f32 v163, v163, v161\n"
  "s_lshl_b32 s87, s35, 3\n"
  "v_add_u32 v173, s87, v172\n"
  "ds_write_b64 v173, v[162:163]\n"
  "s_add_u32 s98, s18, 1\n"
  "s_and_b32 s98, s98, 7\n"
  "s_mul_i32 s98, s98, 524288\n"
  "s_add_u32 s60, s22, s98\n"
  "s_addc_u32 s61, s23, 0\n"
  "s_add_u32 m0, s48, 16384\n"
  "s_nop 0\n"
  "global_load_lds_dwordx4 v220, s[60:61] offset:0\n"
  "global_load_lds_dwordx4 v221, s[60:61] offset:1024\n"
  "global_load_lds_dwordx4 v222, s[60:61] offset:2048\n"
  "global_load_lds_dwordx4 v223, s[60:61] offset:3072\n"
  "s_add_u32 s98, s18, 1\n"
  "s_and_b32 s98, s98, 7\n"
  "s_mul_i32 s98, s98, 128\n"
  "s_add_u32 s60, s20, s98\n"
  "s_addc_u32 s61, s21, 0\n"
  "s_add_u32 m0, s49, 8192\n"
  "s_nop 0\n"
  "global_load_lds_dwordx4 v128, s[60:61] offset:0\n"
  "global_load_lds_dwordx4 v129, s[60:61] offset:1024\n"
  "global_load_lds_dwordx4 v130, s[60:61] offset:2048\n"
  "global_load_lds_dwordx4 v131, s[60:61] offset:3072\n"
  "s_add_u32 m0, s49, 12288\n"
  "s_nop 0\n"
  "global_load_lds_dwordx4 v132, s[60:61] offset:0\n"
  "global_load_lds_dwordx4 v133, s[60:61] offset:1024\n"
  "global_load_lds_dwordx4 v134, s[60:61] offset:2048\n"
  "global_load_lds_dwordx4 v135, s[60:61] offset:3072\n"
  "s_add_u32 s62, s84, 104857600\n"
  "s_addc_u32 s63, s85, 0\n"
  "v_mov_b32 v186, 0x20000\n"
  "global_load_dwordx2 v[184:185], v186, s[62:63] sc1\n"
  "s_lshr_b32 s36, s86, 3\n"
  "s_and_b32 s36, s36, 3\n"
  "s_lshl_b32 s36, s36, 2\n"
  "s_add_u32 s36, s36, s35\n"
  "s_lshl_b32 s36, s36, 14\n"
  "v_and_b32 v176, 15, v233\n"
  "v_lshlrev_b32 v176, 10, v176\n"
  "v_add_u32 v176, s36, v176\n"
  "s_lshl_b32 s36, s34, 2\n"
  "s_add_u32 s36, s36, s35\n"
  "s_lshl_b32 s36, s36, 1\n"
  "v_bfe_u32 v177, v233, 2, 1\n"
  "v_lshlrev_b32 v177, 6, v177\n"
  "v_bfe_u32 v235, v233, 1, 1\n"
  "v_add3_u32 v177, v177, v235, s36\n"
  "v_lshlrev_b32 v177, 13, v177\n"
  "v_and_b32 v235, 1, v233\n"
  "v_lshl_add_u32 v177, v235, 7, v177\n"
  "v_lshrrev_b32 v234, 4, v233\n"
  "v_bfe_u32 v235, v233, 2, 2\n"
  "v_lshl_add_u32 v219, v234, 3, v235\n"
  "v_lshlrev_b32 v208, 8, v219\n"
  "v_lshlrev_b32 v216, 7, v219\n"
  "v_and_b32 v234, 1, v234\n"
  "v_lshl_add_u32 v219, v234, 2, v235\n"
  "v_lshl_add_u32 v208, v219, 5, v208\n"
  "v_lshrrev_b32 v235, 1, v235\n"
  "v_lshl_add_u32 v219, v234, 1, v235\n"
  "v_lshl_add_u32 v216, v219, 5, v216\n"
  "v_and_b32 v219, 3, v233\n"
  "v_lshlrev_b32 v219, 3, v219\n"
  "v_add_u32 v219, s38, v219\n"
  "v_add_u32 v208, v219, v208\n"
  "v_add_u32 v216, v219, v216\n"
  "v_xor_b32 v209, 32, v208\n"
  "v_xor_b32 v210, 64, v208\n"
  "v_xor_b32 v211, 96, v208\n"
  "v_xor_b32 v212, 128, v208\n"
  "v_xor_b32 v213, 160, v208\n"
  "v_xor_b32 v214, 192, v208\n"
  "v_xor_b32 v215, 224, v208\n"
  "v_and_b32 v234, 15, v233\n"
  "v_lshrrev_b32 v235, 1, v234\n"
  "v_lshrrev_b32 v219, 4, v233\n"
  "v_xor_b32 v235, v235, v219\n"
  "v_lshlrev_b32 v234, 7, v234\n"
  "v_lshl_add_u32 v229, v235, 4, v234\n"
  "v_xor_b32 v230, 64, v229\n"
  "v_add_u32 v229, s49, v229\n"
  "v_add_u32 v230, s49, v230\n"
  "v_add_u32 v178, 524288, v177\n"
  "v_mov_b32 v179, v177\n"
  "s_lshr_b32 s36, s59, 4\n"
  "s_lshl_b32 s36, s36, 14\n"
  "s_and_b32 s37, s59, 8\n"
  "s_lshl_b32 s37, s37, 4\n"
  "s_add_u32 s36, s36, s37\n"
  "s_lshl_b32 s37, s35, 12\n"
  "s_add_u32 s36, s36, s37\n"
  "s_add_u32 s30, s84, s36\n"
  "s_addc_u32 s31, s85, 0\n"
  "s_waitcnt vmcnt(13)\n"
  "ds_read_b128 a[0:3], v229 offset:0\n"
  "ds_read_b128 a[4:7], v229 offset:2048\n"
  "ds_read_b128 a[8:11], v229 offset:4096\n"
  "ds_read_b128 a[12:15], v229 offset:6144\n"
  "ds_read_b128 a[16:19], v230 offset:0\n"
  "ds_read_b128 a[20:23], v230 offset:2048\n"
  "ds_read_b128 a[24:27], v230 offset:4096\n"
  "ds_read_b128 a[28:31], v230 offset:6144\n"
  "s_waitcnt lgkmcnt(8)\n"
  "s_barrier\n"
  "ds_read_b64_tr_b16 v[192:193], v208 offset:0\n"
  "ds_read_b64_tr_b16 v[194:195], v208 offset:1024\n"
  "ds_read_b64_tr_b16 v[196:197], v208 offset:8192\n"
  "ds_read_b64_tr_b16 v[198:199], v208 offset:9216\n"
  "ds_read_b64_tr_b16 v[200:201], v209 offset:0\n"
  "ds_read_b64_tr_b16 v[202:203], v209 offset:1024\n"
  "ds_read_b64_tr_b16 v[204:205], v209 offset:8192\n"
  "ds_read_b64_tr_b16 v[206:207], v209 offset:9216\n"
  "L_stg0_%=:\n"
  "s_waitcnt lgkmcnt(6)\n"
  "v_mfma_f32_16x16x32_bf16 v[0:3], v[192:195], a[0:3], 0\n"
  "s_waitcnt lgkmcnt(4)\n"
  "v_mfma_f32_16x16x32_bf16 v[0:3], v[196:199], a[16:19], v[0:3]\n"
  "v_mfma_f32_16x16x32_bf16 v[4:7], v[196:199], a[20:23], 0\n"
  "v_mfma_f32_16x16x32_bf16 v[4:7], v[192:195], a[4:7], v[4:7]\n"
  "v_mfma_f32_16x16x32_bf16 v[8:11], v[192:195], a[8:11], 0\n"
  "s_add_u32 s98, s18, 2\n"
  "v_mfma_f32_16x16x32_bf16 v[8:11], v[196:199], a[24:27], v[8:11]\n"
  "v_mfma_f32_16x16x32_bf16 v[12:15], v[196:199], a[28:31], 0\n"
  "s_and_b32 s98, s98, 7\n"
  "ds_read_b64_tr_b16 v[196:197], v210 offset:0\n"
  "v_mfma_f32_16x16x32_bf16 v[12:15], v[192:195], a[12:15], v[12:15]\n"
  "ds_read_b64_tr_b16 v[198:199], v210 offset:1024\n"
  "s_waitcnt lgkmcnt(4)\n"
  "v_mfma_f32_16x16x32_bf16 v[28:31], v[200:203], a[12:15], 0\n"
  "s_mul_i32 s98, s98, 128\n"
  "ds_read_b64_tr_b16 v[192:193], v210 offset:8192\n"
  "s_waitcnt lgkmcnt(3)\n"
  "v_mfma_f32_16x16x32_bf16 v[28:31], v[204:207], a[28:31], v[28:31]\n"
  "s_add_u32 s60, s20, s98\n"
  "s_addc_u32 s61, s21, 0\n"
  "ds_read_b64_tr_b16 v[194:195], v210 offset:9216\n"
  "v_mfma_f32_16x16x32_bf16 v[24:27], v[204:207], a[24:27], 0\n"
  "ds_read_b128 v[164:167], v172\n"
  "v_mfma_f32_16x16x32_bf16 v[24:27], v[200:203], a[8:11], v[24:27]\n"
  "s_add_u32 m0, s49, 0\n"
  "ds_read_b128 v[168:171], v172 offset:16\n"
  "v_mfma_f32_16x16x32_bf16 v[20:23], v[200:203], a[4:7], 0\n"
  "v_mfma_f32_16x16x32_bf16 v[20:23], v[204:207], a[20:23], v[20:23]\n"
  "global_load_lds_dwordx4 v128, s[60:61] offset:0\n"
  "v_and_b32 v172, 7, v233\n"
  "v_mfma_f32_16x16x32_bf16 v[16:19], v[204:207], a[16:19], 0\n"
  "ds_read_b64_tr_b16 v[204:205], v211 offset:0\n"
  "v_mfma_f32_16x16x32_bf16 v[16:19], v[200:203], a[0:3], v[16:19]\n"
  "ds_read_b64_tr_b16 v[206:207], v211 offset:1024\n"
  "v_lshlrev_b32 v172, 4, v172\n"
  "s_waitcnt lgkmcnt(6)\n"
  "v_mfma_f32_16x16x32_bf16 v[32:35], v[196:199], a[0:3], 0\n"
  "ds_read_b64_tr_b16 v[200:201], v211 offset:8192\n"
  "v_lshrrev_b32 v173, 3, v233\n"
  "s_waitcnt lgkmcnt(5)\n"
  "v_mfma_f32_16x16x32_bf16 v[32:35], v[192:195], a[16:19], v[32:35]\n"
  "ds_read_b64_tr_b16 v[202:203], v211 offset:9216\n"
  "v_mfma_f32_16x16x32_bf16 v[36:39], v[192:195], a[20:23], 0\n"
  "global_load_lds_dwordx4 v129, s[60:61] offset:1024\n"
  "v_lshl_add_u32 v172, v173, 8, v172\n"
  "v_mfma_f32_16x16x32_bf16 v[36:39], v[196:199], a[4:7], v[36:39]\n"
  "global_load_lds_dwordx4 v130, s[60:61] offset:2048\n"
  "v_mfma_f32_16x16x32_bf16 v[40:43], v[196:199], a[8:11], 0\n"
  "global_load_lds_dwordx4 v131, s[60:61] offset:3072\n"
  "s_add_u32 m0, s49, 4096\n"
  "s_waitcnt lgkmcnt(5)\n"
  "v_add_f32 v160, v164, v166\n"
  "v_mfma_f32_16x16x32_bf16 v[40:43], v[192:195], a[24:27], v[40:43]\n"
  "s_waitcnt lgkmcnt(4)\n"
  "v_add_f32 v161, v168, v170\n"
  "v_mfma_f32_16x16x32_bf16 v[44:47], v[192:195], a[28:31], 0\n"
  "ds_read_b64_tr_b16 v[192:193], v212 offset:0\n"
  "v_mfma_f32_16x16x32_bf16 v[44:47], v[196:199], a[12:15], v[44:47]\n"
  "ds_read_b64_tr_b16 v[194:195], v212 offset:1024\n"
  "v_add_f32 v160, v160, v161\n"
  "s_waitcnt lgkmcnt(4)\n"
  "v_mfma_f32_16x16x32_bf16 v[60:63], v[204:207], a[12:15], 0\n"
  "ds_read_b64_tr_b16 v[196:197], v212 offset:8192\n"
  "s_waitcnt lgkmcnt(3)\n"
  "v_mfma_f32_16x16x32_bf16 v[60:63], v[200:203], a[28:31], v[60:63]\n"
  "ds_read_b64_tr_b16 v[198:199], v212 offset:9216\n"
  "v_mul_f32 v160, 0x3e800000, v160\n"
  "v_mfma_f32_16x16x32_bf16 v[56:59], v[200:203], a[24:27], 0\n"
  "global_load_lds_dwordx4 v132, s[60:61] offset:0\n"
  "v_add_f32 v163, v165, v167\n"
  "v_mfma_f32_16x16x32_bf16 v[56:59], v[204:207], a[8:11], v[56:59]\n"
  "global_load_lds_dwordx4 v133, s[60:61] offset:1024\n"
  "v_mfma_f32_16x16x32_bf16 v[52:55], v[204:207], a[4:7], 0\n"
  "global_load_lds_dwordx4 v134, s[60:61] offset:2048\n"
  "v_add_f32 v161, v169, v171\n"
  "v_mfma_f32_16x16x32_bf16 v[52:55], v[200:203], a[20:23], v[52:55]\n"
  "global_load_lds_dwordx4 v135, s[60:61] offset:3072\n"
  "s_add_u32 s98, s18, 2\n"
  "v_mfma_f32_16x16x32_bf16 v[48:51], v[200:203], a[16:19], 0\n"
  "s_and_b32 s98, s98, 7\n"
  "ds_read_b64_tr_b16 v[200:201], v213 offset:0\n"
  "v_add_f32 v163, v163, v161\n"
  "v_mfma_f32_16x16x32_bf16 v[48:51], v[204:207], a[0:3], v[48:51]\n"
  "ds_read_b64_tr_b16 v[202:203], v213 offset:1024\n"
  "v_sub_f32 v164, v164, v160\n"
  "s_waitcnt lgkmcnt(4)\n"
  "v_mfma_f32_16x16x32_bf16 v[64:67], v[192:195], a[0:3], 0\n"
  "s_mul_i32 s98, s98, 524288\n"
  "ds_read_b64_tr_b16 v[204:205], v213 offset:8192\n"
  "s_waitcnt lgkmcnt(3)\n"
  "v_mfma_f32_16x16x32_bf16 v[64:67], v[196:199], a[16:19], v[64:67]\n"
  "ds_read_b64_tr_b16 v[206:207], v213 offset:9216\n"
  "v_sub_f32 v166, v166, v160\n"
  "v_mfma_f32_16x16x32_bf16 v[68:71], v[196:199], a[20:23], 0\n"
  "s_add_u32 s60, s22, s98\n"
  "s_addc_u32 s61, s23, 0\n"
  "v_mfma_f32_16x16x32_bf16 v[68:71], v[192:195], a[4:7], v[68:71]\n"
  "s_add_u32 m0, s48, 32768\n"
  "v_sub_f32 v168, v168, v160\n"
  "v_mfma_f32_16x16x32_bf16 v[72:75], v[192:195], a[8:11], 0\n"
  "v_sub_f32 v170, v170, v160\n"
  "v_mfma_f32_16x16x32_bf16 v[72:75], v[196:199], a[24:27], v[72:75]\n"
  "global_load_lds_dwordx4 v220, s[60:61] offset:0\n"
  "v_mfma_f32_16x16x32_bf16 v[76:79], v[196:199], a[28:31], 0\n"
  "ds_read_b64_tr_b16 v[196:197], v214 offset:0\n"
  "v_mul_f32 v161, v164, v164\n"
  "v_mfma_f32_16x16x32_bf16 v[76:79], v[192:195], a[12:15], v[76:79]\n"
  "ds_read_b64_tr_b16 v[198:199], v214 offset:1024\n"
  "s_waitcnt lgkmcnt(4)\n"
  "v_mfma_f32_16x16x32_bf16 v[92:95], v[200:203], a[12:15], 0\n"
  "s_waitcnt vmcnt(10)\n"
  "ds_read_b64_tr_b16 v[192:193], v214 offset:8192\n"
  "v_fmac_f32 v161, v166, v166\n"
  "s_waitcnt lgkmcnt(3)\n"
  "v_mfma_f32_16x16x32_bf16 v[92:95], v[204:207], a[28:31], v[92:95]\n"
  "ds_read_b64_tr_b16 v[194:195], v214 offset:9216\n"
  "v_fmac_f32 v161, v168, v168\n"
  "v_mfma_f32_16x16x32_bf16 v[88:91], v[204:207], a[24:27], 0\n"
  "ds_read_b128 a[32:35], v229 offset:8192\n"
  "v_mfma_f32_16x16x32_bf16 v[88:91], v[200:203], a[8:11], v[88:91]\n"
  "global_load_lds_dwordx4 v221, s[60:61] offset:1024\n"
  "v_fmac_f32 v161, v170, v170\n"
  "v_mfma_f32_16x16x32_bf16 v[84:87], v[200:203], a[4:7], 0\n"
  "ds_read_b128 a[36:39], v229 offset:10240\n"
  "v_mfma_f32_16x16x32_bf16 v[84:87], v[204:207], a[20:23], v[84:87]\n"
  "global_load_lds_dwordx4 v222, s[60:61] offset:2048\n"
  "v_fmac_f32 v163, 0x43000000, v161\n"
  "v_mfma_f32_16x16x32_bf16 v[80:83], v[204:207], a[16:19], 0\n"
  "ds_read_b64_tr_b16 v[204:205], v215 offset:0\n"
  "v_mov_b32 v161, 0x3727c5ac\n"
  "v_mfma_f32_16x16x32_bf16 v[80:83], v[200:203], a[0:3], v[80:83]\n"
  "ds_read_b64_tr_b16 v[206:207], v215 offset:1024\n"
  "s_waitcnt lgkmcnt(6)\n"
  "v_mfma_f32_16x16x32_bf16 v[96:99], v[196:199], a[0:3], 0\n"
  "ds_read_b64_tr_b16 v[200:201], v215 offset:8192\n"
  "v_fmac_f32 v161, 0x3b000000, v163\n"
  "s_waitcnt lgkmcnt(5)\n"
  "v_mfma_f32_16x16x32_bf16 v[96:99], v[192:195], a[16:19], v[96:99]\n"
  "ds_read_b64_tr_b16 v[202:203], v215 offset:9216\n"
  "v_mfma_f32_16x16x32_bf16 v[100:103], v[192:195], a[20:23], 0\n"
  "ds_read_b128 a[40:43], v229 offset:12288\n"
  "v_mfma_f32_16x16x32_bf16 v[100:103], v[196:199], a[4:7], v[100:103]\n"
  "ds_read_b128 a[44:47], v229 offset:14336\n"
  "v_mfma_f32_16x16x32_bf16 v[104:107], v[196:199], a[8:11], 0\n"
  "ds_read_b128 a[48:51], v230 offset:8192\n"
  "v_mfma_f32_16x16x32_bf16 v[104:107], v[192:195], a[24:27], v[104:107]\n"
  "ds_read_b128 a[52:55], v230 offset:10240\n"
  "v_mfma_f32_16x16x32_bf16 v[108:111], v[192:195], a[28:31], 0\n"
  "s_barrier\n"
  "ds_read_b64_tr_b16 v[192:193], v208 offset:16384\n"
  "v_mfma_f32_16x16x32_bf16 v[108:111], v[196:199], a[12:15], v[108:111]\n"
  "ds_read_b64_tr_b16 v[194:195], v208 offset:17408\n"
  "s_waitcnt lgkmcnt(8)\n"
  "v_mfma_f32_16x16x32_bf16 v[124:127], v[204:207], a[12:15], 0\n"
  "ds_read_b64_tr_b16 v[196:197], v208 offset:24576\n"
  "s_waitcnt lgkmcnt(7)\n"
  "v_mfma_f32_16x16x32_bf16 v[124:127], v[200:203], a[28:31], v[124:127]\n"
  "ds_read_b64_tr_b16 v[198:199], v208 offset:25600\n"
  "v_mfma_f32_16x16x32_bf16 v[120:123], v[200:203], a[24:27], 0\n"
  "ds_read_b128 a[56:59], v230 offset:12288\n"
  "v_mfma_f32_16x16x32_bf16 v[120:123], v[204:207], a[8:11], v[120:123]\n"
  "ds_read_b128 a[60:63], v230 offset:14336\n"
  "v_mfma_f32_16x16x32_bf16 v[116:119], v[204:207], a[4:7], 0\n"
  "global_load_lds_dwordx4 v223, s[60:61] offset:3072\n"
  "s_add_u32 s98, s18, 3\n"
  "s_and_b32 s98, s98, 7\n"
  "v_mfma_f32_16x16x32_bf16 v[116:119], v[200:203], a[20:23], v[116:119]\n"
  "s_mul_i32 s98, s98, 524288\n"
  "s_add_u32 s60, s22, s98\n"
  "s_addc_u32 s61, s23, 0\n"
  "v_mfma_f32_16x16x32_bf16 v[112:115], v[200:203], a[16:19], 0\n"
  "s_add_u32 m0, s48, 49152\n"
  "ds_read_b64_tr_b16 v[200:201], v209 offset:16384\n"
  "v_mfma_f32_16x16x32_bf16 v[112:115], v[204:207], a[0:3], v[112:115]\n"
  "ds_read_b64_tr_b16 v[202:203], v209 offset:17408\n"
  "global_load_lds_dwordx4 v220, s[60:61] offset:0\n"
  "global_load_lds_dwordx4 v221, s[60:61] offset:1024\n"
  "global_load_lds_dwordx4 v222, s[60:61] offset:2048\n"
  "global_load_lds_dwordx4 v223, s[60:61] offset:3072\n"
  "v_rsq_f32 v161, v161\n"
  "v_sub_f32 v162, v162, v160\n"
  "v_mul_f32 v162, v162, v161\n"
  "v_fma_f32 v144, v144, v161, v162\n"
  "v_fma_f32 v145, v145, v161, v162\n"
  "v_fma_f32 v146, v146, v161, v162\n"
  "L_stg1_%=:\n"
  "s_waitcnt lgkmcnt(6)\n"
  "v_mfma_f32_16x16x32_bf16 v[0:3], v[192:195], a[32:35], v[0:3]\n"
  "ds_read_b64_tr_b16 v[204:205], v209 offset:24576\n"
  "s_waitcnt lgkmcnt(5)\n"
  "v_mfma_f32_16x16x32_bf16 v[0:3], v[196:199], a[48:51], v[0:3]\n"
  "s_barrier\n"
  "ds_read_b64_tr_b16 v[206:207], v209 offset:25600\n"
  "v_mfma_f32_16x16x32_bf16 v[4:7], v[196:199], a[52:55], v[4:7]\n"
  "v_fma_f32 v147, v147, v161, v162\n"
  "v_mfma_f32_16x16x32_bf16 v[4:7], v[192:195], a[36:39], v[4:7]\n"
  "s_waitcnt lgkmcnt(4)\n"
  "v_mfma_f32_16x16x32_bf16 v[8:11], v[192:195], a[40:43], v[8:11]\n"
  "v_fma_f32 v148, v148, v161, v162\n"
  "v_mfma_f32_16x16x32_bf16 v[8:11], v[196:199], a[56:59], v[8:11]\n"
  "s_add_u32 s98, s18, 3\n"
  "v_mfma_f32_16x16x32_bf16 v[12:15], v[196:199], a[60:63], v[12:15]\n"
  "ds_read_b64_tr_b16 v[196:197], v210 offset:16384\n"
  "v_fma_f32 v149, v149, v161, v162\n"
  "v_mfma_f32_16x16x32_bf16 v[12:15], v[192:195], a[44:47], v[12:15]\n"
  "s_and_b32 s98, s98, 7\n"
  "ds_read_b64_tr_b16 v[198:199], v210 offset:17408\n"
  "s_waitcnt lgkmcnt(4)\n"
  "v_mfma_f32_16x16x32_bf16 v[28:31], v[200:203], a[44:47], v[28:31]\n"
  "ds_read_b64_tr_b16 v[192:193], v210 offset:24576\n"
  "s_waitcnt lgkmcnt(3)\n"
  "v_mfma_f32_16x16x32_bf16 v[28:31], v[204:207], a[60:63], v[28:31]\n"
  "ds_read_b64_tr_b16 v[194:195], v210 offset:25600\n"
  "v_fma_f32 v150, v150, v161, v162\n"
  "v_mfma_f32_16x16x32_bf16 v[24:27], v[204:207], a[56:59], v[24:27]\n"
  "s_mul_i32 s98, s98, 128\n"
  "v_mfma_f32_16x16x32_bf16 v[24:27], v[200:203], a[40:43], v[24:27]\n"
  "v_fma_f32 v151, v151, v161, v162\n"
  "v_mfma_f32_16x16x32_bf16 v[20:23], v[200:203], a[36:39], v[20:23]\n"
  "s_add_u32 s60, s20, s98\n"
  "s_addc_u32 s61, s21, 0\n"
  "v_mfma_f32_16x16x32_bf16 v[20:23], v[204:207], a[52:55], v[20:23]\n"
  "v_mfma_f32_16x16x32_bf16 v[16:19], v[204:207], a[48:51], v[16:19]\n"
  "s_add_u32 m0, s49, 8192\n"
  "ds_read_b64_tr_b16 v[204:205], v211 offset:16384\n"
  "v_fma_f32 v152, v152, v161, v162\n"
  "v_mfma_f32_16x16x32_bf16 v[16:19], v[200:203], a[32:35], v[16:19]\n"
  "ds_read_b64_tr_b16 v[206:207], v211 offset:17408\n"
  "s_waitcnt lgkmcnt(4)\n"
  "v_mfma_f32_16x16x32_bf16 v[32:35], v[196:199], a[32:35], v[32:35]\n"
  "ds_read_b64_tr_b16 v[200:201], v211 offset:24576\n"
  "v_fma_f32 v153, v153, v161, v162\n"
  "s_waitcnt lgkmcnt(3)\n"
  "v_mfma_f32_16x16x32_bf16 v[32:35], v[192:195], a[48:51], v[32:35]\n"
  "ds_read_b64_tr_b16 v[202:203], v211 offset:25600\n"
  "v_mfma_f32_16x16x32_bf16 v[36:39], v[192:195], a[52:55], v[36:39]\n"
  "global_load_lds_dwordx4 v128, s[60:61] offset:0\n"
  "v_mfma_f32_16x16x32_bf16 v[36:39], v[196:199], a[36:39], v[36:39]\n"
  "global_load_lds_dwordx4 v129, s[60:61] offset:1024\n"
  "v_fma_f32 v154, v154, v161, v162\n"
  "v_mfma_f32_16x16x32_bf16 v[40:43], v[196:199], a[40:43], v[40:43]\n"
  "v_mfma_f32_16x16x32_bf16 v[40:43], v[192:195], a[56:59], v[40:43]\n"
  "v_fma_f32 v155, v155, v161, v162\n"
  "v_mfma_f32_16x16x32_bf16 v[44:47], v[192:195], a[60:63], v[44:47]\n"
  "ds_read_b64_tr_b16 v[192:193], v212 offset:16384\n"
  "v_mfma_f32_16x16x32_bf16 v[44:47], v[196:199], a[44:47], v[44:47]\n"
  "ds_read_b64_tr_b16 v[194:195], v212 offset:17408\n"
  "v_fma_f32 v156, v156, v161, v162\n"
  "s_waitcnt lgkmcnt(4)\n"
  "v_mfma_f32_16x16x32_bf16 v[60:63], v[204:207], a[44:47], v[60:63]\n"
  "ds_read_b64_tr_b16 v[196:197], v212 offset:24576\n"
  "s_waitcnt lgkmcnt(3)\n"
  "v_mfma_f32_16x16x32_bf16 v[60:63], v[200:203], a[60:63], v[60:63]\n"
  "ds_read_b64_tr_b16 v[198:199], v212 offset:25600\n"
  "v_mfma_f32_16x16x32_bf16 v[56:59], v[200:203], a[56:59], v[56:59]\n"
  "global_load_lds_dwordx4 v130, s[60:61] offset:2048\n"
  "v_fma_f32 v157, v157, v161, v162\n"
  "v_mfma_f32_16x16x32_bf16 v[56:59], v[204:207], a[40:43], v[56:59]\n"
  "global_load_lds_dwordx4 v131, s[60:61] offset:3072\n"
  "s_add_u32 m0, s49, 12288\n"
  "v_mfma_f32_16x16x32_bf16 v[52:55], v[204:207], a[36:39], v[52:55]\n"
  "v_fma_f32 v158, v158, v161, v162\n"
  "v_mfma_f32_16x16x32_bf16 v[52:55], v[200:203], a[52:55], v[52:55]\n"
  "global_load_lds_dwordx4 v132, s[60:61] offset:0\n"
  "v_mfma_f32_16x16x32_bf16 v[48:51], v[200:203], a[48:51], v[48:51]\n"
  "ds_read_b64_tr_b16 v[200:201], v213 offset:16384\n"
  "v_mfma_f32_16x16x32_bf16 v[48:51], v[204:207], a[32:35], v[48:51]\n"
  "ds_read_b64_tr_b16 v[202:203], v213 offset:17408\n"
  "v_fma_f32 v159, v159, v161, v162\n"
  "s_waitcnt lgkmcnt(4)\n"
  "v_mfma_f32_16x16x32_bf16 v[64:67], v[192:195], a[32:35], v[64:67]\n"
  "ds_read_b64_tr_b16 v[204:205], v213 offset:24576\n"
  "s_waitcnt lgkmcnt(3)\n"
  "v_mfma_f32_16x16x32_bf16 v[64:67], v[196:199], a[48:51], v[64:67]\n"
  "ds_read_b64_tr_b16 v[206:207], v213 offset:25600\n"
  "v_cvt_pk_bf16_f32 v136, v144, v145\n"
  "v_mfma_f32_16x16x32_bf16 v[68:71], v[196:199], a[52:55], v[68:71]\n"
  "global_load_lds_dwordx4 v133, s[60:61] offset:1024\n"
  "v_mfma_f32_16x16x32_bf16 v[68:71], v[192:195], a[36:39], v[68:71]\n"
  "global_load_lds_dwordx4 v134, s[60:61] offset:2048\n"
  "v_mfma_f32_16x16x32_bf16 v[72:75], v[192:195], a[40:43], v[72:75]\n"
  "global_load_lds_dwordx4 v135, s[60:61] offset:3072\n"
  "v_cvt_pk_bf16_f32 v137, v146, v147\n"
  "v_mfma_f32_16x16x32_bf16 v[72:75], v[196:199], a[56:59], v[72:75]\n"
  "v_mfma_f32_16x16x32_bf16 v[76:79], v[196:199], a[60:63], v[76:79]\n"
  "s_add_u32 s98, s18, 4\n"
  "ds_read_b64_tr_b16 v[196:197], v214 offset:16384\n"
  "v_cvt_pk_bf16_f32 v138, v148, v149\n"
  "v_mfma_f32_16x16x32_bf16 v[76:79], v[192:195], a[44:47], v[76:79]\n"
  "ds_read_b64_tr_b16 v[198:199], v214 offset:17408\n"
  "s_waitcnt lgkmcnt(4)\n"
  "v_mfma_f32_16x16x32_bf16 v[92:95], v[200:203], a[44:47], v[92:95]\n"
  "s_waitcnt vmcnt(16)\n"
  "ds_read_b64_tr_b16 v[192:193], v214 offset:24576\n"
  "v_cvt_pk_bf16_f32 v139, v150, v151\n"
  "s_waitcnt lgkmcnt(3)\n"
  "v_mfma_f32_16x16x32_bf16 v[92:95], v[204:207], a[60:63], v[92:95]\n"
  "s_and_b32 s98, s98, 7\n"
  "ds_read_b64_tr_b16 v[194:195], v214 offset:25600\n"
  "v_mfma_f32_16x16x32_bf16 v[88:91], v[204:207], a[56:59], v[88:91]\n"
  "ds_read_b128 a[64:67], v229 offset:0\n"
  "v_mfma_f32_16x16x32_bf16 v[88:91], v[200:203], a[40:43], v[88:91]\n"
  "s_mul_i32 s98, s98, 524288\n"
  "v_cvt_pk_bf16_f32 v140, v152, v153\n"
  "v_mfma_f32_16x16x32_bf16 v[84:87], v[200:203], a[36:39], v[84:87]\n"
  "ds_read_b128 a[68:71], v229 offset:2048\n"
  "v_mfma_f32_16x16x32_bf16 v[84:87], v[204:207], a[52:55], v[84:87]\n"
  "s_add_u32 s60, s22, s98\n"
  "s_addc_u32 s61, s23, 0\n"
  "v_cvt_pk_bf16_f32 v141, v154, v155\n"
  "v_mfma_f32_16x16x32_bf16 v[80:83], v[204:207], a[48:51], v[80:83]\n"
  "ds_read_b64_tr_b16 v[204:205], v215 offset:16384\n"
  "v_mfma_f32_16x16x32_bf16 v[80:83], v[200:203], a[32:35], v[80:83]\n"
  "ds_read_b64_tr_b16 v[206:207], v215 offset:17408\n"
  "s_waitcnt lgkmcnt(6)\n"
  "v_mfma_f32_16x16x32_bf16 v[96:99], v[196:199], a[32:35], v[96:99]\n"
  "s_add_u32 m0, s48, 0\n"
  "ds_read_b64_tr_b16 v[200:201], v215 offset:24576\n"
  "v_cvt_pk_bf16_f32 v142, v156, v157\n"
  "s_waitcnt lgkmcnt(5)\n"
  "v_mfma_f32_16x16x32_bf16 v[96:99], v[192:195], a[48:51], v[96:99]\n"
  "ds_read_b64_tr_b16 v[202:203], v215 offset:25600\n"
  "v_mfma_f32_16x16x32_bf16 v[100:103], v[192:195], a[52:55], v[100:103]\n"
  "ds_read_b128 a[72:75], v229 offset:4096\n"
  "v_cvt_pk_bf16_f32 v143, v158, v159\n"
  "v_mfma_f32_16x16x32_bf16 v[100:103], v[196:199], a[36:39], v[100:103]\n"
  "ds_read_b128 a[76:79], v229 offset:6144\n"
  "v_mfma_f32_16x16x32_bf16 v[104:107], v[196:199], a[40:43], v[104:107]\n"
  "ds_read_b128 a[80:83], v230 offset:0\n"
  "v_mfma_f32_16x16x32_bf16 v[104:107], v[192:195], a[56:59], v[104:107]\n"
  "ds_read_b128 a[84:87], v230 offset:2048\n"
  "v_mfma_f32_16x16x32_bf16 v[108:111], v[192:195], a[60:63], v[108:111]\n"
  "s_waitcnt vmcnt(12)\n"
  "s_barrier\n"
  "ds_read_b64_tr_b16 v[192:193], v208 offset:32768\n"
  "v_mfma_f32_16x16x32_bf16 v[108:111], v[196:199], a[44:47], v[108:111]\n"
  "ds_read_b64_tr_b16 v[194:195], v208 offset:33792\n"
  "s_waitcnt lgkmcnt(8)\n"
  "v_mfma_f32_16x16x32_bf16 v[124:127], v[204:207], a[44:47], v[124:127]\n"
  "ds_read_b64_tr_b16 v[196:197], v208 offset:40960\n"
  "s_waitcnt lgkmcnt(7)\n"
  "v_mfma_f32_16x16x32_bf16 v[124:127], v[200:203], a[60:63], v[124:127]\n"
  "ds_read_b64_tr_b16 v[198:199], v208 offset:41984\n"
  "v_mfma_f32_16x16x32_bf16 v[120:123], v[200:203], a[56:59], v[120:123]\n"
  "ds_read_b128 a[88:91], v230 offset:4096\n"
  "v_mfma_f32_16x16x32_bf16 v[120:123], v[204:207], a[40:43], v[120:123]\n"
  "ds_read_b128 a[92:95], v230 offset:6144\n"
  "v_mfma_f32_16x16x32_bf16 v[116:119], v[204:207], a[36:39], v[116:119]\n"
  "global_load_lds_dwordx4 v220, s[60:61] offset:0\n"
  "v_mfma_f32_16x16x32_bf16 v[116:119], v[200:203], a[52:55], v[116:119]\n"
  "global_load_lds_dwordx4 v221, s[60:61] offset:1024\n"
  "v_mfma_f32_16x16x32_bf16 v[112:115], v[200:203], a[48:51], v[112:115]\n"
  "ds_read_b64_tr_b16 v[200:201], v209 offset:32768\n"
  "v_mfma_f32_16x16x32_bf16 v[112:115], v[204:207], a[32:35], v[112:115]\n"
  "ds_read_b64_tr_b16 v[202:203], v209 offset:33792\n"
  "global_store_dwordx4 v172, v[136:139], s[30:31] sc1\n"
  "global_load_lds_dwordx4 v222, s[60:61] offset:2048\n"
  "global_store_dwordx4 v172, v[140:143], s[30:31] offset:2048 sc1\n"
  "global_load_lds_dwordx4 v223, s[60:61] offset:3072\n"
  "L_stg2_%=:\n"
  "s_waitcnt lgkmcnt(6)\n"
  "v_mfma_f32_16x16x32_bf16 v[0:3], v[192:195], a[64:67], v[0:3]\n"
  "ds_read_b64_tr_b16 v[204:205], v209 offset:40960\n"
  "s_waitcnt lgkmcnt(5)\n"
  "v_mfma_f32_16x16x32_bf16 v[0:3], v[196:199], a[80:83], v[0:3]\n"
  "s_barrier\n"
  "ds_read_b64_tr_b16 v[206:207], v209 offset:41984\n"
  "v_mfma_f32_16x16x32_bf16 v[4:7], v[196:199], a[84:87], v[4:7]\n"
  "v_lshrrev_b32 v181, 3, v233\n"
  "v_mfma_f32_16x16x32_bf16 v[4:7], v[192:195], a[68:71], v[4:7]\n"
  "s_waitcnt lgkmcnt(4)\n"
  "v_mfma_f32_16x16x32_bf16 v[8:11], v[192:195], a[72:75], v[8:11]\n"
  "s_and_b32 s87, s35, 1\n"
  "v_mfma_f32_16x16x32_bf16 v[8:11], v[196:199], a[88:91], v[8:11]\n"
  "s_add_u32 s98, s18, 4\n"
  "v_mfma_f32_16x16x32_bf16 v[12:15], v[196:199], a[92:95], v[12:15]\n"
  "ds_read_b64_tr_b16 v[196:197], v210 offset:32768\n"
  "v_mfma_f32_16x16x32_bf16 v[12:15], v[192:195], a[76:79], v[12:15]\n"
  "s_and_b32 s98, s98, 7\n"
  "s_lshl_b32 s87, s87, 5\n"
  "ds_read_b64_tr_b16 v[198:199], v210 offset:33792\n"
  "s_waitcnt lgkmcnt(4)\n"
  "v_mfma_f32_16x16x32_bf16 v[28:31], v[200:203], a[76:79], v[28:31]\n"
  "ds_read_b64_tr_b16 v[192:193], v210 offset:40960\n"
  "v_readfirstlane_b32 s16, v184\n"
  "s_waitcnt lgkmcnt(3)\n"
  "v_mfma_f32_16x16x32_bf16 v[28:31], v[204:207], a[92:95], v[28:31]\n"
  "ds_read_b64_tr_b16 v[194:195], v210 offset:41984\n"
  "v_mfma_f32_16x16x32_bf16 v[24:27], v[204:207], a[88:91], v[24:27]\n"
  "s_mul_i32 s98, s98, 128\n"
  "v_readfirstlane_b32 s17, v185\n"
  "v_add_u32 v181, s87, v181\n"
  "v_mfma_f32_16x16x32_bf16 v[24:27], v[200:203], a[72:75], v[24:27]\n"
  "v_mfma_f32_16x16x32_bf16 v[20:23], v[200:203], a[68:71], v[20:23]\n"
  "s_add_u32 s60, s20, s98\n"
  "s_addc_u32 s61, s21, 0\n"
  "s_add_u32 s16, s16, 1\n"
  "v_mfma_f32_16x16x32_bf16 v[20:23], v[204:207], a[84:87], v[20:23]\n"
  "v_mul_u32_u24 v181, 8192, v181\n"
  "v_mfma_f32_16x16x32_bf16 v[16:19], v[204:207], a[80:83], v[16:19]\n"
  "s_add_u32 m0, s49, 0\n"
  "ds_read_b64_tr_b16 v[204:205], v211 offset:32768\n"
  "v_mfma_f32_16x16x32_bf16 v[16:19], v[200:203], a[64:67], v[16:19]\n"
  "ds_read_b64_tr_b16 v[206:207], v211 offset:33792\n"
  "v_bfe_u32 v180, v233, 4, 1\n"
  "s_waitcnt lgkmcnt(4)\n"
  "v_mfma_f32_16x16x32_bf16 v[32:35], v[196:199], a[64:67], v[32:35]\n"
  "ds_read_b64_tr_b16 v[200:201], v211 offset:40960\n"
  "s_waitcnt lgkmcnt(3)\n"
  "v_mfma_f32_16x16x32_bf16 v[32:35], v[192:195], a[80:83], v[32:35]\n"
  "ds_read_b64_tr_b16 v[202:203], v211 offset:41984\n"
  "v_mfma_f32_16x16x32_bf16 v[36:39], v[192:195], a[84:87], v[36:39]\n"
  "global_load_lds_dwordx4 v128, s[60:61] offset:0\n"
  "v_lshlrev_b32 v180, 1, v180\n"
  "v_mfma_f32_16x16x32_bf16 v[36:39], v[196:199], a[68:71], v[36:39]\n"
  "global_load_lds_dwordx4 v129, s[60:61] offset:1024\n"
  "v_mfma_f32_16x16x32_bf16 v[40:43], v[196:199], a[72:75], v[40:43]\n"
  "v_mfma_f32_16x16x32_bf16 v[40:43], v[192:195], a[88:91], v[40:43]\n"
  "v_and_b32 v182, 7, v233\n"
  "v_mfma_f32_16x16x32_bf16 v[44:47], v[192:195], a[92:95], v[44:47]\n"
  "ds_read_b64_tr_b16 v[192:193], v212 offset:32768\n"
  "v_mfma_f32_16x16x32_bf16 v[44:47], v[196:199], a[76:79], v[44:47]\n"
  "ds_read_b64_tr_b16 v[194:195], v212 offset:33792\n"
  "s_waitcnt lgkmcnt(4)\n"
  "v_mfma_f32_16x16x32_bf16 v[60:63], v[204:207], a[76:79], v[60:63]\n"
  "ds_read_b64_tr_b16 v[196:197], v212 offset:40960\n"
  "v_xor_b32 v180, v180, v182\n"
  "s_waitcnt lgkmcnt(3)\n"
  "v_mfma_f32_16x16x32_bf16 v[60:63], v[200:203], a[92:95], v[60:63]\n"
  "ds_read_b64_tr_b16 v[198:199], v212 offset:41984\n"
  "v_mfma_f32_16x16x32_bf16 v[56:59], v[200:203], a[88:91], v[56:59]\n"
  "global_load_lds_dwordx4 v130, s[60:61] offset:2048\n"
  "v_lshl_add_u32 v224, v180, 4, v181\n"
  "v_mfma_f32_16x16x32_bf16 v[56:59], v[204:207], a[72:75], v[56:59]\n"
  "global_load_lds_dwordx4 v131, s[60:61] offset:3072\n"
  "s_add_u32 m0, s49, 4096\n"
  "v_mfma_f32_16x16x32_bf16 v[52:55], v[204:207], a[68:71], v[52:55]\n"
  "v_mfma_f32_16x16x32_bf16 v[52:55], v[200:203], a[84:87], v[52:55]\n"
  "global_load_lds_dwordx4 v132, s[60:61] offset:0\n"
  "v_xor_b32 v182, 4, v180\n"
  "v_mfma_f32_16x16x32_bf16 v[48:51], v[200:203], a[80:83], v[48:51]\n"
  "ds_read_b64_tr_b16 v[200:201], v213 offset:32768\n"
  "v_mfma_f32_16x16x32_bf16 v[48:51], v[204:207], a[64:67], v[48:51]\n"
  "ds_read_b64_tr_b16 v[202:203], v213 offset:33792\n"
  "s_waitcnt lgkmcnt(4)\n"
  "v_mfma_f32_16x16x32_bf16 v[64:67], v[192:195], a[64:67], v[64:67]\n"
  "ds_read_b64_tr_b16 v[204:205], v213 offset:40960\n"
  "v_lshl_add_u32 v225, v182, 4, v181\n"
  "s_waitcnt lgkmcnt(3)\n"
  "v_mfma_f32_16x16x32_bf16 v[64:67], v[196:199], a[80:83], v[64:67]\n"
  "ds_read_b64_tr_b16 v[206:207], v213 offset:41984\n"
  "v_mfma_f32_16x16x32_bf16 v[68:71], v[196:199], a[84:87], v[68:71]\n"
  "global_load_lds_dwordx4 v133, s[60:61] offset:1024\n"
  "v_mfma_f32_16x16x32_bf16 v[68:71], v[192:195], a[68:71], v[68:71]\n"
  "global_load_lds_dwordx4 v134, s[60:61] offset:2048\n"
  "v_add_u32 v225, 64512, v225\n"
  "v_mfma_f32_16x16x32_bf16 v[72:75], v[192:195], a[72:75], v[72:75]\n"
  "global_load_lds_dwordx4 v135, s[60:61] offset:3072\n"
  "v_mfma_f32_16x16x32_bf16 v[72:75], v[196:199], a[88:91], v[72:75]\n"
  "v_lshl_add_u32 v226, v180, 4, v181\n"
  "v_mfma_f32_16x16x32_bf16 v[76:79], v[196:199], a[92:95], v[76:79]\n"
  "s_add_u32 s98, s18, 5\n"
  "ds_read_b64_tr_b16 v[196:197], v214 offset:32768\n"
  "v_mfma_f32_16x16x32_bf16 v[76:79], v[192:195], a[76:79], v[76:79]\n"
  "ds_read_b64_tr_b16 v[198:199], v214 offset:33792\n"
  "s_waitcnt lgkmcnt(4)\n"
  "v_mfma_f32_16x16x32_bf16 v[92:95], v[200:203], a[76:79], v[92:95]\n"
  "s_waitcnt vmcnt(14)\n"
  "ds_read_b64_tr_b16 v[192:193], v214 offset:40960\n"
  "v_add_u32 v226, 129024, v226\n"
  "s_waitcnt lgkmcnt(3)\n"
  "v_mfma_f32_16x16x32_bf16 v[92:95], v[204:207], a[92:95], v[92:95]\n"
  "s_and_b32 s98, s98, 7\n"
  "ds_read_b64_tr_b16 v[194:195], v214 offset:41984\n"
  "v_mfma_f32_16x16x32_bf16 v[88:91], v[204:207], a[88:91], v[88:91]\n"
  "ds_read_b128 a[96:99], v229 offset:8192\n"
  "v_mfma_f32_16x16x32_bf16 v[88:91], v[200:203], a[72:75], v[88:91]\n"
  "s_mul_i32 s98, s98, 524288\n"
  "v_xor_b32 v182, 4, v180\n"
  "v_mfma_f32_16x16x32_bf16 v[84:87], v[200:203], a[68:71], v[84:87]\n"
  "ds_read_b128 a[100:103], v229 offset:10240\n"
  "v_mfma_f32_16x16x32_bf16 v[84:87], v[204:207], a[84:87], v[84:87]\n"
  "s_add_u32 s60, s22, s98\n"
  "s_addc_u32 s61, s23, 0\n"
  "v_mfma_f32_16x16x32_bf16 v[80:83], v[204:207], a[80:83], v[80:83]\n"
  "ds_read_b64_tr_b16 v[204:205], v215 offset:32768\n"
  "v_lshl_add_u32 v227, v182, 4, v181\n"
  "v_mfma_f32_16x16x32_bf16 v[80:83], v[200:203], a[64:67], v[80:83]\n"
  "ds_read_b64_tr_b16 v[206:207], v215 offset:33792\n"
  "s_waitcnt lgkmcnt(6)\n"
  "v_mfma_f32_16x16x32_bf16 v[96:99], v[196:199], a[64:67], v[96:99]\n"
  "s_add_u32 m0, s48, 16384\n"
  "ds_read_b64_tr_b16 v[200:201], v215 offset:40960\n"
  "v_add_u32 v227, 193536, v227\n"
  "s_waitcnt lgkmcnt(5)\n"
  "v_mfma_f32_16x16x32_bf16 v[96:99], v[192:195], a[80:83], v[96:99]\n"
  "ds_read_b64_tr_b16 v[202:203], v215 offset:41984\n"
  "v_mfma_f32_16x16x32_bf16 v[100:103], v[192:195], a[84:87], v[100:103]\n"
  "ds_read_b128 a[104:107], v229 offset:12288\n"
  "v_mfma_f32_16x16x32_bf16 v[100:103], v[196:199], a[68:71], v[100:103]\n"
  "ds_read_b128 a[108:111], v229 offset:14336\n"
  "v_xor_b32 v217, 32, v216\n"
  "v_mfma_f32_16x16x32_bf16 v[104:107], v[196:199], a[72:75], v[104:107]\n"
  "ds_read_b128 a[112:115], v230 offset:8192\n"
  "v_mfma_f32_16x16x32_bf16 v[104:107], v[192:195], a[88:91], v[104:107]\n"
  "ds_read_b128 a[116:119], v230 offset:10240\n"
  "v_mfma_f32_16x16x32_bf16 v[108:111], v[192:195], a[92:95], v[108:111]\n"
  "s_barrier\n"
  "ds_read_b64_tr_b16 v[192:193], v208 offset:49152\n"
  "v_mfma_f32_16x16x32_bf16 v[108:111], v[196:199], a[76:79], v[108:111]\n"
  "ds_read_b64_tr_b16 v[194:195], v208 offset:50176\n"
  "v_xor_b32 v218, 64, v216\n"
  "s_waitcnt lgkmcnt(8)\n"
  "v_mfma_f32_16x16x32_bf16 v[124:127], v[204:207], a[76:79], v[124:127]\n"
  "ds_read_b64_tr_b16 v[196:197], v208 offset:57344\n"
  "s_waitcnt lgkmcnt(7)\n"
  "v_mfma_f32_16x16x32_bf16 v[124:127], v[200:203], a[92:95], v[124:127]\n"
  "ds_read_b64_tr_b16 v[198:199], v208 offset:58368\n"
  "v_xor_b32 v228, 96, v216\n"
  "v_mfma_f32_16x16x32_bf16 v[120:123], v[200:203], a[88:91], v[120:123]\n"
  "ds_read_b128 a[120:123], v230 offset:12288\n"
  "v_mfma_f32_16x16x32_bf16 v[120:123], v[204:207], a[72:75], v[120:123]\n"
  "ds_read_b128 a[124:127], v230 offset:14336\n"
  "v_mfma_f32_16x16x32_bf16 v[116:119], v[204:207], a[68:71], v[116:119]\n"
  "global_load_lds_dwordx4 v220, s[60:61] offset:0\n"
  "v_mfma_f32_16x16x32_bf16 v[116:119], v[200:203], a[84:87], v[116:119]\n"
  "global_load_lds_dwordx4 v221, s[60:61] offset:1024\n"
  "v_mfma_f32_16x16x32_bf16 v[112:115], v[200:203], a[80:83], v[112:115]\n"
  "ds_read_b64_tr_b16 v[200:201], v209 offset:49152\n"
  "v_mfma_f32_16x16x32_bf16 v[112:115], v[204:207], a[64:67], v[112:115]\n"
  "ds_read_b64_tr_b16 v[202:203], v209 offset:50176\n"
  "global_load_lds_dwordx4 v222, s[60:61] offset:2048\n"
  "global_load_lds_dwordx4 v223, s[60:61] offset:3072\n"
  "L_stg3_%=:\n"
  "s_waitcnt lgkmcnt(6)\n"
  "v_mfma_f32_16x16x32_bf16 v[0:3], v[192:195], a[96:99], v[0:3]\n"
  "ds_read_b64_tr_b16 v[204:205], v209 offset:57344\n"
  "s_waitcnt lgkmcnt(5)\n"
  "v_mfma_f32_16x16x32_bf16 v[0:3], v[196:199], a[112:115], v[0:3]\n"
  "s_waitcnt vmcnt(13)\n"
  "s_barrier\n"
  "ds_read_b64_tr_b16 v[206:207], v209 offset:58368\n"
  "v_mfma_f32_16x16x32_bf16 v[4:7], v[196:199], a[116:119], v[4:7]\n"
  "v_mov_b32 v234, s16\n"
  "v_mov_b32 v235, s17\n"
  "v_mfma_f32_16x16x32_bf16 v[4:7], v[192:195], a[100:103], v[4:7]\n"
  "s_lshl_b32 s36, s86, 9\n"
  "v_mov_b32 v219, s36\n"
  "v_mfma_f32_16x16x32_bf16 v[8:11], v[192:195], a[104:107], v[8:11]\n"
  "s_mov_b64 exec, 1\n"
  "global_store_dwordx2 v219, v[234:235], s[62:63] offset:0 sc1\n"
  "s_mov_b64 exec, -1\n"
  "s_waitcnt lgkmcnt(5)\n"
  "v_mfma_f32_16x16x32_bf16 v[8:11], v[196:199], a[120:123], v[8:11]\n"
  "s_waitcnt lgkmcnt(4)\n"
  "s_add_u32 s98, s18, 5\n"
  "v_mfma_f32_16x16x32_bf16 v[12:15], v[196:199], a[124:127], v[12:15]\n"
  "ds_read_b64_tr_b16 v[196:197], v210 offset:49152\n"
  "v_mfma_f32_16x16x32_bf16 v[12:15], v[192:195], a[108:111], v[12:15]\n"
  "s_and_b32 s98, s98, 7\n"
  "ds_read_b64_tr_b16 v[198:199], v210 offset:50176\n"
  "s_waitcnt lgkmcnt(4)\n"
  "v_mfma_f32_16x16x32_bf16 v[28:31], v[200:203], a[108:111], v[28:31]\n"
  "ds_read_b64_tr_b16 v[192:193], v210 offset:57344\n"
  "s_waitcnt lgkmcnt(3)\n"
  "v_mfma_f32_16x16x32_bf16 v[28:31], v[204:207], a[124:127], v[28:31]\n"
  "s_mul_i32 s98, s98, 128\n"
  "ds_read_b64_tr_b16 v[194:195], v210 offset:58368\n"
  "v_mfma_f32_16x16x32_bf16 v[24:27], v[204:207], a[120:123], v[24:27]\n"
  "v_mfma_f32_16x16x32_bf16 v[24:27], v[200:203], a[104:107], v[24:27]\n"
  "s_add_u32 s60, s20, s98\n"
  "s_addc_u32 s61, s21, 0\n"
  "v_mfma_f32_16x16x32_bf16 v[20:23], v[200:203], a[100:103], v[20:23]\n"
  "v_mfma_f32_16x16x32_bf16 v[20:23], v[204:207], a[116:119], v[20:23]\n"
  "s_add_u32 m0, s49, 8192\n"
  "v_mfma_f32_16x16x32_bf16 v[16:19], v[204:207], a[112:115], v[16:19]\n"
  "ds_read_b64_tr_b16 v[204:205], v211 offset:49152\n"
  "v_mfma_f32_16x16x32_bf16 v[16:19], v[200:203], a[96:99], v[16:19]\n"
  "ds_read_b64_tr_b16 v[206:207], v211 offset:50176\n"
  "s_waitcnt lgkmcnt(4)\n"
  "v_mfma_f32_16x16x32_bf16 v[32:35], v[196:199], a[96:99], v[32:35]\n"
  "ds_read_b64_tr_b16 v[200:201], v211 offset:57344\n"
  "s_waitcnt lgkmcnt(3)\n"
  "v_mfma_f32_16x16x32_bf16 v[32:35], v[192:195], a[112:115], v[32:35]\n"
  "ds_read_b64_tr_b16 v[202:203], v211 offset:58368\n"
  "v_mfma_f32_16x16x32_bf16 v[36:39], v[192:195], a[116:119], v[36:39]\n"
  "global_load_lds_dwordx4 v128, s[60:61] offset:0\n"
  "v_mfma_f32_16x16x32_bf16 v[36:39], v[196:199], a[100:103], v[36:39]\n"
  "global_load_lds_dwordx4 v129, s[60:61] offset:1024\n"
  "v_mfma_f32_16x16x32_bf16 v[40:43], v[196:199], a[104:107], v[40:43]\n"
  "global_load_lds_dwordx4 v130, s[60:61] offset:2048\n"
  "v_mfma_f32_16x16x32_bf16 v[40:43], v[192:195], a[120:123], v[40:43]\n"
  "v_mfma_f32_16x16x32_bf16 v[44:47], v[192:195], a[124:127], v[44:47]\n"
  "ds_read_b64_tr_b16 v[192:193], v212 offset:49152\n"
  "v_mfma_f32_16x16x32_bf16 v[44:47], v[196:199], a[108:111], v[44:47]\n"
  "ds_read_b64_tr_b16 v[194:195], v212 offset:50176\n"
  "s_waitcnt lgkmcnt(4)\n"
  "v_mfma_f32_16x16x32_bf16 v[60:63], v[204:207], a[108:111], v[60:63]\n"
  "ds_read_b64_tr_b16 v[196:197], v212 offset:57344\n"
  "s_waitcnt lgkmcnt(3)\n"
  "v_mfma_f32_16x16x32_bf16 v[60:63], v[200:203], a[124:127], v[60:63]\n"
  "ds_read_b64_tr_b16 v[198:199], v212 offset:58368\n"
  "v_mfma_f32_16x16x32_bf16 v[56:59], v[200:203], a[120:123], v[56:59]\n"
  "global_load_lds_dwordx4 v131, s[60:61] offset:3072\n"
  "s_add_u32 m0, s49, 12288\n"
  "v_mfma_f32_16x16x32_bf16 v[56:59], v[204:207], a[104:107], v[56:59]\n"
  "global_load_lds_dwordx4 v132, s[60:61] offset:0\n"
  "v_mfma_f32_16x16x32_bf16 v[52:55], v[204:207], a[100:103], v[52:55]\n"
  "v_mfma_f32_16x16x32_bf16 v[52:55], v[200:203], a[116:119], v[52:55]\n"
  "global_load_lds_dwordx4 v133, s[60:61] offset:1024\n"
  "v_mfma_f32_16x16x32_bf16 v[48:51], v[200:203], a[112:115], v[48:51]\n"
  "ds_read_b64_tr_b16 v[200:201], v213 offset:49152\n"
  "v_mfma_f32_16x16x32_bf16 v[48:51], v[204:207], a[96:99], v[48:51]\n"
  "ds_read_b64_tr_b16 v[202:203], v213 offset:50176\n"
  "s_waitcnt lgkmcnt(4)\n"
  "v_mfma_f32_16x16x32_bf16 v[64:67], v[192:195], a[96:99], v[64:67]\n"
  "ds_read_b64_tr_b16 v[204:205], v213 offset:57344\n"
  "s_waitcnt lgkmcnt(3)\n"
  "v_mfma_f32_16x16x32_bf16 v[64:67], v[196:199], a[112:115], v[64:67]\n"
  "ds_read_b64_tr_b16 v[206:207], v213 offset:58368\n"
  "v_mfma_f32_16x16x32_bf16 v[68:71], v[196:199], a[116:119], v[68:71]\n"
  "global_load_lds_dwordx4 v134, s[60:61] offset:2048\n"
  "v_mfma_f32_16x16x32_bf16 v[68:71], v[192:195], a[100:103], v[68:71]\n"
  "global_load_lds_dwordx4 v135, s[60:61] offset:3072\n"
  "s_add_u32 s98, s18, 6\n"
  "v_mfma_f32_16x16x32_bf16 v[72:75], v[192:195], a[104:107], v[72:75]\n"
  "v_mfma_f32_16x16x32_bf16 v[72:75], v[196:199], a[120:123], v[72:75]\n"
  "v_mfma_f32_16x16x32_bf16 v[76:79], v[196:199], a[124:127], v[76:79]\n"
  "s_and_b32 s98, s98, 7\n"
  "ds_read_b64_tr_b16 v[196:197], v214 offset:49152\n"
  "v_mfma_f32_16x16x32_bf16 v[76:79], v[192:195], a[108:111], v[76:79]\n"
  "ds_read_b64_tr_b16 v[198:199], v214 offset:50176\n"
  "s_waitcnt lgkmcnt(4)\n"
  "v_mfma_f32_16x16x32_bf16 v[92:95], v[200:203], a[108:111], v[92:95]\n"
  "s_waitcnt vmcnt(13)\n"
  "s_mul_i32 s98, s98, 524288\n"
  "ds_read_b64_tr_b16 v[192:193], v214 offset:57344\n"
  "s_waitcnt lgkmcnt(3)\n"
  "v_mfma_f32_16x16x32_bf16 v[92:95], v[204:207], a[124:127], v[92:95]\n"
  "ds_read_b64_tr_b16 v[194:195], v214 offset:58368\n"
  "v_mfma_f32_16x16x32_bf16 v[88:91], v[204:207], a[120:123], v[88:91]\n"
  "s_add_u32 s60, s22, s98\n"
  "s_addc_u32 s61, s23, 0\n"
  "ds_read_b128 a[128:131], v229 offset:0\n"
  "v_mfma_f32_16x16x32_bf16 v[88:91], v[200:203], a[104:107], v[88:91]\n"
  "v_mfma_f32_16x16x32_bf16 v[84:87], v[200:203], a[100:103], v[84:87]\n"
  "s_add_u32 m0, s48, 32768\n"
  "ds_read_b128 a[132:135], v229 offset:2048\n"
  "v_mfma_f32_16x16x32_bf16 v[84:87], v[204:207], a[116:119], v[84:87]\n"
  "v_mfma_f32_16x16x32_bf16 v[80:83], v[204:207], a[112:115], v[80:83]\n"
  "ds_read_b64_tr_b16 v[204:205], v215 offset:49152\n"
  "v_mfma_f32_16x16x32_bf16 v[80:83], v[200:203], a[96:99], v[80:83]\n"
  "ds_read_b64_tr_b16 v[206:207], v215 offset:50176\n"
  "s_waitcnt lgkmcnt(6)\n"
  "v_mfma_f32_16x16x32_bf16 v[96:99], v[196:199], a[96:99], v[96:99]\n"
  "ds_read_b64_tr_b16 v[200:201], v215 offset:57344\n"
  "s_waitcnt lgkmcnt(5)\n"
  "v_mfma_f32_16x16x32_bf16 v[96:99], v[192:195], a[112:115], v[96:99]\n"
  "ds_read_b64_tr_b16 v[202:203], v215 offset:58368\n"
  "v_mfma_f32_16x16x32_bf16 v[100:103], v[192:195], a[116:119], v[100:103]\n"
  "ds_read_b128 a[136:139], v229 offset:4096\n"
  "v_mfma_f32_16x16x32_bf16 v[100:103], v[196:199], a[100:103], v[100:103]\n"
  "ds_read_b128 a[140:143], v229 offset:6144\n"
  "v_mfma_f32_16x16x32_bf16 v[104:107], v[196:199], a[104:107], v[104:107]\n"
  "ds_read_b128 a[144:147], v230 offset:0\n"
  "v_mfma_f32_16x16x32_bf16 v[104:107], v[192:195], a[120:123], v[104:107]\n"
  "ds_read_b128 a[148:151], v230 offset:2048\n"
  "v_mfma_f32_16x16x32_bf16 v[108:111], v[192:195], a[124:127], v[108:111]\n"
  "s_barrier\n"
  "ds_read_b64_tr_b16 v[192:193], v208 offset:0\n"
  "v_mfma_f32_16x16x32_bf16 v[108:111], v[196:199], a[108:111], v[108:111]\n"
  "ds_read_b64_tr_b16 v[194:195], v208 offset:1024\n"
  "s_waitcnt lgkmcnt(8)\n"
  "v_mfma_f32_16x16x32_bf16 v[124:127], v[204:207], a[108:111], v[124:127]\n"
  "ds_read_b64_tr_b16 v[196:197], v208 offset:8192\n"
  "s_waitcnt lgkmcnt(7)\n"
  "v_mfma_f32_16x16x32_bf16 v[124:127], v[200:203], a[124:127], v[124:127]\n"
  "ds_read_b64_tr_b16 v[198:199], v208 offset:9216\n"
  "v_mfma_f32_16x16x32_bf16 v[120:123], v[200:203], a[120:123], v[120:123]\n"
  "ds_read_b128 a[152:155], v230 offset:4096\n"
  "v_mfma_f32_16x16x32_bf16 v[120:123], v[204:207], a[104:107], v[120:123]\n"
  "ds_read_b128 a[156:159], v230 offset:6144\n"
  "v_mfma_f32_16x16x32_bf16 v[116:119], v[204:207], a[100:103], v[116:119]\n"
  "global_load_lds_dwordx4 v220, s[60:61] offset:0\n"
  "v_mfma_f32_16x16x32_bf16 v[116:119], v[200:203], a[116:119], v[116:119]\n"
  "global_load_lds_dwordx4 v221, s[60:61] offset:1024\n"
  "v_mfma_f32_16x16x32_bf16 v[112:115], v[200:203], a[112:115], v[112:115]\n"
  "ds_read_b64_tr_b16 v[200:201], v209 offset:0\n"
  "v_mfma_f32_16x16x32_bf16 v[112:115], v[204:207], a[96:99], v[112:115]\n"
  "ds_read_b64_tr_b16 v[202:203], v209 offset:1024\n"
  "global_load_lds_dwordx4 v222, s[60:61] offset:2048\n"
  "global_load_lds_dwordx4 v223, s[60:61] offset:3072\n"
  "global_load_dword v231, v179, s[24:25] offset:0\n"
  "v_add_u32 v179, 0x100000, v179\n"
  "L_stg4_%=:\n"
  "s_waitcnt lgkmcnt(6)\n"
  "v_mfma_f32_16x16x32_bf16 v[0:3], v[192:195], a[128:131], v[0:3]\n"
  "ds_read_b64_tr_b16 v[204:205], v209 offset:8192\n"
  "s_waitcnt lgkmcnt(5)\n"
  "v_mfma_f32_16x16x32_bf16 v[0:3], v[196:199], a[144:147], v[0:3]\n"
  "s_barrier\n"
  "ds_read_b64_tr_b16 v[206:207], v209 offset:9216\n"
  "v_mfma_f32_16x16x32_bf16 v[4:7], v[196:199], a[148:151], v[4:7]\n"
  "v_lshrrev_b32 v181, 3, v233\n"
  "v_mfma_f32_16x16x32_bf16 v[4:7], v[192:195], a[132:135], v[4:7]\n"
  "s_waitcnt lgkmcnt(4)\n"
  "v_mfma_f32_16x16x32_bf16 v[8:11], v[192:195], a[136:139], v[8:11]\n"
  "s_and_b32 s87, s35, 3\n"
  "v_mfma_f32_16x16x32_bf16 v[8:11], v[196:199], a[152:155], v[8:11]\n"
  "s_add_u32 s98, s18, 6\n"
  "v_mfma_f32_16x16x32_bf16 v[12:15], v[196:199], a[156:159], v[12:15]\n"
  "ds_read_b64_tr_b16 v[196:197], v210 offset:0\n"
  "v_mfma_f32_16x16x32_bf16 v[12:15], v[192:195], a[140:143], v[12:15]\n"
  "s_and_b32 s98, s98, 7\n"
  "s_lshl_b32 s87, s87, 5\n"
  "ds_read_b64_tr_b16 v[198:199], v210 offset:1024\n"
  "s_waitcnt lgkmcnt(4)\n"
  "v_mfma_f32_16x16x32_bf16 v[28:31], v[200:203], a[140:143], v[28:31]\n"
  "ds_read_b64_tr_b16 v[192:193], v210 offset:8192\n"
  "s_waitcnt lgkmcnt(3)\n"
  "v_mfma_f32_16x16x32_bf16 v[28:31], v[204:207], a[156:159], v[28:31]\n"
  "s_mul_i32 s98, s98, 128\n"
  "ds_read_b64_tr_b16 v[194:195], v210 offset:9216\n"
  "v_mfma_f32_16x16x32_bf16 v[24:27], v[204:207], a[152:155], v[24:27]\n"
  "v_add_u32 v181, s87, v181\n"
  "v_mfma_f32_16x16x32_bf16 v[24:27], v[200:203], a[136:139], v[24:27]\n"
  "s_add_u32 s60, s20, s98\n"
  "s_addc_u32 s61, s21, 0\n"
  "v_mfma_f32_16x16x32_bf16 v[20:23], v[200:203], a[132:135], v[20:23]\n"
  "v_mfma_f32_16x16x32_bf16 v[20:23], v[204:207], a[148:151], v[20:23]\n"
  "s_add_u32 m0, s49, 0\n"
  "v_mul_u32_u24 v181, 2752, v181\n"
  "v_mfma_f32_16x16x32_bf16 v[16:19], v[204:207], a[144:147], v[16:19]\n"
  "ds_read_b64_tr_b16 v[204:205], v211 offset:0\n"
  "v_mfma_f32_16x16x32_bf16 v[16:19], v[200:203], a[128:131], v[16:19]\n"
  "ds_read_b64_tr_b16 v[206:207], v211 offset:1024\n"
  "v_bfe_u32 v180, v233, 4, 1\n"
  "s_waitcnt lgkmcnt(4)\n"
  "v_mfma_f32_16x16x32_bf16 v[32:35], v[196:199], a[128:131], v[32:35]\n"
  "ds_read_b64_tr_b16 v[200:201], v211 offset:8192\n"
  "s_waitcnt lgkmcnt(3)\n"
  "v_mfma_f32_16x16x32_bf16 v[32:35], v[192:195], a[144:147], v[32:35]\n"
  "ds_read_b64_tr_b16 v[202:203], v211 offset:9216\n"
  "v_mfma_f32_16x16x32_bf16 v[36:39], v[192:195], a[148:151], v[36:39]\n"
  "global_load_lds_dwordx4 v128, s[60:61] offset:0\n"
  "v_lshlrev_b32 v180, 1, v180\n"
  "v_mfma_f32_16x16x32_bf16 v[36:39], v[196:199], a[132:135], v[36:39]\n"
  "global_load_lds_dwordx4 v129, s[60:61] offset:1024\n"
  "v_mfma_f32_16x16x32_bf16 v[40:43], v[196:199], a[136:139], v[40:43]\n"
  "global_load_lds_dwordx4 v130, s[60:61] offset:2048\n"
  "v_mfma_f32_16x16x32_bf16 v[40:43], v[192:195], a[152:155], v[40:43]\n"
  "v_and_b32 v182, 7, v233\n"
  "v_mfma_f32_16x16x32_bf16 v[44:47], v[192:195], a[156:159], v[44:47]\n"
  "ds_read_b64_tr_b16 v[192:193], v212 offset:0\n"
  "v_mfma_f32_16x16x32_bf16 v[44:47], v[196:199], a[140:143], v[44:47]\n"
  "ds_read_b64_tr_b16 v[194:195], v212 offset:1024\n"
  "s_waitcnt lgkmcnt(4)\n"
  "v_mfma_f32_16x16x32_bf16 v[60:63], v[204:207], a[140:143], v[60:63]\n"
  "ds_read_b64_tr_b16 v[196:197], v212 offset:8192\n"
  "v_xor_b32 v180, v180, v182\n"
  "s_waitcnt lgkmcnt(3)\n"
  "v_mfma_f32_16x16x32_bf16 v[60:63], v[200:203], a[156:159], v[60:63]\n"
  "ds_read_b64_tr_b16 v[198:199], v212 offset:9216\n"
  "v_mfma_f32_16x16x32_bf16 v[56:59], v[200:203], a[152:155], v[56:59]\n"
  "global_load_lds_dwordx4 v131, s[60:61] offset:3072\n"
  "s_add_u32 m0, s49, 4096\n"
  "v_lshl_add_u32 v248, v180, 4, v181\n"
  "v_mfma_f32_16x16x32_bf16 v[56:59], v[204:207], a[136:139], v[56:59]\n"
  "global_load_lds_dwordx4 v132, s[60:61] offset:0\n"
  "v_mfma_f32_16x16x32_bf16 v[52:55], v[204:207], a[132:135], v[52:55]\n"
  "v_mfma_f32_16x16x32_bf16 v[52:55], v[200:203], a[148:151], v[52:55]\n"
  "global_load_lds_dwordx4 v133, s[60:61] offset:1024\n"
  "v_xor_b32 v182, 4, v180\n"
  "v_mfma_f32_16x16x32_bf16 v[48:51], v[200:203], a[144:147], v[48:51]\n"
  "ds_read_b64_tr_b16 v[200:201], v213 offset:0\n"
  "v_mfma_f32_16x16x32_bf16 v[48:51], v[204:207], a[128:131], v[48:51]\n"
  "ds_read_b64_tr_b16 v[202:203], v213 offset:1024\n"
  "s_waitcnt lgkmcnt(4)\n"
  "v_mfma_f32_16x16x32_bf16 v[64:67], v[192:195], a[128:131], v[64:67]\n"
  "ds_read_b64_tr_b16 v[204:205], v213 offset:8192\n"
  "v_lshl_add_u32 v249, v182, 4, v181\n"
  "s_waitcnt lgkmcnt(3)\n"
  "v_mfma_f32_16x16x32_bf16 v[64:67], v[196:199], a[144:147], v[64:67]\n"
  "ds_read_b64_tr_b16 v[206:207], v213 offset:9216\n"
  "v_mfma_f32_16x16x32_bf16 v[68:71], v[196:199], a[148:151], v[68:71]\n"
  "global_load_lds_dwordx4 v134, s[60:61] offset:2048\n"
  "v_mfma_f32_16x16x32_bf16 v[68:71], v[192:195], a[132:135], v[68:71]\n"
  "global_load_lds_dwordx4 v135, s[60:61] offset:3072\n"
  "s_add_u32 s98, s18, 7\n"
  "v_add_u32 v249, 20992, v249\n"
  "v_mfma_f32_16x16x32_bf16 v[72:75], v[192:195], a[136:139], v[72:75]\n"
  "v_mfma_f32_16x16x32_bf16 v[72:75], v[196:199], a[152:155], v[72:75]\n"
  "v_lshl_add_u32 v250, v180, 4, v181\n"
  "v_mfma_f32_16x16x32_bf16 v[76:79], v[196:199], a[156:159], v[76:79]\n"
  "s_and_b32 s98, s98, 7\n"
  "ds_read_b64_tr_b16 v[196:197], v214 offset:0\n"
  "v_mfma_f32_16x16x32_bf16 v[76:79], v[192:195], a[140:143], v[76:79]\n"
  "ds_read_b64_tr_b16 v[198:199], v214 offset:1024\n"
  "s_waitcnt lgkmcnt(4)\n"
  "v_mfma_f32_16x16x32_bf16 v[92:95], v[200:203], a[140:143], v[92:95]\n"
  "s_waitcnt vmcnt(13)\n"
  "s_mul_i32 s98, s98, 524288\n"
  "ds_read_b64_tr_b16 v[192:193], v214 offset:8192\n"
  "s_waitcnt lgkmcnt(3)\n"
  "v_mfma_f32_16x16x32_bf16 v[92:95], v[204:207], a[156:159], v[92:95]\n"
  "ds_read_b64_tr_b16 v[194:195], v214 offset:9216\n"
  "v_add_u32 v250, 41984, v250\n"
  "v_mfma_f32_16x16x32_bf16 v[88:91], v[204:207], a[152:155], v[88:91]\n"
  "s_add_u32 s60, s22, s98\n"
  "s_addc_u32 s61, s23, 0\n"
  "ds_read_b128 a[160:163], v229 offset:8192\n"
  "v_mfma_f32_16x16x32_bf16 v[88:91], v[200:203], a[136:139], v[88:91]\n"
  "v_xor_b32 v182, 4, v180\n"
  "v_mfma_f32_16x16x32_bf16 v[84:87], v[200:203], a[132:135], v[84:87]\n"
  "s_add_u32 m0, s48, 49152\n"
  "ds_read_b128 a[164:167], v229 offset:10240\n"
  "v_mfma_f32_16x16x32_bf16 v[84:87], v[204:207], a[148:151], v[84:87]\n"
  "v_mfma_f32_16x16x32_bf16 v[80:83], v[204:207], a[144:147], v[80:83]\n"
  "ds_read_b64_tr_b16 v[204:205], v215 offset:0\n"
  "v_lshl_add_u32 v251, v182, 4, v181\n"
  "v_mfma_f32_16x16x32_bf16 v[80:83], v[200:203], a[128:131], v[80:83]\n"
  "ds_read_b64_tr_b16 v[206:207], v215 offset:1024\n"
  "s_waitcnt lgkmcnt(6)\n"
  "v_mfma_f32_16x16x32_bf16 v[96:99], v[196:199], a[128:131], v[96:99]\n"
  "ds_read_b64_tr_b16 v[200:201], v215 offset:8192\n"
  "v_add_u32 v251, 62976, v251\n"
  "s_waitcnt lgkmcnt(5)\n"
  "v_mfma_f32_16x16x32_bf16 v[96:99], v[192:195], a[144:147], v[96:99]\n"
  "ds_read_b64_tr_b16 v[202:203], v215 offset:9216\n"
  "v_mfma_f32_16x16x32_bf16 v[100:103], v[192:195], a[148:151], v[100:103]\n"
  "ds_read_b128 a[168:171], v229 offset:12288\n"
  "v_mfma_f32_16x16x32_bf16 v[100:103], v[196:199], a[132:135], v[100:103]\n"
  "s_mul_i32 s55, s39, 3\n"
  "ds_read_b128 a[172:175], v229 offset:14336\n"
  "v_mfma_f32_16x16x32_bf16 v[104:107], v[196:199], a[136:139], v[104:107]\n"
  "ds_read_b128 a[176:179], v230 offset:8192\n"
  "v_mfma_f32_16x16x32_bf16 v[104:107], v[192:195], a[152:155], v[104:107]\n"
  "ds_read_b128 a[180:183], v230 offset:10240\n"
  "v_mfma_f32_16x16x32_bf16 v[108:111], v[192:195], a[156:159], v[108:111]\n"
  "s_barrier\n"
  "ds_read_b64_tr_b16 v[192:193], v208 offset:16384\n"
  "v_mfma_f32_16x16x32_bf16 v[108:111], v[196:199], a[140:143], v[108:111]\n"
  "s_min_u32 s55, s55, 83\n"
  "ds_read_b64_tr_b16 v[194:195], v208 offset:17408\n"
  "s_waitcnt lgkmcnt(8)\n"
  "v_mfma_f32_16x16x32_bf16 v[124:127], v[204:207], a[140:143], v[124:127]\n"
  "ds_read_b64_tr_b16 v[196:197], v208 offset:24576\n"
  "s_waitcnt lgkmcnt(7)\n"
  "v_mfma_f32_16x16x32_bf16 v[124:127], v[200:203], a[156:159], v[124:127]\n"
  "s_lshl_b32 s87, s55, 5\n"
  "ds_read_b64_tr_b16 v[198:199], v208 offset:25600\n"
  "v_mfma_f32_16x16x32_bf16 v[120:123], v[200:203], a[152:155], v[120:123]\n"
  "ds_read_b128 a[184:187], v230 offset:12288\n"
  "v_mfma_f32_16x16x32_bf16 v[120:123], v[204:207], a[136:139], v[120:123]\n"
  "ds_read_b128 a[188:191], v230 offset:14336\n"
  "v_mfma_f32_16x16x32_bf16 v[116:119], v[204:207], a[132:135], v[116:119]\n"
  "global_load_lds_dwordx4 v220, s[60:61] offset:0\n"
  "v_mfma_f32_16x16x32_bf16 v[116:119], v[200:203], a[148:151], v[116:119]\n"
  "global_load_lds_dwordx4 v221, s[60:61] offset:1024\n"
  "v_mfma_f32_16x16x32_bf16 v[112:115], v[200:203], a[144:147], v[112:115]\n"
  "ds_read_b64_tr_b16 v[200:201], v209 offset:16384\n"
  "v_mfma_f32_16x16x32_bf16 v[112:115], v[204:207], a[128:131], v[112:115]\n"
  "ds_read_b64_tr_b16 v[202:203], v209 offset:17408\n"
  "global_load_lds_dwordx4 v222, s[60:61] offset:2048\n"
  "global_load_lds_dwordx4 v223, s[60:61] offset:3072\n"
  "global_load_dword v231, v179, s[24:25] offset:0\n"
  "v_add_u32 v179, 0x100000, v179\n"
  "L_stg5_%=:\n"
  "s_waitcnt lgkmcnt(6)\n"
  "v_mfma_f32_16x16x32_bf16 v[0:3], v[192:195], a[160:163], v[0:3]\n"
  "ds_read_b64_tr_b16 v[204:205], v209 offset:24576\n"
  "s_waitcnt lgkmcnt(5)\n"
  "v_mfma_f32_16x16x32_bf16 v[0:3], v[196:199], a[176:179], v[0:3]\n"
  "s_barrier\n"
  "ds_read_b64_tr_b16 v[206:207], v209 offset:25600\n"
  "v_mfma_f32_16x16x32_bf16 v[4:7], v[196:199], a[180:183], v[4:7]\n"
  "s_min_u32 s99, s87, 2624\n"
  "v_mfma_f32_16x16x32_bf16 v[4:7], v[192:195], a[164:167], v[4:7]\n"
  "s_waitcnt lgkmcnt(4)\n"
  "v_mfma_f32_16x16x32_bf16 v[8:11], v[192:195], a[168:171], v[8:11]\n"
  "s_sub_u32 s59, s87, s99\n"
  "v_mfma_f32_16x16x32_bf16 v[8:11], v[196:199], a[184:187], v[8:11]\n"
  "s_add_u32 s98, s18, 7\n"
  "v_mfma_f32_16x16x32_bf16 v[12:15], v[196:199], a[188:191], v[12:15]\n"
  "ds_read_b64_tr_b16 v[196:197], v210 offset:16384\n"
  "v_mfma_f32_16x16x32_bf16 v[12:15], v[192:195], a[172:175], v[12:15]\n"
  "s_and_b32 s98, s98, 7\n"
  "ds_read_b64_tr_b16 v[198:199], v210 offset:17408\n"
  "s_waitcnt lgkmcnt(4)\n"
  "v_mfma_f32_16x16x32_bf16 v[28:31], v[200:203], a[172:175], v[28:31]\n"
  "s_add_u32 s26, s76, s99\n"
  "s_addc_u32 s27, s77, 0\n"
  "ds_read_b64_tr_b16 v[192:193], v210 offset:24576\n"
  "s_waitcnt lgkmcnt(3)\n"
  "v_mfma_f32_16x16x32_bf16 v[28:31], v[204:207], a[188:191], v[28:31]\n"
  "s_mul_i32 s98, s98, 128\n"
  "ds_read_b64_tr_b16 v[194:195], v210 offset:25600\n"
  "v_mfma_f32_16x16x32_bf16 v[24:27], v[204:207], a[184:187], v[24:27]\n"
  "s_lshl_b32 s87, s39, 6\n"
  "v_mfma_f32_16x16x32_bf16 v[24:27], v[200:203], a[168:171], v[24:27]\n"
  "s_add_u32 s60, s20, s98\n"
  "s_addc_u32 s61, s21, 0\n"
  "v_mfma_f32_16x16x32_bf16 v[20:23], v[200:203], a[164:167], v[20:23]\n"
  "v_mfma_f32_16x16x32_bf16 v[20:23], v[204:207], a[180:183], v[20:23]\n"
  "s_add_u32 m0, s49, 8192\n"
  "s_lshl_b32 s99, s34, 3\n"
  "v_mfma_f32_16x16x32_bf16 v[16:19], v[204:207], a[176:179], v[16:19]\n"
  "ds_read_b64_tr_b16 v[204:205], v211 offset:16384\n"
  "v_mfma_f32_16x16x32_bf16 v[16:19], v[200:203], a[160:163], v[16:19]\n"
  "s_add_u32 s87, s87, s99\n"
  "ds_read_b64_tr_b16 v[206:207], v211 offset:17408\n"
  "s_waitcnt lgkmcnt(4)\n"
  "v_mfma_f32_16x16x32_bf16 v[32:35], v[196:199], a[160:163], v[32:35]\n"
  "ds_read_b64_tr_b16 v[200:201], v211 offset:24576\n"
  "s_waitcnt lgkmcnt(3)\n"
  "v_mfma_f32_16x16x32_bf16 v[32:35], v[192:195], a[176:179], v[32:35]\n"
  "ds_read_b64_tr_b16 v[202:203], v211 offset:25600\n"
  "v_mfma_f32_16x16x32_bf16 v[36:39], v[192:195], a[180:183], v[36:39]\n"
  "s_lshl_b32 s99, s35, 1\n"
  "global_load_lds_dwordx4 v128, s[60:61] offset:0\n"
  "v_mfma_f32_16x16x32_bf16 v[36:39], v[196:199], a[164:167], v[36:39]\n"
  "global_load_lds_dwordx4 v129, s[60:61] offset:1024\n"
  "v_mfma_f32_16x16x32_bf16 v[40:43], v[196:199], a[168:171], v[40:43]\n"
  "global_load_lds_dwordx4 v130, s[60:61] offset:2048\n"
  "v_mfma_f32_16x16x32_bf16 v[40:43], v[192:195], a[184:187], v[40:43]\n"
  "s_add_u32 s87, s87, s99\n"
  "v_mfma_f32_16x16x32_bf16 v[44:47], v[192:195], a[188:191], v[44:47]\n"
  "ds_read_b64_tr_b16 v[192:193], v212 offset:16384\n"
  "v_mfma_f32_16x16x32_bf16 v[44:47], v[196:199], a[172:175], v[44:47]\n"
  "ds_read_b64_tr_b16 v[194:195], v212 offset:17408\n"
  "s_waitcnt lgkmcnt(4)\n"
  "v_mfma_f32_16x16x32_bf16 v[60:63], v[204:207], a[172:175], v[60:63]\n"
  "s_lshl_b32 s87, s87, 14\n"
  "ds_read_b64_tr_b16 v[196:197], v212 offset:24576\n"
  "s_waitcnt lgkmcnt(3)\n"
  "v_mfma_f32_16x16x32_bf16 v[60:63], v[200:203], a[188:191], v[60:63]\n"
  "ds_read_b64_tr_b16 v[198:199], v212 offset:25600\n"
  "v_mfma_f32_16x16x32_bf16 v[56:59], v[200:203], a[184:187], v[56:59]\n"
  "s_add_u32 s87, s87, 2101248\n"
  "global_load_lds_dwordx4 v131, s[60:61] offset:3072\n"
  "s_add_u32 m0, s49, 12288\n"
  "v_mfma_f32_16x16x32_bf16 v[56:59], v[204:207], a[168:171], v[56:59]\n"
  "global_load_lds_dwordx4 v132, s[60:61] offset:0\n"
  "v_mfma_f32_16x16x32_bf16 v[52:55], v[204:207], a[164:167], v[52:55]\n"
  "v_mfma_f32_16x16x32_bf16 v[52:55], v[200:203], a[180:183], v[52:55]\n"
  "s_add_u32 s40, s84, s87\n"
  "s_addc_u32 s41, s85, 0\n"
  "global_load_lds_dwordx4 v133, s[60:61] offset:1024\n"
  "v_mfma_f32_16x16x32_bf16 v[48:51], v[200:203], a[176:179], v[48:51]\n"
  "ds_read_b64_tr_b16 v[200:201], v213 offset:16384\n"
  "v_mfma_f32_16x16x32_bf16 v[48:51], v[204:207], a[160:163], v[48:51]\n"
  "ds_read_b64_tr_b16 v[202:203], v213 offset:17408\n"
  "s_waitcnt lgkmcnt(4)\n"
  "v_mfma_f32_16x16x32_bf16 v[64:67], v[192:195], a[160:163], v[64:67]\n"
  "s_add_u32 s42, s40, 8192\n"
  "s_addc_u32 s43, s41, 0\n"
  "ds_read_b64_tr_b16 v[204:205], v213 offset:24576\n"
  "s_waitcnt lgkmcnt(3)\n"
  "v_mfma_f32_16x16x32_bf16 v[64:67], v[196:199], a[176:179], v[64:67]\n"
  "ds_read_b64_tr_b16 v[206:207], v213 offset:25600\n"
  "v_mfma_f32_16x16x32_bf16 v[68:71], v[196:199], a[180:183], v[68:71]\n"
  "global_load_lds_dwordx4 v134, s[60:61] offset:2048\n"
  "v_mfma_f32_16x16x32_bf16 v[68:71], v[192:195], a[164:167], v[68:71]\n"
  "global_load_lds_dwordx4 v135, s[60:61] offset:3072\n"
  "s_add_u32 s98, s19, 0\n"
  "v_mfma_f32_16x16x32_bf16 v[72:75], v[192:195], a[168:171], v[72:75]\n"
  "s_add_u32 s44, s40, 16384\n"
  "s_addc_u32 s45, s41, 0\n"
  "v_mfma_f32_16x16x32_bf16 v[72:75], v[196:199], a[184:187], v[72:75]\n"
  "s_add_u32 s46, s40, 24576\n"
  "s_addc_u32 s47, s41, 0\n"
  "v_mfma_f32_16x16x32_bf16 v[76:79], v[196:199], a[188:191], v[76:79]\n"
  "s_and_b32 s98, s98, 7\n"
  "ds_read_b64_tr_b16 v[196:197], v214 offset:16384\n"
  "v_mfma_f32_16x16x32_bf16 v[76:79], v[192:195], a[172:175], v[76:79]\n"
  "ds_read_b64_tr_b16 v[198:199], v214 offset:17408\n"
  "s_waitcnt lgkmcnt(4)\n"
  "v_mfma_f32_16x16x32_bf16 v[92:95], v[200:203], a[172:175], v[92:95]\n"
  "s_waitcnt vmcnt(13)\n"
  "s_mul_i32 s98, s98, 524288\n"
  "ds_read_b64_tr_b16 v[192:193], v214 offset:24576\n"
  "s_waitcnt lgkmcnt(3)\n"
  "v_mfma_f32_16x16x32_bf16 v[92:95], v[204:207], a[188:191], v[92:95]\n"
  "s_lshl_b32 s87, s34, 3\n"
  "ds_read_b64_tr_b16 v[194:195], v214 offset:25600\n"
  "v_mfma_f32_16x16x32_bf16 v[88:91], v[204:207], a[184:187], v[88:91]\n"
  "s_add_u32 s60, s24, s98\n"
  "s_addc_u32 s61, s25, 0\n"
  "ds_read_b128 a[192:195], v229 offset:0\n"
  "v_mfma_f32_16x16x32_bf16 v[88:91], v[200:203], a[168:171], v[88:91]\n"
  "s_sub_u32 s99, 120, s87\n"
  "v_mfma_f32_16x16x32_bf16 v[84:87], v[200:203], a[164:167], v[84:87]\n"
  "s_add_u32 m0, s48, 0\n"
  "ds_read_b128 a[196:199], v229 offset:2048\n"
  "v_mfma_f32_16x16x32_bf16 v[84:87], v[204:207], a[180:183], v[84:87]\n"
  "v_mfma_f32_16x16x32_bf16 v[80:83], v[204:207], a[176:179], v[80:83]\n"
  "s_cmp_lt_u32 s35, 2\n"
  "s_cselect_b32 s54, s99, s87\n"
  "ds_read_b64_tr_b16 v[204:205], v215 offset:16384\n"
  "v_mfma_f32_16x16x32_bf16 v[80:83], v[200:203], a[160:163], v[80:83]\n"
  "ds_read_b64_tr_b16 v[206:207], v215 offset:17408\n"
  "s_waitcnt lgkmcnt(6)\n"
  "v_mfma_f32_16x16x32_bf16 v[96:99], v[196:199], a[160:163], v[96:99]\n"
  "s_add_u32 s87, s35, 1\n"
  "ds_read_b64_tr_b16 v[200:201], v215 offset:24576\n"
  "s_waitcnt lgkmcnt(5)\n"
  "v_mfma_f32_16x16x32_bf16 v[96:99], v[192:195], a[176:179], v[96:99]\n"
  "ds_read_b64_tr_b16 v[202:203], v215 offset:25600\n"
  "v_mfma_f32_16x16x32_bf16 v[100:103], v[192:195], a[180:183], v[100:103]\n"
  "ds_read_b128 a[200:203], v229 offset:4096\n"
  "v_mfma_f32_16x16x32_bf16 v[100:103], v[196:199], a[164:167], v[100:103]\n"
  "s_and_b32 s87, s87, 2\n"
  "ds_read_b128 a[204:207], v229 offset:6144\n"
  "v_mfma_f32_16x16x32_bf16 v[104:107], v[196:199], a[168:171], v[104:107]\n"
  "ds_read_b128 a[208:211], v230 offset:0\n"
  "v_mfma_f32_16x16x32_bf16 v[104:107], v[192:195], a[184:187], v[104:107]\n"
  "ds_read_b128 a[212:215], v230 offset:2048\n"
  "v_mfma_f32_16x16x32_bf16 v[108:111], v[192:195], a[188:191], v[108:111]\n"
  "s_barrier\n"
  "ds_read_b64_tr_b16 v[192:193], v208 offset:32768\n"
  "v_mfma_f32_16x16x32_bf16 v[108:111], v[196:199], a[172:175], v[108:111]\n"
  "s_lshl_b32 s87, s87, 1\n"
  "ds_read_b64_tr_b16 v[194:195], v208 offset:33792\n"
  "s_waitcnt lgkmcnt(8)\n"
  "v_mfma_f32_16x16x32_bf16 v[124:127], v[204:207], a[172:175], v[124:127]\n"
  "ds_read_b64_tr_b16 v[196:197], v208 offset:40960\n"
  "s_waitcnt lgkmcnt(7)\n"
  "v_mfma_f32_16x16x32_bf16 v[124:127], v[200:203], a[188:191], v[124:127]\n"
  "s_add_u32 s54, s54, s87\n"
  "ds_read_b64_tr_b16 v[198:199], v208 offset:41984\n"
  "v_mfma_f32_16x16x32_bf16 v[120:123], v[200:203], a[184:187], v[120:123]\n"
  "ds_read_b128 a[216:219], v230 offset:4096\n"
  "v_mfma_f32_16x16x32_bf16 v[120:123], v[204:207], a[168:171], v[120:123]\n"
  "ds_read_b128 a[220:223], v230 offset:6144\n"
  "v_mfma_f32_16x16x32_bf16 v[116:119], v[204:207], a[164:167], v[116:119]\n"
  "global_load_lds_dwordx4 v224, s[60:61] offset:0\n"
  "v_mfma_f32_16x16x32_bf16 v[116:119], v[200:203], a[180:183], v[116:119]\n"
  "global_load_lds_dwordx4 v225, s[60:61] offset:1024\n"
  "v_mfma_f32_16x16x32_bf16 v[112:115], v[200:203], a[176:179], v[112:115]\n"
  "ds_read_b64_tr_b16 v[200:201], v209 offset:32768\n"
  "v_mfma_f32_16x16x32_bf16 v[112:115], v[204:207], a[160:163], v[112:115]\n"
  "ds_read_b64_tr_b16 v[202:203], v209 offset:33792\n"
  "global_load_lds_dwordx4 v226, s[60:61] offset:2048\n"
  "global_load_lds_dwordx4 v227, s[60:61] offset:3072\n"
  "global_load_dword v231, v179, s[24:25] offset:0\n"
  "v_add_u32 v179, 0x100000, v179\n"
  "L_stg6_%=:\n"
  "s_waitcnt lgkmcnt(6)\n"
  "v_mfma_f32_16x16x32_bf16 v[0:3], v[192:195], a[192:195], v[0:3]\n"
  "ds_read_b64_tr_b16 v[204:205], v209 offset:40960\n"
  "s_waitcnt lgkmcnt(5)\n"
  "v_mfma_f32_16x16x32_bf16 v[0:3], v[196:199], a[208:211], v[0:3]\n"
  "s_barrier\n"
  "ds_read_b64_tr_b16 v[206:207], v209 offset:41984\n"
  "v_mfma_f32_16x16x32_bf16 v[4:7], v[196:199], a[212:215], v[4:7]\n"
  "s_lshl_b32 s87, s54, 14\n"
  "v_mfma_f32_16x16x32_bf16 v[4:7], v[192:195], a[196:199], v[4:7]\n"
  "s_add_u32 s98, s19, 2\n"
  "v_mfma_f32_16x16x32_bf16 v[8:11], v[192:195], a[200:203], v[8:11]\n"
  "s_add_u32 s87, s87, 0\n"
  "s_waitcnt lgkmcnt(5)\n"
  "v_mfma_f32_16x16x32_bf16 v[8:11], v[196:199], a[216:219], v[8:11]\n"
  "s_waitcnt lgkmcnt(4)\n"
  "v_mfma_f32_16x16x32_bf16 v[12:15], v[196:199], a[220:223], v[12:15]\n"
  "s_and_b32 s98, s98, 7\n"
  "ds_read_b64_tr_b16 v[196:197], v210 offset:32768\n"
  "v_mfma_f32_16x16x32_bf16 v[12:15], v[192:195], a[204:207], v[12:15]\n"
  "s_add_u32 s50, s84, s87\n"
  "s_addc_u32 s51, s85, 0\n"
  "ds_read_b64_tr_b16 v[198:199], v210 offset:33792\n"
  "s_waitcnt lgkmcnt(4)\n"
  "v_mfma_f32_16x16x32_bf16 v[28:31], v[200:203], a[204:207], v[28:31]\n"
  "ds_read_b64_tr_b16 v[192:193], v210 offset:40960\n"
  "s_waitcnt lgkmcnt(3)\n"
  "v_mfma_f32_16x16x32_bf16 v[28:31], v[204:207], a[220:223], v[28:31]\n"
  "s_mul_i32 s98, s98, 524288\n"
  "ds_read_b64_tr_b16 v[194:195], v210 offset:41984\n"
  "v_add_u32 v252, 16384, v232\n"
  "v_mfma_f32_16x16x32_bf16 v[24:27], v[204:207], a[216:219], v[24:27]\n"
  "v_mfma_f32_16x16x32_bf16 v[24:27], v[200:203], a[200:203], v[24:27]\n"
  "v_mfma_f32_16x16x32_bf16 v[20:23], v[200:203], a[196:199], v[20:23]\n"
  "v_add_u32 v253, 32768, v232\n"
  "v_mfma_f32_16x16x32_bf16 v[20:23], v[204:207], a[212:215], v[20:23]\n"
  "s_add_u32 s60, s24, s98\n"
  "s_addc_u32 s61, s25, 0\n"
  "v_mfma_f32_16x16x32_bf16 v[16:19], v[204:207], a[208:211], v[16:19]\n"
  "ds_read_b64_tr_b16 v[204:205], v211 offset:32768\n"
  "v_mfma_f32_16x16x32_bf16 v[16:19], v[200:203], a[192:195], v[16:19]\n"
  "ds_read_b64_tr_b16 v[206:207], v211 offset:33792\n"
  "v_add_u32 v254, 49152, v232\n"
  "s_waitcnt lgkmcnt(4)\n"
  "v_mfma_f32_16x16x32_bf16 v[32:35], v[196:199], a[192:195], v[32:35]\n"
  "s_add_u32 m0, s48, 16384\n"
  "ds_read_b64_tr_b16 v[200:201], v211 offset:40960\n"
  "s_waitcnt lgkmcnt(3)\n"
  "v_mfma_f32_16x16x32_bf16 v[32:35], v[192:195], a[208:211], v[32:35]\n"
  "s_mov_b32 s88, 0xf0000\n"
  "ds_read_b64_tr_b16 v[202:203], v211 offset:41984\n"
  "v_mfma_f32_16x16x32_bf16 v[36:39], v[192:195], a[212:215], v[36:39]\n"
  "v_mfma_f32_16x16x32_bf16 v[36:39], v[196:199], a[196:199], v[36:39]\n"
  "global_load_lds_dwordx4 v224, s[60:61] offset:0\n"
  "v_mfma_f32_16x16x32_bf16 v[40:43], v[196:199], a[200:203], v[40:43]\n"
  "s_mov_b32 s89, 0xfff00ff\n"
  "v_mfma_f32_16x16x32_bf16 v[40:43], v[192:195], a[216:219], v[40:43]\n"
  "v_mfma_f32_16x16x32_bf16 v[44:47], v[192:195], a[220:223], v[44:47]\n"
  "ds_read_b64_tr_b16 v[192:193], v212 offset:32768\n"
  "v_mfma_f32_16x16x32_bf16 v[44:47], v[196:199], a[204:207], v[44:47]\n"
  "s_mov_b32 s90, 0x1f0001\n"
  "ds_read_b64_tr_b16 v[194:195], v212 offset:33792\n"
  "s_waitcnt lgkmcnt(4)\n"
  "v_mfma_f32_16x16x32_bf16 v[60:63], v[204:207], a[204:207], v[60:63]\n"
  "ds_read_b64_tr_b16 v[196:197], v212 offset:40960\n"
  "s_waitcnt lgkmcnt(3)\n"
  "v_mfma_f32_16x16x32_bf16 v[60:63], v[200:203], a[220:223], v[60:63]\n"
  "s_mov_b32 s91, 0x1fff01ff\n"
  "ds_read_b64_tr_b16 v[198:199], v212 offset:41984\n"
  "v_mfma_f32_16x16x32_bf16 v[56:59], v[200:203], a[216:219], v[56:59]\n"
  "global_load_lds_dwordx4 v225, s[60:61] offset:1024\n"
  "v_mfma_f32_16x16x32_bf16 v[56:59], v[204:207], a[200:203], v[56:59]\n"
  "global_load_lds_dwordx4 v226, s[60:61] offset:2048\n"
  "v_mfma_f32_16x16x32_bf16 v[52:55], v[204:207], a[196:199], v[52:55]\n"
  "s_mov_b32 s92, 0x3f0003\n"
  "v_mfma_f32_16x16x32_bf16 v[52:55], v[200:203], a[212:215], v[52:55]\n"
  "global_load_lds_dwordx4 v227, s[60:61] offset:3072\n"
  "v_mfma_f32_16x16x32_bf16 v[48:51], v[200:203], a[208:211], v[48:51]\n"
  "ds_read_b64_tr_b16 v[200:201], v213 offset:32768\n"
  "v_mfma_f32_16x16x32_bf16 v[48:51], v[204:207], a[192:195], v[48:51]\n"
  "s_mov_b32 s93, 0x3fff03ff\n"
  "ds_read_b64_tr_b16 v[202:203], v213 offset:33792\n"
  "s_waitcnt lgkmcnt(4)\n"
  "v_mfma_f32_16x16x32_bf16 v[64:67], v[192:195], a[192:195], v[64:67]\n"
  "ds_read_b64_tr_b16 v[204:205], v213 offset:40960\n"
  "s_waitcnt lgkmcnt(3)\n"
  "v_mfma_f32_16x16x32_bf16 v[64:67], v[196:199], a[208:211], v[64:67]\n"
  "s_mov_b32 s94, 0x7f0007\n"
  "ds_read_b64_tr_b16 v[206:207], v213 offset:41984\n"
  "v_mfma_f32_16x16x32_bf16 v[68:71], v[196:199], a[212:215], v[68:71]\n"
  "global_load_dword v231, v179, s[24:25] offset:0\n"
  "v_mfma_f32_16x16x32_bf16 v[68:71], v[192:195], a[196:199], v[68:71]\n"
  "v_mfma_f32_16x16x32_bf16 v[72:75], v[192:195], a[200:203], v[72:75]\n"
  "s_mov_b32 s95, 0x7fff07ff\n"
  "v_add_u32 v179, 0x100000, v179\n"
  "v_mfma_f32_16x16x32_bf16 v[72:75], v[196:199], a[216:219], v[72:75]\n"
  "v_mfma_f32_16x16x32_bf16 v[76:79], v[196:199], a[220:223], v[76:79]\n"
  "s_and_b32 s56, s86, 3\n"
  "ds_read_b64_tr_b16 v[196:197], v214 offset:32768\n"
  "v_mfma_f32_16x16x32_bf16 v[76:79], v[192:195], a[204:207], v[76:79]\n"
  "s_lshl_b32 s36, s34, 5\n"
  "ds_read_b64_tr_b16 v[198:199], v214 offset:33792\n"
  "s_waitcnt lgkmcnt(4)\n"
  "v_mfma_f32_16x16x32_bf16 v[92:95], v[200:203], a[204:207], v[92:95]\n"
  "s_waitcnt vmcnt(10)\n"
  "ds_read_b64_tr_b16 v[192:193], v214 offset:40960\n"
  "s_waitcnt lgkmcnt(3)\n"
  "v_mfma_f32_16x16x32_bf16 v[92:95], v[204:207], a[220:223], v[92:95]\n"
  "s_lshr_b32 s87, s86, 2\n"
  "ds_read_b64_tr_b16 v[194:195], v214 offset:41984\n"
  "v_mfma_f32_16x16x32_bf16 v[88:91], v[204:207], a[216:219], v[88:91]\n"
  "ds_read_b128 a[224:227], v229 offset:8192\n"
  "v_mfma_f32_16x16x32_bf16 v[88:91], v[200:203], a[200:203], v[88:91]\n"
  "v_and_b32 v219, 31, v233\n"
  "v_mfma_f32_16x16x32_bf16 v[84:87], v[200:203], a[196:199], v[84:87]\n"
  "s_and_b32 s87, s87, 1\n"
  "ds_read_b128 a[228:231], v229 offset:10240\n"
  "v_mfma_f32_16x16x32_bf16 v[84:87], v[204:207], a[212:215], v[84:87]\n"
  "v_mfma_f32_16x16x32_bf16 v[80:83], v[204:207], a[208:211], v[80:83]\n"
  "s_lshl_b32 s87, s87, 3\n"
  "ds_read_b64_tr_b16 v[204:205], v215 offset:32768\n"
  "v_lshlrev_b32 v219, 0, v219\n"
  "v_mfma_f32_16x16x32_bf16 v[80:83], v[200:203], a[192:195], v[80:83]\n"
  "ds_read_b64_tr_b16 v[206:207], v215 offset:33792\n"
  "s_waitcnt lgkmcnt(6)\n"
  "v_mfma_f32_16x16x32_bf16 v[96:99], v[196:199], a[192:195], v[96:99]\n"
  "ds_read_b64_tr_b16 v[200:201], v215 offset:40960\n"
  "s_waitcnt lgkmcnt(5)\n"
  "v_mfma_f32_16x16x32_bf16 v[96:99], v[192:195], a[208:211], v[96:99]\n"
  "s_lshr_b32 s57, s86, 5\n"
  "ds_read_b64_tr_b16 v[202:203], v215 offset:41984\n"
  "v_add_u32 v219, s36, v219\n"
  "v_mfma_f32_16x16x32_bf16 v[100:103], v[192:195], a[212:215], v[100:103]\n"
  "ds_read_b128 a[232:235], v229 offset:12288\n"
  "v_mfma_f32_16x16x32_bf16 v[100:103], v[196:199], a[196:199], v[100:103]\n"
  "ds_read_b128 a[236:239], v229 offset:14336\n"
  "v_mfma_f32_16x16x32_bf16 v[104:107], v[196:199], a[200:203], v[104:107]\n"
  "s_add_u32 s57, s57, s87\n"
  "ds_read_b128 a[240:243], v230 offset:8192\n"
  "v_mfma_f32_16x16x32_bf16 v[104:107], v[192:195], a[216:219], v[104:107]\n"
  "ds_read_b128 a[244:247], v230 offset:10240\n"
  "v_lshlrev_b32 v219, 9, v219\n"
  "v_mfma_f32_16x16x32_bf16 v[108:111], v[192:195], a[220:223], v[108:111]\n"
  "s_barrier\n"
  "ds_read_b64_tr_b16 v[192:193], v208 offset:49152\n"
  "v_mfma_f32_16x16x32_bf16 v[108:111], v[196:199], a[204:207], v[108:111]\n"
  "s_lshr_b32 s58, s86, 3\n"
  "ds_read_b64_tr_b16 v[194:195], v208 offset:50176\n"
  "s_waitcnt lgkmcnt(8)\n"
  "v_mfma_f32_16x16x32_bf16 v[124:127], v[204:207], a[204:207], v[124:127]\n"
  "ds_read_b64_tr_b16 v[196:197], v208 offset:57344\n"
  "s_waitcnt lgkmcnt(7)\n"
  "v_mfma_f32_16x16x32_bf16 v[124:127], v[200:203], a[220:223], v[124:127]\n"
  "s_and_b32 s58, s58, 3\n"
  "ds_read_b64_tr_b16 v[198:199], v208 offset:58368\n"
  "v_mfma_f32_16x16x32_bf16 v[120:123], v[200:203], a[216:219], v[120:123]\n"
  "ds_read_b128 a[248:251], v230 offset:12288\n"
  "v_mfma_f32_16x16x32_bf16 v[120:123], v[204:207], a[200:203], v[120:123]\n"
  "ds_read_b128 a[252:255], v230 offset:14336\n"
  "v_mfma_f32_16x16x32_bf16 v[116:119], v[204:207], a[196:199], v[116:119]\n"
  "global_load_dwordx2 v[234:235], v219, s[62:63] offset:0 sc1\n"
  "v_mfma_f32_16x16x32_bf16 v[116:119], v[200:203], a[212:215], v[116:119]\n"
  "v_mfma_f32_16x16x32_bf16 v[112:115], v[200:203], a[208:211], v[112:115]\n"
  "ds_read_b64_tr_b16 v[200:201], v209 offset:49152\n"
  "v_mfma_f32_16x16x32_bf16 v[112:115], v[204:207], a[192:195], v[112:115]\n"
  "ds_read_b64_tr_b16 v[202:203], v209 offset:50176\n"
  "L_stg7_%=:\n"
  "s_waitcnt lgkmcnt(6)\n"
  "v_mfma_f32_16x16x32_bf16 v[0:3], v[192:195], a[224:227], v[0:3]\n"
  "ds_read_b64_tr_b16 v[204:205], v209 offset:57344\n"
  "s_waitcnt lgkmcnt(5)\n"
  "v_mfma_f32_16x16x32_bf16 v[0:3], v[196:199], a[240:243], v[0:3]\n"
  "s_barrier\n"
  "ds_read_b64_tr_b16 v[206:207], v209 offset:58368\n"
  "v_mfma_f32_16x16x32_bf16 v[4:7], v[196:199], a[244:247], v[4:7]\n"
  "v_mfma_f32_16x16x32_bf16 v[4:7], v[192:195], a[228:231], v[4:7]\n"
  "s_add_u32 s98, s19, 4\n"
  "v_mfma_f32_16x16x32_bf16 v[8:11], v[192:195], a[232:235], v[8:11]\n"
  "s_waitcnt lgkmcnt(5)\n"
  "v_mfma_f32_16x16x32_bf16 v[8:11], v[196:199], a[248:251], v[8:11]\n"
  "s_waitcnt lgkmcnt(4)\n"
  "v_mfma_f32_16x16x32_bf16 v[12:15], v[196:199], a[252:255], v[12:15]\n"
  "s_and_b32 s98, s98, 7\n"
  "ds_read_b64_tr_b16 v[196:197], v210 offset:49152\n"
  "v_mfma_f32_16x16x32_bf16 v[12:15], v[192:195], a[236:239], v[12:15]\n"
  "ds_read_b64_tr_b16 v[198:199], v210 offset:50176\n"
  "s_waitcnt lgkmcnt(4)\n"
  "v_mfma_f32_16x16x32_bf16 v[28:31], v[200:203], a[236:239], v[28:31]\n"
  "ds_read_b64_tr_b16 v[192:193], v210 offset:57344\n"
  "v_add_u32 v229, s49, v232\n"
  "s_waitcnt lgkmcnt(3)\n"
  "v_mfma_f32_16x16x32_bf16 v[28:31], v[204:207], a[252:255], v[28:31]\n"
  "s_mul_i32 s98, s98, 524288\n"
  "ds_read_b64_tr_b16 v[194:195], v210 offset:58368\n"
  "v_mfma_f32_16x16x32_bf16 v[24:27], v[204:207], a[248:251], v[24:27]\n"
  "s_lshl_b32 s99, s35, 13\n"
  "v_mfma_f32_16x16x32_bf16 v[24:27], v[200:203], a[232:235], v[24:27]\n"
  "v_mfma_f32_16x16x32_bf16 v[20:23], v[200:203], a[228:231], v[20:23]\n"
  "s_add_u32 s60, s24, s98\n"
  "s_addc_u32 s61, s25, 0\n"
  "s_add_u32 s99, s99, s38\n"
  "v_mfma_f32_16x16x32_bf16 v[20:23], v[204:207], a[244:247], v[20:23]\n"
  "v_mfma_f32_16x16x32_bf16 v[16:19], v[204:207], a[240:243], v[16:19]\n"
  "s_add_u32 s99, s99, 131072\n"
  "ds_read_b64_tr_b16 v[204:205], v211 offset:49152\n"
  "v_mfma_f32_16x16x32_bf16 v[16:19], v[200:203], a[224:227], v[16:19]\n"
  "s_add_u32 m0, s48, 32768\n"
  "ds_read_b64_tr_b16 v[206:207], v211 offset:50176\n"
  "s_waitcnt lgkmcnt(4)\n"
  "v_mfma_f32_16x16x32_bf16 v[32:35], v[196:199], a[224:227], v[32:35]\n"
  "ds_read_b64_tr_b16 v[200:201], v211 offset:57344\n"
  "v_add_u32 v230, s99, v232\n"
  "s_waitcnt lgkmcnt(3)\n"
  "v_mfma_f32_16x16x32_bf16 v[32:35], v[192:195], a[240:243], v[32:35]\n"
  "ds_read_b64_tr_b16 v[202:203], v211 offset:58368\n"
  "v_mfma_f32_16x16x32_bf16 v[36:39], v[192:195], a[244:247], v[36:39]\n"
  "global_load_lds_dwordx4 v224, s[60:61] offset:0\n"
  "v_mfma_f32_16x16x32_bf16 v[36:39], v[196:199], a[228:231], v[36:39]\n"
  "v_mfma_f32_16x16x32_bf16 v[40:43], v[196:199], a[232:235], v[40:43]\n"
  "v_mfma_f32_16x16x32_bf16 v[40:43], v[192:195], a[248:251], v[40:43]\n"
  "global_load_lds_dwordx4 v225, s[60:61] offset:1024\n"
  "v_mfma_f32_16x16x32_bf16 v[44:47], v[192:195], a[252:255], v[44:47]\n"
  "ds_read_b64_tr_b16 v[192:193], v212 offset:49152\n"
  "v_mfma_f32_16x16x32_bf16 v[44:47], v[196:199], a[236:239], v[44:47]\n"
  "ds_read_b64_tr_b16 v[194:195], v212 offset:50176\n"
  "s_waitcnt lgkmcnt(4)\n"
  "v_mfma_f32_16x16x32_bf16 v[60:63], v[204:207], a[236:239], v[60:63]\n"
  "ds_read_b64_tr_b16 v[196:197], v212 offset:57344\n"
  "s_waitcnt lgkmcnt(3)\n"
  "v_mfma_f32_16x16x32_bf16 v[60:63], v[200:203], a[252:255], v[60:63]\n"
  "ds_read_b64_tr_b16 v[198:199], v212 offset:58368\n"
  "v_mfma_f32_16x16x32_bf16 v[56:59], v[200:203], a[248:251], v[56:59]\n"
  "global_load_lds_dwordx4 v226, s[60:61] offset:2048\n"
  "v_mfma_f32_16x16x32_bf16 v[56:59], v[204:207], a[232:235], v[56:59]\n"
  "global_load_lds_dwordx4 v227, s[60:61] offset:3072\n"
  "v_mfma_f32_16x16x32_bf16 v[52:55], v[204:207], a[228:231], v[52:55]\n"
  "v_mfma_f32_16x16x32_bf16 v[52:55], v[200:203], a[244:247], v[52:55]\n"
  "v_mfma_f32_16x16x32_bf16 v[48:51], v[200:203], a[240:243], v[48:51]\n"
  "s_lshl_b32 s36, s34, 2\n"
  "ds_read_b64_tr_b16 v[200:201], v213 offset:49152\n"
  "v_mfma_f32_16x16x32_bf16 v[48:51], v[204:207], a[224:227], v[48:51]\n"
  "ds_read_b64_tr_b16 v[202:203], v213 offset:50176\n"
  "s_waitcnt lgkmcnt(4)\n"
  "v_mfma_f32_16x16x32_bf16 v[64:67], v[192:195], a[224:227], v[64:67]\n"
  "ds_read_b64_tr_b16 v[204:205], v213 offset:57344\n"
  "s_waitcnt lgkmcnt(3)\n"
  "v_mfma_f32_16x16x32_bf16 v[64:67], v[196:199], a[240:243], v[64:67]\n"
  "s_add_u32 s36, s36, s35\n"
  "ds_read_b64_tr_b16 v[206:207], v213 offset:58368\n"
  "v_mfma_f32_16x16x32_bf16 v[68:71], v[196:199], a[244:247], v[68:71]\n"
  "v_mfma_f32_16x16x32_bf16 v[68:71], v[192:195], a[228:231], v[68:71]\n"
  "v_mfma_f32_16x16x32_bf16 v[72:75], v[192:195], a[232:235], v[72:75]\n"
  "s_lshl_b32 s36, s36, 4\n"
  "v_mfma_f32_16x16x32_bf16 v[72:75], v[196:199], a[248:251], v[72:75]\n"
  "v_mfma_f32_16x16x32_bf16 v[76:79], v[196:199], a[252:255], v[76:79]\n"
  "ds_read_b64_tr_b16 v[196:197], v214 offset:49152\n"
  "v_mfma_f32_16x16x32_bf16 v[76:79], v[192:195], a[236:239], v[76:79]\n"
  "ds_read_b64_tr_b16 v[198:199], v214 offset:50176\n"
  "v_bfe_u32 v176, v233, 1, 4\n"
  "s_waitcnt lgkmcnt(4)\n"
  "v_mfma_f32_16x16x32_bf16 v[92:95], v[200:203], a[236:239], v[92:95]\n"
  "ds_read_b64_tr_b16 v[192:193], v214 offset:57344\n"
  "s_waitcnt lgkmcnt(3)\n"
  "v_mfma_f32_16x16x32_bf16 v[92:95], v[204:207], a[252:255], v[92:95]\n"
  "ds_read_b64_tr_b16 v[194:195], v214 offset:58368\n"
  "v_mfma_f32_16x16x32_bf16 v[88:91], v[204:207], a[248:251], v[88:91]\n"
  "v_add_u32 v176, s36, v176\n"
  "v_mfma_f32_16x16x32_bf16 v[88:91], v[200:203], a[232:235], v[88:91]\n"
  "v_mfma_f32_16x16x32_bf16 v[84:87], v[200:203], a[228:231], v[84:87]\n"
  "v_mfma_f32_16x16x32_bf16 v[84:87], v[204:207], a[244:247], v[84:87]\n"
  "v_mul_u32_u24 v176, 2752, v176\n"
  "v_mfma_f32_16x16x32_bf16 v[80:83], v[204:207], a[240:243], v[80:83]\n"
  "ds_read_b64_tr_b16 v[204:205], v215 offset:49152\n"
  "v_mfma_f32_16x16x32_bf16 v[80:83], v[200:203], a[224:227], v[80:83]\n"
  "ds_read_b64_tr_b16 v[206:207], v215 offset:50176\n"
  "s_waitcnt lgkmcnt(4)\n"
  "v_mfma_f32_16x16x32_bf16 v[96:99], v[196:199], a[224:227], v[96:99]\n"
  "ds_read_b64_tr_b16 v[200:201], v215 offset:57344\n"
  "v_and_b32 v177, 1, v233\n"
  "s_waitcnt lgkmcnt(3)\n"
  "v_mfma_f32_16x16x32_bf16 v[96:99], v[192:195], a[240:243], v[96:99]\n"
  "ds_read_b64_tr_b16 v[202:203], v215 offset:58368\n"
  "v_mfma_f32_16x16x32_bf16 v[100:103], v[192:195], a[244:247], v[100:103]\n"
  "v_mfma_f32_16x16x32_bf16 v[100:103], v[196:199], a[228:231], v[100:103]\n"
  "v_mul_u32_u24 v177, 124, v177\n"
  "v_mfma_f32_16x16x32_bf16 v[104:107], v[196:199], a[232:235], v[104:107]\n"
  "v_mfma_f32_16x16x32_bf16 v[104:107], v[192:195], a[248:251], v[104:107]\n"
  "v_mfma_f32_16x16x32_bf16 v[108:111], v[192:195], a[252:255], v[108:111]\n"
  "s_waitcnt vmcnt(11)\n"
  "s_barrier\n"
  "ds_read_b64_tr_b16 v[192:193], v216 offset:0\n"
  "v_mfma_f32_16x16x32_bf16 v[108:111], v[196:199], a[236:239], v[108:111]\n"
  "ds_read_b64_tr_b16 v[194:195], v216 offset:512\n"
  "v_add_u32 v176, v177, v176\n"
  "s_waitcnt lgkmcnt(4)\n"
  "v_mfma_f32_16x16x32_bf16 v[124:127], v[204:207], a[236:239], v[124:127]\n"
  "ds_read_b64_tr_b16 v[196:197], v216 offset:4096\n"
  "s_waitcnt lgkmcnt(3)\n"
  "v_mfma_f32_16x16x32_bf16 v[124:127], v[200:203], a[252:255], v[124:127]\n"
  "ds_read_b64_tr_b16 v[198:199], v216 offset:4608\n"
  "v_mfma_f32_16x16x32_bf16 v[120:123], v[200:203], a[248:251], v[120:123]\n"
  "global_load_dword v231, v176, s[26:27] offset:0\n"
  "v_mfma_f32_16x16x32_bf16 v[120:123], v[204:207], a[232:235], v[120:123]\n"
  "v_mfma_f32_16x16x32_bf16 v[116:119], v[204:207], a[228:231], v[116:119]\n"
  "v_mfma_f32_16x16x32_bf16 v[116:119], v[200:203], a[244:247], v[116:119]\n"
  "v_mfma_f32_16x16x32_bf16 v[112:115], v[200:203], a[240:243], v[112:115]\n"
  "ds_read_b64_tr_b16 v[200:201], v217 offset:0\n"
  "v_mfma_f32_16x16x32_bf16 v[112:115], v[204:207], a[224:227], v[112:115]\n"
  "ds_read_b64_tr_b16 v[202:203], v217 offset:512\n"
  "s_waitcnt vmcnt(5)\n"
  "v_cmp_ne_u64 s[96:97], s[16:17], v[234:235]\n"
  "s_nop 0\n"
  "s_cmp_lg_u64 s[96:97], 0\n"
  "s_cbranch_scc1 L_xn_retry_%=\n"
  "L_xn_ok_%=:\n"
  "L_stg8_%=:\n"
  "s_waitcnt lgkmcnt(4)\n"
  "v_mfma_f32_16x16x32_bf16 v[128:131], a[0:3], v[192:195], 0\n"
  "ds_read_b64_tr_b16 v[204:205], v217 offset:4096\n"
  "s_waitcnt lgkmcnt(3)\n"
  "v_mfma_f32_16x16x32_bf16 v[128:131], a[16:19], v[196:199], v[128:131]\n"
  "s_barrier\n"
  "ds_read_b64_tr_b16 v[206:207], v217 offset:4608\n"
  "v_mfma_f32_16x16x32_bf16 v[132:135], a[20:23], v[196:199], 0\n"
  "v_mfma_f32_16x16x32_bf16 v[132:135], a[4:7], v[192:195], v[132:135]\n"
  "s_add_u32 s98, s19, 6\n"
  "v_mfma_f32_16x16x32_bf16 v[136:139], a[8:11], v[192:195], 0\n"
  "v_mul_f32 v0, 0x3e0293ee, v0\n"
  "v_mul_f32 v1, 0x3e0293ee, v1\n"
  "v_mfma_f32_16x16x32_bf16 v[136:139], a[24:27], v[196:199], v[136:139]\n"
  "v_mul_f32 v2, 0x3e0293ee, v2\n"
  "v_mul_f32 v3, 0x3e0293ee, v3\n"
  "v_mfma_f32_16x16x32_bf16 v[140:143], a[28:31], v[196:199], 0\n"
  "s_and_b32 s98, s98, 7\n"
  "ds_read_b64_tr_b16 v[196:197], v218 offset:0\n"
  "v_mul_f32 v16, 0x3e0293ee, v16\n"
  "v_mfma_f32_16x16x32_bf16 v[140:143], a[12:15], v[192:195], v[140:143]\n"
  "ds_read_b64_tr_b16 v[198:199], v218 offset:512\n"
  "v_mul_f32 v17, 0x3e0293ee, v17\n"
  "s_waitcnt lgkmcnt(4)\n"
  "v_mfma_f32_16x16x32_bf16 v[156:159], a[12:15], v[200:203], 0\n"
  "ds_read_b64_tr_b16 v[192:193], v218 offset:4096\n"
  "v_mul_f32 v18, 0x3e0293ee, v18\n"
  "s_waitcnt lgkmcnt(3)\n"
  "v_mfma_f32_16x16x32_bf16 v[156:159], a[28:31], v[204:207], v[156:159]\n"
  "ds_read_b64_tr_b16 v[194:195], v218 offset:4608\n"
  "v_mul_f32 v19, 0x3e0293ee, v19\n"
  "v_mfma_f32_16x16x32_bf16 v[152:155], a[24:27], v[204:207], 0\n"
  "s_mul_i32 s98, s98, 524288\n"
  "v_cvt_pk_bf16_f32 v236, v0, v1\n"
  "v_cvt_pk_bf16_f32 v237, v2, v3\n"
  "v_mfma_f32_16x16x32_bf16 v[152:155], a[8:11], v[200:203], v[152:155]\n"
  "v_cvt_pk_bf16_f32 v238, v16, v17\n"
  "v_cvt_pk_bf16_f32 v239, v18, v19\n"
  "v_mfma_f32_16x16x32_bf16 v[148:151], a[4:7], v[200:203], 0\n"
  "global_store_dwordx4 v232, v[236:239], s[40:41] offset:-4096 sc1\n"
  "v_mfma_f32_16x16x32_bf16 v[148:151], a[20:23], v[204:207], v[148:151]\n"
  "s_add_u32 s60, s24, s98\n"
  "s_addc_u32 s61, s25, 0\n"
  "v_mfma_f32_16x16x32_bf16 v[144:147], a[16:19], v[204:207], 0\n"
  "ds_read_b64_tr_b16 v[204:205], v228 offset:0\n"
  "v_mfma_f32_16x16x32_bf16 v[144:147], a[0:3], v[200:203], v[144:147]\n"
  "ds_read_b64_tr_b16 v[206:207], v228 offset:512\n"
  "s_waitcnt lgkmcnt(4)\n"
  "v_mfma_f32_16x16x32_bf16 v[160:163], a[0:3], v[196:199], 0\n"
  "ds_read_b64_tr_b16 v[200:201], v228 offset:4096\n"
  "s_waitcnt lgkmcnt(3)\n"
  "v_mfma_f32_16x16x32_bf16 v[160:163], a[16:19], v[192:195], v[160:163]\n"
  "s_add_u32 m0, s48, 49152\n"
  "ds_read_b64_tr_b16 v[202:203], v228 offset:4608\n"
  "v_mfma_f32_16x16x32_bf16 v[164:167], a[20:23], v[192:195], 0\n"
  "v_mul_f32 v32, 0x3e0293ee, v32\n"
  "v_mul_f32 v33, 0x3e0293ee, v33\n"
  "v_mfma_f32_16x16x32_bf16 v[164:167], a[4:7], v[196:199], v[164:167]\n"
  "v_mul_f32 v34, 0x3e0293ee, v34\n"
  "v_mul_f32 v35, 0x3e0293ee, v35\n"
  "v_mfma_f32_16x16x32_bf16 v[168:171], a[8:11], v[196:199], 0\n"
  "v_mul_f32 v48, 0x3e0293ee, v48\n"
  "v_mul_f32 v49, 0x3e0293ee, v49\n"
  "v_mfma_f32_16x16x32_bf16 v[168:171], a[24:27], v[192:195], v[168:171]\n"
  "v_mul_f32 v50, 0x3e0293ee, v50\n"
  "v_mul_f32 v51, 0x3e0293ee, v51\n"
  "v_mfma_f32_16x16x32_bf16 v[172:175], a[28:31], v[192:195], 0\n"
  "ds_read_b64_tr_b16 v[192:193], v216 offset:8192\n"
  "v_cvt_pk_bf16_f32 v240, v32, v33\n"
  "v_mfma_f32_16x16x32_bf16 v[172:175], a[12:15], v[196:199], v[172:175]\n"
  "ds_read_b64_tr_b16 v[194:195], v216 offset:8704\n"
  "v_cvt_pk_bf16_f32 v241, v34, v35\n"
  "s_waitcnt lgkmcnt(4)\n"
  "v_mfma_f32_16x16x32_bf16 v[188:191], a[12:15], v[204:207], 0\n"
  "ds_read_b64_tr_b16 v[196:197], v216 offset:12288\n"
  "v_cvt_pk_bf16_f32 v242, v48, v49\n"
  "s_waitcnt lgkmcnt(3)\n"
  "v_mfma_f32_16x16x32_bf16 v[188:191], a[28:31], v[200:203], v[188:191]\n"
  "ds_read_b64_tr_b16 v[198:199], v216 offset:12800\n"
  "v_cvt_pk_bf16_f32 v243, v50, v51\n"
  "v_mfma_f32_16x16x32_bf16 v[184:187], a[24:27], v[200:203], 0\n"
  "global_store_dwordx4 v232, v[240:243], s[40:41] offset:-3072 sc1\n"
  "v_mfma_f32_16x16x32_bf16 v[184:187], a[8:11], v[204:207], v[184:187]\n"
  "global_load_lds_dwordx4 v224, s[60:61] offset:0\n"
  "v_mfma_f32_16x16x32_bf16 v[180:183], a[4:7], v[204:207], 0\n"
  "global_load_lds_dwordx4 v225, s[60:61] offset:1024\n"
  "v_mfma_f32_16x16x32_bf16 v[180:183], a[20:23], v[200:203], v[180:183]\n"
  "global_load_lds_dwordx4 v226, s[60:61] offset:2048\n"
  "v_mfma_f32_16x16x32_bf16 v[176:179], a[16:19], v[200:203], 0\n"
  "ds_read_b64_tr_b16 v[200:201], v217 offset:8192\n"
  "v_mfma_f32_16x16x32_bf16 v[176:179], a[0:3], v[204:207], v[176:179]\n"
  "ds_read_b64_tr_b16 v[202:203], v217 offset:8704\n"
  "s_waitcnt lgkmcnt(4)\n"
  "v_mfma_f32_16x16x32_bf16 v[128:131], a[32:35], v[192:195], v[128:131]\n"
  "ds_read_b64_tr_b16 v[204:205], v217 offset:12288\n"
  "v_mul_f32 v64, 0x3e0293ee, v64\n"
  "s_waitcnt lgkmcnt(3)\n"
  "v_mfma_f32_16x16x32_bf16 v[128:131], a[48:51], v[196:199], v[128:131]\n"
  "ds_read_b64_tr_b16 v[206:207], v217 offset:12800\n"
  "v_mul_f32 v65, 0x3e0293ee, v65\n"
  "v_mfma_f32_16x16x32_bf16 v[132:135], a[52:55], v[196:199], v[132:135]\n"
  "global_load_lds_dwordx4 v227, s[60:61] offset:3072\n"
  "v_mul_f32 v66, 0x3e0293ee, v66\n"
  "v_mfma_f32_16x16x32_bf16 v[132:135], a[36:39], v[192:195], v[132:135]\n"
  "s_add_u32 s98, s18, 0\n"
  "v_mul_f32 v67, 0x3e0293ee, v67\n"
  "v_mul_f32 v80, 0x3e0293ee, v80\n"
  "v_mfma_f32_16x16x32_bf16 v[136:139], a[40:43], v[192:195], v[136:139]\n"
  "v_mul_f32 v81, 0x3e0293ee, v81\n"
  "v_mul_f32 v82, 0x3e0293ee, v82\n"
  "v_mfma_f32_16x16x32_bf16 v[136:139], a[56:59], v[196:199], v[136:139]\n"
  "v_mul_f32 v83, 0x3e0293ee, v83\n"
  "v_cvt_pk_bf16_f32 v244, v64, v65\n"
  "v_mfma_f32_16x16x32_bf16 v[140:143], a[60:63], v[196:199], v[140:143]\n"
  "s_and_b32 s98, s98, 3\n"
  "ds_read_b64_tr_b16 v[196:197], v218 offset:8192\n"
  "v_cvt_pk_bf16_f32 v245, v66, v67\n"
  "v_mfma_f32_16x16x32_bf16 v[140:143], a[44:47], v[192:195], v[140:143]\n"
  "ds_read_b64_tr_b16 v[198:199], v218 offset:8704\n"
  "v_cvt_pk_bf16_f32 v246, v80, v81\n"
  "s_waitcnt lgkmcnt(4)\n"
  "v_mfma_f32_16x16x32_bf16 v[156:159], a[44:47], v[200:203], v[156:159]\n"
  "ds_read_b64_tr_b16 v[192:193], v218 offset:12288\n"
  "v_cvt_pk_bf16_f32 v247, v82, v83\n"
  "s_waitcnt lgkmcnt(3)\n"
  "v_mfma_f32_16x16x32_bf16 v[156:159], a[60:63], v[204:207], v[156:159]\n"
  "ds_read_b64_tr_b16 v[194:195], v218 offset:12800\n"
  "v_mfma_f32_16x16x32_bf16 v[152:155], a[56:59], v[204:207], v[152:155]\n"
  "s_mul_i32 s98, s98, 4096\n"
  "global_store_dwordx4 v232, v[244:247], s[40:41] offset:-2048 sc1\n"
  "v_mfma_f32_16x16x32_bf16 v[152:155], a[40:43], v[200:203], v[152:155]\n"
  "v_mfma_f32_16x16x32_bf16 v[148:151], a[36:39], v[200:203], v[148:151]\n"
  "v_mfma_f32_16x16x32_bf16 v[148:151], a[52:55], v[204:207], v[148:151]\n"
  "s_add_u32 s52, s50, s98\n"
  "s_addc_u32 s53, s51, 0\n"
  "v_mfma_f32_16x16x32_bf16 v[144:147], a[48:51], v[204:207], v[144:147]\n"
  "ds_read_b64_tr_b16 v[204:205], v228 offset:8192\n"
  "v_mul_f32 v96, 0x3e0293ee, v96\n"
  "v_mfma_f32_16x16x32_bf16 v[144:147], a[32:35], v[200:203], v[144:147]\n"
  "ds_read_b64_tr_b16 v[206:207], v228 offset:8704\n"
  "v_mul_f32 v97, 0x3e0293ee, v97\n"
  "s_waitcnt lgkmcnt(4)\n"
  "v_mfma_f32_16x16x32_bf16 v[160:163], a[32:35], v[196:199], v[160:163]\n"
  "ds_read_b64_tr_b16 v[200:201], v228 offset:12288\n"
  "v_mul_f32 v98, 0x3e0293ee, v98\n"
  "s_waitcnt lgkmcnt(3)\n"
  "v_mfma_f32_16x16x32_bf16 v[160:163], a[48:51], v[192:195], v[160:163]\n"
  "s_add_u32 m0, s99, 0xfffff800\n"
  "ds_read_b64_tr_b16 v[202:203], v228 offset:12800\n"
  "v_mul_f32 v99, 0x3e0293ee, v99\n"
  "v_mfma_f32_16x16x32_bf16 v[164:167], a[52:55], v[192:195], v[164:167]\n"
  "v_mul_f32 v112, 0x3e0293ee, v112\n"
  "v_mul_f32 v113, 0x3e0293ee, v113\n"
  "v_mfma_f32_16x16x32_bf16 v[164:167], a[36:39], v[196:199], v[164:167]\n"
  "v_mul_f32 v114, 0x3e0293ee, v114\n"
  "v_mul_f32 v115, 0x3e0293ee, v115\n"
  "v_mfma_f32_16x16x32_bf16 v[168:171], a[40:43], v[196:199], v[168:171]\n"
  "v_cvt_pk_bf16_f32 v236, v96, v97\n"
  "v_cvt_pk_bf16_f32 v237, v98, v99\n"
  "v_mfma_f32_16x16x32_bf16 v[168:171], a[56:59], v[192:195], v[168:171]\n"
  "v_cvt_pk_bf16_f32 v238, v112, v113\n"
  "v_cvt_pk_bf16_f32 v239, v114, v115\n"
  "v_mfma_f32_16x16x32_bf16 v[172:175], a[60:63], v[192:195], v[172:175]\n"
  "s_barrier\n"
  "ds_read_b64_tr_b16 v[192:193], v216 offset:16384\n"
  "v_mfma_f32_16x16x32_bf16 v[172:175], a[44:47], v[196:199], v[172:175]\n"
  "ds_read_b64_tr_b16 v[194:195], v216 offset:16896\n"
  "s_waitcnt lgkmcnt(4)\n"
  "v_mfma_f32_16x16x32_bf16 v[188:191], a[44:47], v[204:207], v[188:191]\n"
  "ds_read_b64_tr_b16 v[196:197], v216 offset:20480\n"
  "s_waitcnt lgkmcnt(3)\n"
  "v_mfma_f32_16x16x32_bf16 v[188:191], a[60:63], v[200:203], v[188:191]\n"
  "ds_read_b64_tr_b16 v[198:199], v216 offset:20992\n"
  "v_mfma_f32_16x16x32_bf16 v[184:187], a[56:59], v[200:203], v[184:187]\n"
  "ds_write_b128 v229, v[236:239] offset:0\n"
  "v_mfma_f32_16x16x32_bf16 v[184:187], a[40:43], v[204:207], v[184:187]\n"
  "global_load_lds_dwordx4 v232, s[52:53] offset:2048 sc1\n"
  "v_mfma_f32_16x16x32_bf16 v[180:183], a[36:39], v[204:207], v[180:183]\n"
  "global_load_lds_dwordx4 v232, s[52:53] offset:3072 sc1\n"
  "v_mfma_f32_16x16x32_bf16 v[180:183], a[52:55], v[200:203], v[180:183]\n"
  "v_mfma_f32_16x16x32_bf16 v[176:179], a[48:51], v[200:203], v[176:179]\n"
  "ds_read_b64_tr_b16 v[200:201], v217 offset:16384\n"
  "v_mfma_f32_16x16x32_bf16 v[176:179], a[32:35], v[204:207], v[176:179]\n"
  "ds_read_b64_tr_b16 v[202:203], v217 offset:16896\n"
  "L_stg9_%=:\n"
  "s_waitcnt lgkmcnt(5)\n"
  "v_mfma_f32_16x16x32_bf16 v[128:131], a[64:67], v[192:195], v[128:131]\n"
  "ds_read_b64_tr_b16 v[204:205], v217 offset:20480\n"
  "s_waitcnt lgkmcnt(4)\n"
  "v_mfma_f32_16x16x32_bf16 v[128:131], a[80:83], v[196:199], v[128:131]\n"
  "s_barrier\n"
  "ds_read_b64_tr_b16 v[206:207], v217 offset:20992\n"
  "v_mfma_f32_16x16x32_bf16 v[132:135], a[84:87], v[196:199], v[132:135]\n"
  "v_mfma_f32_16x16x32_bf16 v[132:135], a[68:71], v[192:195], v[132:135]\n"
  "s_add_u32 s98, s19, 0\n"
  "v_mfma_f32_16x16x32_bf16 v[136:139], a[72:75], v[192:195], v[136:139]\n"
  "v_mul_f32 v4, 0x3e0293ee, v4\n"
  "v_mul_f32 v5, 0x3e0293ee, v5\n"
  "v_mfma_f32_16x16x32_bf16 v[136:139], a[88:91], v[196:199], v[136:139]\n"
  "s_and_b32 s98, s98, 7\n"
  "v_mul_f32 v6, 0x3e0293ee, v6\n"
  "v_mul_f32 v7, 0x3e0293ee, v7\n"
  "v_mfma_f32_16x16x32_bf16 v[140:143], a[92:95], v[196:199], v[140:143]\n"
  "ds_read_b64_tr_b16 v[196:197], v218 offset:16384\n"
  "v_mul_f32 v20, 0x3e0293ee, v20\n"
  "v_mfma_f32_16x16x32_bf16 v[140:143], a[76:79], v[192:195], v[140:143]\n"
  "s_mul_i32 s98, s98, 524288\n"
  "ds_read_b64_tr_b16 v[198:199], v218 offset:16896\n"
  "v_mul_f32 v21, 0x3e0293ee, v21\n"
  "s_waitcnt lgkmcnt(4)\n"
  "v_mfma_f32_16x16x32_bf16 v[156:159], a[76:79], v[200:203], v[156:159]\n"
  "ds_read_b64_tr_b16 v[192:193], v218 offset:20480\n"
  "v_mul_f32 v22, 0x3e0293ee, v22\n"
  "s_waitcnt lgkmcnt(3)\n"
  "v_mfma_f32_16x16x32_bf16 v[156:159], a[92:95], v[204:207], v[156:159]\n"
  "s_add_u32 s98, s98, 128\n"
  "ds_read_b64_tr_b16 v[194:195], v218 offset:20992\n"
  "v_mul_f32 v23, 0x3e0293ee, v23\n"
  "v_mfma_f32_16x16x32_bf16 v[152:155], a[88:91], v[204:207], v[152:155]\n"
  "v_cvt_pk_bf16_f32 v240, v4, v5\n"
  "v_cvt_pk_bf16_f32 v241, v6, v7\n"
  "v_mfma_f32_16x16x32_bf16 v[152:155], a[72:75], v[200:203], v[152:155]\n"
  "v_cvt_pk_bf16_f32 v242, v20, v21\n"
  "v_cvt_pk_bf16_f32 v243, v22, v23\n"
  "v_mfma_f32_16x16x32_bf16 v[148:151], a[68:71], v[200:203], v[148:151]\n"
  "s_add_u32 s60, s24, s98\n"
  "s_addc_u32 s61, s25, 0\n"
  "global_store_dwordx4 v232, v[240:243], s[40:41] offset:0 sc1\n"
  "v_mfma_f32_16x16x32_bf16 v[148:151], a[84:87], v[204:207], v[148:151]\n"
  "s_add_u32 m0, s48, 0\n"
  "v_mfma_f32_16x16x32_bf16 v[144:147], a[80:83], v[204:207], v[144:147]\n"
  "ds_read_b64_tr_b16 v[204:205], v228 offset:16384\n"
  "v_mfma_f32_16x16x32_bf16 v[144:147], a[64:67], v[200:203], v[144:147]\n"
  "ds_read_b64_tr_b16 v[206:207], v228 offset:16896\n"
  "s_waitcnt lgkmcnt(4)\n"
  "v_mfma_f32_16x16x32_bf16 v[160:163], a[64:67], v[196:199], v[160:163]\n"
  "ds_read_b64_tr_b16 v[200:201], v228 offset:20480\n"
  "s_waitcnt lgkmcnt(3)\n"
  "v_mfma_f32_16x16x32_bf16 v[160:163], a[80:83], v[192:195], v[160:163]\n"
  "ds_read_b64_tr_b16 v[202:203], v228 offset:20992\n"
  "v_mfma_f32_16x16x32_bf16 v[164:167], a[84:87], v[192:195], v[164:167]\n"
  "global_load_lds_dwordx4 v224, s[60:61] offset:0\n"
  "v_mul_f32 v36, 0x3e0293ee, v36\n"
  "v_mfma_f32_16x16x32_bf16 v[164:167], a[68:71], v[196:199], v[164:167]\n"
  "global_load_lds_dwordx4 v225, s[60:61] offset:1024\n"
  "v_mul_f32 v37, 0x3e0293ee, v37\n"
  "v_mfma_f32_16x16x32_bf16 v[168:171], a[72:75], v[196:199], v[168:171]\n"
  "v_mul_f32 v38, 0x3e0293ee, v38\n"
  "v_mul_f32 v39, 0x3e0293ee, v39\n"
  "v_mfma_f32_16x16x32_bf16 v[168:171], a[88:91], v[192:195], v[168:171]\n"
  "v_mul_f32 v52, 0x3e0293ee, v52\n"
  "v_mul_f32 v53, 0x3e0293ee, v53\n"
  "v_mfma_f32_16x16x32_bf16 v[172:175], a[92:95], v[192:195], v[172:175]\n"
  "ds_read_b64_tr_b16 v[192:193], v216 offset:24576\n"
  "v_mul_f32 v54, 0x3e0293ee, v54\n"
  "v_mfma_f32_16x16x32_bf16 v[172:175], a[76:79], v[196:199], v[172:175]\n"
  "ds_read_b64_tr_b16 v[194:195], v216 offset:25088\n"
  "v_mul_f32 v55, 0x3e0293ee, v55\n"
  "s_waitcnt lgkmcnt(4)\n"
  "v_mfma_f32_16x16x32_bf16 v[188:191], a[76:79], v[204:207], v[188:191]\n"
  "ds_read_b64_tr_b16 v[196:197], v216 offset:28672\n"
  "v_cvt_pk_bf16_f32 v244, v36, v37\n"
  "s_waitcnt lgkmcnt(3)\n"
  "v_mfma_f32_16x16x32_bf16 v[188:191], a[92:95], v[200:203], v[188:191]\n"
  "ds_read_b64_tr_b16 v[198:199], v216 offset:29184\n"
  "v_cvt_pk_bf16_f32 v245, v38, v39\n"
  "v_mfma_f32_16x16x32_bf16 v[184:187], a[88:91], v[200:203], v[184:187]\n"
  "v_cvt_pk_bf16_f32 v246, v52, v53\n"
  "v_cvt_pk_bf16_f32 v247, v54, v55\n"
  "v_mfma_f32_16x16x32_bf16 v[184:187], a[72:75], v[204:207], v[184:187]\n"
  "global_store_dwordx4 v232, v[244:247], s[40:41] offset:1024 sc1\n"
  "v_mfma_f32_16x16x32_bf16 v[180:183], a[68:71], v[204:207], v[180:183]\n"
  "global_load_lds_dwordx4 v226, s[60:61] offset:2048\n"
  "v_mfma_f32_16x16x32_bf16 v[180:183], a[84:87], v[200:203], v[180:183]\n"
  "global_load_lds_dwordx4 v227, s[60:61] offset:3072\n"
  "s_add_u32 m0, s99, 0x0\n"
  "v_mfma_f32_16x16x32_bf16 v[176:179], a[80:83], v[200:203], v[176:179]\n"
  "ds_read_b64_tr_b16 v[200:201], v217 offset:24576\n"
  "v_mfma_f32_16x16x32_bf16 v[176:179], a[64:67], v[204:207], v[176:179]\n"
  "ds_read_b64_tr_b16 v[202:203], v217 offset:25088\n"
  "s_waitcnt lgkmcnt(4)\n"
  "v_mfma_f32_16x16x32_bf16 v[128:131], a[96:99], v[192:195], v[128:131]\n"
  "ds_read_b64_tr_b16 v[204:205], v217 offset:28672\n"
  "v_mul_f32 v68, 0x3e0293ee, v68\n"
  "s_waitcnt lgkmcnt(3)\n"
  "v_mfma_f32_16x16x32_bf16 v[128:131], a[112:115], v[196:199], v[128:131]\n"
  "ds_read_b64_tr_b16 v[206:207], v217 offset:29184\n"
  "v_mul_f32 v69, 0x3e0293ee, v69\n"
  "v_mfma_f32_16x16x32_bf16 v[132:135], a[116:119], v[196:199], v[132:135]\n"
  "global_load_lds_dwordx4 v252, s[52:53] offset:2048 sc1\n"
  "v_mul_f32 v70, 0x3e0293ee, v70\n"
  "v_mfma_f32_16x16x32_bf16 v[132:135], a[100:103], v[192:195], v[132:135]\n"
  "global_load_lds_dwordx4 v252, s[52:53] offset:3072 sc1\n"
  "s_add_u32 m0, s49, 4096\n"
  "v_mul_f32 v71, 0x3e0293ee, v71\n"
  "v_mfma_f32_16x16x32_bf16 v[136:139], a[104:107], v[192:195], v[136:139]\n"
  "global_load_lds_dwordx4 v232, s[52:53] offset:0 sc1\n"
  "v_mul_f32 v84, 0x3e0293ee, v84\n"
  "v_mfma_f32_16x16x32_bf16 v[136:139], a[120:123], v[196:199], v[136:139]\n"
  "v_mul_f32 v85, 0x3e0293ee, v85\n"
  "v_mul_f32 v86, 0x3e0293ee, v86\n"
  "v_mfma_f32_16x16x32_bf16 v[140:143], a[124:127], v[196:199], v[140:143]\n"
  "ds_read_b64_tr_b16 v[196:197], v218 offset:24576\n"
  "v_mul_f32 v87, 0x3e0293ee, v87\n"
  "v_mfma_f32_16x16x32_bf16 v[140:143], a[108:111], v[192:195], v[140:143]\n"
  "ds_read_b64_tr_b16 v[198:199], v218 offset:25088\n"
  "v_cvt_pk_bf16_f32 v236, v68, v69\n"
  "s_waitcnt lgkmcnt(4)\n"
  "v_mfma_f32_16x16x32_bf16 v[156:159], a[108:111], v[200:203], v[156:159]\n"
  "ds_read_b64_tr_b16 v[192:193], v218 offset:28672\n"
  "v_cvt_pk_bf16_f32 v237, v70, v71\n"
  "s_waitcnt lgkmcnt(3)\n"
  "v_mfma_f32_16x16x32_bf16 v[156:159], a[124:127], v[204:207], v[156:159]\n"
  "ds_read_b64_tr_b16 v[194:195], v218 offset:29184\n"
  "v_cvt_pk_bf16_f32 v238, v84, v85\n"
  "v_mfma_f32_16x16x32_bf16 v[152:155], a[120:123], v[204:207], v[152:155]\n"
  "global_load_lds_dwordx4 v232, s[52:53] offset:1024 sc1\n"
  "s_lshl_b32 s36, s34, 2\n"
  "v_cvt_pk_bf16_f32 v239, v86, v87\n"
  "v_mfma_f32_16x16x32_bf16 v[152:155], a[104:107], v[200:203], v[152:155]\n"
  "s_add_u32 s36, s36, s35\n"
  "s_lshl_b32 s36, s36, 1\n"
  "global_store_dwordx4 v232, v[236:239], s[40:41] offset:2048 sc1\n"
  "v_mfma_f32_16x16x32_bf16 v[148:151], a[100:103], v[200:203], v[148:151]\n"
  "v_bfe_u32 v234, v233, 2, 1\n"
  "v_lshlrev_b32 v234, 6, v234\n"
  "v_mfma_f32_16x16x32_bf16 v[148:151], a[116:119], v[204:207], v[148:151]\n"
  "v_bfe_u32 v219, v233, 1, 1\n"
  "v_mfma_f32_16x16x32_bf16 v[144:147], a[112:115], v[204:207], v[144:147]\n"
  "ds_read_b64_tr_b16 v[204:205], v228 offset:24576\n"
  "v_mul_f32 v100, 0x3e0293ee, v100\n"
  "v_mfma_f32_16x16x32_bf16 v[144:147], a[96:99], v[200:203], v[144:147]\n"
  "ds_read_b64_tr_b16 v[206:207], v228 offset:25088\n"
  "v_mul_f32 v101, 0x3e0293ee, v101\n"
  "s_waitcnt lgkmcnt(4)\n"
  "v_mfma_f32_16x16x32_bf16 v[160:163], a[96:99], v[196:199], v[160:163]\n"
  "ds_read_b64_tr_b16 v[200:201], v228 offset:28672\n"
  "v_mul_f32 v102, 0x3e0293ee, v102\n"
  "s_waitcnt lgkmcnt(3)\n"
  "v_mfma_f32_16x16x32_bf16 v[160:163], a[112:115], v[192:195], v[160:163]\n"
  "ds_read_b64_tr_b16 v[202:203], v228 offset:29184\n"
  "v_mul_f32 v103, 0x3e0293ee, v103\n"
  "v_mfma_f32_16x16x32_bf16 v[164:167], a[116:119], v[192:195], v[164:167]\n"
  "v_mul_f32 v116, 0x3e0293ee, v116\n"
  "v_mul_f32 v117, 0x3e0293ee, v117\n"
  "v_mfma_f32_16x16x32_bf16 v[164:167], a[100:103], v[196:199], v[164:167]\n"
  "v_mul_f32 v118, 0x3e0293ee, v118\n"
  "v_mul_f32 v119, 0x3e0293ee, v119\n"
  "v_mfma_f32_16x16x32_bf16 v[168:171], a[104:107], v[196:199], v[168:171]\n"
  "v_cvt_pk_bf16_f32 v240, v100, v101\n"
  "v_cvt_pk_bf16_f32 v241, v102, v103\n"
  "v_mfma_f32_16x16x32_bf16 v[168:171], a[120:123], v[192:195], v[168:171]\n"
  "v_cvt_pk_bf16_f32 v242, v116, v117\n"
  "v_cvt_pk_bf16_f32 v243, v118, v119\n"
  "v_mfma_f32_16x16x32_bf16 v[172:175], a[124:127], v[192:195], v[172:175]\n"
  "s_waitcnt vmcnt(21)\n"
  "s_barrier\n"
  "ds_read_b64_tr_b16 v[192:193], v216 offset:32768\n"
  "v_mfma_f32_16x16x32_bf16 v[172:175], a[108:111], v[196:199], v[172:175]\n"
  "ds_read_b64_tr_b16 v[194:195], v216 offset:33280\n"
  "v_add3_u32 v234, v234, v219, s36\n"
  "s_waitcnt lgkmcnt(4)\n"
  "v_mfma_f32_16x16x32_bf16 v[188:191], a[108:111], v[204:207], v[188:191]\n"
  "ds_read_b64_tr_b16 v[196:197], v216 offset:36864\n"
  "v_lshlrev_b32 v234, 13, v234\n"
  "s_waitcnt lgkmcnt(3)\n"
  "v_mfma_f32_16x16x32_bf16 v[188:191], a[124:127], v[200:203], v[188:191]\n"
  "ds_read_b64_tr_b16 v[198:199], v216 offset:37376\n"
  "v_and_b32 v219, 1, v233\n"
  "v_mfma_f32_16x16x32_bf16 v[184:187], a[120:123], v[200:203], v[184:187]\n"
  "ds_write_b128 v229, v[240:243] offset:1024\n"
  "v_mfma_f32_16x16x32_bf16 v[184:187], a[104:107], v[204:207], v[184:187]\n"
  "v_lshl_add_u32 v234, v219, 7, v234\n"
  "v_mfma_f32_16x16x32_bf16 v[180:183], a[100:103], v[204:207], v[180:183]\n"
  "global_load_dword v231, v234, s[28:29] offset:0\n"
  "v_add_u32 v234, 0x100000, v234\n"
  "v_mfma_f32_16x16x32_bf16 v[180:183], a[116:119], v[200:203], v[180:183]\n"
  "v_mfma_f32_16x16x32_bf16 v[176:179], a[112:115], v[200:203], v[176:179]\n"
  "ds_read_b64_tr_b16 v[200:201], v217 offset:32768\n"
  "v_mfma_f32_16x16x32_bf16 v[176:179], a[96:99], v[204:207], v[176:179]\n"
  "ds_read_b64_tr_b16 v[202:203], v217 offset:33280\n"
  "L_stg10_%=:\n"
  "s_waitcnt lgkmcnt(5)\n"
  "v_mfma_f32_16x16x32_bf16 v[128:131], a[128:131], v[192:195], v[128:131]\n"
  "ds_read_b64_tr_b16 v[204:205], v217 offset:36864\n"
  "s_waitcnt lgkmcnt(4)\n"
  "v_mfma_f32_16x16x32_bf16 v[128:131], a[144:147], v[196:199], v[128:131]\n"
  "s_barrier\n"
  "ds_read_b64_tr_b16 v[206:207], v217 offset:37376\n"
  "v_mfma_f32_16x16x32_bf16 v[132:135], a[148:151], v[196:199], v[132:135]\n"
  "v_mfma_f32_16x16x32_bf16 v[132:135], a[132:135], v[192:195], v[132:135]\n"
  "s_add_u32 s98, s19, 2\n"
  "v_mfma_f32_16x16x32_bf16 v[136:139], a[136:139], v[192:195], v[136:139]\n"
  "v_mul_f32 v8, 0x3e0293ee, v8\n"
  "v_mul_f32 v9, 0x3e0293ee, v9\n"
  "v_mfma_f32_16x16x32_bf16 v[136:139], a[152:155], v[196:199], v[136:139]\n"
  "v_mul_f32 v10, 0x3e0293ee, v10\n"
  "v_mul_f32 v11, 0x3e0293ee, v11\n"
  "v_mfma_f32_16x16x32_bf16 v[140:143], a[156:159], v[196:199], v[140:143]\n"
  "s_and_b32 s98, s98, 7\n"
  "ds_read_b64_tr_b16 v[196:197], v218 offset:32768\n"
  "v_mul_f32 v24, 0x3e0293ee, v24\n"
  "v_mfma_f32_16x16x32_bf16 v[140:143], a[140:143], v[192:195], v[140:143]\n"
  "ds_read_b64_tr_b16 v[198:199], v218 offset:33280\n"
  "v_mul_f32 v25, 0x3e0293ee, v25\n"
  "s_waitcnt lgkmcnt(4)\n"
  "v_mfma_f32_16x16x32_bf16 v[156:159], a[140:143], v[200:203], v[156:159]\n"
  "ds_read_b64_tr_b16 v[192:193], v218 offset:36864\n"
  "v_mul_f32 v26, 0x3e0293ee, v26\n"
  "s_waitcnt lgkmcnt(3)\n"
  "v_mfma_f32_16x16x32_bf16 v[156:159], a[156:159], v[204:207], v[156:159]\n"
  "s_mul_i32 s98, s98, 524288\n"
  "ds_read_b64_tr_b16 v[194:195], v218 offset:37376\n"
  "v_mul_f32 v27, 0x3e0293ee, v27\n"
  "v_mfma_f32_16x16x32_bf16 v[152:155], a[152:155], v[204:207], v[152:155]\n"
  "v_cvt_pk_bf16_f32 v244, v8, v9\n"
  "v_cvt_pk_bf16_f32 v245, v10, v11\n"
  "v_mfma_f32_16x16x32_bf16 v[152:155], a[136:139], v[200:203], v[152:155]\n"
  "v_cvt_pk_bf16_f32 v246, v24, v25\n"
  "v_cvt_pk_bf16_f32 v247, v26, v27\n"
  "v_mfma_f32_16x16x32_bf16 v[148:151], a[132:135], v[200:203], v[148:151]\n"
  "s_add_u32 s98, s98, 128\n"
  "global_store_dwordx4 v232, v[244:247], s[44:45] offset:-4096 sc1\n"
  "v_mfma_f32_16x16x32_bf16 v[148:151], a[148:151], v[204:207], v[148:151]\n"
  "v_mfma_f32_16x16x32_bf16 v[144:147], a[144:147], v[204:207], v[144:147]\n"
  "ds_read_b64_tr_b16 v[204:205], v228 offset:32768\n"
  "v_mfma_f32_16x16x32_bf16 v[144:147], a[128:131], v[200:203], v[144:147]\n"
  "s_add_u32 s60, s24, s98\n"
  "s_addc_u32 s61, s25, 0\n"
  "ds_read_b64_tr_b16 v[206:207], v228 offset:33280\n"
  "s_waitcnt lgkmcnt(4)\n"
  "v_mfma_f32_16x16x32_bf16 v[160:163], a[128:131], v[196:199], v[160:163]\n"
  "ds_read_b64_tr_b16 v[200:201], v228 offset:36864\n"
  "s_waitcnt lgkmcnt(3)\n"
  "v_mfma_f32_16x16x32_bf16 v[160:163], a[144:147], v[192:195], v[160:163]\n"
  "ds_read_b64_tr_b16 v[202:203], v228 offset:37376\n"
  "v_mfma_f32_16x16x32_bf16 v[164:167], a[148:151], v[192:195], v[164:167]\n"
  "s_add_u32 m0, s48, 16384\n"
  "v_mul_f32 v40, 0x3e0293ee, v40\n"
  "v_mul_f32 v41, 0x3e0293ee, v41\n"
  "v_mfma_f32_16x16x32_bf16 v[164:167], a[132:135], v[196:199], v[164:167]\n"
  "v_mul_f32 v42, 0x3e0293ee, v42\n"
  "v_mul_f32 v43, 0x3e0293ee, v43\n"
  "v_mfma_f32_16x16x32_bf16 v[168:171], a[136:139], v[196:199], v[168:171]\n"
  "v_mul_f32 v56, 0x3e0293ee, v56\n"
  "v_mul_f32 v57, 0x3e0293ee, v57\n"
  "v_mfma_f32_16x16x32_bf16 v[168:171], a[152:155], v[192:195], v[168:171]\n"
  "v_mul_f32 v58, 0x3e0293ee, v58\n"
  "v_mul_f32 v59, 0x3e0293ee, v59\n"
  "v_mfma_f32_16x16x32_bf16 v[172:175], a[156:159], v[192:195], v[172:175]\n"
  "ds_read_b64_tr_b16 v[192:193], v216 offset:40960\n"
  "v_cvt_pk_bf16_f32 v236, v40, v41\n"
  "v_mfma_f32_16x16x32_bf16 v[172:175], a[140:143], v[196:199], v[172:175]\n"
  "ds_read_b64_tr_b16 v[194:195], v216 offset:41472\n"
  "v_cvt_pk_bf16_f32 v237, v42, v43\n"
  "s_waitcnt lgkmcnt(4)\n"
  "v_mfma_f32_16x16x32_bf16 v[188:191], a[140:143], v[204:207], v[188:191]\n"
  "ds_read_b64_tr_b16 v[196:197], v216 offset:45056\n"
  "v_cvt_pk_bf16_f32 v238, v56, v57\n"
  "s_waitcnt lgkmcnt(3)\n"
  "v_mfma_f32_16x16x32_bf16 v[188:191], a[156:159], v[200:203], v[188:191]\n"
  "ds_read_b64_tr_b16 v[198:199], v216 offset:45568\n"
  "v_cvt_pk_bf16_f32 v239, v58, v59\n"
  "v_mfma_f32_16x16x32_bf16 v[184:187], a[152:155], v[200:203], v[184:187]\n"
  "global_store_dwordx4 v232, v[236:239], s[44:45] offset:-3072 sc1\n"
  "v_mfma_f32_16x16x32_bf16 v[184:187], a[136:139], v[204:207], v[184:187]\n"
  "global_load_lds_dwordx4 v224, s[60:61] offset:0\n"
  "v_mfma_f32_16x16x32_bf16 v[180:183], a[132:135], v[204:207], v[180:183]\n"
  "global_load_lds_dwordx4 v225, s[60:61] offset:1024\n"
  "v_mfma_f32_16x16x32_bf16 v[180:183], a[148:151], v[200:203], v[180:183]\n"
  "global_load_lds_dwordx4 v226, s[60:61] offset:2048\n"
  "v_mfma_f32_16x16x32_bf16 v[176:179], a[144:147], v[200:203], v[176:179]\n"
  "ds_read_b64_tr_b16 v[200:201], v217 offset:40960\n"
  "v_mfma_f32_16x16x32_bf16 v[176:179], a[128:131], v[204:207], v[176:179]\n"
  "ds_read_b64_tr_b16 v[202:203], v217 offset:41472\n"
  "s_waitcnt lgkmcnt(4)\n"
  "v_mfma_f32_16x16x32_bf16 v[128:131], a[160:163], v[192:195], v[128:131]\n"
  "ds_read_b64_tr_b16 v[204:205], v217 offset:45056\n"
  "v_mul_f32 v72, 0x3e0293ee, v72\n"
  "s_waitcnt lgkmcnt(3)\n"
  "v_mfma_f32_16x16x32_bf16 v[128:131], a[176:179], v[196:199], v[128:131]\n"
  "ds_read_b64_tr_b16 v[206:207], v217 offset:45568\n"
  "v_mul_f32 v73, 0x3e0293ee, v73\n"
  "v_mfma_f32_16x16x32_bf16 v[132:135], a[180:183], v[196:199], v[132:135]\n"
  "global_load_lds_dwordx4 v227, s[60:61] offset:3072\n"
  "s_add_u32 m0, s99, 0x800\n"
  "v_mul_f32 v74, 0x3e0293ee, v74\n"
  "v_mfma_f32_16x16x32_bf16 v[132:135], a[164:167], v[192:195], v[132:135]\n"
  "v_mul_f32 v75, 0x3e0293ee, v75\n"
  "v_mul_f32 v88, 0x3e0293ee, v88\n"
  "v_mfma_f32_16x16x32_bf16 v[136:139], a[168:171], v[192:195], v[136:139]\n"
  "v_mul_f32 v89, 0x3e0293ee, v89\n"
  "v_mul_f32 v90, 0x3e0293ee, v90\n"
  "v_mfma_f32_16x16x32_bf16 v[136:139], a[184:187], v[196:199], v[136:139]\n"
  "v_mul_f32 v91, 0x3e0293ee, v91\n"
  "v_cvt_pk_bf16_f32 v240, v72, v73\n"
  "v_mfma_f32_16x16x32_bf16 v[140:143], a[188:191], v[196:199], v[140:143]\n"
  "ds_read_b64_tr_b16 v[196:197], v218 offset:40960\n"
  "v_cvt_pk_bf16_f32 v241, v74, v75\n"
  "v_mfma_f32_16x16x32_bf16 v[140:143], a[172:175], v[192:195], v[140:143]\n"
  "ds_read_b64_tr_b16 v[198:199], v218 offset:41472\n"
  "v_cvt_pk_bf16_f32 v242, v88, v89\n"
  "s_waitcnt lgkmcnt(4)\n"
  "v_mfma_f32_16x16x32_bf16 v[156:159], a[172:175], v[200:203], v[156:159]\n"
  "ds_read_b64_tr_b16 v[192:193], v218 offset:45056\n"
  "v_cvt_pk_bf16_f32 v243, v90, v91\n"
  "s_waitcnt lgkmcnt(3)\n"
  "v_mfma_f32_16x16x32_bf16 v[156:159], a[188:191], v[204:207], v[156:159]\n"
  "ds_read_b64_tr_b16 v[194:195], v218 offset:45568\n"
  "v_mfma_f32_16x16x32_bf16 v[152:155], a[184:187], v[204:207], v[152:155]\n"
  "ds_write_b128 v229, v[240:243] offset:2048\n"
  "v_mfma_f32_16x16x32_bf16 v[152:155], a[168:171], v[200:203], v[152:155]\n"
  "global_load_lds_dwordx4 v253, s[52:53] offset:2048 sc1\n"
  "v_mfma_f32_16x16x32_bf16 v[148:151], a[164:167], v[200:203], v[148:151]\n"
  "global_load_lds_dwordx4 v253, s[52:53] offset:3072 sc1\n"
  "s_add_u32 m0, s49, 6144\n"
  "v_mfma_f32_16x16x32_bf16 v[148:151], a[180:183], v[204:207], v[148:151]\n"
  "v_mfma_f32_16x16x32_bf16 v[144:147], a[176:179], v[204:207], v[144:147]\n"
  "ds_read_b64_tr_b16 v[204:205], v228 offset:40960\n"
  "v_mul_f32 v104, 0x3e0293ee, v104\n"
  "v_mfma_f32_16x16x32_bf16 v[144:147], a[160:163], v[200:203], v[144:147]\n"
  "ds_read_b64_tr_b16 v[206:207], v228 offset:41472\n"
  "v_mul_f32 v105, 0x3e0293ee, v105\n"
  "s_waitcnt lgkmcnt(5)\n"
  "v_mfma_f32_16x16x32_bf16 v[160:163], a[160:163], v[196:199], v[160:163]\n"
  "ds_read_b64_tr_b16 v[200:201], v228 offset:45056\n"
  "v_mul_f32 v106, 0x3e0293ee, v106\n"
  "s_waitcnt lgkmcnt(4)\n"
  "v_mfma_f32_16x16x32_bf16 v[160:163], a[176:179], v[192:195], v[160:163]\n"
  "ds_read_b64_tr_b16 v[202:203], v228 offset:45568\n"
  "v_mul_f32 v107, 0x3e0293ee, v107\n"
  "v_mfma_f32_16x16x32_bf16 v[164:167], a[180:183], v[192:195], v[164:167]\n"
  "v_mul_f32 v120, 0x3e0293ee, v120\n"
  "v_mul_f32 v121, 0x3e0293ee, v121\n"
  "v_mfma_f32_16x16x32_bf16 v[164:167], a[164:167], v[196:199], v[164:167]\n"
  "v_mul_f32 v122, 0x3e0293ee, v122\n"
  "v_mul_f32 v123, 0x3e0293ee, v123\n"
  "v_mfma_f32_16x16x32_bf16 v[168:171], a[168:171], v[196:199], v[168:171]\n"
  "v_cvt_pk_bf16_f32 v244, v104, v105\n"
  "v_cvt_pk_bf16_f32 v245, v106, v107\n"
  "v_mfma_f32_16x16x32_bf16 v[168:171], a[184:187], v[192:195], v[168:171]\n"
  "v_cvt_pk_bf16_f32 v246, v120, v121\n"
  "v_cvt_pk_bf16_f32 v247, v122, v123\n"
  "v_mfma_f32_16x16x32_bf16 v[172:175], a[188:191], v[192:195], v[172:175]\n"
  "s_waitcnt vmcnt(23)\n"
  "s_barrier\n"
  "ds_read_b64_tr_b16 v[192:193], v216 offset:49152\n"
  "v_mfma_f32_16x16x32_bf16 v[172:175], a[172:175], v[196:199], v[172:175]\n"
  "ds_read_b64_tr_b16 v[194:195], v216 offset:49664\n"
  "s_waitcnt lgkmcnt(4)\n"
  "v_mfma_f32_16x16x32_bf16 v[188:191], a[172:175], v[204:207], v[188:191]\n"
  "ds_read_b64_tr_b16 v[196:197], v216 offset:53248\n"
  "s_waitcnt lgkmcnt(3)\n"
  "v_mfma_f32_16x16x32_bf16 v[188:191], a[188:191], v[200:203], v[188:191]\n"
  "ds_read_b64_tr_b16 v[198:199], v216 offset:53760\n"
  "v_mfma_f32_16x16x32_bf16 v[184:187], a[184:187], v[200:203], v[184:187]\n"
  "ds_write_b128 v229, v[244:247] offset:3072\n"
  "v_mfma_f32_16x16x32_bf16 v[184:187], a[168:171], v[204:207], v[184:187]\n"
  "global_load_lds_dwordx4 v252, s[52:53] offset:0 sc1\n"
  "v_mfma_f32_16x16x32_bf16 v[180:183], a[164:167], v[204:207], v[180:183]\n"
  "global_load_lds_dwordx4 v252, s[52:53] offset:1024 sc1\n"
  "v_mfma_f32_16x16x32_bf16 v[180:183], a[180:183], v[200:203], v[180:183]\n"
  "global_load_dword v231, v234, s[28:29] offset:0\n"
  "v_add_u32 v234, 0x100000, v234\n"
  "v_mfma_f32_16x16x32_bf16 v[176:179], a[176:179], v[200:203], v[176:179]\n"
  "ds_read_b64_tr_b16 v[200:201], v217 offset:49152\n"
  "v_mfma_f32_16x16x32_bf16 v[176:179], a[160:163], v[204:207], v[176:179]\n"
  "ds_read_b64_tr_b16 v[202:203], v217 offset:49664\n"
  "L_stg11_%=:\n"
  "s_waitcnt lgkmcnt(5)\n"
  "v_mfma_f32_16x16x32_bf16 v[128:131], a[192:195], v[192:195], v[128:131]\n"
  "ds_read_b64_tr_b16 v[204:205], v217 offset:53248\n"
  "s_waitcnt lgkmcnt(4)\n"
  "v_mfma_f32_16x16x32_bf16 v[128:131], a[208:211], v[196:199], v[128:131]\n"
  "s_barrier\n"
  "ds_read_b64_tr_b16 v[206:207], v217 offset:53760\n"
  "v_mfma_f32_16x16x32_bf16 v[132:135], a[212:215], v[196:199], v[132:135]\n"
  "v_mfma_f32_16x16x32_bf16 v[132:135], a[196:199], v[192:195], v[132:135]\n"
  "s_add_u32 s98, s19, 4\n"
  "v_mfma_f32_16x16x32_bf16 v[136:139], a[200:203], v[192:195], v[136:139]\n"
  "v_mul_f32 v12, 0x3e0293ee, v12\n"
  "v_mul_f32 v13, 0x3e0293ee, v13\n"
  "v_mfma_f32_16x16x32_bf16 v[136:139], a[216:219], v[196:199], v[136:139]\n"
  "v_mul_f32 v14, 0x3e0293ee, v14\n"
  "v_mul_f32 v15, 0x3e0293ee, v15\n"
  "v_mfma_f32_16x16x32_bf16 v[140:143], a[220:223], v[196:199], v[140:143]\n"
  "s_and_b32 s98, s98, 7\n"
  "ds_read_b64_tr_b16 v[196:197], v218 offset:49152\n"
  "v_mul_f32 v28, 0x3e0293ee, v28\n"
  "v_mfma_f32_16x16x32_bf16 v[140:143], a[204:207], v[192:195], v[140:143]\n"
  "ds_read_b64_tr_b16 v[198:199], v218 offset:49664\n"
  "v_mul_f32 v29, 0x3e0293ee, v29\n"
  "s_waitcnt lgkmcnt(4)\n"
  "v_mfma_f32_16x16x32_bf16 v[156:159], a[204:207], v[200:203], v[156:159]\n"
  "ds_read_b64_tr_b16 v[192:193], v218 offset:53248\n"
  "v_mul_f32 v30, 0x3e0293ee, v30\n"
  "s_waitcnt lgkmcnt(3)\n"
  "v_mfma_f32_16x16x32_bf16 v[156:159], a[220:223], v[204:207], v[156:159]\n"
  "s_mul_i32 s98, s98, 524288\n"
  "ds_read_b64_tr_b16 v[194:195], v218 offset:53760\n"
  "v_mul_f32 v31, 0x3e0293ee, v31\n"
  "v_mfma_f32_16x16x32_bf16 v[152:155], a[216:219], v[204:207], v[152:155]\n"
  "v_cvt_pk_bf16_f32 v236, v12, v13\n"
  "v_cvt_pk_bf16_f32 v237, v14, v15\n"
  "v_mfma_f32_16x16x32_bf16 v[152:155], a[200:203], v[200:203], v[152:155]\n"
  "v_cvt_pk_bf16_f32 v238, v28, v29\n"
  "v_cvt_pk_bf16_f32 v239, v30, v31\n"
  "v_mfma_f32_16x16x32_bf16 v[148:151], a[196:199], v[200:203], v[148:151]\n"
  "s_add_u32 s98, s98, 128\n"
  "global_store_dwordx4 v232, v[236:239], s[44:45] offset:0 sc1\n"
  "v_mfma_f32_16x16x32_bf16 v[148:151], a[212:215], v[204:207], v[148:151]\n"
  "v_mfma_f32_16x16x32_bf16 v[144:147], a[208:211], v[204:207], v[144:147]\n"
  "ds_read_b64_tr_b16 v[204:205], v228 offset:49152\n"
  "v_mfma_f32_16x16x32_bf16 v[144:147], a[192:195], v[200:203], v[144:147]\n"
  "s_add_u32 s60, s24, s98\n"
  "s_addc_u32 s61, s25, 0\n"
  "ds_read_b64_tr_b16 v[206:207], v228 offset:49664\n"
  "s_waitcnt lgkmcnt(4)\n"
  "v_mfma_f32_16x16x32_bf16 v[160:163], a[192:195], v[196:199], v[160:163]\n"
  "ds_read_b64_tr_b16 v[200:201], v228 offset:53248\n"
  "s_waitcnt lgkmcnt(3)\n"
  "v_mfma_f32_16x16x32_bf16 v[160:163], a[208:211], v[192:195], v[160:163]\n"
  "ds_read_b64_tr_b16 v[202:203], v228 offset:53760\n"
  "v_mfma_f32_16x16x32_bf16 v[164:167], a[212:215], v[192:195], v[164:167]\n"
  "s_add_u32 m0, s48, 32768\n"
  "v_mul_f32 v44, 0x3e0293ee, v44\n"
  "v_mul_f32 v45, 0x3e0293ee, v45\n"
  "v_mfma_f32_16x16x32_bf16 v[164:167], a[196:199], v[196:199], v[164:167]\n"
  "v_mul_f32 v46, 0x3e0293ee, v46\n"
  "v_mul_f32 v47, 0x3e0293ee, v47\n"
  "v_mfma_f32_16x16x32_bf16 v[168:171], a[200:203], v[196:199], v[168:171]\n"
  "v_mul_f32 v60, 0x3e0293ee, v60\n"
  "v_mul_f32 v61, 0x3e0293ee, v61\n"
  "v_mfma_f32_16x16x32_bf16 v[168:171], a[216:219], v[192:195], v[168:171]\n"
  "v_mul_f32 v62, 0x3e0293ee, v62\n"
  "v_mul_f32 v63, 0x3e0293ee, v63\n"
  "v_mfma_f32_16x16x32_bf16 v[172:175], a[220:223], v[192:195], v[172:175]\n"
  "ds_read_b64_tr_b16 v[192:193], v216 offset:57344\n"
  "v_cvt_pk_bf16_f32 v240, v44, v45\n"
  "v_mfma_f32_16x16x32_bf16 v[172:175], a[204:207], v[196:199], v[172:175]\n"
  "ds_read_b64_tr_b16 v[194:195], v216 offset:57856\n"
  "v_cvt_pk_bf16_f32 v241, v46, v47\n"
  "s_waitcnt lgkmcnt(4)\n"
  "v_mfma_f32_16x16x32_bf16 v[188:191], a[204:207], v[204:207], v[188:191]\n"
  "ds_read_b64_tr_b16 v[196:197], v216 offset:61440\n"
  "v_cvt_pk_bf16_f32 v242, v60, v61\n"
  "s_waitcnt lgkmcnt(3)\n"
  "v_mfma_f32_16x16x32_bf16 v[188:191], a[220:223], v[200:203], v[188:191]\n"
  "ds_read_b64_tr_b16 v[198:199], v216 offset:61952\n"
  "v_cvt_pk_bf16_f32 v243, v62, v63\n"
  "v_mfma_f32_16x16x32_bf16 v[184:187], a[216:219], v[200:203], v[184:187]\n"
  "global_store_dwordx4 v232, v[240:243], s[44:45] offset:1024 sc1\n"
  "v_mfma_f32_16x16x32_bf16 v[184:187], a[200:203], v[204:207], v[184:187]\n"
  "global_load_lds_dwordx4 v224, s[60:61] offset:0\n"
  "v_mfma_f32_16x16x32_bf16 v[180:183], a[196:199], v[204:207], v[180:183]\n"
  "global_load_lds_dwordx4 v225, s[60:61] offset:1024\n"
  "v_mfma_f32_16x16x32_bf16 v[180:183], a[212:215], v[200:203], v[180:183]\n"
  "global_load_lds_dwordx4 v226, s[60:61] offset:2048\n"
  "v_mfma_f32_16x16x32_bf16 v[176:179], a[208:211], v[200:203], v[176:179]\n"
  "ds_read_b64_tr_b16 v[200:201], v217 offset:57344\n"
  "v_mfma_f32_16x16x32_bf16 v[176:179], a[192:195], v[204:207], v[176:179]\n"
  "ds_read_b64_tr_b16 v[202:203], v217 offset:57856\n"
  "s_waitcnt lgkmcnt(4)\n"
  "v_mfma_f32_16x16x32_bf16 v[128:131], a[224:227], v[192:195], v[128:131]\n"
  "ds_read_b64_tr_b16 v[204:205], v217 offset:61440\n"
  "v_mul_f32 v76, 0x3e0293ee, v76\n"
  "s_waitcnt lgkmcnt(3)\n"
  "v_mfma_f32_16x16x32_bf16 v[128:131], a[240:243], v[196:199], v[128:131]\n"
  "ds_read_b64_tr_b16 v[206:207], v217 offset:61952\n"
  "v_mul_f32 v77, 0x3e0293ee, v77\n"
  "v_mfma_f32_16x16x32_bf16 v[132:135], a[244:247], v[196:199], v[132:135]\n"
  "global_load_lds_dwordx4 v227, s[60:61] offset:3072\n"
  "s_add_u32 m0, s99, 0x1000\n"
  "v_mul_f32 v78, 0x3e0293ee, v78\n"
  "v_mfma_f32_16x16x32_bf16 v[132:135], a[228:231], v[192:195], v[132:135]\n"
  "v_mul_f32 v79, 0x3e0293ee, v79\n"
  "v_mul_f32 v92, 0x3e0293ee, v92\n"
  "v_mfma_f32_16x16x32_bf16 v[136:139], a[232:235], v[192:195], v[136:139]\n"
  "v_mul_f32 v93, 0x3e0293ee, v93\n"
  "v_mul_f32 v94, 0x3e0293ee, v94\n"
  "v_mfma_f32_16x16x32_bf16 v[136:139], a[248:251], v[196:199], v[136:139]\n"
  "v_mul_f32 v95, 0x3e0293ee, v95\n"
  "v_cvt_pk_bf16_f32 v244, v76, v77\n"
  "v_mfma_f32_16x16x32_bf16 v[140:143], a[252:255], v[196:199], v[140:143]\n"
  "ds_read_b64_tr_b16 v[196:197], v218 offset:57344\n"
  "v_cvt_pk_bf16_f32 v245, v78, v79\n"
  "v_mfma_f32_16x16x32_bf16 v[140:143], a[236:239], v[192:195], v[140:143]\n"
  "ds_read_b64_tr_b16 v[198:199], v218 offset:57856\n"
  "v_cvt_pk_bf16_f32 v246, v92, v93\n"
  "s_waitcnt lgkmcnt(4)\n"
  "v_mfma_f32_16x16x32_bf16 v[156:159], a[236:239], v[200:203], v[156:159]\n"
  "ds_read_b64_tr_b16 v[192:193], v218 offset:61440\n"
  "v_cvt_pk_bf16_f32 v247, v94, v95\n"
  "s_waitcnt lgkmcnt(3)\n"
  "v_mfma_f32_16x16x32_bf16 v[156:159], a[252:255], v[204:207], v[156:159]\n"
  "ds_read_b64_tr_b16 v[194:195], v218 offset:61952\n"
  "v_mfma_f32_16x16x32_bf16 v[152:155], a[248:251], v[204:207], v[152:155]\n"
  "ds_write_b128 v229, v[244:247] offset:8192\n"
  "v_mfma_f32_16x16x32_bf16 v[152:155], a[232:235], v[200:203], v[152:155]\n"
  "global_load_lds_dwordx4 v254, s[52:53] offset:2048 sc1\n"
  "v_mfma_f32_16x16x32_bf16 v[148:151], a[228:231], v[200:203], v[148:151]\n"
  "global_load_lds_dwordx4 v254, s[52:53] offset:3072 sc1\n"
  "s_add_u32 m0, s49, 12288\n"
  "v_mfma_f32_16x16x32_bf16 v[148:151], a[244:247], v[204:207], v[148:151]\n"
  "v_mfma_f32_16x16x32_bf16 v[144:147], a[240:243], v[204:207], v[144:147]\n"
  "ds_read_b64_tr_b16 v[204:205], v228 offset:57344\n"
  "v_mul_f32 v108, 0x3e0293ee, v108\n"
  "v_mfma_f32_16x16x32_bf16 v[144:147], a[224:227], v[200:203], v[144:147]\n"
  "ds_read_b64_tr_b16 v[206:207], v228 offset:57856\n"
  "v_mul_f32 v109, 0x3e0293ee, v109\n"
  "s_waitcnt lgkmcnt(5)\n"
  "v_mfma_f32_16x16x32_bf16 v[160:163], a[224:227], v[196:199], v[160:163]\n"
  "ds_read_b64_tr_b16 v[200:201], v228 offset:61440\n"
  "v_mul_f32 v110, 0x3e0293ee, v110\n"
  "s_waitcnt lgkmcnt(4)\n"
  "v_mfma_f32_16x16x32_bf16 v[160:163], a[240:243], v[192:195], v[160:163]\n"
  "ds_read_b64_tr_b16 v[202:203], v228 offset:61952\n"
  "v_mul_f32 v111, 0x3e0293ee, v111\n"
  "v_mfma_f32_16x16x32_bf16 v[164:167], a[244:247], v[192:195], v[164:167]\n"
  "v_mul_f32 v124, 0x3e0293ee, v124\n"
  "v_mul_f32 v125, 0x3e0293ee, v125\n"
  "v_mfma_f32_16x16x32_bf16 v[164:167], a[228:231], v[196:199], v[164:167]\n"
  "v_mul_f32 v126, 0x3e0293ee, v126\n"
  "v_mul_f32 v127, 0x3e0293ee, v127\n"
  "v_mfma_f32_16x16x32_bf16 v[168:171], a[232:235], v[196:199], v[168:171]\n"
  "v_cvt_pk_bf16_f32 v236, v108, v109\n"
  "v_cvt_pk_bf16_f32 v237, v110, v111\n"
  "v_mfma_f32_16x16x32_bf16 v[168:171], a[248:251], v[192:195], v[168:171]\n"
  "v_cvt_pk_bf16_f32 v238, v124, v125\n"
  "v_cvt_pk_bf16_f32 v239, v126, v127\n"
  "v_mfma_f32_16x16x32_bf16 v[172:175], a[252:255], v[192:195], v[172:175]\n"
  "s_waitcnt vmcnt(25)\n"
  "s_barrier\n"
  "ds_read_b64_tr_b16 v[192:193], v216 offset:0\n"
  "v_mfma_f32_16x16x32_bf16 v[172:175], a[236:239], v[196:199], v[172:175]\n"
  "ds_read_b64_tr_b16 v[194:195], v216 offset:512\n"
  "s_waitcnt lgkmcnt(4)\n"
  "v_mfma_f32_16x16x32_bf16 v[188:191], a[236:239], v[204:207], v[188:191]\n"
  "ds_read_b64_tr_b16 v[196:197], v216 offset:4096\n"
  "s_waitcnt lgkmcnt(3)\n"
  "v_mfma_f32_16x16x32_bf16 v[188:191], a[252:255], v[200:203], v[188:191]\n"
  "ds_read_b64_tr_b16 v[198:199], v216 offset:4608\n"
  "v_mfma_f32_16x16x32_bf16 v[184:187], a[248:251], v[200:203], v[184:187]\n"
  "ds_write_b128 v229, v[236:239] offset:9216\n"
  "v_mfma_f32_16x16x32_bf16 v[184:187], a[232:235], v[204:207], v[184:187]\n"
  "global_load_lds_dwordx4 v253, s[52:53] offset:0 sc1\n"
  "v_mfma_f32_16x16x32_bf16 v[180:183], a[228:231], v[204:207], v[180:183]\n"
  "global_load_lds_dwordx4 v253, s[52:53] offset:1024 sc1\n"
  "v_mfma_f32_16x16x32_bf16 v[180:183], a[244:247], v[200:203], v[180:183]\n"
  "global_load_dword v231, v234, s[28:29] offset:0\n"
  "v_add_u32 v234, 0x100000, v234\n"
  "v_mfma_f32_16x16x32_bf16 v[176:179], a[240:243], v[200:203], v[176:179]\n"
  "ds_read_b64_tr_b16 v[200:201], v217 offset:0\n"
  "v_mfma_f32_16x16x32_bf16 v[176:179], a[224:227], v[204:207], v[176:179]\n"
  "ds_read_b64_tr_b16 v[202:203], v217 offset:512\n"
  "L_stg12_%=:\n"
  "s_waitcnt lgkmcnt(5)\n"
  "v_mfma_f32_16x16x32_bf16 v[0:3], a[0:3], v[192:195], 0\n"
  "ds_read_b64_tr_b16 v[204:205], v217 offset:4096\n"
  "s_waitcnt lgkmcnt(4)\n"
  "v_mfma_f32_16x16x32_bf16 v[0:3], a[16:19], v[196:199], v[0:3]\n"
  "s_barrier\n"
  "ds_read_b64_tr_b16 v[206:207], v217 offset:4608\n"
  "v_mfma_f32_16x16x32_bf16 v[4:7], a[20:23], v[196:199], 0\n"
  "ds_read_b128 v[88:91], v229 offset:0\n"
  "v_mfma_f32_16x16x32_bf16 v[4:7], a[4:7], v[192:195], v[4:7]\n"
  "s_add_u32 s98, s19, 6\n"
  "v_mfma_f32_16x16x32_bf16 v[8:11], a[8:11], v[192:195], 0\n"
  "v_cvt_pk_bf16_f32 v240, v128, v129\n"
  "v_cvt_pk_bf16_f32 v241, v130, v131\n"
  "v_mfma_f32_16x16x32_bf16 v[8:11], a[24:27], v[196:199], v[8:11]\n"
  "v_cvt_pk_bf16_f32 v242, v132, v133\n"
  "v_cvt_pk_bf16_f32 v243, v134, v135\n"
  "v_mfma_f32_16x16x32_bf16 v[12:15], a[28:31], v[196:199], 0\n"
  "s_and_b32 s98, s98, 7\n"
  "ds_read_b64_tr_b16 v[196:197], v218 offset:0\n"
  "v_mfma_f32_16x16x32_bf16 v[12:15], a[12:15], v[192:195], v[12:15]\n"
  "ds_read_b64_tr_b16 v[198:199], v218 offset:512\n"
  "s_waitcnt lgkmcnt(5)\n"
  "v_mfma_f32_16x16x32_bf16 v[28:31], a[12:15], v[200:203], 0\n"
  "s_mov_b64 s[20:21], s[42:43]\n"
  "ds_read_b64_tr_b16 v[192:193], v218 offset:4096\n"
  "s_waitcnt lgkmcnt(4)\n"
  "v_mfma_f32_16x16x32_bf16 v[28:31], a[28:31], v[204:207], v[28:31]\n"
  "s_mul_i32 s98, s98, 524288\n"
  "ds_read_b64_tr_b16 v[194:195], v218 offset:4608\n"
  "v_mfma_f32_16x16x32_bf16 v[24:27], a[24:27], v[204:207], 0\n"
  "s_mov_b64 s[22:23], s[46:47]\n"
  "ds_write_b128 v229, v[240:243] offset:10240\n"
  "v_mfma_f32_16x16x32_bf16 v[24:27], a[8:11], v[200:203], v[24:27]\n"
  "s_waitcnt lgkmcnt(5)\n"
  "global_store_dwordx4 v232, v[88:91], s[40:41] offset:-1024 sc1\n"
  "v_mfma_f32_16x16x32_bf16 v[20:23], a[4:7], v[200:203], 0\n"
  "s_add_u32 s98, s98, 128\n"
  "v_mfma_f32_16x16x32_bf16 v[20:23], a[20:23], v[204:207], v[20:23]\n"
  "v_mfma_f32_16x16x32_bf16 v[16:19], a[16:19], v[204:207], 0\n"
  "ds_read_b64_tr_b16 v[204:205], v228 offset:0\n"
  "v_mfma_f32_16x16x32_bf16 v[16:19], a[0:3], v[200:203], v[16:19]\n"
  "s_add_u32 s60, s24, s98\n"
  "s_addc_u32 s61, s25, 0\n"
  "ds_read_b64_tr_b16 v[206:207], v228 offset:512\n"
  "s_waitcnt lgkmcnt(5)\n"
  "v_mfma_f32_16x16x32_bf16 v[32:35], a[0:3], v[196:199], 0\n"
  "ds_read_b64_tr_b16 v[200:201], v228 offset:4096\n"
  "s_waitcnt lgkmcnt(4)\n"
  "v_mfma_f32_16x16x32_bf16 v[32:35], a[16:19], v[192:195], v[32:35]\n"
  "ds_read_b64_tr_b16 v[202:203], v228 offset:4608\n"
  "v_mfma_f32_16x16x32_bf16 v[36:39], a[20:23], v[192:195], 0\n"
  "s_add_u32 m0, s48, 49152\n"
  "v_mfma_f32_16x16x32_bf16 v[36:39], a[4:7], v[196:199], v[36:39]\n"
  "v_mfma_f32_16x16x32_bf16 v[40:43], a[8:11], v[196:199], 0\n"
  "v_mfma_f32_16x16x32_bf16 v[40:43], a[24:27], v[192:195], v[40:43]\n"
  "global_load_lds_dwordx4 v224, s[60:61] offset:0\n"
  "v_mfma_f32_16x16x32_bf16 v[44:47], a[28:31], v[192:195], 0\n"
  "ds_read_b64_tr_b16 v[192:193], v216 offset:8192\n"
  "v_mfma_f32_16x16x32_bf16 v[44:47], a[12:15], v[196:199], v[44:47]\n"
  "ds_read_b64_tr_b16 v[194:195], v216 offset:8704\n"
  "s_waitcnt lgkmcnt(4)\n"
  "v_mfma_f32_16x16x32_bf16 v[60:63], a[12:15], v[204:207], 0\n"
  "ds_read_b64_tr_b16 v[196:197], v216 offset:12288\n"
  "s_waitcnt lgkmcnt(3)\n"
  "v_mfma_f32_16x16x32_bf16 v[60:63], a[28:31], v[200:203], v[60:63]\n"
  "ds_read_b64_tr_b16 v[198:199], v216 offset:12800\n"
  "v_mfma_f32_16x16x32_bf16 v[56:59], a[24:27], v[200:203], 0\n"
  "global_load_lds_dwordx4 v225, s[60:61] offset:1024\n"
  "v_mfma_f32_16x16x32_bf16 v[56:59], a[8:11], v[204:207], v[56:59]\n"
  "global_load_lds_dwordx4 v226, s[60:61] offset:2048\n"
  "v_mfma_f32_16x16x32_bf16 v[52:55], a[4:7], v[204:207], 0\n"
  "ds_read_b128 v[92:95], v229 offset:1024\n"
  "v_mfma_f32_16x16x32_bf16 v[52:55], a[20:23], v[200:203], v[52:55]\n"
  "v_mfma_f32_16x16x32_bf16 v[48:51], a[16:19], v[200:203], 0\n"
  "ds_read_b64_tr_b16 v[200:201], v217 offset:8192\n"
  "v_mfma_f32_16x16x32_bf16 v[48:51], a[0:3], v[204:207], v[48:51]\n"
  "ds_read_b64_tr_b16 v[202:203], v217 offset:8704\n"
  "s_waitcnt lgkmcnt(5)\n"
  "v_mfma_f32_16x16x32_bf16 v[0:3], a[32:35], v[192:195], v[0:3]\n"
  "ds_read_b64_tr_b16 v[204:205], v217 offset:12288\n"
  "v_cvt_pk_bf16_f32 v244, v144, v145\n"
  "s_waitcnt lgkmcnt(4)\n"
  "v_mfma_f32_16x16x32_bf16 v[0:3], a[48:51], v[196:199], v[0:3]\n"
  "ds_read_b64_tr_b16 v[206:207], v217 offset:12800\n"
  "v_cvt_pk_bf16_f32 v245, v146, v147\n"
  "v_mfma_f32_16x16x32_bf16 v[4:7], a[52:55], v[196:199], v[4:7]\n"
  "global_load_lds_dwordx4 v227, s[60:61] offset:3072\n"
  "s_add_u32 m0, s49, 14336\n"
  "v_cvt_pk_bf16_f32 v246, v148, v149\n"
  "v_mfma_f32_16x16x32_bf16 v[4:7], a[36:39], v[192:195], v[4:7]\n"
  "v_cvt_pk_bf16_f32 v247, v150, v151\n"
  "v_mfma_f32_16x16x32_bf16 v[8:11], a[40:43], v[192:195], v[8:11]\n"
  "ds_write_b128 v229, v[244:247] offset:11264\n"
  "v_mfma_f32_16x16x32_bf16 v[8:11], a[56:59], v[196:199], v[8:11]\n"
  "global_load_lds_dwordx4 v254, s[52:53] offset:0 sc1\n"
  "v_mfma_f32_16x16x32_bf16 v[12:15], a[60:63], v[196:199], v[12:15]\n"
  "ds_read_b64_tr_b16 v[196:197], v218 offset:8192\n"
  "v_mfma_f32_16x16x32_bf16 v[12:15], a[44:47], v[192:195], v[12:15]\n"
  "ds_read_b64_tr_b16 v[198:199], v218 offset:8704\n"
  "s_waitcnt lgkmcnt(5)\n"
  "v_mfma_f32_16x16x32_bf16 v[28:31], a[44:47], v[200:203], v[28:31]\n"
  "ds_read_b64_tr_b16 v[192:193], v218 offset:12288\n"
  "s_waitcnt lgkmcnt(4)\n"
  "v_mfma_f32_16x16x32_bf16 v[28:31], a[60:63], v[204:207], v[28:31]\n"
  "ds_read_b64_tr_b16 v[194:195], v218 offset:12800\n"
  "v_mfma_f32_16x16x32_bf16 v[24:27], a[56:59], v[204:207], v[24:27]\n"
  "global_store_dwordx4 v232, v[92:95], s[40:41] offset:3072 sc1\n"
  "v_mfma_f32_16x16x32_bf16 v[24:27], a[40:43], v[200:203], v[24:27]\n"
  "global_load_lds_dwordx4 v254, s[52:53] offset:1024 sc1\n"
  "v_mfma_f32_16x16x32_bf16 v[20:23], a[36:39], v[200:203], v[20:23]\n"
  "global_load_dword v231, v234, s[28:29] offset:0\n"
  "v_mfma_f32_16x16x32_bf16 v[20:23], a[52:55], v[204:207], v[20:23]\n"
  "v_add_u32 v234, 0x100000, v234\n"
  "v_mfma_f32_16x16x32_bf16 v[16:19], a[48:51], v[204:207], v[16:19]\n"
  "ds_read_b64_tr_b16 v[204:205], v228 offset:8192\n"
  "v_mfma_f32_16x16x32_bf16 v[16:19], a[32:35], v[200:203], v[16:19]\n"
  "ds_read_b64_tr_b16 v[206:207], v228 offset:8704\n"
  "s_waitcnt lgkmcnt(4)\n"
  "v_mfma_f32_16x16x32_bf16 v[32:35], a[32:35], v[196:199], v[32:35]\n"
  "s_lshl_b32 s36, s55, 5\n"
  "ds_read_b64_tr_b16 v[200:201], v228 offset:12288\n"
  "s_waitcnt lgkmcnt(3)\n"
  "v_mfma_f32_16x16x32_bf16 v[32:35], a[48:51], v[192:195], v[32:35]\n"
  "s_add_u32 s60, s78, s36\n"
  "s_addc_u32 s61, s79, 0\n"
  "ds_read_b64_tr_b16 v[202:203], v228 offset:12800\n"
  "v_mfma_f32_16x16x32_bf16 v[36:39], a[52:55], v[192:195], v[36:39]\n"
  "v_lshrrev_b32 v86, 4, v233\n"
  "v_lshlrev_b32 v86, 3, v86\n"
  "v_mfma_f32_16x16x32_bf16 v[36:39], a[36:39], v[196:199], v[36:39]\n"
  "global_load_dwordx2 v[80:81], v86, s[60:61] offset:0\n"
  "v_mfma_f32_16x16x32_bf16 v[40:43], a[40:43], v[196:199], v[40:43]\n"
  "v_mfma_f32_16x16x32_bf16 v[40:43], a[56:59], v[192:195], v[40:43]\n"
  "v_mfma_f32_16x16x32_bf16 v[44:47], a[60:63], v[192:195], v[44:47]\n"
  "s_waitcnt vmcnt(26)\n"
  "s_barrier\n"
  "ds_read_b64_tr_b16 v[192:193], v216 offset:16384\n"
  "v_mfma_f32_16x16x32_bf16 v[44:47], a[44:47], v[196:199], v[44:47]\n"
  "ds_read_b64_tr_b16 v[194:195], v216 offset:16896\n"
  "s_waitcnt lgkmcnt(4)\n"
  "v_mfma_f32_16x16x32_bf16 v[60:63], a[44:47], v[204:207], v[60:63]\n"
  "ds_read_b64_tr_b16 v[196:197], v216 offset:20480\n"
  "s_waitcnt lgkmcnt(3)\n"
  "v_mfma_f32_16x16x32_bf16 v[60:63], a[60:63], v[200:203], v[60:63]\n"
  "ds_read_b64_tr_b16 v[198:199], v216 offset:20992\n"
  "v_mfma_f32_16x16x32_bf16 v[56:59], a[56:59], v[200:203], v[56:59]\n"
  "global_load_dwordx2 v[82:83], v86, s[60:61] offset:32\n"
  "v_mfma_f32_16x16x32_bf16 v[56:59], a[40:43], v[204:207], v[56:59]\n"
  "global_load_dwordx2 v[84:85], v86, s[60:61] offset:64\n"
  "v_mfma_f32_16x16x32_bf16 v[52:55], a[36:39], v[204:207], v[52:55]\n"
  "v_mfma_f32_16x16x32_bf16 v[52:55], a[52:55], v[200:203], v[52:55]\n"
  "v_mfma_f32_16x16x32_bf16 v[48:51], a[48:51], v[200:203], v[48:51]\n"
  "ds_read_b64_tr_b16 v[200:201], v217 offset:16384\n"
  "v_mfma_f32_16x16x32_bf16 v[48:51], a[32:35], v[204:207], v[48:51]\n"
  "ds_read_b64_tr_b16 v[202:203], v217 offset:16896\n"
  "L_stg13_%=:\n"
  "s_waitcnt lgkmcnt(4)\n"
  "v_mfma_f32_16x16x32_bf16 v[0:3], a[64:67], v[192:195], v[0:3]\n"
  "ds_read_b64_tr_b16 v[204:205], v217 offset:20480\n"
  "s_waitcnt lgkmcnt(3)\n"
  "v_mfma_f32_16x16x32_bf16 v[0:3], a[80:83], v[196:199], v[0:3]\n"
  "s_barrier\n"
  "ds_read_b64_tr_b16 v[206:207], v217 offset:20992\n"
  "v_mfma_f32_16x16x32_bf16 v[4:7], a[84:87], v[196:199], v[4:7]\n"
  "s_waitcnt vmcnt(4)\n"
  "ds_read_b128 v[88:91], v229 offset:2048\n"
  "v_mfma_f32_16x16x32_bf16 v[4:7], a[68:71], v[192:195], v[4:7]\n"
  "s_add_u32 s98, s18, 0\n"
  "v_mfma_f32_16x16x32_bf16 v[8:11], a[72:75], v[192:195], v[8:11]\n"
  "ds_read_b128 a[0:3], v229 offset:4096\n"
  "v_cvt_pk_bf16_f32 v236, v160, v161\n"
  "v_mfma_f32_16x16x32_bf16 v[8:11], a[88:91], v[196:199], v[8:11]\n"
  "v_cvt_pk_bf16_f32 v237, v162, v163\n"
  "v_cvt_pk_bf16_f32 v238, v164, v165\n"
  "v_mfma_f32_16x16x32_bf16 v[12:15], a[92:95], v[196:199], v[12:15]\n"
  "ds_read_b64_tr_b16 v[196:197], v218 offset:16384\n"
  "v_cvt_pk_bf16_f32 v239, v166, v167\n"
  "v_mfma_f32_16x16x32_bf16 v[12:15], a[76:79], v[192:195], v[12:15]\n"
  "ds_read_b64_tr_b16 v[198:199], v218 offset:16896\n"
  "s_waitcnt lgkmcnt(6)\n"
  "v_mfma_f32_16x16x32_bf16 v[28:31], a[76:79], v[200:203], v[28:31]\n"
  "ds_read_b64_tr_b16 v[192:193], v218 offset:20480\n"
  "v_and_b32 v79, 0xffff, v216\n"
  "s_waitcnt lgkmcnt(5)\n"
  "v_mfma_f32_16x16x32_bf16 v[28:31], a[92:95], v[204:207], v[28:31]\n"
  "s_and_b32 s98, s98, 3\n"
  "ds_read_b64_tr_b16 v[194:195], v218 offset:20992\n"
  "v_mfma_f32_16x16x32_bf16 v[24:27], a[88:91], v[204:207], v[24:27]\n"
  "ds_read_b128 a[4:7], v229 offset:6144\n"
  "v_xor_b32 v76, s59, v79\n"
  "v_mfma_f32_16x16x32_bf16 v[24:27], a[72:75], v[200:203], v[24:27]\n"
  "ds_read_b128 a[8:11], v229 offset:12288\n"
  "v_mfma_f32_16x16x32_bf16 v[20:23], a[68:71], v[200:203], v[20:23]\n"
  "s_add_u32 s59, s59, 32\n"
  "ds_read_b128 a[12:15], v229 offset:14336\n"
  "v_mfma_f32_16x16x32_bf16 v[20:23], a[84:87], v[204:207], v[20:23]\n"
  "ds_read_b128 a[16:19], v229 offset:5120\n"
  "v_mfma_f32_16x16x32_bf16 v[16:19], a[80:83], v[204:207], v[16:19]\n"
  "ds_read_b64_tr_b16 v[204:205], v228 offset:16384\n"
  "v_xor_b32 v77, s59, v79\n"
  "v_mfma_f32_16x16x32_bf16 v[16:19], a[64:67], v[200:203], v[16:19]\n"
  "s_mul_i32 s98, s98, 352256\n"
  "ds_read_b64_tr_b16 v[206:207], v228 offset:16896\n"
  "s_waitcnt lgkmcnt(8)\n"
  "v_mfma_f32_16x16x32_bf16 v[32:35], a[64:67], v[196:199], v[32:35]\n"
  "s_add_u32 s59, s59, 32\n"
  "ds_read_b64_tr_b16 v[200:201], v228 offset:20480\n"
  "s_waitcnt lgkmcnt(7)\n"
  "v_mfma_f32_16x16x32_bf16 v[32:35], a[80:83], v[192:195], v[32:35]\n"
  "ds_read_b64_tr_b16 v[202:203], v228 offset:20992\n"
  "v_mfma_f32_16x16x32_bf16 v[36:39], a[84:87], v[192:195], v[36:39]\n"
  "ds_read_b128 a[20:23], v229 offset:7168\n"
  "v_xor_b32 v78, s59, v79\n"
  "v_mfma_f32_16x16x32_bf16 v[36:39], a[68:71], v[196:199], v[36:39]\n"
  "ds_read_b128 a[24:27], v229 offset:13312\n"
  "v_mfma_f32_16x16x32_bf16 v[40:43], a[72:75], v[196:199], v[40:43]\n"
  "s_add_u32 s59, s59, 32\n"
  "ds_read_b128 a[28:31], v229 offset:15360\n"
  "v_mfma_f32_16x16x32_bf16 v[40:43], a[88:91], v[192:195], v[40:43]\n"
  "ds_read_b128 a[32:35], v230 offset:0\n"
  "v_mfma_f32_16x16x32_bf16 v[44:47], a[92:95], v[192:195], v[44:47]\n"
  "s_add_u32 s60, s26, s98\n"
  "s_addc_u32 s61, s27, 0\n"
  "ds_read_b64_tr_b16 v[192:193], v216 offset:24576\n"
  "v_mfma_f32_16x16x32_bf16 v[44:47], a[76:79], v[196:199], v[44:47]\n"
  "ds_read_b64_tr_b16 v[194:195], v216 offset:25088\n"
  "s_waitcnt lgkmcnt(8)\n"
  "v_mfma_f32_16x16x32_bf16 v[60:63], a[76:79], v[204:207], v[60:63]\n"
  "ds_read_b64_tr_b16 v[196:197], v216 offset:28672\n"
  "s_waitcnt lgkmcnt(7)\n"
  "v_mfma_f32_16x16x32_bf16 v[60:63], a[92:95], v[200:203], v[60:63]\n"
  "ds_read_b64_tr_b16 v[198:199], v216 offset:29184\n"
  "v_mfma_f32_16x16x32_bf16 v[56:59], a[88:91], v[200:203], v[56:59]\n"
  "ds_read_b128 a[36:39], v230 offset:2048\n"
  "v_mfma_f32_16x16x32_bf16 v[56:59], a[72:75], v[204:207], v[56:59]\n"
  "ds_read_b128 a[40:43], v230 offset:4096\n"
  "v_mfma_f32_16x16x32_bf16 v[52:55], a[68:71], v[204:207], v[52:55]\n"
  "s_add_u32 m0, s48, 0\n"
  "ds_read_b128 a[44:47], v230 offset:6144\n"
  "v_mfma_f32_16x16x32_bf16 v[52:55], a[84:87], v[200:203], v[52:55]\n"
  "ds_read_b128 a[48:51], v230 offset:1024\n"
  "v_mfma_f32_16x16x32_bf16 v[48:51], a[80:83], v[200:203], v[48:51]\n"
  "ds_read_b64_tr_b16 v[200:201], v217 offset:24576\n"
  "v_mfma_f32_16x16x32_bf16 v[48:51], a[64:67], v[204:207], v[48:51]\n"
  "ds_read_b64_tr_b16 v[202:203], v217 offset:25088\n"
  "s_waitcnt lgkmcnt(8)\n"
  "v_mfma_f32_16x16x32_bf16 v[0:3], a[96:99], v[192:195], v[0:3]\n"
  "ds_read_b64_tr_b16 v[204:205], v217 offset:28672\n"
  "s_waitcnt lgkmcnt(7)\n"
  "v_mfma_f32_16x16x32_bf16 v[0:3], a[112:115], v[196:199], v[0:3]\n"
  "ds_read_b64_tr_b16 v[206:207], v217 offset:29184\n"
  "v_mfma_f32_16x16x32_bf16 v[4:7], a[116:119], v[196:199], v[4:7]\n"
  "ds_read_b128 a[52:55], v230 offset:3072\n"
  "v_mfma_f32_16x16x32_bf16 v[4:7], a[100:103], v[192:195], v[4:7]\n"
  "ds_read_b128 a[56:59], v230 offset:5120\n"
  "v_mfma_f32_16x16x32_bf16 v[8:11], a[104:107], v[192:195], v[8:11]\n"
  "ds_read_b128 a[60:63], v230 offset:7168\n"
  "v_mfma_f32_16x16x32_bf16 v[8:11], a[120:123], v[196:199], v[8:11]\n"
  "ds_write_b128 v229, v[236:239] offset:0\n"
  "v_mfma_f32_16x16x32_bf16 v[12:15], a[124:127], v[196:199], v[12:15]\n"
  "ds_read_b64_tr_b16 v[196:197], v218 offset:24576\n"
  "v_cvt_pk_bf16_f32 v240, v176, v177\n"
  "v_mfma_f32_16x16x32_bf16 v[12:15], a[108:111], v[192:195], v[12:15]\n"
  "ds_read_b64_tr_b16 v[198:199], v218 offset:25088\n"
  "v_cvt_pk_bf16_f32 v241, v178, v179\n"
  "s_waitcnt lgkmcnt(8)\n"
  "v_mfma_f32_16x16x32_bf16 v[28:31], a[108:111], v[200:203], v[28:31]\n"
  "ds_read_b64_tr_b16 v[192:193], v218 offset:28672\n"
  "v_cvt_pk_bf16_f32 v242, v180, v181\n"
  "s_waitcnt lgkmcnt(7)\n"
  "v_mfma_f32_16x16x32_bf16 v[28:31], a[124:127], v[204:207], v[28:31]\n"
  "ds_read_b64_tr_b16 v[194:195], v218 offset:29184\n"
  "v_cvt_pk_bf16_f32 v243, v182, v183\n"
  "v_mfma_f32_16x16x32_bf16 v[24:27], a[120:123], v[204:207], v[24:27]\n"
  "global_store_dwordx4 v232, v[88:91], s[44:45] offset:-2048 sc1\n"
  "v_mfma_f32_16x16x32_bf16 v[24:27], a[104:107], v[200:203], v[24:27]\n"
  "ds_read_b128 v[92:95], v229 offset:3072\n"
  "v_mfma_f32_16x16x32_bf16 v[20:23], a[100:103], v[200:203], v[20:23]\n"
  "ds_write_b128 v229, v[240:243] offset:1024\n"
  "v_mfma_f32_16x16x32_bf16 v[20:23], a[116:119], v[204:207], v[20:23]\n"
  "global_load_lds_dwordx4 v248, s[60:61] offset:0\n"
  "v_mfma_f32_16x16x32_bf16 v[16:19], a[112:115], v[204:207], v[16:19]\n"
  "ds_read_b64_tr_b16 v[204:205], v228 offset:24576\n"
  "v_mfma_f32_16x16x32_bf16 v[16:19], a[96:99], v[200:203], v[16:19]\n"
  "ds_read_b64_tr_b16 v[206:207], v228 offset:25088\n"
  "s_waitcnt lgkmcnt(6)\n"
  "v_mfma_f32_16x16x32_bf16 v[32:35], a[96:99], v[196:199], v[32:35]\n"
  "ds_read_b64_tr_b16 v[200:201], v228 offset:28672\n"
  "s_waitcnt lgkmcnt(5)\n"
  "v_mfma_f32_16x16x32_bf16 v[32:35], a[112:115], v[192:195], v[32:35]\n"
  "ds_read_b64_tr_b16 v[202:203], v228 offset:29184\n"
  "v_mfma_f32_16x16x32_bf16 v[36:39], a[116:119], v[192:195], v[36:39]\n"
  "s_waitcnt lgkmcnt(5)\n"
  "global_store_dwordx4 v232, v[92:95], s[44:45] offset:-1024 sc1\n"
  "v_mfma_f32_16x16x32_bf16 v[36:39], a[100:103], v[196:199], v[36:39]\n"
  "global_load_lds_dwordx4 v249, s[60:61] offset:1024\n"
  "v_mfma_f32_16x16x32_bf16 v[40:43], a[104:107], v[196:199], v[40:43]\n"
  "global_load_lds_dwordx4 v250, s[60:61] offset:2048\n"
  "v_mfma_f32_16x16x32_bf16 v[40:43], a[120:123], v[192:195], v[40:43]\n"
  "global_load_lds_dwordx4 v251, s[60:61] offset:3072\n"
  "v_mfma_f32_16x16x32_bf16 v[44:47], a[124:127], v[192:195], v[44:47]\n"
  "s_barrier\n"
  "ds_read_b64_tr_b16 v[192:193], v216 offset:32768\n"
  "v_mfma_f32_16x16x32_bf16 v[44:47], a[108:111], v[196:199], v[44:47]\n"
  "ds_read_b64_tr_b16 v[194:195], v216 offset:33280\n"
  "s_waitcnt lgkmcnt(4)\n"
  "v_mfma_f32_16x16x32_bf16 v[60:63], a[108:111], v[204:207], v[60:63]\n"
  "ds_read_b64_tr_b16 v[196:197], v216 offset:36864\n"
  "s_waitcnt lgkmcnt(3)\n"
  "v_mfma_f32_16x16x32_bf16 v[60:63], a[124:127], v[200:203], v[60:63]\n"
  "ds_read_b64_tr_b16 v[198:199], v216 offset:37376\n"
  "v_mfma_f32_16x16x32_bf16 v[56:59], a[120:123], v[200:203], v[56:59]\n"
  "v_mfma_f32_16x16x32_bf16 v[56:59], a[104:107], v[204:207], v[56:59]\n"
  "v_mfma_f32_16x16x32_bf16 v[52:55], a[100:103], v[204:207], v[52:55]\n"
  "v_mfma_f32_16x16x32_bf16 v[52:55], a[116:119], v[200:203], v[52:55]\n"
  "v_mfma_f32_16x16x32_bf16 v[48:51], a[112:115], v[200:203], v[48:51]\n"
  "ds_read_b64_tr_b16 v[200:201], v217 offset:32768\n"
  "v_mfma_f32_16x16x32_bf16 v[48:51], a[96:99], v[204:207], v[48:51]\n"
  "ds_read_b64_tr_b16 v[202:203], v217 offset:33280\n"
  "L_stg14_%=:\n"
  "s_waitcnt lgkmcnt(4)\n"
  "v_mfma_f32_16x16x32_bf16 v[0:3], a[128:131], v[192:195], v[0:3]\n"
  "ds_read_b64_tr_b16 v[204:205], v217 offset:36864\n"
  "s_waitcnt lgkmcnt(3)\n"
  "v_mfma_f32_16x16x32_bf16 v[0:3], a[144:147], v[196:199], v[0:3]\n"
  "s_barrier\n"
  "ds_read_b64_tr_b16 v[206:207], v217 offset:37376\n"
  "v_mfma_f32_16x16x32_bf16 v[4:7], a[148:151], v[196:199], v[4:7]\n"
  "ds_read_b128 v[88:91], v229 offset:8192\n"
  "v_mfma_f32_16x16x32_bf16 v[4:7], a[132:135], v[192:195], v[4:7]\n"
  "s_add_u32 s98, s18, 1\n"
  "v_mfma_f32_16x16x32_bf16 v[8:11], a[136:139], v[192:195], v[8:11]\n"
  "v_cvt_pk_bf16_f32 v244, v136, v137\n"
  "v_cvt_pk_bf16_f32 v245, v138, v139\n"
  "v_mfma_f32_16x16x32_bf16 v[8:11], a[152:155], v[196:199], v[8:11]\n"
  "s_and_b32 s98, s98, 3\n"
  "v_cvt_pk_bf16_f32 v246, v140, v141\n"
  "v_cvt_pk_bf16_f32 v247, v142, v143\n"
  "v_mfma_f32_16x16x32_bf16 v[12:15], a[156:159], v[196:199], v[12:15]\n"
  "ds_read_b64_tr_b16 v[196:197], v218 offset:32768\n"
  "v_mfma_f32_16x16x32_bf16 v[12:15], a[140:143], v[192:195], v[12:15]\n"
  "s_mul_i32 s98, s98, 4096\n"
  "ds_read_b64_tr_b16 v[198:199], v218 offset:33280\n"
  "s_waitcnt lgkmcnt(5)\n"
  "v_mfma_f32_16x16x32_bf16 v[28:31], a[140:143], v[200:203], v[28:31]\n"
  "ds_read_b64_tr_b16 v[192:193], v218 offset:36864\n"
  "s_waitcnt vmcnt(8)\n"
  "v_lshlrev_b32 v64, 16, v80\n"
  "s_waitcnt lgkmcnt(4)\n"
  "v_mfma_f32_16x16x32_bf16 v[28:31], a[156:159], v[204:207], v[28:31]\n"
  "s_add_u32 s52, s50, s98\n"
  "s_addc_u32 s53, s51, 0\n"
  "ds_read_b64_tr_b16 v[194:195], v218 offset:37376\n"
  "v_mfma_f32_16x16x32_bf16 v[24:27], a[152:155], v[204:207], v[24:27]\n"
  "ds_write_b128 v229, v[244:247] offset:2048\n"
  "v_mfma_f32_16x16x32_bf16 v[24:27], a[136:139], v[200:203], v[24:27]\n"
  "s_waitcnt lgkmcnt(5)\n"
  "global_store_dwordx4 v232, v[88:91], s[44:45] offset:2048 sc1\n"
  "v_mfma_f32_16x16x32_bf16 v[20:23], a[132:135], v[200:203], v[20:23]\n"
  "global_load_dwordx4 a[64:67], v232, s[52:53] offset:0 sc1\n"
  "v_and_b32 v65, 0xffff0000, v80\n"
  "v_mfma_f32_16x16x32_bf16 v[20:23], a[148:151], v[204:207], v[20:23]\n"
  "v_lshlrev_b32 v66, 16, v81\n"
  "v_mfma_f32_16x16x32_bf16 v[16:19], a[144:147], v[204:207], v[16:19]\n"
  "ds_read_b64_tr_b16 v[204:205], v228 offset:32768\n"
  "v_and_b32 v67, 0xffff0000, v81\n"
  "v_mfma_f32_16x16x32_bf16 v[16:19], a[128:131], v[200:203], v[16:19]\n"
  "ds_read_b64_tr_b16 v[206:207], v228 offset:33280\n"
  "s_waitcnt lgkmcnt(5)\n"
  "v_mfma_f32_16x16x32_bf16 v[32:35], a[128:131], v[196:199], v[32:35]\n"
  "ds_read_b64_tr_b16 v[200:201], v228 offset:36864\n"
  "s_waitcnt vmcnt(9)\n"
  "v_lshlrev_b32 v68, 16, v82\n"
  "s_waitcnt lgkmcnt(4)\n"
  "v_mfma_f32_16x16x32_bf16 v[32:35], a[144:147], v[192:195], v[32:35]\n"
  "ds_read_b64_tr_b16 v[202:203], v228 offset:37376\n"
  "v_mfma_f32_16x16x32_bf16 v[36:39], a[148:151], v[192:195], v[36:39]\n"
  "global_load_dwordx4 a[68:71], v252, s[52:53] offset:0 sc1\n"
  "v_and_b32 v69, 0xffff0000, v82\n"
  "v_mfma_f32_16x16x32_bf16 v[36:39], a[132:135], v[196:199], v[36:39]\n"
  "global_load_dwordx4 a[72:75], v253, s[52:53] offset:0 sc1\n"
  "v_mfma_f32_16x16x32_bf16 v[40:43], a[136:139], v[196:199], v[40:43]\n"
  "global_load_dwordx4 a[76:79], v254, s[52:53] offset:0 sc1\n"
  "v_lshlrev_b32 v70, 16, v83\n"
  "v_mfma_f32_16x16x32_bf16 v[40:43], a[152:155], v[192:195], v[40:43]\n"
  "global_load_dwordx4 a[80:83], v232, s[52:53] offset:1024 sc1\n"
  "v_mfma_f32_16x16x32_bf16 v[44:47], a[156:159], v[192:195], v[44:47]\n"
  "ds_read_b64_tr_b16 v[192:193], v216 offset:40960\n"
  "v_and_b32 v71, 0xffff0000, v83\n"
  "v_mfma_f32_16x16x32_bf16 v[44:47], a[140:143], v[196:199], v[44:47]\n"
  "ds_read_b64_tr_b16 v[194:195], v216 offset:41472\n"
  "s_waitcnt lgkmcnt(4)\n"
  "v_mfma_f32_16x16x32_bf16 v[60:63], a[140:143], v[204:207], v[60:63]\n"
  "ds_read_b64_tr_b16 v[196:197], v216 offset:45056\n"
  "s_waitcnt vmcnt(12)\n"
  "v_lshlrev_b32 v72, 16, v84\n"
  "s_waitcnt lgkmcnt(3)\n"
  "v_mfma_f32_16x16x32_bf16 v[60:63], a[156:159], v[200:203], v[60:63]\n"
  "ds_read_b64_tr_b16 v[198:199], v216 offset:45568\n"
  "v_mfma_f32_16x16x32_bf16 v[56:59], a[152:155], v[200:203], v[56:59]\n"
  "global_load_dwordx4 a[84:87], v252, s[52:53] offset:1024 sc1\n"
  "v_and_b32 v73, 0xffff0000, v84\n"
  "v_mfma_f32_16x16x32_bf16 v[56:59], a[136:139], v[204:207], v[56:59]\n"
  "global_load_dwordx4 a[88:91], v253, s[52:53] offset:1024 sc1\n"
  "v_mfma_f32_16x16x32_bf16 v[52:55], a[132:135], v[204:207], v[52:55]\n"
  "global_load_dwordx4 a[92:95], v254, s[52:53] offset:1024 sc1\n"
  "v_lshlrev_b32 v74, 16, v85\n"
  "v_mfma_f32_16x16x32_bf16 v[52:55], a[148:151], v[200:203], v[52:55]\n"
  "ds_read_b128 v[92:95], v229 offset:9216\n"
  "v_mfma_f32_16x16x32_bf16 v[48:51], a[144:147], v[200:203], v[48:51]\n"
  "s_add_u32 s98, s18, 1\n"
  "ds_read_b64_tr_b16 v[200:201], v217 offset:40960\n"
  "v_and_b32 v75, 0xffff0000, v85\n"
  "v_mfma_f32_16x16x32_bf16 v[48:51], a[128:131], v[204:207], v[48:51]\n"
  "ds_read_b64_tr_b16 v[202:203], v217 offset:41472\n"
  "s_waitcnt lgkmcnt(5)\n"
  "v_mfma_f32_16x16x32_bf16 v[0:3], a[160:163], v[192:195], v[0:3]\n"
  "s_and_b32 s98, s98, 3\n"
  "ds_read_b64_tr_b16 v[204:205], v217 offset:45056\n"
  "v_cvt_pk_bf16_f32 v236, v152, v153\n"
  "s_waitcnt lgkmcnt(4)\n"
  "v_mfma_f32_16x16x32_bf16 v[0:3], a[176:179], v[196:199], v[0:3]\n"
  "ds_read_b64_tr_b16 v[206:207], v217 offset:45568\n"
  "v_cvt_pk_bf16_f32 v237, v154, v155\n"
  "v_mfma_f32_16x16x32_bf16 v[4:7], a[180:183], v[196:199], v[4:7]\n"
  "s_mul_i32 s98, s98, 352256\n"
  "v_cvt_pk_bf16_f32 v238, v156, v157\n"
  "v_cvt_pk_bf16_f32 v239, v158, v159\n"
  "v_mfma_f32_16x16x32_bf16 v[4:7], a[164:167], v[192:195], v[4:7]\n"
  "ds_write_b128 v229, v[236:239] offset:3072\n"
  "v_mfma_f32_16x16x32_bf16 v[8:11], a[168:171], v[192:195], v[8:11]\n"
  "s_waitcnt lgkmcnt(5)\n"
  "global_store_dwordx4 v232, v[92:95], s[44:45] offset:3072 sc1\n"
  "v_mfma_f32_16x16x32_bf16 v[8:11], a[184:187], v[196:199], v[8:11]\n"
  "s_add_u32 s60, s26, s98\n"
  "s_addc_u32 s61, s27, 0\n"
  "v_mfma_f32_16x16x32_bf16 v[12:15], a[188:191], v[196:199], v[12:15]\n"
  "ds_read_b64_tr_b16 v[196:197], v218 offset:40960\n"
  "v_mfma_f32_16x16x32_bf16 v[12:15], a[172:175], v[192:195], v[12:15]\n"
  "s_add_u32 m0, s48, 16384\n"
  "ds_read_b64_tr_b16 v[198:199], v218 offset:41472\n"
  "s_waitcnt lgkmcnt(5)\n"
  "v_mfma_f32_16x16x32_bf16 v[28:31], a[172:175], v[200:203], v[28:31]\n"
  "ds_read_b64_tr_b16 v[192:193], v218 offset:45056\n"
  "s_waitcnt lgkmcnt(4)\n"
  "v_mfma_f32_16x16x32_bf16 v[28:31], a[188:191], v[204:207], v[28:31]\n"
  "ds_read_b64_tr_b16 v[194:195], v218 offset:45568\n"
  "v_mfma_f32_16x16x32_bf16 v[24:27], a[184:187], v[204:207], v[24:27]\n"
  "global_load_lds_dwordx4 v248, s[60:61] offset:0\n"
  "v_mfma_f32_16x16x32_bf16 v[24:27], a[168:171], v[200:203], v[24:27]\n"
  "v_mfma_f32_16x16x32_bf16 v[20:23], a[164:167], v[200:203], v[20:23]\n"
  "global_load_lds_dwordx4 v249, s[60:61] offset:1024\n"
  "v_mfma_f32_16x16x32_bf16 v[20:23], a[180:183], v[204:207], v[20:23]\n"
  "v_mfma_f32_16x16x32_bf16 v[16:19], a[176:179], v[204:207], v[16:19]\n"
  "ds_read_b64_tr_b16 v[204:205], v228 offset:40960\n"
  "v_mfma_f32_16x16x32_bf16 v[16:19], a[160:163], v[200:203], v[16:19]\n"
  "ds_read_b64_tr_b16 v[206:207], v228 offset:41472\n"
  "s_waitcnt lgkmcnt(4)\n"
  "v_mfma_f32_16x16x32_bf16 v[32:35], a[160:163], v[196:199], v[32:35]\n"
  "ds_read_b64_tr_b16 v[200:201], v228 offset:45056\n"
  "s_waitcnt lgkmcnt(3)\n"
  "v_mfma_f32_16x16x32_bf16 v[32:35], a[176:179], v[192:195], v[32:35]\n"
  "ds_read_b64_tr_b16 v[202:203], v228 offset:45568\n"
  "v_mfma_f32_16x16x32_bf16 v[36:39], a[180:183], v[192:195], v[36:39]\n"
  "global_load_lds_dwordx4 v250, s[60:61] offset:2048\n"
  "v_mfma_f32_16x16x32_bf16 v[36:39], a[164:167], v[196:199], v[36:39]\n"
  "global_load_lds_dwordx4 v251, s[60:61] offset:3072\n"
  "v_mfma_f32_16x16x32_bf16 v[40:43], a[168:171], v[196:199], v[40:43]\n"
  "global_load_dwordx4 a[96:99], v232, s[52:53] offset:2048 sc1\n"
  "v_mfma_f32_16x16x32_bf16 v[40:43], a[184:187], v[192:195], v[40:43]\n"
  "global_load_dwordx4 a[100:103], v252, s[52:53] offset:2048 sc1\n"
  "v_mfma_f32_16x16x32_bf16 v[44:47], a[188:191], v[192:195], v[44:47]\n"
  "s_barrier\n"
  "ds_read_b64_tr_b16 v[192:193], v216 offset:49152\n"
  "v_mfma_f32_16x16x32_bf16 v[44:47], a[172:175], v[196:199], v[44:47]\n"
  "ds_read_b64_tr_b16 v[194:195], v216 offset:49664\n"
  "s_waitcnt lgkmcnt(4)\n"
  "v_mfma_f32_16x16x32_bf16 v[60:63], a[172:175], v[204:207], v[60:63]\n"
  "ds_read_b64_tr_b16 v[196:197], v216 offset:53248\n"
  "s_waitcnt lgkmcnt(3)\n"
  "v_mfma_f32_16x16x32_bf16 v[60:63], a[188:191], v[200:203], v[60:63]\n"
  "ds_read_b64_tr_b16 v[198:199], v216 offset:53760\n"
  "v_mfma_f32_16x16x32_bf16 v[56:59], a[184:187], v[200:203], v[56:59]\n"
  "global_load_dwordx4 a[104:107], v253, s[52:53] offset:2048 sc1\n"
  "v_mfma_f32_16x16x32_bf16 v[56:59], a[168:171], v[204:207], v[56:59]\n"
  "global_load_dwordx4 a[108:111], v254, s[52:53] offset:2048 sc1\n"
  "v_mfma_f32_16x16x32_bf16 v[52:55], a[164:167], v[204:207], v[52:55]\n"
  "v_mfma_f32_16x16x32_bf16 v[52:55], a[180:183], v[200:203], v[52:55]\n"
  "v_mfma_f32_16x16x32_bf16 v[48:51], a[176:179], v[200:203], v[48:51]\n"
  "ds_read_b64_tr_b16 v[200:201], v217 offset:49152\n"
  "v_mfma_f32_16x16x32_bf16 v[48:51], a[160:163], v[204:207], v[48:51]\n"
  "ds_read_b64_tr_b16 v[202:203], v217 offset:49664\n"
  "L_stg15_%=:\n"
  "s_waitcnt lgkmcnt(4)\n"
  "v_mfma_f32_16x16x32_bf16 v[0:3], a[192:195], v[192:195], v[0:3]\n"
  "ds_read_b64_tr_b16 v[204:205], v217 offset:53248\n"
  "s_waitcnt lgkmcnt(3)\n"
  "v_mfma_f32_16x16x32_bf16 v[0:3], a[208:211], v[196:199], v[0:3]\n"
  "s_barrier\n"
  "ds_read_b64_tr_b16 v[206:207], v217 offset:53760\n"
  "v_mfma_f32_16x16x32_bf16 v[4:7], a[212:215], v[196:199], v[4:7]\n"
  "ds_read_b128 v[88:91], v229 offset:10240\n"
  "v_mfma_f32_16x16x32_bf16 v[4:7], a[196:199], v[192:195], v[4:7]\n"
  "global_load_dwordx4 a[112:115], v232, s[52:53] offset:3072 sc1\n"
  "v_mfma_f32_16x16x32_bf16 v[8:11], a[200:203], v[192:195], v[8:11]\n"
  "v_cvt_pk_bf16_f32 v240, v168, v169\n"
  "v_cvt_pk_bf16_f32 v241, v170, v171\n"
  "v_mfma_f32_16x16x32_bf16 v[8:11], a[216:219], v[196:199], v[8:11]\n"
  "v_cvt_pk_bf16_f32 v242, v172, v173\n"
  "v_cvt_pk_bf16_f32 v243, v174, v175\n"
  "v_mfma_f32_16x16x32_bf16 v[12:15], a[220:223], v[196:199], v[12:15]\n"
  "ds_read_b64_tr_b16 v[196:197], v218 offset:49152\n"
  "v_mfma_f32_16x16x32_bf16 v[12:15], a[204:207], v[192:195], v[12:15]\n"
  "ds_read_b64_tr_b16 v[198:199], v218 offset:49664\n"
  "s_waitcnt lgkmcnt(5)\n"
  "v_mfma_f32_16x16x32_bf16 v[28:31], a[204:207], v[200:203], v[28:31]\n"
  "ds_read_b64_tr_b16 v[192:193], v218 offset:53248\n"
  "s_waitcnt lgkmcnt(4)\n"
  "v_mfma_f32_16x16x32_bf16 v[28:31], a[220:223], v[204:207], v[28:31]\n"
  "ds_read_b64_tr_b16 v[194:195], v218 offset:53760\n"
  "v_mfma_f32_16x16x32_bf16 v[24:27], a[216:219], v[204:207], v[24:27]\n"
  "ds_write_b128 v229, v[240:243] offset:4096\n"
  "v_mfma_f32_16x16x32_bf16 v[24:27], a[200:203], v[200:203], v[24:27]\n"
  "global_load_dwordx4 a[116:119], v252, s[52:53] offset:3072 sc1\n"
  "v_mfma_f32_16x16x32_bf16 v[20:23], a[196:199], v[200:203], v[20:23]\n"
  "global_load_dwordx4 a[120:123], v253, s[52:53] offset:3072 sc1\n"
  "v_mfma_f32_16x16x32_bf16 v[20:23], a[212:215], v[204:207], v[20:23]\n"
  "global_load_dwordx4 a[124:127], v254, s[52:53] offset:3072 sc1\n"
  "s_add_u32 s98, s18, 2\n"
  "v_mfma_f32_16x16x32_bf16 v[16:19], a[208:211], v[204:207], v[16:19]\n"
  "s_and_b32 s98, s98, 3\n"
  "ds_read_b64_tr_b16 v[204:205], v228 offset:49152\n"
  "v_mfma_f32_16x16x32_bf16 v[16:19], a[192:195], v[200:203], v[16:19]\n"
  "ds_read_b64_tr_b16 v[206:207], v228 offset:49664\n"
  "s_waitcnt lgkmcnt(5)\n"
  "v_mfma_f32_16x16x32_bf16 v[32:35], a[192:195], v[196:199], v[32:35]\n"
  "s_mul_i32 s98, s98, 4096\n"
  "ds_read_b64_tr_b16 v[200:201], v228 offset:53248\n"
  "s_waitcnt lgkmcnt(4)\n"
  "v_mfma_f32_16x16x32_bf16 v[32:35], a[208:211], v[192:195], v[32:35]\n"
  "ds_read_b64_tr_b16 v[202:203], v228 offset:53760\n"
  "v_mfma_f32_16x16x32_bf16 v[36:39], a[212:215], v[192:195], v[36:39]\n"
  "s_add_u32 s52, s50, s98\n"
  "s_addc_u32 s53, s51, 0\n"
  "global_store_dwordx4 v232, v[88:91], s[42:43] offset:-4096 sc1\n"
  "v_mfma_f32_16x16x32_bf16 v[36:39], a[196:199], v[196:199], v[36:39]\n"
  "v_mfma_f32_16x16x32_bf16 v[40:43], a[200:203], v[196:199], v[40:43]\n"
  "v_mfma_f32_16x16x32_bf16 v[40:43], a[216:219], v[192:195], v[40:43]\n"
  "global_load_dwordx4 a[128:131], v232, s[52:53] offset:0 sc1\n"
  "v_mfma_f32_16x16x32_bf16 v[44:47], a[220:223], v[192:195], v[44:47]\n"
  "ds_read_b64_tr_b16 v[192:193], v216 offset:57344\n"
  "v_mfma_f32_16x16x32_bf16 v[44:47], a[204:207], v[196:199], v[44:47]\n"
  "ds_read_b64_tr_b16 v[194:195], v216 offset:57856\n"
  "s_waitcnt lgkmcnt(4)\n"
  "v_mfma_f32_16x16x32_bf16 v[60:63], a[204:207], v[204:207], v[60:63]\n"
  "ds_read_b64_tr_b16 v[196:197], v216 offset:61440\n"
  "s_waitcnt lgkmcnt(3)\n"
  "v_mfma_f32_16x16x32_bf16 v[60:63], a[220:223], v[200:203], v[60:63]\n"
  "ds_read_b64_tr_b16 v[198:199], v216 offset:61952\n"
  "v_mfma_f32_16x16x32_bf16 v[56:59], a[216:219], v[200:203], v[56:59]\n"
  "global_load_dwordx4 a[132:135], v252, s[52:53] offset:0 sc1\n"
  "v_mfma_f32_16x16x32_bf16 v[56:59], a[200:203], v[204:207], v[56:59]\n"
  "global_load_dwordx4 a[136:139], v253, s[52:53] offset:0 sc1\n"
  "v_mfma_f32_16x16x32_bf16 v[52:55], a[196:199], v[204:207], v[52:55]\n"
  "global_load_dwordx4 a[140:143], v254, s[52:53] offset:0 sc1\n"
  "v_mfma_f32_16x16x32_bf16 v[52:55], a[212:215], v[200:203], v[52:55]\n"
  "ds_read_b128 v[92:95], v229 offset:11264\n"
  "v_mfma_f32_16x16x32_bf16 v[48:51], a[208:211], v[200:203], v[48:51]\n"
  "s_add_u32 s98, s18, 2\n"
  "ds_read_b64_tr_b16 v[200:201], v217 offset:57344\n"
  "v_mfma_f32_16x16x32_bf16 v[48:51], a[192:195], v[204:207], v[48:51]\n"
  "ds_read_b64_tr_b16 v[202:203], v217 offset:57856\n"
  "s_waitcnt lgkmcnt(5)\n"
  "v_mfma_f32_16x16x32_bf16 v[0:3], a[224:227], v[192:195], v[0:3]\n"
  "s_and_b32 s98, s98, 3\n"
  "ds_read_b64_tr_b16 v[204:205], v217 offset:61440\n"
  "v_cvt_pk_bf16_f32 v244, v184, v185\n"
  "s_waitcnt lgkmcnt(4)\n"
  "v_mfma_f32_16x16x32_bf16 v[0:3], a[240:243], v[196:199], v[0:3]\n"
  "ds_read_b64_tr_b16 v[206:207], v217 offset:61952\n"
  "v_cvt_pk_bf16_f32 v245, v186, v187\n"
  "v_mfma_f32_16x16x32_bf16 v[4:7], a[244:247], v[196:199], v[4:7]\n"
  "s_mul_i32 s98, s98, 352256\n"
  "v_cvt_pk_bf16_f32 v246, v188, v189\n"
  "v_cvt_pk_bf16_f32 v247, v190, v191\n"
  "v_mfma_f32_16x16x32_bf16 v[4:7], a[228:231], v[192:195], v[4:7]\n"
  "ds_write_b128 v229, v[244:247] offset:5120\n"
  "v_mfma_f32_16x16x32_bf16 v[8:11], a[232:235], v[192:195], v[8:11]\n"
  "s_waitcnt lgkmcnt(5)\n"
  "global_store_dwordx4 v232, v[92:95], s[42:43] offset:-3072 sc1\n"
  "v_mfma_f32_16x16x32_bf16 v[8:11], a[248:251], v[196:199], v[8:11]\n"
  "s_add_u32 s60, s26, s98\n"
  "s_addc_u32 s61, s27, 0\n"
  "v_mfma_f32_16x16x32_bf16 v[12:15], a[252:255], v[196:199], v[12:15]\n"
  "ds_read_b64_tr_b16 v[196:197], v218 offset:57344\n"
  "v_mfma_f32_16x16x32_bf16 v[12:15], a[236:239], v[192:195], v[12:15]\n"
  "s_add_u32 m0, s48, 32768\n"
  "ds_read_b64_tr_b16 v[198:199], v218 offset:57856\n"
  "s_waitcnt lgkmcnt(5)\n"
  "v_mfma_f32_16x16x32_bf16 v[28:31], a[236:239], v[200:203], v[28:31]\n"
  "ds_read_b64_tr_b16 v[192:193], v218 offset:61440\n"
  "s_waitcnt lgkmcnt(4)\n"
  "v_mfma_f32_16x16x32_bf16 v[28:31], a[252:255], v[204:207], v[28:31]\n"
  "ds_read_b64_tr_b16 v[194:195], v218 offset:61952\n"
  "v_mfma_f32_16x16x32_bf16 v[24:27], a[248:251], v[204:207], v[24:27]\n"
  "global_load_lds_dwordx4 v248, s[60:61] offset:0\n"
  "v_mfma_f32_16x16x32_bf16 v[24:27], a[232:235], v[200:203], v[24:27]\n"
  "v_mfma_f32_16x16x32_bf16 v[20:23], a[228:231], v[200:203], v[20:23]\n"
  "global_load_lds_dwordx4 v249, s[60:61] offset:1024\n"
  "v_mfma_f32_16x16x32_bf16 v[20:23], a[244:247], v[204:207], v[20:23]\n"
  "v_mfma_f32_16x16x32_bf16 v[16:19], a[240:243], v[204:207], v[16:19]\n"
  "ds_read_b64_tr_b16 v[204:205], v228 offset:57344\n"
  "v_mfma_f32_16x16x32_bf16 v[16:19], a[224:227], v[200:203], v[16:19]\n"
  "ds_read_b64_tr_b16 v[206:207], v228 offset:57856\n"
  "s_waitcnt lgkmcnt(4)\n"
  "v_mfma_f32_16x16x32_bf16 v[32:35], a[224:227], v[196:199], v[32:35]\n"
  "ds_read_b64_tr_b16 v[200:201], v228 offset:61440\n"
  "s_waitcnt lgkmcnt(3)\n"
  "v_mfma_f32_16x16x32_bf16 v[32:35], a[240:243], v[192:195], v[32:35]\n"
  "ds_read_b64_tr_b16 v[202:203], v228 offset:61952\n"
  "v_mfma_f32_16x16x32_bf16 v[36:39], a[244:247], v[192:195], v[36:39]\n"
  "global_load_lds_dwordx4 v250, s[60:61] offset:2048\n"
  "v_mfma_f32_16x16x32_bf16 v[36:39], a[228:231], v[196:199], v[36:39]\n"
  "global_load_lds_dwordx4 v251, s[60:61] offset:3072\n"
  "v_mfma_f32_16x16x32_bf16 v[40:43], a[232:235], v[196:199], v[40:43]\n"
  "global_load_dwordx4 a[144:147], v232, s[52:53] offset:1024 sc1\n"
  "v_mfma_f32_16x16x32_bf16 v[40:43], a[248:251], v[192:195], v[40:43]\n"
  "global_load_dwordx4 a[148:151], v252, s[52:53] offset:1024 sc1\n"
  "v_mfma_f32_16x16x32_bf16 v[44:47], a[252:255], v[192:195], v[44:47]\n"
  "s_waitcnt vmcnt(34)\n"
  "s_barrier\n"
  "ds_read_b64_tr_b16 v[192:193], v76 offset:0\n"
  "v_mfma_f32_16x16x32_bf16 v[44:47], a[236:239], v[196:199], v[44:47]\n"
  "ds_read_b64_tr_b16 v[194:195], v76 offset:512\n"
  "s_waitcnt lgkmcnt(4)\n"
  "v_mfma_f32_16x16x32_bf16 v[60:63], a[236:239], v[204:207], v[60:63]\n"
  "ds_read_b64_tr_b16 v[196:197], v76 offset:4096\n"
  "s_waitcnt lgkmcnt(3)\n"
  "v_mfma_f32_16x16x32_bf16 v[60:63], a[252:255], v[200:203], v[60:63]\n"
  "ds_read_b64_tr_b16 v[198:199], v76 offset:4608\n"
  "v_mfma_f32_16x16x32_bf16 v[56:59], a[248:251], v[200:203], v[56:59]\n"
  "global_load_dwordx4 a[152:155], v253, s[52:53] offset:1024 sc1\n"
  "v_mfma_f32_16x16x32_bf16 v[56:59], a[232:235], v[204:207], v[56:59]\n"
  "global_load_dwordx4 a[156:159], v254, s[52:53] offset:1024 sc1\n"
  "v_mfma_f32_16x16x32_bf16 v[52:55], a[228:231], v[204:207], v[52:55]\n"
  "v_mfma_f32_16x16x32_bf16 v[52:55], a[244:247], v[200:203], v[52:55]\n"
  "v_mfma_f32_16x16x32_bf16 v[48:51], a[240:243], v[200:203], v[48:51]\n"
  "ds_read_b64_tr_b16 v[200:201], v77 offset:0\n"
  "v_mfma_f32_16x16x32_bf16 v[48:51], a[224:227], v[204:207], v[48:51]\n"
  "ds_read_b64_tr_b16 v[202:203], v77 offset:512\n"
  "L_stg16_%=:\n"
  "s_waitcnt lgkmcnt(4)\n"
  "v_mfma_f32_16x16x32_bf16 v[128:131], v[192:195], a[0:3], v[64:67]\n"
  "ds_read_b64_tr_b16 v[204:205], v77 offset:4096\n"
  "s_waitcnt lgkmcnt(3)\n"
  "v_mfma_f32_16x16x32_bf16 v[128:131], v[196:199], a[16:19], v[128:131]\n"
  "s_barrier\n"
  "ds_read_b64_tr_b16 v[206:207], v77 offset:4608\n"
  "v_mfma_f32_16x16x32_bf16 v[132:135], v[196:199], a[20:23], v[64:67]\n"
  "ds_read_b128 v[88:91], v229 offset:0\n"
  "v_mfma_f32_16x16x32_bf16 v[132:135], v[192:195], a[4:7], v[132:135]\n"
  "global_load_dwordx4 a[160:163], v232, s[52:53] offset:2048 sc1\n"
  "v_mfma_f32_16x16x32_bf16 v[136:139], v[192:195], a[8:11], v[64:67]\n"
  "v_cvt_pk_bf16_f32 v236, v0, v1\n"
  "v_cvt_pk_bf16_f32 v237, v2, v3\n"
  "v_mfma_f32_16x16x32_bf16 v[136:139], v[196:199], a[24:27], v[136:139]\n"
  "v_cvt_pk_bf16_f32 v238, v4, v5\n"
  "v_cvt_pk_bf16_f32 v239, v6, v7\n"
  "v_mfma_f32_16x16x32_bf16 v[140:143], v[196:199], a[28:31], v[64:67]\n"
  "ds_read_b64_tr_b16 v[196:197], v78 offset:0\n"
  "v_mfma_f32_16x16x32_bf16 v[140:143], v[192:195], a[12:15], v[140:143]\n"
  "ds_read_b64_tr_b16 v[198:199], v78 offset:512\n"
  "s_waitcnt lgkmcnt(5)\n"
  "v_mfma_f32_16x16x32_bf16 v[156:159], v[200:203], a[12:15], v[68:71]\n"
  "ds_read_b64_tr_b16 v[192:193], v78 offset:4096\n"
  "s_waitcnt lgkmcnt(4)\n"
  "v_mfma_f32_16x16x32_bf16 v[156:159], v[204:207], a[28:31], v[156:159]\n"
  "ds_read_b64_tr_b16 v[194:195], v78 offset:4608\n"
  "v_mfma_f32_16x16x32_bf16 v[152:155], v[204:207], a[24:27], v[68:71]\n"
  "ds_write_b128 v229, v[236:239] offset:6144\n"
  "v_mfma_f32_16x16x32_bf16 v[152:155], v[200:203], a[8:11], v[152:155]\n"
  "global_load_dwordx4 a[164:167], v252, s[52:53] offset:2048 sc1\n"
  "v_mfma_f32_16x16x32_bf16 v[148:151], v[200:203], a[4:7], v[68:71]\n"
  "global_load_dwordx4 a[168:171], v253, s[52:53] offset:2048 sc1\n"
  "v_mfma_f32_16x16x32_bf16 v[148:151], v[204:207], a[20:23], v[148:151]\n"
  "s_waitcnt lgkmcnt(5)\n"
  "global_store_dwordx4 v232, v[88:91], s[42:43] offset:-2048 sc1\n"
  "v_mfma_f32_16x16x32_bf16 v[144:147], v[204:207], a[16:19], v[68:71]\n"
  "ds_read_b64_tr_b16 v[204:205], v76 offset:8192\n"
  "v_mfma_f32_16x16x32_bf16 v[144:147], v[200:203], a[0:3], v[144:147]\n"
  "ds_read_b64_tr_b16 v[206:207], v76 offset:8704\n"
  "s_waitcnt lgkmcnt(5)\n"
  "v_mfma_f32_16x16x32_bf16 v[160:163], v[196:199], a[0:3], v[72:75]\n"
  "ds_read_b64_tr_b16 v[200:201], v76 offset:12288\n"
  "s_waitcnt lgkmcnt(4)\n"
  "v_mfma_f32_16x16x32_bf16 v[160:163], v[192:195], a[16:19], v[160:163]\n"
  "ds_read_b64_tr_b16 v[202:203], v76 offset:12800\n"
  "v_mfma_f32_16x16x32_bf16 v[164:167], v[192:195], a[20:23], v[72:75]\n"
  "global_load_dwordx4 a[172:175], v254, s[52:53] offset:2048 sc1\n"
  "v_mfma_f32_16x16x32_bf16 v[164:167], v[196:199], a[4:7], v[164:167]\n"
  "global_load_dwordx4 a[176:179], v232, s[52:53] offset:3072 sc1\n"
  "v_mfma_f32_16x16x32_bf16 v[168:171], v[196:199], a[8:11], v[72:75]\n"
  "global_load_dwordx4 a[180:183], v252, s[52:53] offset:3072 sc1\n"
  "v_mfma_f32_16x16x32_bf16 v[168:171], v[192:195], a[24:27], v[168:171]\n"
  "global_load_dwordx4 a[184:187], v253, s[52:53] offset:3072 sc1\n"
  "v_mfma_f32_16x16x32_bf16 v[172:175], v[192:195], a[28:31], v[72:75]\n"
  "ds_read_b64_tr_b16 v[192:193], v77 offset:8192\n"
  "v_mfma_f32_16x16x32_bf16 v[172:175], v[196:199], a[12:15], v[172:175]\n"
  "ds_read_b64_tr_b16 v[194:195], v77 offset:8704\n"
  "s_waitcnt lgkmcnt(4)\n"
  "v_mfma_f32_16x16x32_bf16 v[140:143], v[204:207], a[44:47], v[140:143]\n"
  "ds_read_b64_tr_b16 v[196:197], v77 offset:12288\n"
  "v_cvt_pk_bf16_f32 v240, v16, v17\n"
  "s_waitcnt lgkmcnt(3)\n"
  "v_mfma_f32_16x16x32_bf16 v[140:143], v[200:203], a[60:63], v[140:143]\n"
  "ds_read_b64_tr_b16 v[198:199], v77 offset:12800\n"
  "v_cvt_pk_bf16_f32 v241, v18, v19\n"
  "v_mfma_f32_16x16x32_bf16 v[136:139], v[200:203], a[56:59], v[136:139]\n"
  "global_load_dwordx4 a[188:191], v254, s[52:53] offset:3072 sc1\n"
  "s_add_u32 s98, s18, 3\n"
  "s_and_b32 s98, s98, 3\n"
  "v_mfma_f32_16x16x32_bf16 v[136:139], v[204:207], a[40:43], v[136:139]\n"
  "s_mul_i32 s98, s98, 352256\n"
  "ds_read_b128 v[92:95], v229 offset:1024\n"
  "v_cvt_pk_bf16_f32 v242, v20, v21\n"
  "v_mfma_f32_16x16x32_bf16 v[132:135], v[204:207], a[36:39], v[132:135]\n"
  "v_cvt_pk_bf16_f32 v243, v22, v23\n"
  "v_mfma_f32_16x16x32_bf16 v[132:135], v[200:203], a[52:55], v[132:135]\n"
  "s_add_u32 s60, s26, s98\n"
  "s_addc_u32 s61, s27, 0\n"
  "ds_write_b128 v229, v[240:243] offset:7168\n"
  "v_mfma_f32_16x16x32_bf16 v[128:131], v[200:203], a[48:51], v[128:131]\n"
  "ds_read_b64_tr_b16 v[200:201], v78 offset:8192\n"
  "v_mfma_f32_16x16x32_bf16 v[128:131], v[204:207], a[32:35], v[128:131]\n"
  "s_add_u32 m0, s48, 49152\n"
  "ds_read_b64_tr_b16 v[202:203], v78 offset:8704\n"
  "s_waitcnt lgkmcnt(6)\n"
  "v_mfma_f32_16x16x32_bf16 v[144:147], v[192:195], a[32:35], v[144:147]\n"
  "ds_read_b64_tr_b16 v[204:205], v78 offset:12288\n"
  "s_waitcnt lgkmcnt(5)\n"
  "v_mfma_f32_16x16x32_bf16 v[144:147], v[196:199], a[48:51], v[144:147]\n"
  "ds_read_b64_tr_b16 v[206:207], v78 offset:12800\n"
  "v_mfma_f32_16x16x32_bf16 v[148:151], v[196:199], a[52:55], v[148:151]\n"
  "s_waitcnt lgkmcnt(5)\n"
  "global_store_dwordx4 v232, v[92:95], s[42:43] offset:-1024 sc1\n"
  "v_mfma_f32_16x16x32_bf16 v[148:151], v[192:195], a[36:39], v[148:151]\n"
  "global_load_lds_dwordx4 v248, s[60:61] offset:0\n"
  "v_mfma_f32_16x16x32_bf16 v[152:155], v[192:195], a[40:43], v[152:155]\n"
  "global_load_lds_dwordx4 v249, s[60:61] offset:1024\n"
  "v_mfma_f32_16x16x32_bf16 v[152:155], v[196:199], a[56:59], v[152:155]\n"
  "v_mfma_f32_16x16x32_bf16 v[156:159], v[196:199], a[60:63], v[156:159]\n"
  "s_waitcnt vmcnt(34)\n"
  "s_barrier\n"
  "ds_read_b64_tr_b16 v[196:197], v76 offset:16384\n"
  "v_mfma_f32_16x16x32_bf16 v[156:159], v[192:195], a[44:47], v[156:159]\n"
  "ds_read_b64_tr_b16 v[198:199], v76 offset:16896\n"
  "s_waitcnt lgkmcnt(4)\n"
  "v_mfma_f32_16x16x32_bf16 v[172:175], v[200:203], a[44:47], v[172:175]\n"
  "ds_read_b64_tr_b16 v[192:193], v76 offset:20480\n"
  "s_waitcnt lgkmcnt(3)\n"
  "v_mfma_f32_16x16x32_bf16 v[172:175], v[204:207], a[60:63], v[172:175]\n"
  "ds_read_b64_tr_b16 v[194:195], v76 offset:20992\n"
  "v_mfma_f32_16x16x32_bf16 v[168:171], v[204:207], a[56:59], v[168:171]\n"
  "global_load_lds_dwordx4 v250, s[60:61] offset:2048\n"
  "v_mfma_f32_16x16x32_bf16 v[168:171], v[200:203], a[40:43], v[168:171]\n"
  "global_load_lds_dwordx4 v251, s[60:61] offset:3072\n"
  "v_mfma_f32_16x16x32_bf16 v[164:167], v[200:203], a[36:39], v[164:167]\n"
  "v_mfma_f32_16x16x32_bf16 v[164:167], v[204:207], a[52:55], v[164:167]\n"
  "v_mfma_f32_16x16x32_bf16 v[160:163], v[204:207], a[48:51], v[160:163]\n"
  "ds_read_b64_tr_b16 v[204:205], v77 offset:16384\n"
  "v_mfma_f32_16x16x32_bf16 v[160:163], v[200:203], a[32:35], v[160:163]\n"
  "ds_read_b64_tr_b16 v[206:207], v77 offset:16896\n"
  "L_stg17_%=:\n"
  "s_waitcnt lgkmcnt(4)\n"
  "v_mfma_f32_16x16x32_bf16 v[128:131], v[196:199], a[64:67], v[128:131]\n"
  "ds_read_b64_tr_b16 v[200:201], v77 offset:20480\n"
  "s_waitcnt lgkmcnt(3)\n"
  "v_mfma_f32_16x16x32_bf16 v[128:131], v[192:195], a[80:83], v[128:131]\n"
  "s_barrier\n"
  "ds_read_b64_tr_b16 v[202:203], v77 offset:20992\n"
  "v_mfma_f32_16x16x32_bf16 v[132:135], v[192:195], a[84:87], v[132:135]\n"
  "ds_read_b128 v[88:91], v229 offset:2048\n"
  "v_mfma_f32_16x16x32_bf16 v[132:135], v[196:199], a[68:71], v[132:135]\n"
  "s_add_u32 s98, s18, 3\n"
  "v_mfma_f32_16x16x32_bf16 v[136:139], v[196:199], a[72:75], v[136:139]\n"
  "s_and_b32 s98, s98, 3\n"
  "v_cvt_pk_bf16_f32 v236, v32, v33\n"
  "v_cvt_pk_bf16_f32 v237, v34, v35\n"
  "v_mfma_f32_16x16x32_bf16 v[136:139], v[192:195], a[88:91], v[136:139]\n"
  "v_cvt_pk_bf16_f32 v238, v36, v37\n"
  "v_cvt_pk_bf16_f32 v239, v38, v39\n"
  "v_mfma_f32_16x16x32_bf16 v[140:143], v[192:195], a[92:95], v[140:143]\n"
  "s_mul_i32 s98, s98, 4096\n"
  "ds_read_b64_tr_b16 v[192:193], v78 offset:16384\n"
  "v_mfma_f32_16x16x32_bf16 v[140:143], v[196:199], a[76:79], v[140:143]\n"
  "ds_read_b64_tr_b16 v[194:195], v78 offset:16896\n"
  "s_waitcnt lgkmcnt(5)\n"
  "v_mfma_f32_16x16x32_bf16 v[156:159], v[204:207], a[76:79], v[156:159]\n"
  "s_add_u32 s52, s50, s98\n"
  "s_addc_u32 s53, s51, 0\n"
  "ds_read_b64_tr_b16 v[196:197], v78 offset:20480\n"
  "s_waitcnt lgkmcnt(4)\n"
  "v_mfma_f32_16x16x32_bf16 v[156:159], v[200:203], a[92:95], v[156:159]\n"
  "ds_read_b64_tr_b16 v[198:199], v78 offset:20992\n"
  "v_mfma_f32_16x16x32_bf16 v[152:155], v[200:203], a[88:91], v[152:155]\n"
  "ds_write_b128 v229, v[236:239] offset:12288\n"
  "v_mfma_f32_16x16x32_bf16 v[152:155], v[204:207], a[72:75], v[152:155]\n"
  "s_waitcnt lgkmcnt(5)\n"
  "global_store_dwordx4 v232, v[88:91], s[46:47] offset:-4096 sc1\n"
  "v_mfma_f32_16x16x32_bf16 v[148:151], v[204:207], a[68:71], v[148:151]\n"
  "global_load_dwordx4 a[192:195], v232, s[52:53] offset:0 sc1\n"
  "v_mfma_f32_16x16x32_bf16 v[148:151], v[200:203], a[84:87], v[148:151]\n"
  "global_load_dwordx4 a[196:199], v252, s[52:53] offset:0 sc1\n"
  "v_mfma_f32_16x16x32_bf16 v[144:147], v[200:203], a[80:83], v[144:147]\n"
  "ds_read_b64_tr_b16 v[200:201], v76 offset:24576\n"
  "v_mfma_f32_16x16x32_bf16 v[144:147], v[204:207], a[64:67], v[144:147]\n"
  "ds_read_b64_tr_b16 v[202:203], v76 offset:25088\n"
  "s_waitcnt lgkmcnt(5)\n"
  "v_mfma_f32_16x16x32_bf16 v[160:163], v[192:195], a[64:67], v[160:163]\n"
  "ds_read_b64_tr_b16 v[204:205], v76 offset:28672\n"
  "s_waitcnt lgkmcnt(4)\n"
  "v_mfma_f32_16x16x32_bf16 v[160:163], v[196:199], a[80:83], v[160:163]\n"
  "ds_read_b64_tr_b16 v[206:207], v76 offset:29184\n"
  "v_mfma_f32_16x16x32_bf16 v[164:167], v[196:199], a[84:87], v[164:167]\n"
  "global_load_dwordx4 a[200:203], v253, s[52:53] offset:0 sc1\n"
  "v_mfma_f32_16x16x32_bf16 v[164:167], v[192:195], a[68:71], v[164:167]\n"
  "global_load_dwordx4 a[204:207], v254, s[52:53] offset:0 sc1\n"
  "v_mfma_f32_16x16x32_bf16 v[168:171], v[192:195], a[72:75], v[168:171]\n"
  "global_load_dwordx4 a[208:211], v232, s[52:53] offset:1024 sc1\n"
  "v_mfma_f32_16x16x32_bf16 v[168:171], v[196:199], a[88:91], v[168:171]\n"
  "global_load_dwordx4 a[212:215], v252, s[52:53] offset:1024 sc1\n"
  "v_mfma_f32_16x16x32_bf16 v[172:175], v[196:199], a[92:95], v[172:175]\n"
  "ds_read_b64_tr_b16 v[196:197], v77 offset:24576\n"
  "v_mfma_f32_16x16x32_bf16 v[172:175], v[192:195], a[76:79], v[172:175]\n"
  "ds_read_b64_tr_b16 v[198:199], v77 offset:25088\n"
  "s_waitcnt vmcnt(39) lgkmcnt(4)\n"
  "v_mfma_f32_16x16x32_bf16 v[140:143], v[200:203], a[108:111], v[140:143]\n"
  "ds_read_b64_tr_b16 v[192:193], v77 offset:28672\n"
  "v_cvt_pk_bf16_f32 v240, v48, v49\n"
  "s_waitcnt vmcnt(35) lgkmcnt(3)\n"
  "v_mfma_f32_16x16x32_bf16 v[140:143], v[204:207], a[124:127], v[140:143]\n"
  "ds_read_b64_tr_b16 v[194:195], v77 offset:29184\n"
  "v_cvt_pk_bf16_f32 v241, v50, v51\n"
  "v_mfma_f32_16x16x32_bf16 v[136:139], v[204:207], a[120:123], v[136:139]\n"
  "ds_read_b128 v[92:95], v229 offset:3072\n"
  "v_cvt_pk_bf16_f32 v242, v52, v53\n"
  "v_mfma_f32_16x16x32_bf16 v[136:139], v[200:203], a[104:107], v[136:139]\n"
  "global_load_dwordx4 a[216:219], v253, s[52:53] offset:1024 sc1\n"
  "v_cvt_pk_bf16_f32 v243, v54, v55\n"
  "v_mfma_f32_16x16x32_bf16 v[132:135], v[200:203], a[100:103], v[132:135]\n"
  "global_load_dwordx4 a[220:223], v254, s[52:53] offset:1024 sc1\n"
  "s_lshl_b32 s98, s18, 1\n"
  "s_add_u32 s98, s98, 0\n"
  "v_mfma_f32_16x16x32_bf16 v[132:135], v[204:207], a[116:119], v[132:135]\n"
  "s_and_b32 s98, s98, 7\n"
  "ds_write_b128 v229, v[240:243] offset:13312\n"
  "v_mfma_f32_16x16x32_bf16 v[128:131], v[204:207], a[112:115], v[128:131]\n"
  "s_mul_i32 s98, s98, 524288\n"
  "ds_read_b64_tr_b16 v[204:205], v78 offset:24576\n"
  "v_mfma_f32_16x16x32_bf16 v[128:131], v[200:203], a[96:99], v[128:131]\n"
  "ds_read_b64_tr_b16 v[206:207], v78 offset:25088\n"
  "s_waitcnt lgkmcnt(6)\n"
  "v_mfma_f32_16x16x32_bf16 v[144:147], v[196:199], a[96:99], v[144:147]\n"
  "s_add_u32 s60, s28, s98\n"
  "s_addc_u32 s61, s29, 0\n"
  "ds_read_b64_tr_b16 v[200:201], v78 offset:28672\n"
  "s_waitcnt lgkmcnt(5)\n"
  "v_mfma_f32_16x16x32_bf16 v[144:147], v[192:195], a[112:115], v[144:147]\n"
  "ds_read_b64_tr_b16 v[202:203], v78 offset:29184\n"
  "v_mfma_f32_16x16x32_bf16 v[148:151], v[192:195], a[116:119], v[148:151]\n"
  "s_add_u32 m0, s48, 0\n"
  "s_waitcnt lgkmcnt(5)\n"
  "global_store_dwordx4 v232, v[92:95], s[46:47] offset:-3072 sc1\n"
  "v_mfma_f32_16x16x32_bf16 v[148:151], v[196:199], a[100:103], v[148:151]\n"
  "v_mfma_f32_16x16x32_bf16 v[152:155], v[196:199], a[104:107], v[152:155]\n"
  "global_load_lds_dwordx4 v220, s[60:61] offset:0\n"
  "v_mfma_f32_16x16x32_bf16 v[152:155], v[192:195], a[120:123], v[152:155]\n"
  "v_mfma_f32_16x16x32_bf16 v[156:159], v[192:195], a[124:127], v[156:159]\n"
  "s_waitcnt vmcnt(29)\n"
  "s_barrier\n"
  "ds_read_b64_tr_b16 v[192:193], v76 offset:32768\n"
  "v_mfma_f32_16x16x32_bf16 v[156:159], v[196:199], a[108:111], v[156:159]\n"
  "ds_read_b64_tr_b16 v[194:195], v76 offset:33280\n"
  "s_waitcnt lgkmcnt(4)\n"
  "v_mfma_f32_16x16x32_bf16 v[172:175], v[204:207], a[108:111], v[172:175]\n"
  "ds_read_b64_tr_b16 v[196:197], v76 offset:36864\n"
  "s_waitcnt lgkmcnt(3)\n"
  "v_mfma_f32_16x16x32_bf16 v[172:175], v[200:203], a[124:127], v[172:175]\n"
  "ds_read_b64_tr_b16 v[198:199], v76 offset:37376\n"
  "v_mfma_f32_16x16x32_bf16 v[168:171], v[200:203], a[120:123], v[168:171]\n"
  "global_load_lds_dwordx4 v221, s[60:61] offset:1024\n"
  "v_mfma_f32_16x16x32_bf16 v[168:171], v[204:207], a[104:107], v[168:171]\n"
  "global_load_lds_dwordx4 v222, s[60:61] offset:2048\n"
  "v_mfma_f32_16x16x32_bf16 v[164:167], v[204:207], a[100:103], v[164:167]\n"
  "global_load_lds_dwordx4 v223, s[60:61] offset:3072\n"
  "v_mfma_f32_16x16x32_bf16 v[164:167], v[200:203], a[116:119], v[164:167]\n"
  "v_mfma_f32_16x16x32_bf16 v[160:163], v[200:203], a[112:115], v[160:163]\n"
  "ds_read_b64_tr_b16 v[200:201], v77 offset:32768\n"
  "v_mfma_f32_16x16x32_bf16 v[160:163], v[204:207], a[96:99], v[160:163]\n"
  "ds_read_b64_tr_b16 v[202:203], v77 offset:33280\n"
  "L_stg18_%=:\n"
  "s_waitcnt lgkmcnt(4)\n"
  "v_mfma_f32_16x16x32_bf16 v[128:131], v[192:195], a[128:131], v[128:131]\n"
  "ds_read_b64_tr_b16 v[204:205], v77 offset:36864\n"
  "s_waitcnt vmcnt(31) lgkmcnt(3)\n"
  "v_mfma_f32_16x16x32_bf16 v[128:131], v[196:199], a[144:147], v[128:131]\n"
  "s_barrier\n"
  "ds_read_b64_tr_b16 v[206:207], v77 offset:37376\n"
  "s_waitcnt vmcnt(30)\n"
  "v_mfma_f32_16x16x32_bf16 v[132:135], v[196:199], a[148:151], v[132:135]\n"
  "ds_read_b128 v[88:91], v229 offset:4096\n"
  "v_mfma_f32_16x16x32_bf16 v[132:135], v[192:195], a[132:135], v[132:135]\n"
  "global_load_dwordx4 a[224:227], v232, s[52:53] offset:2048 sc1\n"
  "v_mfma_f32_16x16x32_bf16 v[136:139], v[192:195], a[136:139], v[136:139]\n"
  "v_cvt_pk_bf16_f32 v236, v8, v9\n"
  "v_cvt_pk_bf16_f32 v237, v10, v11\n"
  "s_waitcnt vmcnt(30)\n"
  "v_mfma_f32_16x16x32_bf16 v[136:139], v[196:199], a[152:155], v[136:139]\n"
  "v_cvt_pk_bf16_f32 v238, v12, v13\n"
  "v_cvt_pk_bf16_f32 v239, v14, v15\n"
  "s_waitcnt vmcnt(29)\n"
  "v_mfma_f32_16x16x32_bf16 v[140:143], v[196:199], a[156:159], v[140:143]\n"
  "ds_read_b64_tr_b16 v[196:197], v78 offset:32768\n"
  "v_mfma_f32_16x16x32_bf16 v[140:143], v[192:195], a[140:143], v[140:143]\n"
  "ds_read_b64_tr_b16 v[198:199], v78 offset:33280\n"
  "s_waitcnt lgkmcnt(5)\n"
  "v_mfma_f32_16x16x32_bf16 v[156:159], v[200:203], a[140:143], v[156:159]\n"
  "ds_read_b64_tr_b16 v[192:193], v78 offset:36864\n"
  "s_waitcnt lgkmcnt(4)\n"
  "v_mfma_f32_16x16x32_bf16 v[156:159], v[204:207], a[156:159], v[156:159]\n"
  "ds_read_b64_tr_b16 v[194:195], v78 offset:37376\n"
  "v_mfma_f32_16x16x32_bf16 v[152:155], v[204:207], a[152:155], v[152:155]\n"
  "ds_write_b128 v229, v[236:239] offset:14336\n"
  "v_mfma_f32_16x16x32_bf16 v[152:155], v[200:203], a[136:139], v[152:155]\n"
  "global_load_dwordx4 a[228:231], v252, s[52:53] offset:2048 sc1\n"
  "v_mfma_f32_16x16x32_bf16 v[148:151], v[200:203], a[132:135], v[148:151]\n"
  "global_load_dwordx4 a[232:235], v253, s[52:53] offset:2048 sc1\n"
  "v_mfma_f32_16x16x32_bf16 v[148:151], v[204:207], a[148:151], v[148:151]\n"
  "global_load_dwordx4 a[236:239], v254, s[52:53] offset:2048 sc1\n"
  "v_mfma_f32_16x16x32_bf16 v[144:147], v[204:207], a[144:147], v[144:147]\n"
  "ds_read_b64_tr_b16 v[204:205], v76 offset:40960\n"
  "v_mfma_f32_16x16x32_bf16 v[144:147], v[200:203], a[128:131], v[144:147]\n"
  "ds_read_b64_tr_b16 v[206:207], v76 offset:41472\n"
  "s_waitcnt lgkmcnt(5)\n"
  "v_mfma_f32_16x16x32_bf16 v[160:163], v[196:199], a[128:131], v[160:163]\n"
  "ds_read_b64_tr_b16 v[200:201], v76 offset:45056\n"
  "s_waitcnt lgkmcnt(4)\n"
  "v_mfma_f32_16x16x32_bf16 v[160:163], v[192:195], a[144:147], v[160:163]\n"
  "ds_read_b64_tr_b16 v[202:203], v76 offset:45568\n"
  "v_mfma_f32_16x16x32_bf16 v[164:167], v[192:195], a[148:151], v[164:167]\n"
  "global_store_dwordx4 v232, v[88:91], s[46:47] offset:-2048 sc1\n"
  "v_mfma_f32_16x16x32_bf16 v[164:167], v[196:199], a[132:135], v[164:167]\n"
  "global_load_dwordx4 a[240:243], v232, s[52:53] offset:3072 sc1\n"
  "v_mfma_f32_16x16x32_bf16 v[168:171], v[196:199], a[136:139], v[168:171]\n"
  "global_load_dwordx4 a[244:247], v252, s[52:53] offset:3072 sc1\n"
  "v_mfma_f32_16x16x32_bf16 v[168:171], v[192:195], a[152:155], v[168:171]\n"
  "global_load_dwordx4 a[248:251], v253, s[52:53] offset:3072 sc1\n"
  "v_mfma_f32_16x16x32_bf16 v[172:175], v[192:195], a[156:159], v[172:175]\n"
  "ds_read_b64_tr_b16 v[192:193], v77 offset:40960\n"
  "v_mfma_f32_16x16x32_bf16 v[172:175], v[196:199], a[140:143], v[172:175]\n"
  "ds_read_b64_tr_b16 v[194:195], v77 offset:41472\n"
  "s_waitcnt vmcnt(31) lgkmcnt(4)\n"
  "v_mfma_f32_16x16x32_bf16 v[140:143], v[204:207], a[172:175], v[140:143]\n"
  "ds_read_b64_tr_b16 v[196:197], v77 offset:45056\n"
  "v_cvt_pk_bf16_f32 v240, v24, v25\n"
  "s_waitcnt vmcnt(27) lgkmcnt(3)\n"
  "v_mfma_f32_16x16x32_bf16 v[140:143], v[200:203], a[188:191], v[140:143]\n"
  "ds_read_b64_tr_b16 v[198:199], v77 offset:45568\n"
  "v_cvt_pk_bf16_f32 v241, v26, v27\n"
  "v_mfma_f32_16x16x32_bf16 v[136:139], v[200:203], a[184:187], v[136:139]\n"
  "global_load_dwordx4 a[252:255], v254, s[52:53] offset:3072 sc1\n"
  "s_lshl_b32 s98, s18, 1\n"
  "s_add_u32 s98, s98, 1\n"
  "v_mfma_f32_16x16x32_bf16 v[136:139], v[204:207], a[168:171], v[136:139]\n"
  "s_and_b32 s98, s98, 7\n"
  "ds_read_b128 v[92:95], v229 offset:5120\n"
  "v_cvt_pk_bf16_f32 v242, v28, v29\n"
  "v_mfma_f32_16x16x32_bf16 v[132:135], v[204:207], a[164:167], v[132:135]\n"
  "s_mul_i32 s98, s98, 524288\n"
  "v_cvt_pk_bf16_f32 v243, v30, v31\n"
  "v_mfma_f32_16x16x32_bf16 v[132:135], v[200:203], a[180:183], v[132:135]\n"
  "ds_write_b128 v229, v[240:243] offset:15360\n"
  "v_mfma_f32_16x16x32_bf16 v[128:131], v[200:203], a[176:179], v[128:131]\n"
  "s_add_u32 s60, s28, s98\n"
  "s_addc_u32 s61, s29, 0\n"
  "ds_read_b64_tr_b16 v[200:201], v78 offset:40960\n"
  "v_mfma_f32_16x16x32_bf16 v[128:131], v[204:207], a[160:163], v[128:131]\n"
  "ds_read_b64_tr_b16 v[202:203], v78 offset:41472\n"
  "s_waitcnt lgkmcnt(6)\n"
  "v_mfma_f32_16x16x32_bf16 v[144:147], v[192:195], a[160:163], v[144:147]\n"
  "s_add_u32 m0, s48, 16384\n"
  "ds_read_b64_tr_b16 v[204:205], v78 offset:45056\n"
  "s_waitcnt lgkmcnt(5)\n"
  "v_mfma_f32_16x16x32_bf16 v[144:147], v[196:199], a[176:179], v[144:147]\n"
  "ds_read_b64_tr_b16 v[206:207], v78 offset:45568\n"
  "v_mfma_f32_16x16x32_bf16 v[148:151], v[196:199], a[180:183], v[148:151]\n"
  "s_waitcnt lgkmcnt(5)\n"
  "global_store_dwordx4 v232, v[92:95], s[46:47] offset:-1024 sc1\n"
  "v_mfma_f32_16x16x32_bf16 v[148:151], v[192:195], a[164:167], v[148:151]\n"
  "global_load_lds_dwordx4 v220, s[60:61] offset:0\n"
  "v_mfma_f32_16x16x32_bf16 v[152:155], v[192:195], a[168:171], v[152:155]\n"
  "v_mfma_f32_16x16x32_bf16 v[152:155], v[196:199], a[184:187], v[152:155]\n"
  "global_load_lds_dwordx4 v221, s[60:61] offset:1024\n"
  "v_mfma_f32_16x16x32_bf16 v[156:159], v[196:199], a[188:191], v[156:159]\n"
  "s_waitcnt vmcnt(26)\n"
  "s_barrier\n"
  "ds_read_b64_tr_b16 v[196:197], v76 offset:49152\n"
  "v_mfma_f32_16x16x32_bf16 v[156:159], v[192:195], a[172:175], v[156:159]\n"
  "ds_read_b64_tr_b16 v[198:199], v76 offset:49664\n"
  "s_waitcnt lgkmcnt(4)\n"
  "v_mfma_f32_16x16x32_bf16 v[172:175], v[200:203], a[172:175], v[172:175]\n"
  "ds_read_b64_tr_b16 v[192:193], v76 offset:53248\n"
  "s_waitcnt lgkmcnt(3)\n"
  "v_mfma_f32_16x16x32_bf16 v[172:175], v[204:207], a[188:191], v[172:175]\n"
  "ds_read_b64_tr_b16 v[194:195], v76 offset:53760\n"
  "v_mfma_f32_16x16x32_bf16 v[168:171], v[204:207], a[184:187], v[168:171]\n"
  "global_load_lds_dwordx4 v222, s[60:61] offset:2048\n"
  "v_mfma_f32_16x16x32_bf16 v[168:171], v[200:203], a[168:171], v[168:171]\n"
  "global_load_lds_dwordx4 v223, s[60:61] offset:3072\n"
  "v_mfma_f32_16x16x32_bf16 v[164:167], v[200:203], a[164:167], v[164:167]\n"
  "v_mfma_f32_16x16x32_bf16 v[164:167], v[204:207], a[180:183], v[164:167]\n"
  "v_mfma_f32_16x16x32_bf16 v[160:163], v[204:207], a[176:179], v[160:163]\n"
  "ds_read_b64_tr_b16 v[204:205], v77 offset:49152\n"
  "v_mfma_f32_16x16x32_bf16 v[160:163], v[200:203], a[160:163], v[160:163]\n"
  "ds_read_b64_tr_b16 v[206:207], v77 offset:49664\n"
  "L_stg19_%=:\n"
  "s_waitcnt vmcnt(26) lgkmcnt(4)\n"
  "v_mfma_f32_16x16x32_bf16 v[128:131], v[196:199], a[192:195], v[128:131]\n"
  "ds_read_b64_tr_b16 v[200:201], v77 offset:53248\n"
  "s_waitcnt vmcnt(22) lgkmcnt(3)\n"
  "v_mfma_f32_16x16x32_bf16 v[128:131], v[192:195], a[208:211], v[128:131]\n"
  "s_barrier\n"
  "ds_read_b64_tr_b16 v[202:203], v77 offset:53760\n"
  "s_waitcnt vmcnt(21)\n"
  "v_mfma_f32_16x16x32_bf16 v[132:135], v[192:195], a[212:215], v[132:135]\n"
  "ds_read_b128 v[88:91], v229 offset:6144\n"
  "v_mfma_f32_16x16x32_bf16 v[132:135], v[196:199], a[196:199], v[132:135]\n"
  "s_lshl_b32 s98, s18, 1\n"
  "v_mfma_f32_16x16x32_bf16 v[136:139], v[196:199], a[200:203], v[136:139]\n"
  "s_add_u32 s98, s98, 2\n"
  "v_cvt_pk_bf16_f32 v236, v40, v41\n"
  "v_cvt_pk_bf16_f32 v237, v42, v43\n"
  "s_waitcnt vmcnt(20)\n"
  "v_mfma_f32_16x16x32_bf16 v[136:139], v[192:195], a[216:219], v[136:139]\n"
  "v_cvt_pk_bf16_f32 v238, v44, v45\n"
  "v_cvt_pk_bf16_f32 v239, v46, v47\n"
  "s_waitcnt vmcnt(19)\n"
  "v_mfma_f32_16x16x32_bf16 v[140:143], v[192:195], a[220:223], v[140:143]\n"
  "s_and_b32 s98, s98, 7\n"
  "ds_read_b64_tr_b16 v[192:193], v78 offset:49152\n"
  "v_mfma_f32_16x16x32_bf16 v[140:143], v[196:199], a[204:207], v[140:143]\n"
  "s_mul_i32 s98, s98, 524288\n"
  "ds_read_b64_tr_b16 v[194:195], v78 offset:49664\n"
  "s_waitcnt lgkmcnt(5)\n"
  "v_mfma_f32_16x16x32_bf16 v[156:159], v[204:207], a[204:207], v[156:159]\n"
  "ds_read_b64_tr_b16 v[196:197], v78 offset:53248\n"
  "s_waitcnt lgkmcnt(4)\n"
  "v_mfma_f32_16x16x32_bf16 v[156:159], v[200:203], a[220:223], v[156:159]\n"
  "s_add_u32 s60, s28, s98\n"
  "s_addc_u32 s61, s29, 0\n"
  "ds_read_b64_tr_b16 v[198:199], v78 offset:53760\n"
  "v_mfma_f32_16x16x32_bf16 v[152:155], v[200:203], a[216:219], v[152:155]\n"
  "s_add_u32 m0, s48, 32768\n"
  "ds_write_b128 v229, v[236:239] offset:8192\n"
  "v_mfma_f32_16x16x32_bf16 v[152:155], v[204:207], a[200:203], v[152:155]\n"
  "s_waitcnt lgkmcnt(5)\n"
  "global_store_dwordx4 v232, v[88:91], s[42:43] offset:0 sc1\n"
  "v_mfma_f32_16x16x32_bf16 v[148:151], v[204:207], a[196:199], v[148:151]\n"
  "global_load_lds_dwordx4 v220, s[60:61] offset:0\n"
  "v_mfma_f32_16x16x32_bf16 v[148:151], v[200:203], a[212:215], v[148:151]\n"
  "global_load_lds_dwordx4 v221, s[60:61] offset:1024\n"
  "v_mfma_f32_16x16x32_bf16 v[144:147], v[200:203], a[208:211], v[144:147]\n"
  "ds_read_b64_tr_b16 v[200:201], v76 offset:57344\n"
  "v_mfma_f32_16x16x32_bf16 v[144:147], v[204:207], a[192:195], v[144:147]\n"
  "ds_read_b64_tr_b16 v[202:203], v76 offset:57856\n"
  "s_waitcnt lgkmcnt(5)\n"
  "v_mfma_f32_16x16x32_bf16 v[160:163], v[192:195], a[192:195], v[160:163]\n"
  "ds_read_b64_tr_b16 v[204:205], v76 offset:61440\n"
  "s_waitcnt lgkmcnt(4)\n"
  "v_mfma_f32_16x16x32_bf16 v[160:163], v[196:199], a[208:211], v[160:163]\n"
  "ds_read_b64_tr_b16 v[206:207], v76 offset:61952\n"
  "v_mfma_f32_16x16x32_bf16 v[164:167], v[196:199], a[212:215], v[164:167]\n"
  "ds_read_b128 v[92:95], v229 offset:7168\n"
  "v_mfma_f32_16x16x32_bf16 v[164:167], v[192:195], a[196:199], v[164:167]\n"
  "global_load_lds_dwordx4 v222, s[60:61] offset:2048\n"
  "v_mfma_f32_16x16x32_bf16 v[168:171], v[192:195], a[200:203], v[168:171]\n"
  "global_load_lds_dwordx4 v223, s[60:61] offset:3072\n"
  "s_mul_i32 s60, s56, 11\n"
  "s_min_u32 s60, s60, 32\n"
  "v_mfma_f32_16x16x32_bf16 v[168:171], v[196:199], a[216:219], v[168:171]\n"
  "s_lshl_b32 s37, s57, 2\n"
  "s_waitcnt lgkmcnt(0)\n"
  "global_store_dwordx4 v232, v[92:95], s[42:43] offset:1024 sc1\n"
  "v_mfma_f32_16x16x32_bf16 v[172:175], v[196:199], a[220:223], v[172:175]\n"
  "s_add_u32 s37, s37, s35\n"
  "ds_read_b64_tr_b16 v[196:197], v77 offset:57344\n"
  "v_mfma_f32_16x16x32_bf16 v[172:175], v[192:195], a[204:207], v[172:175]\n"
  "ds_read_b64_tr_b16 v[198:199], v77 offset:57856\n"
  "s_waitcnt vmcnt(16)\n"
  "v_mfma_f32_16x16x32_bf16 v[140:143], v[200:203], a[236:239], v[140:143]\n"
  "s_mul_i32 s37, s37, 342\n"
  "ds_read_b64_tr_b16 v[192:193], v77 offset:61440\n"
  "v_cvt_pk_bf16_f32 v240, v56, v57\n"
  "s_waitcnt vmcnt(11)\n"
  "v_mfma_f32_16x16x32_bf16 v[140:143], v[204:207], a[252:255], v[140:143]\n"
  "s_add_u32 s37, s37, s60\n"
  "ds_read_b64_tr_b16 v[194:195], v77 offset:61952\n"
  "v_cvt_pk_bf16_f32 v241, v58, v59\n"
  "v_mfma_f32_16x16x32_bf16 v[136:139], v[204:207], a[248:251], v[136:139]\n"
  "v_cvt_pk_bf16_f32 v242, v60, v61\n"
  "v_cvt_pk_bf16_f32 v243, v62, v63\n"
  "v_mfma_f32_16x16x32_bf16 v[136:139], v[200:203], a[232:235], v[136:139]\n"
  "s_add_u32 s37, s37, 128\n"
  "ds_write_b128 v229, v[240:243] offset:9216\n"
  "v_mfma_f32_16x16x32_bf16 v[132:135], v[200:203], a[228:231], v[132:135]\n"
  "s_lshl_b32 s37, s37, 10\n"
  "ds_read_b128 v[88:91], v229 offset:12288\n"
  "v_mfma_f32_16x16x32_bf16 v[132:135], v[204:207], a[244:247], v[132:135]\n"
  "v_mfma_f32_16x16x32_bf16 v[128:131], v[204:207], a[240:243], v[128:131]\n"
  "s_add_u32 s37, s37, 35651584\n"
  "ds_read_b64_tr_b16 v[204:205], v78 offset:57344\n"
  "v_mfma_f32_16x16x32_bf16 v[128:131], v[200:203], a[224:227], v[128:131]\n"
  "s_add_u32 s50, s84, s37\n"
  "s_addc_u32 s51, s85, 0\n"
  "ds_read_b64_tr_b16 v[206:207], v78 offset:57856\n"
  "s_waitcnt lgkmcnt(6)\n"
  "v_mfma_f32_16x16x32_bf16 v[144:147], v[196:199], a[224:227], v[144:147]\n"
  "ds_read_b64_tr_b16 v[200:201], v78 offset:61440\n"
  "s_waitcnt lgkmcnt(5)\n"
  "v_mfma_f32_16x16x32_bf16 v[144:147], v[192:195], a[240:243], v[144:147]\n"
  "s_lshl_b32 s60, s60, 15\n"
  "ds_read_b64_tr_b16 v[202:203], v78 offset:61952\n"
  "v_mfma_f32_16x16x32_bf16 v[148:151], v[192:195], a[244:247], v[148:151]\n"
  "s_lshl_b32 s37, s35, 13\n"
  "s_waitcnt lgkmcnt(4)\n"
  "global_store_dwordx4 v232, v[88:91], s[42:43] offset:2048 sc1\n"
  "v_mfma_f32_16x16x32_bf16 v[148:151], v[196:199], a[228:231], v[148:151]\n"
  "v_mfma_f32_16x16x32_bf16 v[152:155], v[196:199], a[232:235], v[152:155]\n"
  "s_add_u32 s60, s60, s37\n"
  "v_mfma_f32_16x16x32_bf16 v[152:155], v[192:195], a[248:251], v[152:155]\n"
  "s_lshl_b32 s37, s58, 8\n"
  "v_mfma_f32_16x16x32_bf16 v[156:159], v[192:195], a[252:255], v[156:159]\n"
  "s_barrier\n"
  "ds_read_b64_tr_b16 v[192:193], v208 offset:0\n"
  "v_mfma_f32_16x16x32_bf16 v[156:159], v[196:199], a[236:239], v[156:159]\n"
  "s_add_u32 s60, s60, s37\n"
  "ds_read_b64_tr_b16 v[194:195], v208 offset:1024\n"
  "s_waitcnt lgkmcnt(4)\n"
  "v_mfma_f32_16x16x32_bf16 v[172:175], v[204:207], a[236:239], v[172:175]\n"
  "s_add_u32 s52, s80, s60\n"
  "s_addc_u32 s53, s81, 0\n"
  "ds_read_b64_tr_b16 v[196:197], v208 offset:8192\n"
  "s_waitcnt lgkmcnt(3)\n"
  "v_mfma_f32_16x16x32_bf16 v[172:175], v[200:203], a[252:255], v[172:175]\n"
  "ds_read_b64_tr_b16 v[198:199], v208 offset:9216\n"
  "v_mfma_f32_16x16x32_bf16 v[168:171], v[200:203], a[248:251], v[168:171]\n"
  "s_sub_u32 s52, s52, 4096\n"
  "s_subb_u32 s53, s53, 0\n"
  "v_mfma_f32_16x16x32_bf16 v[168:171], v[204:207], a[232:235], v[168:171]\n"
  "v_mfma_f32_16x16x32_bf16 v[164:167], v[204:207], a[228:231], v[164:167]\n"
  "v_mfma_f32_16x16x32_bf16 v[164:167], v[200:203], a[244:247], v[164:167]\n"
  "v_mfma_f32_16x16x32_bf16 v[160:163], v[200:203], a[240:243], v[160:163]\n"
  "ds_read_b64_tr_b16 v[200:201], v209 offset:0\n"
  "v_mfma_f32_16x16x32_bf16 v[160:163], v[204:207], a[224:227], v[160:163]\n"
  "ds_read_b64_tr_b16 v[202:203], v209 offset:1024\n"
  "L_stg20_%=:\n"
  "s_waitcnt lgkmcnt(4)\n"
  "v_mfma_f32_16x16x32_bf16 v[0:3], v[192:195], a[0:3], 0\n"
  "ds_read_b64_tr_b16 v[204:205], v209 offset:8192\n"
  "s_waitcnt lgkmcnt(3)\n"
  "v_mfma_f32_16x16x32_bf16 v[0:3], v[196:199], a[16:19], v[0:3]\n"
  "s_barrier\n"
  "ds_read_b64_tr_b16 v[206:207], v209 offset:9216\n"
  "v_mfma_f32_16x16x32_bf16 v[4:7], v[196:199], a[20:23], 0\n"
  "ds_read_b128 v[248:251], v229 offset:13312\n"
  "v_mfma_f32_16x16x32_bf16 v[4:7], v[192:195], a[4:7], v[4:7]\n"
  "s_lshl_b32 s98, s18, 1\n"
  "s_mul_i32 s36, s54, 175104\n"
  "s_add_u32 s36, s36, 35651584\n"
  "v_mfma_f32_16x16x32_bf16 v[8:11], v[192:195], a[8:11], 0\n"
  "s_add_u32 s40, s84, s36\n"
  "s_addc_u32 s41, s85, 0\n"
  "v_mfma_f32_16x16x32_bf16 v[8:11], v[196:199], a[24:27], v[8:11]\n"
  "s_add_u32 s42, s40, 175104\n"
  "s_addc_u32 s43, s41, 0\n"
  "v_mfma_f32_16x16x32_bf16 v[12:15], v[196:199], a[28:31], 0\n"
  "s_add_u32 s44, s42, 175104\n"
  "s_addc_u32 s45, s43, 0\n"
  "ds_read_b64_tr_b16 v[196:197], v210 offset:0\n"
  "v_mfma_f32_16x16x32_bf16 v[12:15], v[192:195], a[12:15], v[12:15]\n"
  "s_add_u32 s46, s44, 175104\n"
  "s_addc_u32 s47, s45, 0\n"
  "ds_read_b64_tr_b16 v[198:199], v210 offset:1024\n"
  "s_waitcnt lgkmcnt(5)\n"
  "v_mfma_f32_16x16x32_bf16 v[28:31], v[200:203], a[12:15], 0\n"
  "s_add_u32 s36, s55, 0\n"
  "s_add_u32 s98, s98, 3\n"
  "ds_read_b64_tr_b16 v[192:193], v210 offset:8192\n"
  "s_waitcnt lgkmcnt(4)\n"
  "v_mfma_f32_16x16x32_bf16 v[28:31], v[204:207], a[28:31], v[28:31]\n"
  "s_and_b32 s37, s36, 1\n"
  "s_lshl_b32 s37, s37, 3\n"
  "ds_read_b64_tr_b16 v[194:195], v210 offset:9216\n"
  "v_mfma_f32_16x16x32_bf16 v[24:27], v[204:207], a[24:27], 0\n"
  "s_lshr_b32 s36, s36, 1\n"
  "s_lshl_b32 s36, s36, 10\n"
  "s_waitcnt lgkmcnt(4)\n"
  "global_store_dwordx4 v232, v[248:251], s[20:21] offset:3072 sc1\n"
  "v_mfma_f32_16x16x32_bf16 v[24:27], v[200:203], a[8:11], v[24:27]\n"
  "s_add_u32 s36, s36, s37\n"
  "s_add_u32 s36, s36, 131072\n"
  "v_mfma_f32_16x16x32_bf16 v[20:23], v[200:203], a[4:7], 0\n"
  "v_add_u32 v236, s36, v232\n"
  "v_mfma_f32_16x16x32_bf16 v[20:23], v[204:207], a[20:23], v[20:23]\n"
  "s_add_u32 s36, s55, 1\n"
  "s_and_b32 s37, s36, 1\n"
  "v_mfma_f32_16x16x32_bf16 v[16:19], v[204:207], a[16:19], 0\n"
  "s_and_b32 s98, s98, 7\n"
  "s_lshl_b32 s37, s37, 3\n"
  "ds_read_b64_tr_b16 v[204:205], v211 offset:0\n"
  "v_mfma_f32_16x16x32_bf16 v[16:19], v[200:203], a[0:3], v[16:19]\n"
  "s_lshr_b32 s36, s36, 1\n"
  "s_lshl_b32 s36, s36, 10\n"
  "ds_read_b64_tr_b16 v[206:207], v211 offset:1024\n"
  "s_waitcnt lgkmcnt(4)\n"
  "v_mfma_f32_16x16x32_bf16 v[32:35], v[196:199], a[0:3], 0\n"
  "s_add_u32 s36, s36, s37\n"
  "ds_read_b64_tr_b16 v[200:201], v211 offset:8192\n"
  "s_waitcnt lgkmcnt(3)\n"
  "v_mfma_f32_16x16x32_bf16 v[32:35], v[192:195], a[16:19], v[32:35]\n"
  "s_add_u32 s36, s36, 131072\n"
  "ds_read_b64_tr_b16 v[202:203], v211 offset:9216\n"
  "v_mfma_f32_16x16x32_bf16 v[36:39], v[192:195], a[20:23], 0\n"
  "v_add_u32 v237, s36, v232\n"
  "v_mfma_f32_16x16x32_bf16 v[36:39], v[196:199], a[4:7], v[36:39]\n"
  "s_add_u32 s36, s55, 2\n"
  "s_and_b32 s37, s36, 1\n"
  "ds_read_b128 v[244:247], v229 offset:14336\n"
  "v_mfma_f32_16x16x32_bf16 v[40:43], v[196:199], a[8:11], 0\n"
  "s_mul_i32 s98, s98, 524288\n"
  "s_lshl_b32 s37, s37, 3\n"
  "v_mfma_f32_16x16x32_bf16 v[40:43], v[192:195], a[24:27], v[40:43]\n"
  "s_lshr_b32 s36, s36, 1\n"
  "s_lshl_b32 s36, s36, 10\n"
  "v_mfma_f32_16x16x32_bf16 v[44:47], v[192:195], a[28:31], 0\n"
  "s_add_u32 s36, s36, s37\n"
  "ds_read_b64_tr_b16 v[192:193], v212 offset:0\n"
  "v_mfma_f32_16x16x32_bf16 v[44:47], v[196:199], a[12:15], v[44:47]\n"
  "s_add_u32 s36, s36, 131072\n"
  "ds_read_b64_tr_b16 v[194:195], v212 offset:1024\n"
  "v_add_u32 v238, s36, v232\n"
  "s_waitcnt lgkmcnt(5)\n"
  "v_mfma_f32_16x16x32_bf16 v[60:63], v[204:207], a[12:15], 0\n"
  "ds_read_b64_tr_b16 v[196:197], v212 offset:8192\n"
  "v_mul_f32 v176, 0x3fb8aa3b, v128\n"
  "s_waitcnt lgkmcnt(4)\n"
  "v_mfma_f32_16x16x32_bf16 v[60:63], v[200:203], a[28:31], v[60:63]\n"
  "s_add_u32 s60, s28, s98\n"
  "s_addc_u32 s61, s29, 0\n"
  "ds_read_b64_tr_b16 v[198:199], v212 offset:9216\n"
  "v_mfma_f32_16x16x32_bf16 v[56:59], v[200:203], a[24:27], 0\n"
  "v_mul_f32 v177, 0x3fb8aa3b, v129\n"
  "v_mul_f32 v178, 0x3fb8aa3b, v130\n"
  "v_mfma_f32_16x16x32_bf16 v[56:59], v[204:207], a[8:11], v[56:59]\n"
  "v_mul_f32 v179, 0x3fb8aa3b, v131\n"
  "v_mfma_f32_16x16x32_bf16 v[52:55], v[204:207], a[4:7], 0\n"
  "s_waitcnt lgkmcnt(4)\n"
  "global_store_dwordx4 v232, v[244:247], s[22:23] offset:0 sc1\n"
  "v_mfma_f32_16x16x32_bf16 v[52:55], v[200:203], a[20:23], v[52:55]\n"
  "v_exp_f32 v176, v176\n"
  "v_mfma_f32_16x16x32_bf16 v[48:51], v[200:203], a[16:19], 0\n"
  "ds_read_b64_tr_b16 v[200:201], v213 offset:0\n"
  "v_mfma_f32_16x16x32_bf16 v[48:51], v[204:207], a[0:3], v[48:51]\n"
  "s_add_u32 m0, s48, 49152\n"
  "ds_read_b64_tr_b16 v[202:203], v213 offset:1024\n"
  "s_waitcnt lgkmcnt(4)\n"
  "v_mfma_f32_16x16x32_bf16 v[64:67], v[192:195], a[0:3], 0\n"
  "ds_read_b64_tr_b16 v[204:205], v213 offset:8192\n"
  "s_waitcnt lgkmcnt(3)\n"
  "v_mfma_f32_16x16x32_bf16 v[64:67], v[196:199], a[16:19], v[64:67]\n"
  "ds_read_b64_tr_b16 v[206:207], v213 offset:9216\n"
  "v_mfma_f32_16x16x32_bf16 v[68:71], v[196:199], a[20:23], 0\n"
  "v_exp_f32 v177, v177\n"
  "v_mfma_f32_16x16x32_bf16 v[68:71], v[192:195], a[4:7], v[68:71]\n"
  "v_exp_f32 v178, v178\n"
  "v_mfma_f32_16x16x32_bf16 v[72:75], v[192:195], a[8:11], 0\n"
  "v_exp_f32 v179, v179\n"
  "v_mfma_f32_16x16x32_bf16 v[72:75], v[196:199], a[24:27], v[72:75]\n"
  "v_add_f32 v180, 2.0, v176\n"
  "v_add_f32 v181, 2.0, v177\n"
  "v_mfma_f32_16x16x32_bf16 v[76:79], v[196:199], a[28:31], 0\n"
  "ds_read_b64_tr_b16 v[196:197], v214 offset:0\n"
  "v_add_f32 v182, 2.0, v178\n"
  "v_mfma_f32_16x16x32_bf16 v[76:79], v[192:195], a[12:15], v[76:79]\n"
  "ds_read_b64_tr_b16 v[198:199], v214 offset:1024\n"
  "v_add_f32 v183, 2.0, v179\n"
  "s_waitcnt lgkmcnt(4)\n"
  "v_mfma_f32_16x16x32_bf16 v[92:95], v[200:203], a[12:15], 0\n"
  "ds_read_b64_tr_b16 v[192:193], v214 offset:8192\n"
  "v_fma_f32 v180, v176, v180, 2.0\n"
  "s_waitcnt lgkmcnt(3)\n"
  "v_mfma_f32_16x16x32_bf16 v[92:95], v[204:207], a[28:31], v[92:95]\n"
  "ds_read_b64_tr_b16 v[194:195], v214 offset:9216\n"
  "v_fma_f32 v181, v177, v181, 2.0\n"
  "v_mfma_f32_16x16x32_bf16 v[88:91], v[204:207], a[24:27], 0\n"
  "v_fma_f32 v182, v178, v182, 2.0\n"
  "v_fma_f32 v183, v179, v183, 2.0\n"
  "v_mfma_f32_16x16x32_bf16 v[88:91], v[200:203], a[8:11], v[88:91]\n"
  "v_rcp_f32 v180, v180\n"
  "v_mfma_f32_16x16x32_bf16 v[84:87], v[200:203], a[4:7], 0\n"
  "v_rcp_f32 v181, v181\n"
  "v_mfma_f32_16x16x32_bf16 v[84:87], v[204:207], a[20:23], v[84:87]\n"
  "global_load_lds_dwordx4 v220, s[60:61] offset:0\n"
  "v_mfma_f32_16x16x32_bf16 v[80:83], v[204:207], a[16:19], 0\n"
  "ds_read_b64_tr_b16 v[204:205], v215 offset:0\n"
  "v_mfma_f32_16x16x32_bf16 v[80:83], v[200:203], a[0:3], v[80:83]\n"
  "ds_read_b64_tr_b16 v[206:207], v215 offset:1024\n"
  "s_waitcnt lgkmcnt(4)\n"
  "v_mfma_f32_16x16x32_bf16 v[96:99], v[196:199], a[0:3], 0\n"
  "ds_read_b64_tr_b16 v[200:201], v215 offset:8192\n"
  "s_waitcnt lgkmcnt(3)\n"
  "v_mfma_f32_16x16x32_bf16 v[96:99], v[192:195], a[16:19], v[96:99]\n"
  "ds_read_b64_tr_b16 v[202:203], v215 offset:9216\n"
  "v_mfma_f32_16x16x32_bf16 v[100:103], v[192:195], a[20:23], 0\n"
  "ds_read_b128 v[248:251], v229 offset:15360\n"
  "v_mfma_f32_16x16x32_bf16 v[100:103], v[196:199], a[4:7], v[100:103]\n"
  "v_rcp_f32 v182, v182\n"
  "v_mfma_f32_16x16x32_bf16 v[104:107], v[196:199], a[8:11], 0\n"
  "v_rcp_f32 v183, v183\n"
  "v_mfma_f32_16x16x32_bf16 v[104:107], v[192:195], a[24:27], v[104:107]\n"
  "v_fma_f32 v180, -2.0, v180, 1.0\n"
  "v_fma_f32 v181, -2.0, v181, 1.0\n"
  "v_mfma_f32_16x16x32_bf16 v[108:111], v[192:195], a[28:31], 0\n"
  "s_waitcnt vmcnt(10)\n"
  "s_barrier\n"
  "ds_read_b64_tr_b16 v[192:193], v208 offset:16384\n"
  "v_mfma_f32_16x16x32_bf16 v[108:111], v[196:199], a[12:15], v[108:111]\n"
  "ds_read_b64_tr_b16 v[194:195], v208 offset:17408\n"
  "v_fma_f32 v182, -2.0, v182, 1.0\n"
  "s_waitcnt lgkmcnt(5)\n"
  "v_mfma_f32_16x16x32_bf16 v[124:127], v[204:207], a[12:15], 0\n"
  "ds_read_b64_tr_b16 v[196:197], v208 offset:24576\n"
  "v_fma_f32 v183, -2.0, v183, 1.0\n"
  "s_waitcnt lgkmcnt(4)\n"
  "v_mfma_f32_16x16x32_bf16 v[124:127], v[200:203], a[28:31], v[124:127]\n"
  "ds_read_b64_tr_b16 v[198:199], v208 offset:25600\n"
  "v_mul_f32 v128, v128, v180\n"
  "v_mfma_f32_16x16x32_bf16 v[120:123], v[200:203], a[24:27], 0\n"
  "global_load_lds_dwordx4 v221, s[60:61] offset:1024\n"
  "v_mul_f32 v129, v129, v181\n"
  "v_mfma_f32_16x16x32_bf16 v[120:123], v[204:207], a[8:11], v[120:123]\n"
  "v_mul_f32 v130, v130, v182\n"
  "v_mul_f32 v131, v131, v183\n"
  "v_mfma_f32_16x16x32_bf16 v[116:119], v[204:207], a[4:7], 0\n"
  "v_cvt_pk_bf16_f32 v240, v128, v129\n"
  "v_cvt_pk_bf16_f32 v241, v130, v131\n"
  "v_mfma_f32_16x16x32_bf16 v[116:119], v[200:203], a[20:23], v[116:119]\n"
  "s_waitcnt lgkmcnt(4)\n"
  "global_store_dwordx4 v232, v[248:251], s[22:23] offset:1024 sc1\n"
  "v_mfma_f32_16x16x32_bf16 v[112:115], v[200:203], a[16:19], 0\n"
  "ds_read_b64_tr_b16 v[200:201], v209 offset:16384\n"
  "v_mfma_f32_16x16x32_bf16 v[112:115], v[204:207], a[0:3], v[112:115]\n"
  "ds_read_b64_tr_b16 v[202:203], v209 offset:17408\n"
  "global_store_dwordx2 v236, v[240:241], s[40:41] sc1\n"
  "global_load_lds_dwordx4 v222, s[60:61] offset:2048\n"
  "global_load_lds_dwordx4 v223, s[60:61] offset:3072\n"
  "L_stg21_%=:\n"
  "s_waitcnt lgkmcnt(4)\n"
  "v_mfma_f32_16x16x32_bf16 v[0:3], v[192:195], a[32:35], v[0:3]\n"
  "ds_read_b64_tr_b16 v[204:205], v209 offset:24576\n"
  "v_mul_f32 v184, 0x3fb8aa3b, v132\n"
  "s_waitcnt lgkmcnt(3)\n"
  "v_mfma_f32_16x16x32_bf16 v[0:3], v[196:199], a[48:51], v[0:3]\n"
  "s_barrier\n"
  "ds_read_b64_tr_b16 v[206:207], v209 offset:25600\n"
  "v_mul_f32 v185, 0x3fb8aa3b, v133\n"
  "v_mfma_f32_16x16x32_bf16 v[4:7], v[196:199], a[52:55], v[4:7]\n"
  "ds_read_b128 v[244:247], v229 offset:8192\n"
  "v_mul_f32 v186, 0x3fb8aa3b, v134\n"
  "v_mfma_f32_16x16x32_bf16 v[4:7], v[192:195], a[36:39], v[4:7]\n"
  "s_lshl_b32 s98, s18, 1\n"
  "v_mul_f32 v187, 0x3fb8aa3b, v135\n"
  "v_mfma_f32_16x16x32_bf16 v[8:11], v[192:195], a[40:43], v[8:11]\n"
  "v_exp_f32 v184, v184\n"
  "v_mfma_f32_16x16x32_bf16 v[8:11], v[196:199], a[56:59], v[8:11]\n"
  "v_exp_f32 v185, v185\n"
  "v_mfma_f32_16x16x32_bf16 v[12:15], v[196:199], a[60:63], v[12:15]\n"
  "ds_read_b64_tr_b16 v[196:197], v210 offset:16384\n"
  "v_mfma_f32_16x16x32_bf16 v[12:15], v[192:195], a[44:47], v[12:15]\n"
  "ds_read_b64_tr_b16 v[198:199], v210 offset:17408\n"
  "s_waitcnt lgkmcnt(5)\n"
  "v_mfma_f32_16x16x32_bf16 v[28:31], v[200:203], a[44:47], v[28:31]\n"
  "s_add_u32 s98, s98, 4\n"
  "ds_read_b64_tr_b16 v[192:193], v210 offset:24576\n"
  "s_waitcnt lgkmcnt(4)\n"
  "v_mfma_f32_16x16x32_bf16 v[28:31], v[204:207], a[60:63], v[28:31]\n"
  "ds_read_b64_tr_b16 v[194:195], v210 offset:25600\n"
  "v_mfma_f32_16x16x32_bf16 v[24:27], v[204:207], a[56:59], v[24:27]\n"
  "v_exp_f32 v186, v186\n"
  "v_mfma_f32_16x16x32_bf16 v[24:27], v[200:203], a[40:43], v[24:27]\n"
  "v_exp_f32 v187, v187\n"
  "v_mfma_f32_16x16x32_bf16 v[20:23], v[200:203], a[36:39], v[20:23]\n"
  "v_add_f32 v188, 2.0, v184\n"
  "v_add_f32 v189, 2.0, v185\n"
  "v_mfma_f32_16x16x32_bf16 v[20:23], v[204:207], a[52:55], v[20:23]\n"
  "v_add_f32 v190, 2.0, v186\n"
  "v_add_f32 v191, 2.0, v187\n"
  "v_mfma_f32_16x16x32_bf16 v[16:19], v[204:207], a[48:51], v[16:19]\n"
  "s_and_b32 s98, s98, 7\n"
  "ds_read_b64_tr_b16 v[204:205], v211 offset:16384\n"
  "v_fma_f32 v188, v184, v188, 2.0\n"
  "v_mfma_f32_16x16x32_bf16 v[16:19], v[200:203], a[32:35], v[16:19]\n"
  "ds_read_b64_tr_b16 v[206:207], v211 offset:17408\n"
  "v_fma_f32 v189, v185, v189, 2.0\n"
  "s_waitcnt lgkmcnt(4)\n"
  "v_mfma_f32_16x16x32_bf16 v[32:35], v[196:199], a[32:35], v[32:35]\n"
  "ds_read_b64_tr_b16 v[200:201], v211 offset:24576\n"
  "v_fma_f32 v190, v186, v190, 2.0\n"
  "s_waitcnt lgkmcnt(3)\n"
  "v_mfma_f32_16x16x32_bf16 v[32:35], v[192:195], a[48:51], v[32:35]\n"
  "ds_read_b64_tr_b16 v[202:203], v211 offset:25600\n"
  "v_fma_f32 v191, v187, v191, 2.0\n"
  "v_mfma_f32_16x16x32_bf16 v[36:39], v[192:195], a[52:55], v[36:39]\n"
  "global_store_dwordx4 v232, v[244:247], s[22:23] offset:2048 sc1\n"
  "v_mfma_f32_16x16x32_bf16 v[36:39], v[196:199], a[36:39], v[36:39]\n"
  "v_rcp_f32 v188, v188\n"
  "v_mfma_f32_16x16x32_bf16 v[40:43], v[196:199], a[40:43], v[40:43]\n"
  "s_mul_i32 s98, s98, 524288\n"
  "v_rcp_f32 v189, v189\n"
  "v_mfma_f32_16x16x32_bf16 v[40:43], v[192:195], a[56:59], v[40:43]\n"
  "v_rcp_f32 v190, v190\n"
  "v_mfma_f32_16x16x32_bf16 v[44:47], v[192:195], a[60:63], v[44:47]\n"
  "ds_read_b64_tr_b16 v[192:193], v212 offset:16384\n"
  "v_mfma_f32_16x16x32_bf16 v[44:47], v[196:199], a[44:47], v[44:47]\n"
  "ds_read_b64_tr_b16 v[194:195], v212 offset:17408\n"
  "s_waitcnt lgkmcnt(4)\n"
  "v_mfma_f32_16x16x32_bf16 v[60:63], v[204:207], a[44:47], v[60:63]\n"
  "ds_read_b64_tr_b16 v[196:197], v212 offset:24576\n"
  "s_waitcnt lgkmcnt(3)\n"
  "v_mfma_f32_16x16x32_bf16 v[60:63], v[200:203], a[60:63], v[60:63]\n"
  "s_add_u32 s60, s28, s98\n"
  "s_addc_u32 s61, s29, 0\n"
  "ds_read_b64_tr_b16 v[198:199], v212 offset:25600\n"
  "v_mfma_f32_16x16x32_bf16 v[56:59], v[200:203], a[56:59], v[56:59]\n"
  "v_rcp_f32 v191, v191\n"
  "v_mfma_f32_16x16x32_bf16 v[56:59], v[204:207], a[40:43], v[56:59]\n"
  "v_fma_f32 v188, -2.0, v188, 1.0\n"
  "v_fma_f32 v189, -2.0, v189, 1.0\n"
  "v_mfma_f32_16x16x32_bf16 v[52:55], v[204:207], a[36:39], v[52:55]\n"
  "v_fma_f32 v190, -2.0, v190, 1.0\n"
  "v_fma_f32 v191, -2.0, v191, 1.0\n"
  "v_mfma_f32_16x16x32_bf16 v[52:55], v[200:203], a[52:55], v[52:55]\n"
  "v_mul_f32 v132, v132, v188\n"
  "v_mul_f32 v133, v133, v189\n"
  "v_mfma_f32_16x16x32_bf16 v[48:51], v[200:203], a[48:51], v[48:51]\n"
  "ds_read_b64_tr_b16 v[200:201], v213 offset:16384\n"
  "v_mul_f32 v134, v134, v190\n"
  "v_mfma_f32_16x16x32_bf16 v[48:51], v[204:207], a[32:35], v[48:51]\n"
  "s_add_u32 m0, s48, 0\n"
  "ds_read_b64_tr_b16 v[202:203], v213 offset:17408\n"
  "v_mul_f32 v135, v135, v191\n"
  "s_waitcnt lgkmcnt(4)\n"
  "v_mfma_f32_16x16x32_bf16 v[64:67], v[192:195], a[32:35], v[64:67]\n"
  "ds_read_b64_tr_b16 v[204:205], v213 offset:24576\n"
  "v_cvt_pk_bf16_f32 v242, v132, v133\n"
  "s_waitcnt lgkmcnt(3)\n"
  "v_mfma_f32_16x16x32_bf16 v[64:67], v[196:199], a[48:51], v[64:67]\n"
  "ds_read_b64_tr_b16 v[206:207], v213 offset:25600\n"
  "v_cvt_pk_bf16_f32 v243, v134, v135\n"
  "v_mfma_f32_16x16x32_bf16 v[68:71], v[196:199], a[52:55], v[68:71]\n"
  "global_store_dwordx2 v236, v[242:243], s[42:43] sc1\n"
  "v_mfma_f32_16x16x32_bf16 v[68:71], v[192:195], a[36:39], v[68:71]\n"
  "v_mul_f32 v176, 0x3fb8aa3b, v136\n"
  "v_mul_f32 v177, 0x3fb8aa3b, v137\n"
  "v_mfma_f32_16x16x32_bf16 v[72:75], v[192:195], a[40:43], v[72:75]\n"
  "v_mul_f32 v178, 0x3fb8aa3b, v138\n"
  "v_mul_f32 v179, 0x3fb8aa3b, v139\n"
  "v_mfma_f32_16x16x32_bf16 v[72:75], v[196:199], a[56:59], v[72:75]\n"
  "v_exp_f32 v176, v176\n"
  "v_mfma_f32_16x16x32_bf16 v[76:79], v[196:199], a[60:63], v[76:79]\n"
  "ds_read_b64_tr_b16 v[196:197], v214 offset:16384\n"
  "v_mfma_f32_16x16x32_bf16 v[76:79], v[192:195], a[44:47], v[76:79]\n"
  "ds_read_b64_tr_b16 v[198:199], v214 offset:17408\n"
  "s_waitcnt lgkmcnt(4)\n"
  "v_mfma_f32_16x16x32_bf16 v[92:95], v[200:203], a[44:47], v[92:95]\n"
  "ds_read_b64_tr_b16 v[192:193], v214 offset:24576\n"
  "s_waitcnt lgkmcnt(3)\n"
  "v_mfma_f32_16x16x32_bf16 v[92:95], v[204:207], a[60:63], v[92:95]\n"
  "ds_read_b64_tr_b16 v[194:195], v214 offset:25600\n"
  "v_mfma_f32_16x16x32_bf16 v[88:91], v[204:207], a[56:59], v[88:91]\n"
  "v_exp_f32 v177, v177\n"
  "v_mfma_f32_16x16x32_bf16 v[88:91], v[200:203], a[40:43], v[88:91]\n"
  "v_exp_f32 v178, v178\n"
  "v_mfma_f32_16x16x32_bf16 v[84:87], v[200:203], a[36:39], v[84:87]\n"
  "ds_read_b128 v[248:251], v229 offset:9216\n"
  "v_mfma_f32_16x16x32_bf16 v[84:87], v[204:207], a[52:55], v[84:87]\n"
  "v_exp_f32 v179, v179\n"
  "v_mfma_f32_16x16x32_bf16 v[80:83], v[204:207], a[48:51], v[80:83]\n"
  "ds_read_b64_tr_b16 v[204:205], v215 offset:16384\n"
  "v_add_f32 v180, 2.0, v176\n"
  "v_mfma_f32_16x16x32_bf16 v[80:83], v[200:203], a[32:35], v[80:83]\n"
  "ds_read_b64_tr_b16 v[206:207], v215 offset:17408\n"
  "v_add_f32 v181, 2.0, v177\n"
  "s_waitcnt lgkmcnt(5)\n"
  "v_mfma_f32_16x16x32_bf16 v[96:99], v[196:199], a[32:35], v[96:99]\n"
  "ds_read_b64_tr_b16 v[200:201], v215 offset:24576\n"
  "v_add_f32 v182, 2.0, v178\n"
  "s_waitcnt lgkmcnt(4)\n"
  "v_mfma_f32_16x16x32_bf16 v[96:99], v[192:195], a[48:51], v[96:99]\n"
  "ds_read_b64_tr_b16 v[202:203], v215 offset:25600\n"
  "v_add_f32 v183, 2.0, v179\n"
  "v_mfma_f32_16x16x32_bf16 v[100:103], v[192:195], a[52:55], v[100:103]\n"
  "v_fma_f32 v180, v176, v180, 2.0\n"
  "v_fma_f32 v181, v177, v181, 2.0\n"
  "v_mfma_f32_16x16x32_bf16 v[100:103], v[196:199], a[36:39], v[100:103]\n"
  "v_fma_f32 v182, v178, v182, 2.0\n"
  "v_fma_f32 v183, v179, v183, 2.0\n"
  "v_mfma_f32_16x16x32_bf16 v[104:107], v[196:199], a[40:43], v[104:107]\n"
  "v_rcp_f32 v180, v180\n"
  "v_mfma_f32_16x16x32_bf16 v[104:107], v[192:195], a[56:59], v[104:107]\n"
  "v_rcp_f32 v181, v181\n"
  "v_mfma_f32_16x16x32_bf16 v[108:111], v[192:195], a[60:63], v[108:111]\n"
  "s_waitcnt vmcnt(12)\n"
  "s_barrier\n"
  "ds_read_b64_tr_b16 v[192:193], v208 offset:32768\n"
  "v_mfma_f32_16x16x32_bf16 v[108:111], v[196:199], a[44:47], v[108:111]\n"
  "ds_read_b64_tr_b16 v[194:195], v208 offset:33792\n"
  "s_waitcnt lgkmcnt(4)\n"
  "v_mfma_f32_16x16x32_bf16 v[124:127], v[204:207], a[44:47], v[124:127]\n"
  "ds_read_b64_tr_b16 v[196:197], v208 offset:40960\n"
  "s_waitcnt lgkmcnt(3)\n"
  "v_mfma_f32_16x16x32_bf16 v[124:127], v[200:203], a[60:63], v[124:127]\n"
  "ds_read_b64_tr_b16 v[198:199], v208 offset:41984\n"
  "v_mfma_f32_16x16x32_bf16 v[120:123], v[200:203], a[56:59], v[120:123]\n"
  "global_store_dwordx4 v232, v[248:251], s[22:23] offset:3072 sc1\n"
  "v_mfma_f32_16x16x32_bf16 v[120:123], v[204:207], a[40:43], v[120:123]\n"
  "v_rcp_f32 v182, v182\n"
  "v_mfma_f32_16x16x32_bf16 v[116:119], v[204:207], a[36:39], v[116:119]\n"
  "global_load_lds_dwordx4 v220, s[60:61] offset:0\n"
  "v_mfma_f32_16x16x32_bf16 v[116:119], v[200:203], a[52:55], v[116:119]\n"
  "v_rcp_f32 v183, v183\n"
  "v_mfma_f32_16x16x32_bf16 v[112:115], v[200:203], a[48:51], v[112:115]\n"
  "ds_read_b64_tr_b16 v[200:201], v209 offset:32768\n"
  "v_fma_f32 v180, -2.0, v180, 1.0\n"
  "v_mfma_f32_16x16x32_bf16 v[112:115], v[204:207], a[32:35], v[112:115]\n"
  "ds_read_b64_tr_b16 v[202:203], v209 offset:33792\n"
  "global_load_lds_dwordx4 v221, s[60:61] offset:1024\n"
  "global_load_lds_dwordx4 v222, s[60:61] offset:2048\n"
  "global_load_lds_dwordx4 v223, s[60:61] offset:3072\n"
  "v_fma_f32 v181, -2.0, v181, 1.0\n"
  "v_fma_f32 v182, -2.0, v182, 1.0\n"
  "v_fma_f32 v183, -2.0, v183, 1.0\n"
  "v_mul_f32 v136, v136, v180\n"
  "v_mul_f32 v137, v137, v181\n"
  "v_mul_f32 v138, v138, v182\n"
  "v_mul_f32 v139, v139, v183\n"
  "L_stg22_%=:\n"
  "s_waitcnt lgkmcnt(4)\n"
  "v_mfma_f32_16x16x32_bf16 v[0:3], v[192:195], a[64:67], v[0:3]\n"
  "ds_read_b64_tr_b16 v[204:205], v209 offset:40960\n"
  "v_cvt_pk_bf16_f32 v240, v136, v137\n"
  "s_waitcnt lgkmcnt(3)\n"
  "v_mfma_f32_16x16x32_bf16 v[0:3], v[196:199], a[80:83], v[0:3]\n"
  "s_barrier\n"
  "ds_read_b64_tr_b16 v[206:207], v209 offset:41984\n"
  "v_cvt_pk_bf16_f32 v241, v138, v139\n"
  "v_mfma_f32_16x16x32_bf16 v[4:7], v[196:199], a[84:87], v[4:7]\n"
  "global_store_dwordx2 v236, v[240:241], s[44:45] sc1\n"
  "v_mfma_f32_16x16x32_bf16 v[4:7], v[192:195], a[68:71], v[4:7]\n"
  "s_lshl_b32 s98, s18, 1\n"
  "v_mul_f32 v184, 0x3fb8aa3b, v140\n"
  "v_mul_f32 v185, 0x3fb8aa3b, v141\n"
  "v_mfma_f32_16x16x32_bf16 v[8:11], v[192:195], a[72:75], v[8:11]\n"
  "v_mul_f32 v186, 0x3fb8aa3b, v142\n"
  "v_mul_f32 v187, 0x3fb8aa3b, v143\n"
  "v_mfma_f32_16x16x32_bf16 v[8:11], v[196:199], a[88:91], v[8:11]\n"
  "v_exp_f32 v184, v184\n"
  "v_mfma_f32_16x16x32_bf16 v[12:15], v[196:199], a[92:95], v[12:15]\n"
  "ds_read_b64_tr_b16 v[196:197], v210 offset:32768\n"
  "v_mfma_f32_16x16x32_bf16 v[12:15], v[192:195], a[76:79], v[12:15]\n"
  "ds_read_b64_tr_b16 v[198:199], v210 offset:33792\n"
  "s_waitcnt lgkmcnt(4)\n"
  "v_mfma_f32_16x16x32_bf16 v[28:31], v[200:203], a[76:79], v[28:31]\n"
  "s_add_u32 s98, s98, 5\n"
  "ds_read_b64_tr_b16 v[192:193], v210 offset:40960\n"
  "s_waitcnt lgkmcnt(3)\n"
  "v_mfma_f32_16x16x32_bf16 v[28:31], v[204:207], a[92:95], v[28:31]\n"
  "ds_read_b64_tr_b16 v[194:195], v210 offset:41984\n"
  "v_mfma_f32_16x16x32_bf16 v[24:27], v[204:207], a[88:91], v[24:27]\n"
  "v_exp_f32 v185, v185\n"
  "v_mfma_f32_16x16x32_bf16 v[24:27], v[200:203], a[72:75], v[24:27]\n"
  "v_exp_f32 v186, v186\n"
  "v_mfma_f32_16x16x32_bf16 v[20:23], v[200:203], a[68:71], v[20:23]\n"
  "v_exp_f32 v187, v187\n"
  "v_mfma_f32_16x16x32_bf16 v[20:23], v[204:207], a[84:87], v[20:23]\n"
  "v_add_f32 v188, 2.0, v184\n"
  "v_add_f32 v189, 2.0, v185\n"
  "v_mfma_f32_16x16x32_bf16 v[16:19], v[204:207], a[80:83], v[16:19]\n"
  "s_and_b32 s98, s98, 7\n"
  "ds_read_b64_tr_b16 v[204:205], v211 offset:32768\n"
  "v_add_f32 v190, 2.0, v186\n"
  "v_mfma_f32_16x16x32_bf16 v[16:19], v[200:203], a[64:67], v[16:19]\n"
  "ds_read_b64_tr_b16 v[206:207], v211 offset:33792\n"
  "v_add_f32 v191, 2.0, v187\n"
  "s_waitcnt lgkmcnt(4)\n"
  "v_mfma_f32_16x16x32_bf16 v[32:35], v[196:199], a[64:67], v[32:35]\n"
  "ds_read_b64_tr_b16 v[200:201], v211 offset:40960\n"
  "v_fma_f32 v188, v184, v188, 2.0\n"
  "s_waitcnt lgkmcnt(3)\n"
  "v_mfma_f32_16x16x32_bf16 v[32:35], v[192:195], a[80:83], v[32:35]\n"
  "ds_read_b64_tr_b16 v[202:203], v211 offset:41984\n"
  "v_fma_f32 v189, v185, v189, 2.0\n"
  "v_mfma_f32_16x16x32_bf16 v[36:39], v[192:195], a[84:87], v[36:39]\n"
  "v_fma_f32 v190, v186, v190, 2.0\n"
  "v_fma_f32 v191, v187, v191, 2.0\n"
  "v_mfma_f32_16x16x32_bf16 v[36:39], v[196:199], a[68:71], v[36:39]\n"
  "v_rcp_f32 v188, v188\n"
  "v_mfma_f32_16x16x32_bf16 v[40:43], v[196:199], a[72:75], v[40:43]\n"
  "s_mul_i32 s98, s98, 524288\n"
  "v_rcp_f32 v189, v189\n"
  "v_mfma_f32_16x16x32_bf16 v[40:43], v[192:195], a[88:91], v[40:43]\n"
  "v_rcp_f32 v190, v190\n"
  "v_mfma_f32_16x16x32_bf16 v[44:47], v[192:195], a[92:95], v[44:47]\n"
  "ds_read_b64_tr_b16 v[192:193], v212 offset:32768\n"
  "v_mfma_f32_16x16x32_bf16 v[44:47], v[196:199], a[76:79], v[44:47]\n"
  "ds_read_b64_tr_b16 v[194:195], v212 offset:33792\n"
  "s_waitcnt lgkmcnt(4)\n"
  "v_mfma_f32_16x16x32_bf16 v[60:63], v[204:207], a[76:79], v[60:63]\n"
  "ds_read_b64_tr_b16 v[196:197], v212 offset:40960\n"
  "s_waitcnt lgkmcnt(3)\n"
  "v_mfma_f32_16x16x32_bf16 v[60:63], v[200:203], a[92:95], v[60:63]\n"
  "s_add_u32 s60, s28, s98\n"
  "s_addc_u32 s61, s29, 0\n"
  "ds_read_b64_tr_b16 v[198:199], v212 offset:41984\n"
  "v_mfma_f32_16x16x32_bf16 v[56:59], v[200:203], a[88:91], v[56:59]\n"
  "v_rcp_f32 v191, v191\n"
  "v_mfma_f32_16x16x32_bf16 v[56:59], v[204:207], a[72:75], v[56:59]\n"
  "v_fma_f32 v188, -2.0, v188, 1.0\n"
  "v_fma_f32 v189, -2.0, v189, 1.0\n"
  "v_mfma_f32_16x16x32_bf16 v[52:55], v[204:207], a[68:71], v[52:55]\n"
  "v_fma_f32 v190, -2.0, v190, 1.0\n"
  "v_fma_f32 v191, -2.0, v191, 1.0\n"
  "v_mfma_f32_16x16x32_bf16 v[52:55], v[200:203], a[84:87], v[52:55]\n"
  "v_mul_f32 v140, v140, v188\n"
  "v_mul_f32 v141, v141, v189\n"
  "v_mfma_f32_16x16x32_bf16 v[48:51], v[200:203], a[80:83], v[48:51]\n"
  "ds_read_b64_tr_b16 v[200:201], v213 offset:32768\n"
  "v_mul_f32 v142, v142, v190\n"
  "v_mfma_f32_16x16x32_bf16 v[48:51], v[204:207], a[64:67], v[48:51]\n"
  "s_add_u32 m0, s48, 16384\n"
  "ds_read_b64_tr_b16 v[202:203], v213 offset:33792\n"
  "v_mul_f32 v143, v143, v191\n"
  "s_waitcnt lgkmcnt(4)\n"
  "v_mfma_f32_16x16x32_bf16 v[64:67], v[192:195], a[64:67], v[64:67]\n"
  "ds_read_b64_tr_b16 v[204:205], v213 offset:40960\n"
  "v_cvt_pk_bf16_f32 v242, v140, v141\n"
  "s_waitcnt lgkmcnt(3)\n"
  "v_mfma_f32_16x16x32_bf16 v[64:67], v[196:199], a[80:83], v[64:67]\n"
  "ds_read_b64_tr_b16 v[206:207], v213 offset:41984\n"
  "v_cvt_pk_bf16_f32 v243, v142, v143\n"
  "v_mfma_f32_16x16x32_bf16 v[68:71], v[196:199], a[84:87], v[68:71]\n"
  "global_store_dwordx2 v236, v[242:243], s[46:47] sc1\n"
  "v_mfma_f32_16x16x32_bf16 v[68:71], v[192:195], a[68:71], v[68:71]\n"
  "v_mul_f32 v176, 0x3fb8aa3b, v144\n"
  "v_mul_f32 v177, 0x3fb8aa3b, v145\n"
  "v_mfma_f32_16x16x32_bf16 v[72:75], v[192:195], a[72:75], v[72:75]\n"
  "v_mul_f32 v178, 0x3fb8aa3b, v146\n"
  "v_mul_f32 v179, 0x3fb8aa3b, v147\n"
  "v_mfma_f32_16x16x32_bf16 v[72:75], v[196:199], a[88:91], v[72:75]\n"
  "v_exp_f32 v176, v176\n"
  "v_mfma_f32_16x16x32_bf16 v[76:79], v[196:199], a[92:95], v[76:79]\n"
  "ds_read_b64_tr_b16 v[196:197], v214 offset:32768\n"
  "v_mfma_f32_16x16x32_bf16 v[76:79], v[192:195], a[76:79], v[76:79]\n"
  "ds_read_b64_tr_b16 v[198:199], v214 offset:33792\n"
  "s_waitcnt lgkmcnt(4)\n"
  "v_mfma_f32_16x16x32_bf16 v[92:95], v[200:203], a[76:79], v[92:95]\n"
  "ds_read_b64_tr_b16 v[192:193], v214 offset:40960\n"
  "s_waitcnt lgkmcnt(3)\n"
  "v_mfma_f32_16x16x32_bf16 v[92:95], v[204:207], a[92:95], v[92:95]\n"
  "ds_read_b64_tr_b16 v[194:195], v214 offset:41984\n"
  "v_mfma_f32_16x16x32_bf16 v[88:91], v[204:207], a[88:91], v[88:91]\n"
  "v_exp_f32 v177, v177\n"
  "v_mfma_f32_16x16x32_bf16 v[88:91], v[200:203], a[72:75], v[88:91]\n"
  "v_exp_f32 v178, v178\n"
  "v_mfma_f32_16x16x32_bf16 v[84:87], v[200:203], a[68:71], v[84:87]\n"
  "v_exp_f32 v179, v179\n"
  "v_mfma_f32_16x16x32_bf16 v[84:87], v[204:207], a[84:87], v[84:87]\n"
  "v_add_f32 v180, 2.0, v176\n"
  "v_add_f32 v181, 2.0, v177\n"
  "v_mfma_f32_16x16x32_bf16 v[80:83], v[204:207], a[80:83], v[80:83]\n"
  "ds_read_b64_tr_b16 v[204:205], v215 offset:32768\n"
  "v_add_f32 v182, 2.0, v178\n"
  "v_mfma_f32_16x16x32_bf16 v[80:83], v[200:203], a[64:67], v[80:83]\n"
  "ds_read_b64_tr_b16 v[206:207], v215 offset:33792\n"
  "v_add_f32 v183, 2.0, v179\n"
  "s_waitcnt lgkmcnt(4)\n"
  "v_mfma_f32_16x16x32_bf16 v[96:99], v[196:199], a[64:67], v[96:99]\n"
  "ds_read_b64_tr_b16 v[200:201], v215 offset:40960\n"
  "v_fma_f32 v180, v176, v180, 2.0\n"
  "s_waitcnt lgkmcnt(3)\n"
  "v_mfma_f32_16x16x32_bf16 v[96:99], v[192:195], a[80:83], v[96:99]\n"
  "ds_read_b64_tr_b16 v[202:203], v215 offset:41984\n"
  "v_fma_f32 v181, v177, v181, 2.0\n"
  "v_mfma_f32_16x16x32_bf16 v[100:103], v[192:195], a[84:87], v[100:103]\n"
  "global_load_lds_dwordx4 v220, s[60:61] offset:0\n"
  "v_fma_f32 v182, v178, v182, 2.0\n"
  "v_mfma_f32_16x16x32_bf16 v[100:103], v[196:199], a[68:71], v[100:103]\n"
  "global_load_lds_dwordx4 v221, s[60:61] offset:1024\n"
  "v_fma_f32 v183, v179, v183, 2.0\n"
  "v_mfma_f32_16x16x32_bf16 v[104:107], v[196:199], a[72:75], v[104:107]\n"
  "v_rcp_f32 v180, v180\n"
  "v_mfma_f32_16x16x32_bf16 v[104:107], v[192:195], a[88:91], v[104:107]\n"
  "v_rcp_f32 v181, v181\n"
  "v_mfma_f32_16x16x32_bf16 v[108:111], v[192:195], a[92:95], v[108:111]\n"
  "s_waitcnt vmcnt(11)\n"
  "s_barrier\n"
  "ds_read_b64_tr_b16 v[192:193], v208 offset:49152\n"
  "v_mfma_f32_16x16x32_bf16 v[108:111], v[196:199], a[76:79], v[108:111]\n"
  "ds_read_b64_tr_b16 v[194:195], v208 offset:50176\n"
  "s_waitcnt lgkmcnt(4)\n"
  "v_mfma_f32_16x16x32_bf16 v[124:127], v[204:207], a[76:79], v[124:127]\n"
  "ds_read_b64_tr_b16 v[196:197], v208 offset:57344\n"
  "s_waitcnt lgkmcnt(3)\n"
  "v_mfma_f32_16x16x32_bf16 v[124:127], v[200:203], a[92:95], v[124:127]\n"
  "ds_read_b64_tr_b16 v[198:199], v208 offset:58368\n"
  "v_mfma_f32_16x16x32_bf16 v[120:123], v[200:203], a[88:91], v[120:123]\n"
  "v_rcp_f32 v182, v182\n"
  "v_mfma_f32_16x16x32_bf16 v[120:123], v[204:207], a[72:75], v[120:123]\n"
  "global_load_lds_dwordx4 v222, s[60:61] offset:2048\n"
  "v_mfma_f32_16x16x32_bf16 v[116:119], v[204:207], a[68:71], v[116:119]\n"
  "global_load_lds_dwordx4 v223, s[60:61] offset:3072\n"
  "v_mfma_f32_16x16x32_bf16 v[116:119], v[200:203], a[84:87], v[116:119]\n"
  "v_mfma_f32_16x16x32_bf16 v[112:115], v[200:203], a[80:83], v[112:115]\n"
  "ds_read_b64_tr_b16 v[200:201], v209 offset:49152\n"
  "v_mfma_f32_16x16x32_bf16 v[112:115], v[204:207], a[64:67], v[112:115]\n"
  "ds_read_b64_tr_b16 v[202:203], v209 offset:50176\n"
  "L_stg23_%=:\n"
  "s_waitcnt lgkmcnt(4)\n"
  "v_mfma_f32_16x16x32_bf16 v[0:3], v[192:195], a[96:99], v[0:3]\n"
  "ds_read_b64_tr_b16 v[204:205], v209 offset:57344\n"
  "s_waitcnt lgkmcnt(3)\n"
  "v_mfma_f32_16x16x32_bf16 v[0:3], v[196:199], a[112:115], v[0:3]\n"
  "s_waitcnt vmcnt(10)\n"
  "s_barrier\n"
  "ds_read_b64_tr_b16 v[206:207], v209 offset:58368\n"
  "v_mfma_f32_16x16x32_bf16 v[4:7], v[196:199], a[116:119], v[4:7]\n"
  "s_lshl_b32 s31, s34, 1\n"
  "v_rcp_f32 v183, v183\n"
  "v_mfma_f32_16x16x32_bf16 v[4:7], v[192:195], a[100:103], v[4:7]\n"
  "v_fma_f32 v180, -2.0, v180, 1.0\n"
  "v_fma_f32 v181, -2.0, v181, 1.0\n"
  "v_mfma_f32_16x16x32_bf16 v[8:11], v[192:195], a[104:107], v[8:11]\n"
  "v_mov_b32 v234, s16\n"
  "v_mov_b32 v235, s17\n"
  "v_mfma_f32_16x16x32_bf16 v[8:11], v[196:199], a[120:123], v[8:11]\n"
  "s_lshl_b32 s36, s86, 9\n"
  "v_mov_b32 v219, s36\n"
  "v_fma_f32 v182, -2.0, v182, 1.0\n"
  "v_mfma_f32_16x16x32_bf16 v[12:15], v[196:199], a[124:127], v[12:15]\n"
  "s_sub_u32 s25, 7, s34\n"
  "ds_read_b64_tr_b16 v[196:197], v210 offset:49152\n"
  "v_fma_f32 v183, -2.0, v183, 1.0\n"
  "v_mfma_f32_16x16x32_bf16 v[12:15], v[192:195], a[108:111], v[12:15]\n"
  "ds_read_b64_tr_b16 v[198:199], v210 offset:50176\n"
  "v_mul_f32 v144, v144, v180\n"
  "s_waitcnt lgkmcnt(4)\n"
  "v_mfma_f32_16x16x32_bf16 v[28:31], v[200:203], a[108:111], v[28:31]\n"
  "ds_read_b64_tr_b16 v[192:193], v210 offset:57344\n"
  "v_mul_f32 v145, v145, v181\n"
  "s_waitcnt lgkmcnt(3)\n"
  "v_mfma_f32_16x16x32_bf16 v[28:31], v[204:207], a[124:127], v[28:31]\n"
  "s_lshl_b32 s25, s25, 17\n"
  "ds_read_b64_tr_b16 v[194:195], v210 offset:58368\n"
  "v_mul_f32 v146, v146, v182\n"
  "v_mfma_f32_16x16x32_bf16 v[24:27], v[204:207], a[120:123], v[24:27]\n"
  "s_mov_b64 exec, 1\n"
  "global_store_dwordx2 v219, v[234:235], s[62:63] offset:64 sc1\n"
  "s_mov_b64 exec, -1\n"
  "v_mfma_f32_16x16x32_bf16 v[24:27], v[200:203], a[104:107], v[24:27]\n"
  "s_lshl_b32 s98, s18, 1\n"
  "v_mul_f32 v147, v147, v183\n"
  "v_cvt_pk_bf16_f32 v240, v144, v145\n"
  "v_mfma_f32_16x16x32_bf16 v[20:23], v[200:203], a[100:103], v[20:23]\n"
  "s_add_u32 s98, s98, 6\n"
  "v_cvt_pk_bf16_f32 v241, v146, v147\n"
  "v_mfma_f32_16x16x32_bf16 v[20:23], v[204:207], a[116:119], v[20:23]\n"
  "s_and_b32 s98, s98, 7\n"
  "s_add_u32 s25, s25, 0x4000\n"
  "global_store_dwordx2 v237, v[240:241], s[40:41] sc1\n"
  "v_mfma_f32_16x16x32_bf16 v[16:19], v[204:207], a[112:115], v[16:19]\n"
  "ds_read_b64_tr_b16 v[204:205], v211 offset:49152\n"
  "v_mul_f32 v184, 0x3fb8aa3b, v148\n"
  "v_mfma_f32_16x16x32_bf16 v[16:19], v[200:203], a[96:99], v[16:19]\n"
  "ds_read_b64_tr_b16 v[206:207], v211 offset:50176\n"
  "v_mul_f32 v185, 0x3fb8aa3b, v149\n"
  "s_waitcnt lgkmcnt(4)\n"
  "v_mfma_f32_16x16x32_bf16 v[32:35], v[196:199], a[96:99], v[32:35]\n"
  "ds_read_b64_tr_b16 v[200:201], v211 offset:57344\n"
  "v_mul_f32 v186, 0x3fb8aa3b, v150\n"
  "s_waitcnt lgkmcnt(3)\n"
  "v_mfma_f32_16x16x32_bf16 v[32:35], v[192:195], a[112:115], v[32:35]\n"
  "ds_read_b64_tr_b16 v[202:203], v211 offset:58368\n"
  "v_mul_f32 v187, 0x3fb8aa3b, v151\n"
  "v_mfma_f32_16x16x32_bf16 v[36:39], v[192:195], a[116:119], v[36:39]\n"
  "s_mul_i32 s98, s98, 524288\n"
  "v_exp_f32 v184, v184\n"
  "v_mfma_f32_16x16x32_bf16 v[36:39], v[196:199], a[100:103], v[36:39]\n"
  "v_exp_f32 v185, v185\n"
  "v_mfma_f32_16x16x32_bf16 v[40:43], v[196:199], a[104:107], v[40:43]\n"
  "v_exp_f32 v186, v186\n"
  "v_mfma_f32_16x16x32_bf16 v[40:43], v[192:195], a[120:123], v[40:43]\n"
  "v_exp_f32 v187, v187\n"
  "v_mfma_f32_16x16x32_bf16 v[44:47], v[192:195], a[124:127], v[44:47]\n"
  "ds_read_b64_tr_b16 v[192:193], v212 offset:49152\n"
  "v_add_f32 v188, 2.0, v184\n"
  "v_mfma_f32_16x16x32_bf16 v[44:47], v[196:199], a[108:111], v[44:47]\n"
  "ds_read_b64_tr_b16 v[194:195], v212 offset:50176\n"
  "v_add_f32 v189, 2.0, v185\n"
  "s_waitcnt lgkmcnt(4)\n"
  "v_mfma_f32_16x16x32_bf16 v[60:63], v[204:207], a[108:111], v[60:63]\n"
  "ds_read_b64_tr_b16 v[196:197], v212 offset:57344\n"
  "v_add_f32 v190, 2.0, v186\n"
  "s_waitcnt lgkmcnt(3)\n"
  "v_mfma_f32_16x16x32_bf16 v[60:63], v[200:203], a[124:127], v[60:63]\n"
  "ds_read_b64_tr_b16 v[198:199], v212 offset:58368\n"
  "v_add_f32 v191, 2.0, v187\n"
  "v_mfma_f32_16x16x32_bf16 v[56:59], v[200:203], a[120:123], v[56:59]\n"
  "v_fma_f32 v188, v184, v188, 2.0\n"
  "v_mov_b32 v252, L_PFF_%=-L_start_%=\n"
  "v_mfma_f32_16x16x32_bf16 v[56:59], v[204:207], a[104:107], v[56:59]\n"
  "v_fma_f32 v189, v185, v189, 2.0\n"
  "v_fma_f32 v190, v186, v190, 2.0\n"
  "v_mfma_f32_16x16x32_bf16 v[52:55], v[204:207], a[100:103], v[52:55]\n"
  "v_fma_f32 v191, v187, v191, 2.0\n"
  "v_mov_b32 v251, L_SFF_%=-L_start_%=\n"
  "v_mfma_f32_16x16x32_bf16 v[52:55], v[200:203], a[116:119], v[52:55]\n"
  "v_rcp_f32 v188, v188\n"
  "v_mfma_f32_16x16x32_bf16 v[48:51], v[200:203], a[112:115], v[48:51]\n"
  "s_add_u32 s60, s28, s98\n"
  "s_addc_u32 s61, s29, 0\n"
  "ds_read_b64_tr_b16 v[200:201], v213 offset:49152\n"
  "v_mfma_f32_16x16x32_bf16 v[48:51], v[204:207], a[96:99], v[48:51]\n"
  "s_add_u32 m0, s48, 32768\n"
  "ds_read_b64_tr_b16 v[202:203], v213 offset:50176\n"
  "v_cmp_gt_u32 vcc, s31, v233\n"
  "s_waitcnt lgkmcnt(4)\n"
  "v_mfma_f32_16x16x32_bf16 v[64:67], v[192:195], a[96:99], v[64:67]\n"
  "s_nop 0\n"
  "ds_read_b64_tr_b16 v[204:205], v213 offset:57344\n"
  "v_cndmask_b32 v252, v252, v251, vcc\n"
  "s_waitcnt lgkmcnt(3)\n"
  "v_mfma_f32_16x16x32_bf16 v[64:67], v[196:199], a[112:115], v[64:67]\n"
  "ds_read_b64_tr_b16 v[206:207], v213 offset:58368\n"
  "v_mfma_f32_16x16x32_bf16 v[68:71], v[196:199], a[116:119], v[68:71]\n"
  "v_rcp_f32 v189, v189\n"
  "v_mfma_f32_16x16x32_bf16 v[68:71], v[192:195], a[100:103], v[68:71]\n"
  "v_rcp_f32 v190, v190\n"
  "v_mfma_f32_16x16x32_bf16 v[72:75], v[192:195], a[104:107], v[72:75]\n"
  "s_mov_b32 s87, L_AFF_%=-L_start_%=\n"
  "v_rcp_f32 v191, v191\n"
  "v_mfma_f32_16x16x32_bf16 v[72:75], v[196:199], a[120:123], v[72:75]\n"
  "v_fma_f32 v188, -2.0, v188, 1.0\n"
  "v_fma_f32 v189, -2.0, v189, 1.0\n"
  "v_mfma_f32_16x16x32_bf16 v[76:79], v[196:199], a[124:127], v[76:79]\n"
  "ds_read_b64_tr_b16 v[196:197], v214 offset:49152\n"
  "v_fma_f32 v190, -2.0, v190, 1.0\n"
  "v_mfma_f32_16x16x32_bf16 v[76:79], v[192:195], a[108:111], v[76:79]\n"
  "ds_read_b64_tr_b16 v[198:199], v214 offset:50176\n"
  "v_fma_f32 v191, -2.0, v191, 1.0\n"
  "s_waitcnt lgkmcnt(4)\n"
  "v_mfma_f32_16x16x32_bf16 v[92:95], v[200:203], a[108:111], v[92:95]\n"
  "ds_read_b64_tr_b16 v[192:193], v214 offset:57344\n"
  "v_mul_f32 v148, v148, v188\n"
  "s_waitcnt lgkmcnt(3)\n"
  "v_mfma_f32_16x16x32_bf16 v[92:95], v[204:207], a[124:127], v[92:95]\n"
  "ds_read_b64_tr_b16 v[194:195], v214 offset:58368\n"
  "v_mul_f32 v149, v149, v189\n"
  "v_mfma_f32_16x16x32_bf16 v[88:91], v[204:207], a[120:123], v[88:91]\n"
  "v_mul_f32 v150, v150, v190\n"
  "v_mul_f32 v151, v151, v191\n"
  "v_mfma_f32_16x16x32_bf16 v[88:91], v[200:203], a[104:107], v[88:91]\n"
  "v_cvt_pk_bf16_f32 v242, v148, v149\n"
  "v_cvt_pk_bf16_f32 v243, v150, v151\n"
  "v_mfma_f32_16x16x32_bf16 v[84:87], v[200:203], a[100:103], v[84:87]\n"
  "global_store_dwordx2 v237, v[242:243], s[42:43] sc1\n"
  "v_mfma_f32_16x16x32_bf16 v[84:87], v[204:207], a[116:119], v[84:87]\n"
  "v_mul_f32 v176, 0x3fb8aa3b, v152\n"
  "v_mul_f32 v177, 0x3fb8aa3b, v153\n"
  "v_mfma_f32_16x16x32_bf16 v[80:83], v[204:207], a[112:115], v[80:83]\n"
  "ds_read_b64_tr_b16 v[204:205], v215 offset:49152\n"
  "v_mul_f32 v178, 0x3fb8aa3b, v154\n"
  "v_mfma_f32_16x16x32_bf16 v[80:83], v[200:203], a[96:99], v[80:83]\n"
  "ds_read_b64_tr_b16 v[206:207], v215 offset:50176\n"
  "v_mul_f32 v179, 0x3fb8aa3b, v155\n"
  "s_waitcnt lgkmcnt(4)\n"
  "v_mfma_f32_16x16x32_bf16 v[96:99], v[196:199], a[96:99], v[96:99]\n"
  "ds_read_b64_tr_b16 v[200:201], v215 offset:57344\n"
  "v_writelane_b32 v252, s87, 15\n"
  "s_waitcnt lgkmcnt(3)\n"
  "v_mfma_f32_16x16x32_bf16 v[96:99], v[192:195], a[112:115], v[96:99]\n"
  "s_mov_b32 s87, L_BFX_%=-L_start_%=\n"
  "ds_read_b64_tr_b16 v[202:203], v215 offset:58368\n"
  "v_writelane_b32 v252, s87, 16\n"
  "v_mfma_f32_16x16x32_bf16 v[100:103], v[192:195], a[116:119], v[100:103]\n"
  "v_exp_f32 v176, v176\n"
  "v_mfma_f32_16x16x32_bf16 v[100:103], v[196:199], a[100:103], v[100:103]\n"
  "global_load_lds_dwordx4 v220, s[60:61] offset:0\n"
  "v_mfma_f32_16x16x32_bf16 v[104:107], v[196:199], a[104:107], v[104:107]\n"
  "s_mov_b32 s87, L_final_%=-L_start_%=\n"
  "v_exp_f32 v177, v177\n"
  "v_mfma_f32_16x16x32_bf16 v[104:107], v[192:195], a[120:123], v[104:107]\n"
  "v_exp_f32 v178, v178\n"
  "v_mfma_f32_16x16x32_bf16 v[108:111], v[192:195], a[124:127], v[108:111]\n"
  "s_waitcnt vmcnt(10)\n"
  "s_barrier\n"
  "ds_read_b64_tr_b16 v[192:193], v208 offset:0\n"
  "v_mfma_f32_16x16x32_bf16 v[108:111], v[196:199], a[108:111], v[108:111]\n"
  "ds_read_b64_tr_b16 v[194:195], v208 offset:1024\n"
  "s_waitcnt lgkmcnt(4)\n"
  "v_mfma_f32_16x16x32_bf16 v[124:127], v[204:207], a[108:111], v[124:127]\n"
  "ds_read_b64_tr_b16 v[196:197], v208 offset:8192\n"
  "v_writelane_b32 v252, s87, 17\n"
  "s_waitcnt lgkmcnt(3)\n"
  "v_mfma_f32_16x16x32_bf16 v[124:127], v[200:203], a[124:127], v[124:127]\n"
  "ds_read_b64_tr_b16 v[198:199], v208 offset:9216\n"
  "v_mfma_f32_16x16x32_bf16 v[120:123], v[200:203], a[120:123], v[120:123]\n"
  "v_exp_f32 v179, v179\n"
  "v_mfma_f32_16x16x32_bf16 v[120:123], v[204:207], a[104:107], v[120:123]\n"
  "v_add_f32 v180, 2.0, v176\n"
  "v_add_f32 v181, 2.0, v177\n"
  "v_mfma_f32_16x16x32_bf16 v[116:119], v[204:207], a[100:103], v[116:119]\n"
  "v_add_f32 v182, 2.0, v178\n"
  "v_add_f32 v183, 2.0, v179\n"
  "v_mfma_f32_16x16x32_bf16 v[116:119], v[200:203], a[116:119], v[116:119]\n"
  "global_load_lds_dwordx4 v221, s[60:61] offset:1024\n"
  "v_fma_f32 v180, v176, v180, 2.0\n"
  "v_mfma_f32_16x16x32_bf16 v[112:115], v[200:203], a[112:115], v[112:115]\n"
  "ds_read_b64_tr_b16 v[200:201], v209 offset:0\n"
  "v_fma_f32 v181, v177, v181, 2.0\n"
  "v_mfma_f32_16x16x32_bf16 v[112:115], v[204:207], a[96:99], v[112:115]\n"
  "ds_read_b64_tr_b16 v[202:203], v209 offset:1024\n"
  "global_load_lds_dwordx4 v222, s[60:61] offset:2048\n"
  "global_load_lds_dwordx4 v223, s[60:61] offset:3072\n"
  "s_lshl_b32 s36, s39, 20\n"
  "s_lshl_b32 s37, s35, 12\n"
  "s_add_u32 s36, s36, s37\n"
  "s_add_u32 s36, s36, 2097152\n"
  "s_add_u32 s20, s84, s36\n"
  "s_addc_u32 s21, s85, 0\n"
  "s_add_u32 s49, s48, 65536\n"
  "v_fma_f32 v182, v178, v182, 2.0\n"
  "v_fma_f32 v183, v179, v183, 2.0\n"
  "v_rcp_f32 v180, v180\n"
  "v_rcp_f32 v181, v181\n"
  "L_stg24_%=:\n"
  "s_waitcnt lgkmcnt(4)\n"
  "v_mfma_f32_16x16x32_bf16 v[0:3], v[192:195], a[128:131], v[0:3]\n"
  "ds_read_b64_tr_b16 v[204:205], v209 offset:8192\n"
  "s_waitcnt lgkmcnt(3)\n"
  "v_mfma_f32_16x16x32_bf16 v[0:3], v[196:199], a[144:147], v[0:3]\n"
  "s_barrier\n"
  "ds_read_b64_tr_b16 v[206:207], v209 offset:9216\n"
  "v_mfma_f32_16x16x32_bf16 v[4:7], v[196:199], a[148:151], v[4:7]\n"
  "s_mov_b32 s99, 60\n"
  "v_rcp_f32 v182, v182\n"
  "v_mfma_f32_16x16x32_bf16 v[4:7], v[192:195], a[132:135], v[4:7]\n"
  "v_rcp_f32 v183, v183\n"
  "v_mfma_f32_16x16x32_bf16 v[8:11], v[192:195], a[136:139], v[8:11]\n"
  "v_fma_f32 v180, -2.0, v180, 1.0\n"
  "v_fma_f32 v181, -2.0, v181, 1.0\n"
  "v_mfma_f32_16x16x32_bf16 v[8:11], v[196:199], a[152:155], v[8:11]\n"
  "v_and_b32 v219, 7, v233\n"
  "v_lshlrev_b32 v219, 5, v219\n"
  "v_mfma_f32_16x16x32_bf16 v[12:15], v[196:199], a[156:159], v[12:15]\n"
  "ds_read_b64_tr_b16 v[196:197], v210 offset:0\n"
  "v_fma_f32 v182, -2.0, v182, 1.0\n"
  "v_mfma_f32_16x16x32_bf16 v[12:15], v[192:195], a[140:143], v[12:15]\n"
  "s_and_b32 s36, s86, 31\n"
  "ds_read_b64_tr_b16 v[198:199], v210 offset:1024\n"
  "v_fma_f32 v183, -2.0, v183, 1.0\n"
  "s_waitcnt lgkmcnt(4)\n"
  "v_mfma_f32_16x16x32_bf16 v[28:31], v[200:203], a[140:143], v[28:31]\n"
  "ds_read_b64_tr_b16 v[192:193], v210 offset:8192\n"
  "v_mul_f32 v152, v152, v180\n"
  "s_waitcnt lgkmcnt(3)\n"
  "v_mfma_f32_16x16x32_bf16 v[28:31], v[204:207], a[156:159], v[28:31]\n"
  "s_cmp_eq_u32 s35, 0\n"
  "s_cselect_b32 s99, 0x1, s99\n"
  "ds_read_b64_tr_b16 v[194:195], v210 offset:9216\n"
  "v_mfma_f32_16x16x32_bf16 v[24:27], v[204:207], a[152:155], v[24:27]\n"
  "v_mul_f32 v153, v153, v181\n"
  "v_mul_f32 v154, v154, v182\n"
  "v_mfma_f32_16x16x32_bf16 v[24:27], v[200:203], a[136:139], v[24:27]\n"
  "s_cmp_eq_u32 s35, 2\n"
  "s_cselect_b32 s99, 0x0, s99\n"
  "v_add_u32 v219, s36, v219\n"
  "v_mfma_f32_16x16x32_bf16 v[20:23], v[200:203], a[132:135], v[20:23]\n"
  "v_lshlrev_b32 v219, 9, v219\n"
  "v_mul_f32 v155, v155, v183\n"
  "v_mfma_f32_16x16x32_bf16 v[20:23], v[204:207], a[148:151], v[20:23]\n"
  "s_cmp_eq_u32 s35, 3\n"
  "s_cselect_b32 s99, 0xffffffff, s99\n"
  "v_cvt_pk_bf16_f32 v240, v152, v153\n"
  "v_mfma_f32_16x16x32_bf16 v[16:19], v[204:207], a[144:147], v[16:19]\n"
  "s_add_u32 s99, s99, s31\n"
  "ds_read_b64_tr_b16 v[204:205], v211 offset:0\n"
  "v_cvt_pk_bf16_f32 v241, v154, v155\n"
  "v_mfma_f32_16x16x32_bf16 v[16:19], v[200:203], a[128:131], v[16:19]\n"
  "s_min_u32 s99, s99, 63\n"
  "ds_read_b64_tr_b16 v[206:207], v211 offset:1024\n"
  "v_cmp_eq_u32 vcc, s99, v233\n"
  "s_waitcnt lgkmcnt(4)\n"
  "v_mfma_f32_16x16x32_bf16 v[32:35], v[196:199], a[128:131], v[32:35]\n"
  "ds_read_b64_tr_b16 v[200:201], v211 offset:8192\n"
  "s_waitcnt lgkmcnt(3)\n"
  "v_mfma_f32_16x16x32_bf16 v[32:35], v[192:195], a[144:147], v[32:35]\n"
  "s_mov_b32 s87, L_PFD_%=-L_start_%=\n"
  "ds_read_b64_tr_b16 v[202:203], v211 offset:9216\n"
  "v_mfma_f32_16x16x32_bf16 v[36:39], v[192:195], a[148:151], v[36:39]\n"
  "global_store_dwordx2 v237, v[240:241], s[44:45] sc1\n"
  "v_mfma_f32_16x16x32_bf16 v[36:39], v[196:199], a[132:135], v[36:39]\n"
  "global_load_dwordx2 v[234:235], v219, s[62:63] offset:64 sc1\n"
  "s_lshl_b32 s98, s18, 1\n"
  "v_mul_f32 v184, 0x3fb8aa3b, v156\n"
  "v_mfma_f32_16x16x32_bf16 v[40:43], v[196:199], a[136:139], v[40:43]\n"
  "s_add_u32 s98, s98, 7\n"
  "v_mul_f32 v185, 0x3fb8aa3b, v157\n"
  "v_mul_f32 v186, 0x3fb8aa3b, v158\n"
  "v_mfma_f32_16x16x32_bf16 v[40:43], v[192:195], a[152:155], v[40:43]\n"
  "s_cmp_eq_u32 s35, 3\n"
  "s_cselect_b32 s87, L_SFD_%=-L_start_%=, s87\n"
  "v_mul_f32 v187, 0x3fb8aa3b, v159\n"
  "v_mfma_f32_16x16x32_bf16 v[44:47], v[192:195], a[156:159], v[44:47]\n"
  "s_cmp_eq_u32 s99, 15\n"
  "s_cselect_b32 s87, L_AFD_%=-L_start_%=, s87\n"
  "ds_read_b64_tr_b16 v[192:193], v212 offset:0\n"
  "v_mfma_f32_16x16x32_bf16 v[44:47], v[196:199], a[140:143], v[44:47]\n"
  "ds_read_b64_tr_b16 v[194:195], v212 offset:1024\n"
  "s_waitcnt lgkmcnt(4)\n"
  "v_mfma_f32_16x16x32_bf16 v[60:63], v[204:207], a[140:143], v[60:63]\n"
  "s_and_b32 s98, s98, 7\n"
  "ds_read_b64_tr_b16 v[196:197], v212 offset:8192\n"
  "v_mov_b32 v251, s87\n"
  "s_waitcnt lgkmcnt(3)\n"
  "v_mfma_f32_16x16x32_bf16 v[60:63], v[200:203], a[156:159], v[60:63]\n"
  "ds_read_b64_tr_b16 v[198:199], v212 offset:9216\n"
  "v_mfma_f32_16x16x32_bf16 v[56:59], v[200:203], a[152:155], v[56:59]\n"
  "v_exp_f32 v184, v184\n"
  "v_mfma_f32_16x16x32_bf16 v[56:59], v[204:207], a[136:139], v[56:59]\n"
  "v_exp_f32 v185, v185\n"
  "v_mfma_f32_16x16x32_bf16 v[52:55], v[204:207], a[132:135], v[52:55]\n"
  "v_exp_f32 v186, v186\n"
  "v_mfma_f32_16x16x32_bf16 v[52:55], v[200:203], a[148:151], v[52:55]\n"
  "s_mul_i32 s98, s98, 524288\n"
  "v_exp_f32 v187, v187\n"
  "v_mfma_f32_16x16x32_bf16 v[48:51], v[200:203], a[144:147], v[48:51]\n"
  "ds_read_b64_tr_b16 v[200:201], v213 offset:0\n"
  "v_add_f32 v188, 2.0, v184\n"
  "v_mfma_f32_16x16x32_bf16 v[48:51], v[204:207], a[128:131], v[48:51]\n"
  "ds_read_b64_tr_b16 v[202:203], v213 offset:1024\n"
  "v_add_f32 v189, 2.0, v185\n"
  "s_waitcnt lgkmcnt(4)\n"
  "v_mfma_f32_16x16x32_bf16 v[64:67], v[192:195], a[128:131], v[64:67]\n"
  "ds_read_b64_tr_b16 v[204:205], v213 offset:8192\n"
  "v_add_f32 v190, 2.0, v186\n"
  "s_waitcnt lgkmcnt(3)\n"
  "v_mfma_f32_16x16x32_bf16 v[64:67], v[196:199], a[144:147], v[64:67]\n"
  "ds_read_b64_tr_b16 v[206:207], v213 offset:9216\n"
  "v_add_f32 v191, 2.0, v187\n"
  "v_mfma_f32_16x16x32_bf16 v[68:71], v[196:199], a[148:151], v[68:71]\n"
  "v_fma_f32 v188, v184, v188, 2.0\n"
  "v_fma_f32 v189, v185, v189, 2.0\n"
  "v_mfma_f32_16x16x32_bf16 v[68:71], v[192:195], a[132:135], v[68:71]\n"
  "v_fma_f32 v190, v186, v190, 2.0\n"
  "v_fma_f32 v191, v187, v191, 2.0\n"
  "v_mfma_f32_16x16x32_bf16 v[72:75], v[192:195], a[136:139], v[72:75]\n"
  "v_rcp_f32 v188, v188\n"
  "v_mfma_f32_16x16x32_bf16 v[72:75], v[196:199], a[152:155], v[72:75]\n"
  "v_rcp_f32 v189, v189\n"
  "v_mfma_f32_16x16x32_bf16 v[76:79], v[196:199], a[156:159], v[76:79]\n"
  "ds_read_b64_tr_b16 v[196:197], v214 offset:0\n"
  "v_cndmask_b32 v252, v252, v251, vcc\n"
  "v_mfma_f32_16x16x32_bf16 v[76:79], v[192:195], a[140:143], v[76:79]\n"
  "s_mov_b32 s99, 60\n"
  "ds_read_b64_tr_b16 v[198:199], v214 offset:1024\n"
  "s_waitcnt lgkmcnt(4)\n"
  "v_mfma_f32_16x16x32_bf16 v[92:95], v[200:203], a[140:143], v[92:95]\n"
  "s_cmp_eq_u32 s35, 2\n"
  "s_cselect_b32 s99, 0x1, s99\n"
  "ds_read_b64_tr_b16 v[192:193], v214 offset:8192\n"
  "s_waitcnt lgkmcnt(3)\n"
  "v_mfma_f32_16x16x32_bf16 v[92:95], v[204:207], a[156:159], v[92:95]\n"
  "s_add_u32 s60, s28, s98\n"
  "s_addc_u32 s61, s29, 0\n"
  "ds_read_b64_tr_b16 v[194:195], v214 offset:9216\n"
  "v_mfma_f32_16x16x32_bf16 v[88:91], v[204:207], a[152:155], v[88:91]\n"
  "s_add_u32 m0, s48, 49152\n"
  "v_rcp_f32 v190, v190\n"
  "v_mfma_f32_16x16x32_bf16 v[88:91], v[200:203], a[136:139], v[88:91]\n"
  "v_rcp_f32 v191, v191\n"
  "v_mfma_f32_16x16x32_bf16 v[84:87], v[200:203], a[132:135], v[84:87]\n"
  "v_fma_f32 v188, -2.0, v188, 1.0\n"
  "v_fma_f32 v189, -2.0, v189, 1.0\n"
  "v_mfma_f32_16x16x32_bf16 v[84:87], v[204:207], a[148:151], v[84:87]\n"
  "v_fma_f32 v190, -2.0, v190, 1.0\n"
  "v_fma_f32 v191, -2.0, v191, 1.0\n"
  "v_mfma_f32_16x16x32_bf16 v[80:83], v[204:207], a[144:147], v[80:83]\n"
  "ds_read_b64_tr_b16 v[204:205], v215 offset:0\n"
  "v_mul_f32 v156, v156, v188\n"
  "v_mfma_f32_16x16x32_bf16 v[80:83], v[200:203], a[128:131], v[80:83]\n"
  "ds_read_b64_tr_b16 v[206:207], v215 offset:1024\n"
  "v_mul_f32 v157, v157, v189\n"
  "s_waitcnt lgkmcnt(4)\n"
  "v_mfma_f32_16x16x32_bf16 v[96:99], v[196:199], a[128:131], v[96:99]\n"
  "ds_read_b64_tr_b16 v[200:201], v215 offset:8192\n"
  "v_mul_f32 v158, v158, v190\n"
  "s_waitcnt lgkmcnt(3)\n"
  "v_mfma_f32_16x16x32_bf16 v[96:99], v[192:195], a[144:147], v[96:99]\n"
  "ds_read_b64_tr_b16 v[202:203], v215 offset:9216\n"
  "v_mul_f32 v159, v159, v191\n"
  "v_mfma_f32_16x16x32_bf16 v[100:103], v[192:195], a[148:151], v[100:103]\n"
  "s_cmp_eq_u32 s35, 3\n"
  "s_cselect_b32 s99, 0x0, s99\n"
  "v_cvt_pk_bf16_f32 v242, v156, v157\n"
  "v_mfma_f32_16x16x32_bf16 v[100:103], v[196:199], a[132:135], v[100:103]\n"
  "s_add_u32 s99, s99, s31\n"
  "s_min_u32 s99, s99, 63\n"
  "v_cvt_pk_bf16_f32 v243, v158, v159\n"
  "v_mfma_f32_16x16x32_bf16 v[104:107], v[196:199], a[136:139], v[104:107]\n"
  "global_store_dwordx2 v237, v[242:243], s[46:47] sc1\n"
  "v_mfma_f32_16x16x32_bf16 v[104:107], v[192:195], a[152:155], v[104:107]\n"
  "v_mul_f32 v176, 0x3fb8aa3b, v160\n"
  "v_mul_f32 v177, 0x3fb8aa3b, v161\n"
  "v_mfma_f32_16x16x32_bf16 v[108:111], v[192:195], a[156:159], v[108:111]\n"
  "s_waitcnt vmcnt(10)\n"
  "s_barrier\n"
  "ds_read_b64_tr_b16 v[192:193], v208 offset:16384\n"
  "v_mfma_f32_16x16x32_bf16 v[108:111], v[196:199], a[140:143], v[108:111]\n"
  "ds_read_b64_tr_b16 v[194:195], v208 offset:17408\n"
  "v_mul_f32 v178, 0x3fb8aa3b, v162\n"
  "s_waitcnt lgkmcnt(4)\n"
  "v_mfma_f32_16x16x32_bf16 v[124:127], v[204:207], a[140:143], v[124:127]\n"
  "ds_read_b64_tr_b16 v[196:197], v208 offset:24576\n"
  "v_mul_f32 v179, 0x3fb8aa3b, v163\n"
  "s_waitcnt lgkmcnt(3)\n"
  "v_mfma_f32_16x16x32_bf16 v[124:127], v[200:203], a[156:159], v[124:127]\n"
  "ds_read_b64_tr_b16 v[198:199], v208 offset:25600\n"
  "v_cmp_eq_u32 vcc, s99, v233\n"
  "v_mfma_f32_16x16x32_bf16 v[120:123], v[200:203], a[152:155], v[120:123]\n"
  "s_mov_b32 s87, L_PDX_%=-L_start_%=\n"
  "v_exp_f32 v176, v176\n"
  "v_mfma_f32_16x16x32_bf16 v[120:123], v[204:207], a[136:139], v[120:123]\n"
  "v_exp_f32 v177, v177\n"
  "v_mfma_f32_16x16x32_bf16 v[116:119], v[204:207], a[132:135], v[116:119]\n"
  "v_exp_f32 v178, v178\n"
  "v_mfma_f32_16x16x32_bf16 v[116:119], v[200:203], a[148:151], v[116:119]\n"
  "s_cmp_eq_u32 s99, 15\n"
  "s_cselect_b32 s87, L_ADX_%=-L_start_%=, s87\n"
  "global_load_lds_dwordx4 v220, s[60:61] offset:0\n"
  "v_mfma_f32_16x16x32_bf16 v[112:115], v[200:203], a[144:147], v[112:115]\n"
  "ds_read_b64_tr_b16 v[200:201], v209 offset:16384\n"
  "v_mov_b32 v251, s87\n"
  "v_mfma_f32_16x16x32_bf16 v[112:115], v[204:207], a[128:131], v[112:115]\n"
  "ds_read_b64_tr_b16 v[202:203], v209 offset:17408\n"
  "global_load_lds_dwordx4 v221, s[60:61] offset:1024\n"
  "global_load_lds_dwordx4 v222, s[60:61] offset:2048\n"
  "global_load_lds_dwordx4 v223, s[60:61] offset:3072\n"
  "v_exp_f32 v179, v179\n"
  "v_add_f32 v180, 2.0, v176\n"
  "v_cndmask_b32 v252, v252, v251, vcc\n"
  "s_mov_b32 s99, 60\n"
  "s_cmp_eq_u32 s35, 0\n"
  "s_cselect_b32 s99, 0x2, s99\n"
  "s_cmp_eq_u32 s35, 3\n"
  "s_cselect_b32 s99, 0x2, s99\n"
  "s_add_u32 s99, s99, s31\n"
  "L_stg25_%=:\n"
  "s_waitcnt lgkmcnt(4)\n"
  "v_mfma_f32_16x16x32_bf16 v[0:3], v[192:195], a[160:163], v[0:3]\n"
  "ds_read_b64_tr_b16 v[204:205], v209 offset:24576\n"
  "v_add_f32 v181, 2.0, v177\n"
  "s_waitcnt lgkmcnt(3)\n"
  "v_mfma_f32_16x16x32_bf16 v[0:3], v[196:199], a[176:179], v[0:3]\n"
  "ds_read_b64_tr_b16 v[206:207], v209 offset:25600\n"
  "v_add_f32 v182, 2.0, v178\n"
  "v_mfma_f32_16x16x32_bf16 v[4:7], v[196:199], a[180:183], v[4:7]\n"
  "s_min_u32 s99, s99, 63\n"
  "v_add_f32 v183, 2.0, v179\n"
  "v_fma_f32 v180, v176, v180, 2.0\n"
  "v_mfma_f32_16x16x32_bf16 v[4:7], v[192:195], a[164:167], v[4:7]\n"
  "v_fma_f32 v181, v177, v181, 2.0\n"
  "v_mfma_f32_16x16x32_bf16 v[8:11], v[192:195], a[168:171], v[8:11]\n"
  "v_fma_f32 v182, v178, v182, 2.0\n"
  "v_fma_f32 v183, v179, v183, 2.0\n"
  "v_mfma_f32_16x16x32_bf16 v[8:11], v[196:199], a[184:187], v[8:11]\n"
  "v_cmp_eq_u32 vcc, s99, v233\n"
  "v_mfma_f32_16x16x32_bf16 v[12:15], v[196:199], a[188:191], v[12:15]\n"
  "s_mov_b32 s87, L_PDF_%=-L_start_%=\n"
  "ds_read_b64_tr_b16 v[196:197], v210 offset:16384\n"
  "v_mfma_f32_16x16x32_bf16 v[12:15], v[192:195], a[172:175], v[12:15]\n"
  "ds_read_b64_tr_b16 v[198:199], v210 offset:17408\n"
  "s_waitcnt lgkmcnt(4)\n"
  "v_mfma_f32_16x16x32_bf16 v[28:31], v[200:203], a[172:175], v[28:31]\n"
  "s_cmp_eq_u32 s99, 16\n"
  "s_cselect_b32 s87, L_BDX_%=-L_start_%=, s87\n"
  "ds_read_b64_tr_b16 v[192:193], v210 offset:24576\n"
  "s_waitcnt lgkmcnt(3)\n"
  "v_mfma_f32_16x16x32_bf16 v[28:31], v[204:207], a[188:191], v[28:31]\n"
  "ds_read_b64_tr_b16 v[194:195], v210 offset:25600\n"
  "v_mfma_f32_16x16x32_bf16 v[24:27], v[204:207], a[184:187], v[24:27]\n"
  "v_rcp_f32 v180, v180\n"
  "v_mfma_f32_16x16x32_bf16 v[24:27], v[200:203], a[168:171], v[24:27]\n"
  "v_rcp_f32 v181, v181\n"
  "v_mfma_f32_16x16x32_bf16 v[20:23], v[200:203], a[164:167], v[20:23]\n"
  "v_rcp_f32 v182, v182\n"
  "v_mfma_f32_16x16x32_bf16 v[20:23], v[204:207], a[180:183], v[20:23]\n"
  "v_rcp_f32 v183, v183\n"
  "v_mfma_f32_16x16x32_bf16 v[16:19], v[204:207], a[176:179], v[16:19]\n"
  "ds_read_b64_tr_b16 v[204:205], v211 offset:16384\n"
  "v_fma_f32 v180, -2.0, v180, 1.0\n"
  "v_mfma_f32_16x16x32_bf16 v[16:19], v[200:203], a[160:163], v[16:19]\n"
  "ds_read_b64_tr_b16 v[206:207], v211 offset:17408\n"
  "v_fma_f32 v181, -2.0, v181, 1.0\n"
  "s_waitcnt lgkmcnt(4)\n"
  "v_mfma_f32_16x16x32_bf16 v[32:35], v[196:199], a[160:163], v[32:35]\n"
  "ds_read_b64_tr_b16 v[200:201], v211 offset:24576\n"
  "v_fma_f32 v182, -2.0, v182, 1.0\n"
  "s_waitcnt lgkmcnt(3)\n"
  "v_mfma_f32_16x16x32_bf16 v[32:35], v[192:195], a[176:179], v[32:35]\n"
  "ds_read_b64_tr_b16 v[202:203], v211 offset:25600\n"
  "v_fma_f32 v183, -2.0, v183, 1.0\n"
  "v_mfma_f32_16x16x32_bf16 v[36:39], v[192:195], a[180:183], v[36:39]\n"
  "v_mov_b32 v251, s87\n"
  "v_mul_f32 v160, v160, v180\n"
  "v_mfma_f32_16x16x32_bf16 v[36:39], v[196:199], a[164:167], v[36:39]\n"
  "v_mul_f32 v161, v161, v181\n"
  "v_cndmask_b32 v252, v252, v251, vcc\n"
  "v_mfma_f32_16x16x32_bf16 v[40:43], v[196:199], a[168:171], v[40:43]\n"
  "s_mov_b32 s99, 60\n"
  "v_mul_f32 v162, v162, v182\n"
  "v_mul_f32 v163, v163, v183\n"
  "v_mfma_f32_16x16x32_bf16 v[40:43], v[192:195], a[184:187], v[40:43]\n"
  "v_cvt_pk_bf16_f32 v240, v160, v161\n"
  "v_cvt_pk_bf16_f32 v241, v162, v163\n"
  "v_mfma_f32_16x16x32_bf16 v[44:47], v[192:195], a[188:191], v[44:47]\n"
  "s_cmp_eq_u32 s35, 2\n"
  "s_cselect_b32 s99, 0x2, s99\n"
  "ds_read_b64_tr_b16 v[192:193], v212 offset:16384\n"
  "v_mfma_f32_16x16x32_bf16 v[44:47], v[196:199], a[172:175], v[44:47]\n"
  "s_cmp_eq_u32 s35, 3\n"
  "s_cselect_b32 s99, 0x1, s99\n"
  "ds_read_b64_tr_b16 v[194:195], v212 offset:17408\n"
  "s_waitcnt lgkmcnt(4)\n"
  "v_mfma_f32_16x16x32_bf16 v[60:63], v[204:207], a[172:175], v[60:63]\n"
  "s_add_u32 s99, s99, s31\n"
  "ds_read_b64_tr_b16 v[196:197], v212 offset:24576\n"
  "s_waitcnt lgkmcnt(3)\n"
  "v_mfma_f32_16x16x32_bf16 v[60:63], v[200:203], a[188:191], v[60:63]\n"
  "s_min_u32 s99, s99, 63\n"
  "ds_read_b64_tr_b16 v[198:199], v212 offset:25600\n"
  "v_mfma_f32_16x16x32_bf16 v[56:59], v[200:203], a[184:187], v[56:59]\n"
  "global_store_dwordx2 v238, v[240:241], s[40:41] sc1\n"
  "v_mfma_f32_16x16x32_bf16 v[56:59], v[204:207], a[168:171], v[56:59]\n"
  "v_mul_f32 v184, 0x3fb8aa3b, v164\n"
  "v_mul_f32 v185, 0x3fb8aa3b, v165\n"
  "v_mfma_f32_16x16x32_bf16 v[52:55], v[204:207], a[164:167], v[52:55]\n"
  "v_mul_f32 v186, 0x3fb8aa3b, v166\n"
  "v_mul_f32 v187, 0x3fb8aa3b, v167\n"
  "v_mfma_f32_16x16x32_bf16 v[52:55], v[200:203], a[180:183], v[52:55]\n"
  "v_exp_f32 v184, v184\n"
  "v_mfma_f32_16x16x32_bf16 v[48:51], v[200:203], a[176:179], v[48:51]\n"
  "ds_read_b64_tr_b16 v[200:201], v213 offset:16384\n"
  "v_cmp_eq_u32 vcc, s99, v233\n"
  "v_mfma_f32_16x16x32_bf16 v[48:51], v[204:207], a[160:163], v[48:51]\n"
  "s_mov_b32 s87, L_IP2_%=-L_start_%=\n"
  "ds_read_b64_tr_b16 v[202:203], v213 offset:17408\n"
  "s_waitcnt lgkmcnt(4)\n"
  "v_mfma_f32_16x16x32_bf16 v[64:67], v[192:195], a[160:163], v[64:67]\n"
  "s_cmp_eq_u32 s35, 3\n"
  "s_cselect_b32 s87, L_IP3_%=-L_start_%=, s87\n"
  "ds_read_b64_tr_b16 v[204:205], v213 offset:24576\n"
  "s_waitcnt lgkmcnt(3)\n"
  "v_mfma_f32_16x16x32_bf16 v[64:67], v[196:199], a[176:179], v[64:67]\n"
  "ds_read_b64_tr_b16 v[206:207], v213 offset:25600\n"
  "v_mfma_f32_16x16x32_bf16 v[68:71], v[196:199], a[180:183], v[68:71]\n"
  "v_exp_f32 v185, v185\n"
  "v_mfma_f32_16x16x32_bf16 v[68:71], v[192:195], a[164:167], v[68:71]\n"
  "v_exp_f32 v186, v186\n"
  "v_mfma_f32_16x16x32_bf16 v[72:75], v[192:195], a[168:171], v[72:75]\n"
  "v_exp_f32 v187, v187\n"
  "v_mfma_f32_16x16x32_bf16 v[72:75], v[196:199], a[184:187], v[72:75]\n"
  "v_add_f32 v188, 2.0, v184\n"
  "v_add_f32 v189, 2.0, v185\n"
  "v_mfma_f32_16x16x32_bf16 v[76:79], v[196:199], a[188:191], v[76:79]\n"
  "ds_read_b64_tr_b16 v[196:197], v214 offset:16384\n"
  "v_add_f32 v190, 2.0, v186\n"
  "v_mfma_f32_16x16x32_bf16 v[76:79], v[192:195], a[172:175], v[76:79]\n"
  "ds_read_b64_tr_b16 v[198:199], v214 offset:17408\n"
  "v_add_f32 v191, 2.0, v187\n"
  "s_waitcnt lgkmcnt(4)\n"
  "v_mfma_f32_16x16x32_bf16 v[92:95], v[200:203], a[172:175], v[92:95]\n"
  "ds_read_b64_tr_b16 v[192:193], v214 offset:24576\n"
  "v_fma_f32 v188, v184, v188, 2.0\n"
  "s_waitcnt lgkmcnt(3)\n"
  "v_mfma_f32_16x16x32_bf16 v[92:95], v[204:207], a[188:191], v[92:95]\n"
  "ds_read_b64_tr_b16 v[194:195], v214 offset:25600\n"
  "v_fma_f32 v189, v185, v189, 2.0\n"
  "v_mfma_f32_16x16x32_bf16 v[88:91], v[204:207], a[184:187], v[88:91]\n"
  "v_fma_f32 v190, v186, v190, 2.0\n"
  "v_fma_f32 v191, v187, v191, 2.0\n"
  "v_mfma_f32_16x16x32_bf16 v[88:91], v[200:203], a[168:171], v[88:91]\n"
  "v_rcp_f32 v188, v188\n"
  "v_mfma_f32_16x16x32_bf16 v[84:87], v[200:203], a[164:167], v[84:87]\n"
  "v_rcp_f32 v189, v189\n"
  "v_mfma_f32_16x16x32_bf16 v[84:87], v[204:207], a[180:183], v[84:87]\n"
  "v_rcp_f32 v190, v190\n"
  "v_mfma_f32_16x16x32_bf16 v[80:83], v[204:207], a[176:179], v[80:83]\n"
  "s_cmp_eq_u32 s99, 15\n"
  "s_cselect_b32 s87, L_IA_%=-L_start_%=, s87\n"
  "ds_read_b64_tr_b16 v[204:205], v215 offset:16384\n"
  "v_mfma_f32_16x16x32_bf16 v[80:83], v[200:203], a[160:163], v[80:83]\n"
  "s_cmp_eq_u32 s99, 16\n"
  "s_cselect_b32 s87, L_IB_%=-L_start_%=, s87\n"
  "ds_read_b64_tr_b16 v[206:207], v215 offset:17408\n"
  "s_waitcnt lgkmcnt(4)\n"
  "v_mfma_f32_16x16x32_bf16 v[96:99], v[196:199], a[160:163], v[96:99]\n"
  "ds_read_b64_tr_b16 v[200:201], v215 offset:24576\n"
  "v_mov_b32 v251, s87\n"
  "s_waitcnt lgkmcnt(3)\n"
  "v_mfma_f32_16x16x32_bf16 v[96:99], v[192:195], a[176:179], v[96:99]\n"
  "ds_read_b64_tr_b16 v[202:203], v215 offset:25600\n"
  "v_cndmask_b32 v252, v252, v251, vcc\n"
  "v_mfma_f32_16x16x32_bf16 v[100:103], v[192:195], a[180:183], v[100:103]\n"
  "v_rcp_f32 v191, v191\n"
  "v_mfma_f32_16x16x32_bf16 v[100:103], v[196:199], a[164:167], v[100:103]\n"
  "v_fma_f32 v188, -2.0, v188, 1.0\n"
  "v_fma_f32 v189, -2.0, v189, 1.0\n"
  "v_mfma_f32_16x16x32_bf16 v[104:107], v[196:199], a[168:171], v[104:107]\n"
  "v_fma_f32 v190, -2.0, v190, 1.0\n"
  "v_fma_f32 v191, -2.0, v191, 1.0\n"
  "v_mfma_f32_16x16x32_bf16 v[104:107], v[192:195], a[184:187], v[104:107]\n"
  "v_mul_f32 v164, v164, v188\n"
  "v_mul_f32 v165, v165, v189\n"
  "v_mfma_f32_16x16x32_bf16 v[108:111], v[192:195], a[188:191], v[108:111]\n"
  "s_waitcnt vmcnt(8)\n"
  "s_barrier\n"
  "ds_read_b64_tr_b16 v[192:193], v208 offset:32768\n"
  "v_mfma_f32_16x16x32_bf16 v[108:111], v[196:199], a[172:175], v[108:111]\n"
  "ds_read_b64_tr_b16 v[194:195], v208 offset:33792\n"
  "v_mul_f32 v166, v166, v190\n"
  "s_waitcnt lgkmcnt(4)\n"
  "v_mfma_f32_16x16x32_bf16 v[124:127], v[204:207], a[172:175], v[124:127]\n"
  "ds_read_b64_tr_b16 v[196:197], v208 offset:40960\n"
  "v_mul_f32 v167, v167, v191\n"
  "s_waitcnt lgkmcnt(3)\n"
  "v_mfma_f32_16x16x32_bf16 v[124:127], v[200:203], a[188:191], v[124:127]\n"
  "ds_read_b64_tr_b16 v[198:199], v208 offset:41984\n"
  "v_cvt_pk_bf16_f32 v242, v164, v165\n"
  "v_mfma_f32_16x16x32_bf16 v[120:123], v[200:203], a[184:187], v[120:123]\n"
  "v_cvt_pk_bf16_f32 v243, v166, v167\n"
  "v_mov_b32 v250, L_PFF_%=-L_PF2_%=\n"
  "v_mfma_f32_16x16x32_bf16 v[120:123], v[204:207], a[168:171], v[120:123]\n"
  "global_store_dwordx2 v238, v[242:243], s[42:43] sc1\n"
  "v_mfma_f32_16x16x32_bf16 v[116:119], v[204:207], a[164:167], v[116:119]\n"
  "v_mov_b32 v251, L_SFF_%=-L_SF2_%=\n"
  "v_cmp_gt_u32 vcc, s31, v233\n"
  "v_mfma_f32_16x16x32_bf16 v[116:119], v[200:203], a[180:183], v[116:119]\n"
  "s_nop 0\n"
  "v_cndmask_b32 v251, v250, v251, vcc\n"
  "v_mov_b32 v250, 0\n"
  "v_mfma_f32_16x16x32_bf16 v[112:115], v[200:203], a[176:179], v[112:115]\n"
  "ds_read_b64_tr_b16 v[200:201], v209 offset:32768\n"
  "v_sub_u32 v251, v252, v251\n"
  "v_mfma_f32_16x16x32_bf16 v[112:115], v[204:207], a[160:163], v[112:115]\n"
  "ds_read_b64_tr_b16 v[202:203], v209 offset:33792\n"
  "s_waitcnt vmcnt(7)\n"
  "v_cmp_ne_u64 s[96:97], s[16:17], v[234:235]\n"
  "s_nop 0\n"
  "s_cmp_lg_u64 s[96:97], 0\n"
  "s_cbranch_scc1 L_kv_retry_%=\n"
  "L_kv_ok_%=:\n"
  "L_stg26_%=:\n"
  "s_waitcnt lgkmcnt(4)\n"
  "v_mfma_f32_16x16x32_bf16 v[0:3], v[192:195], a[192:195], v[0:3]\n"
  "ds_read_b64_tr_b16 v[204:205], v209 offset:40960\n"
  "v_mul_f32 v176, 0x3fb8aa3b, v168\n"
  "s_waitcnt lgkmcnt(3)\n"
  "v_mfma_f32_16x16x32_bf16 v[0:3], v[196:199], a[208:211], v[0:3]\n"
  "ds_read_b64_tr_b16 v[206:207], v209 offset:41984\n"
  "v_mul_f32 v177, 0x3fb8aa3b, v169\n"
  "v_mfma_f32_16x16x32_bf16 v[4:7], v[196:199], a[212:215], v[4:7]\n"
  "v_mul_f32 v178, 0x3fb8aa3b, v170\n"
  "v_mul_f32 v179, 0x3fb8aa3b, v171\n"
  "v_mfma_f32_16x16x32_bf16 v[4:7], v[192:195], a[196:199], v[4:7]\n"
  "s_add_u32 m0, s49, 0\n"
  "v_exp_f32 v176, v176\n"
  "v_mfma_f32_16x16x32_bf16 v[8:11], v[192:195], a[200:203], v[8:11]\n"
  "v_mov_b32_dpp v250, v252 row_shl:1 row_mask:0xf bank_mask:0xf\n"
  "v_cmp_eq_u32 vcc, v250, v252\n"
  "v_mfma_f32_16x16x32_bf16 v[8:11], v[196:199], a[216:219], v[8:11]\n"
  "v_exp_f32 v177, v177\n"
  "v_mfma_f32_16x16x32_bf16 v[12:15], v[196:199], a[220:223], v[12:15]\n"
  "ds_read_b64_tr_b16 v[196:197], v210 offset:32768\n"
  "v_mfma_f32_16x16x32_bf16 v[12:15], v[192:195], a[204:207], v[12:15]\n"
  "s_nop 0\n"
  "ds_read_b64_tr_b16 v[198:199], v210 offset:33792\n"
  "s_waitcnt lgkmcnt(4)\n"
  "v_mfma_f32_16x16x32_bf16 v[28:31], v[200:203], a[204:207], v[28:31]\n"
  "ds_read_b64_tr_b16 v[192:193], v210 offset:40960\n"
  "s_waitcnt lgkmcnt(3)\n"
  "v_mfma_f32_16x16x32_bf16 v[28:31], v[204:207], a[220:223], v[28:31]\n"
  "ds_read_b64_tr_b16 v[194:195], v210 offset:41984\n"
  "v_mfma_f32_16x16x32_bf16 v[24:27], v[204:207], a[216:219], v[24:27]\n"
  "v_exp_f32 v178, v178\n"
  "v_mfma_f32_16x16x32_bf16 v[24:27], v[200:203], a[200:203], v[24:27]\n"
  "v_exp_f32 v179, v179\n"
  "v_mfma_f32_16x16x32_bf16 v[20:23], v[200:203], a[196:199], v[20:23]\n"
  "v_add_f32 v180, 2.0, v176\n"
  "v_add_f32 v181, 2.0, v177\n"
  "v_mfma_f32_16x16x32_bf16 v[20:23], v[204:207], a[212:215], v[20:23]\n"
  "v_add_f32 v182, 2.0, v178\n"
  "v_add_f32 v183, 2.0, v179\n"
  "v_mfma_f32_16x16x32_bf16 v[16:19], v[204:207], a[208:211], v[16:19]\n"
  "ds_read_b64_tr_b16 v[204:205], v211 offset:32768\n"
  "v_fma_f32 v180, v176, v180, 2.0\n"
  "v_mfma_f32_16x16x32_bf16 v[16:19], v[200:203], a[192:195], v[16:19]\n"
  "ds_read_b64_tr_b16 v[206:207], v211 offset:33792\n"
  "v_fma_f32 v181, v177, v181, 2.0\n"
  "s_waitcnt lgkmcnt(4)\n"
  "v_mfma_f32_16x16x32_bf16 v[32:35], v[196:199], a[192:195], v[32:35]\n"
  "ds_read_b64_tr_b16 v[200:201], v211 offset:40960\n"
  "v_fma_f32 v182, v178, v182, 2.0\n"
  "s_waitcnt lgkmcnt(3)\n"
  "v_mfma_f32_16x16x32_bf16 v[32:35], v[192:195], a[208:211], v[32:35]\n"
  "ds_read_b64_tr_b16 v[202:203], v211 offset:41984\n"
  "v_fma_f32 v183, v179, v183, 2.0\n"
  "v_mfma_f32_16x16x32_bf16 v[36:39], v[192:195], a[212:215], v[36:39]\n"
  "global_load_lds_dwordx4 v232, s[20:21] offset:0 sc1\n"
  "v_cndmask_b32 v252, v252, v251, vcc\n"
  "v_mfma_f32_16x16x32_bf16 v[36:39], v[196:199], a[196:199], v[36:39]\n"
  "s_sub_u32 s87, 15, s57\n"
  "v_rcp_f32 v180, v180\n"
  "v_mfma_f32_16x16x32_bf16 v[40:43], v[196:199], a[200:203], v[40:43]\n"
  "s_min_u32 s87, s87, s57\n"
  "v_rcp_f32 v181, v181\n"
  "v_mfma_f32_16x16x32_bf16 v[40:43], v[192:195], a[216:219], v[40:43]\n"
  "s_lshl_b32 s87, s87, 5\n"
  "v_rcp_f32 v182, v182\n"
  "v_mfma_f32_16x16x32_bf16 v[44:47], v[192:195], a[220:223], v[44:47]\n"
  "ds_read_b64_tr_b16 v[192:193], v212 offset:32768\n"
  "v_and_b32 v253, 31, v233\n"
  "v_mfma_f32_16x16x32_bf16 v[44:47], v[196:199], a[204:207], v[44:47]\n"
  "ds_read_b64_tr_b16 v[194:195], v212 offset:33792\n"
  "s_waitcnt lgkmcnt(4)\n"
  "v_mfma_f32_16x16x32_bf16 v[60:63], v[204:207], a[204:207], v[60:63]\n"
  "ds_read_b64_tr_b16 v[196:197], v212 offset:40960\n"
  "v_lshlrev_b32 v253, 0, v253\n"
  "s_waitcnt lgkmcnt(3)\n"
  "v_mfma_f32_16x16x32_bf16 v[60:63], v[200:203], a[220:223], v[60:63]\n"
  "ds_read_b64_tr_b16 v[198:199], v212 offset:41984\n"
  "v_mfma_f32_16x16x32_bf16 v[56:59], v[200:203], a[216:219], v[56:59]\n"
  "v_rcp_f32 v183, v183\n"
  "v_mfma_f32_16x16x32_bf16 v[56:59], v[204:207], a[200:203], v[56:59]\n"
  "v_fma_f32 v180, -2.0, v180, 1.0\n"
  "v_fma_f32 v181, -2.0, v181, 1.0\n"
  "v_mfma_f32_16x16x32_bf16 v[52:55], v[204:207], a[196:199], v[52:55]\n"
  "v_fma_f32 v182, -2.0, v182, 1.0\n"
  "v_fma_f32 v183, -2.0, v183, 1.0\n"
  "v_mfma_f32_16x16x32_bf16 v[52:55], v[200:203], a[212:215], v[52:55]\n"
  "v_mul_f32 v168, v168, v180\n"
  "v_mul_f32 v169, v169, v181\n"
  "v_mfma_f32_16x16x32_bf16 v[48:51], v[200:203], a[208:211], v[48:51]\n"
  "ds_read_b64_tr_b16 v[200:201], v213 offset:32768\n"
  "v_mul_f32 v170, v170, v182\n"
  "v_mfma_f32_16x16x32_bf16 v[48:51], v[204:207], a[192:195], v[48:51]\n"
  "ds_read_b64_tr_b16 v[202:203], v213 offset:33792\n"
  "v_mul_f32 v171, v171, v183\n"
  "s_waitcnt lgkmcnt(4)\n"
  "v_mfma_f32_16x16x32_bf16 v[64:67], v[192:195], a[192:195], v[64:67]\n"
  "ds_read_b64_tr_b16 v[204:205], v213 offset:40960\n"
  "v_cvt_pk_bf16_f32 v240, v168, v169\n"
  "s_waitcnt lgkmcnt(3)\n"
  "v_mfma_f32_16x16x32_bf16 v[64:67], v[196:199], a[208:211], v[64:67]\n"
  "ds_read_b64_tr_b16 v[206:207], v213 offset:41984\n"
  "v_cvt_pk_bf16_f32 v241, v170, v171\n"
  "v_mfma_f32_16x16x32_bf16 v[68:71], v[196:199], a[212:215], v[68:71]\n"
  "global_store_dwordx2 v238, v[240:241], s[44:45] sc1\n"
  "v_mfma_f32_16x16x32_bf16 v[68:71], v[192:195], a[196:199], v[68:71]\n"
  "global_load_lds_dwordx4 v232, s[20:21] offset:1024 sc1\n"
  "v_mul_f32 v184, 0x3fb8aa3b, v172\n"
  "v_mfma_f32_16x16x32_bf16 v[72:75], v[192:195], a[200:203], v[72:75]\n"
  "v_mul_f32 v185, 0x3fb8aa3b, v173\n"
  "v_mul_f32 v186, 0x3fb8aa3b, v174\n"
  "v_mfma_f32_16x16x32_bf16 v[72:75], v[196:199], a[216:219], v[72:75]\n"
  "v_mul_f32 v187, 0x3fb8aa3b, v175\n"
  "v_add_u32 v253, s87, v253\n"
  "v_mfma_f32_16x16x32_bf16 v[76:79], v[196:199], a[220:223], v[76:79]\n"
  "ds_read_b64_tr_b16 v[196:197], v214 offset:32768\n"
  "v_lshlrev_b32 v253, 9, v253\n"
  "v_mfma_f32_16x16x32_bf16 v[76:79], v[192:195], a[204:207], v[76:79]\n"
  "s_lshl_b32 s87, s35, 14\n"
  "s_add_u32 s87, s87, 131072\n"
  "ds_read_b64_tr_b16 v[198:199], v214 offset:33792\n"
  "s_waitcnt lgkmcnt(4)\n"
  "v_mfma_f32_16x16x32_bf16 v[92:95], v[200:203], a[204:207], v[92:95]\n"
  "s_lshl_b32 s99, s35, 15\n"
  "s_add_u32 s99, s99, 16384\n"
  "ds_read_b64_tr_b16 v[192:193], v214 offset:40960\n"
  "s_waitcnt lgkmcnt(3)\n"
  "v_mfma_f32_16x16x32_bf16 v[92:95], v[204:207], a[220:223], v[92:95]\n"
  "ds_read_b64_tr_b16 v[194:195], v214 offset:41984\n"
  "v_mfma_f32_16x16x32_bf16 v[88:91], v[204:207], a[216:219], v[88:91]\n"
  "v_exp_f32 v184, v184\n"
  "v_mfma_f32_16x16x32_bf16 v[88:91], v[200:203], a[200:203], v[88:91]\n"
  "v_exp_f32 v185, v185\n"
  "v_mfma_f32_16x16x32_bf16 v[84:87], v[200:203], a[196:199], v[84:87]\n"
  "v_exp_f32 v186, v186\n"
  "v_mfma_f32_16x16x32_bf16 v[84:87], v[204:207], a[212:215], v[84:87]\n"
  "v_exp_f32 v187, v187\n"
  "v_mfma_f32_16x16x32_bf16 v[80:83], v[204:207], a[208:211], v[80:83]\n"
  "ds_read_b64_tr_b16 v[204:205], v215 offset:32768\n"
  "v_add_f32 v188, 2.0, v184\n"
  "v_mfma_f32_16x16x32_bf16 v[80:83], v[200:203], a[192:195], v[80:83]\n"
  "ds_read_b64_tr_b16 v[206:207], v215 offset:33792\n"
  "v_add_f32 v189, 2.0, v185\n"
  "s_waitcnt lgkmcnt(4)\n"
  "v_mfma_f32_16x16x32_bf16 v[96:99], v[196:199], a[192:195], v[96:99]\n"
  "ds_read_b64_tr_b16 v[200:201], v215 offset:40960\n"
  "v_add_f32 v190, 2.0, v186\n"
  "s_waitcnt lgkmcnt(3)\n"
  "v_mfma_f32_16x16x32_bf16 v[96:99], v[192:195], a[208:211], v[96:99]\n"
  "ds_read_b64_tr_b16 v[202:203], v215 offset:41984\n"
  "v_add_f32 v191, 2.0, v187\n"
  "v_mfma_f32_16x16x32_bf16 v[100:103], v[192:195], a[212:215], v[100:103]\n"
  "global_load_lds_dwordx4 v232, s[20:21] offset:2048 sc1\n"
  "v_fma_f32 v188, v184, v188, 2.0\n"
  "v_mfma_f32_16x16x32_bf16 v[100:103], v[196:199], a[196:199], v[100:103]\n"
  "v_fma_f32 v189, v185, v189, 2.0\n"
  "v_fma_f32 v190, v186, v190, 2.0\n"
  "v_mfma_f32_16x16x32_bf16 v[104:107], v[196:199], a[200:203], v[104:107]\n"
  "global_load_lds_dwordx4 v232, s[20:21] offset:3072 sc1\n"
  "v_fma_f32 v191, v187, v191, 2.0\n"
  "v_mfma_f32_16x16x32_bf16 v[104:107], v[192:195], a[216:219], v[104:107]\n"
  "v_rcp_f32 v188, v188\n"
  "v_mfma_f32_16x16x32_bf16 v[108:111], v[192:195], a[220:223], v[108:111]\n"
  "s_waitcnt vmcnt(7)\n"
  "s_barrier\n"
  "ds_read_b64_tr_b16 v[192:193], v208 offset:49152\n"
  "v_mfma_f32_16x16x32_bf16 v[108:111], v[196:199], a[204:207], v[108:111]\n"
  "s_cmp_lt_u32 s35, 2\n"
  "s_cselect_b32 s87, s87, s99\n"
  "ds_read_b64_tr_b16 v[194:195], v208 offset:50176\n"
  "s_waitcnt lgkmcnt(4)\n"
  "v_mfma_f32_16x16x32_bf16 v[124:127], v[204:207], a[204:207], v[124:127]\n"
  "s_add_u32 s87, s87, s38\n"
  "ds_read_b64_tr_b16 v[196:197], v208 offset:57344\n"
  "v_add_u32 v254, s87, v232\n"
  "s_waitcnt lgkmcnt(3)\n"
  "v_mfma_f32_16x16x32_bf16 v[124:127], v[200:203], a[220:223], v[124:127]\n"
  "s_add_u32 s20, s20, 16384\n"
  "s_addc_u32 s21, s21, 0\n"
  "ds_read_b64_tr_b16 v[198:199], v208 offset:58368\n"
  "v_mfma_f32_16x16x32_bf16 v[120:123], v[200:203], a[216:219], v[120:123]\n"
  "v_rcp_f32 v189, v189\n"
  "v_mfma_f32_16x16x32_bf16 v[120:123], v[204:207], a[200:203], v[120:123]\n"
  "v_rcp_f32 v190, v190\n"
  "v_mfma_f32_16x16x32_bf16 v[116:119], v[204:207], a[196:199], v[116:119]\n"
  "v_rcp_f32 v191, v191\n"
  "v_mfma_f32_16x16x32_bf16 v[116:119], v[200:203], a[212:215], v[116:119]\n"
  "v_fma_f32 v188, -2.0, v188, 1.0\n"
  "v_fma_f32 v189, -2.0, v189, 1.0\n"
  "v_mfma_f32_16x16x32_bf16 v[112:115], v[200:203], a[208:211], v[112:115]\n"
  "ds_read_b64_tr_b16 v[200:201], v209 offset:49152\n"
  "v_fma_f32 v190, -2.0, v190, 1.0\n"
  "v_mfma_f32_16x16x32_bf16 v[112:115], v[204:207], a[192:195], v[112:115]\n"
  "ds_read_b64_tr_b16 v[202:203], v209 offset:50176\n"
  "v_fma_f32 v191, -2.0, v191, 1.0\n"
  "v_mul_f32 v172, v172, v188\n"
  "v_mul_f32 v173, v173, v189\n"
  "v_mul_f32 v174, v174, v190\n"
  "v_mul_f32 v175, v175, v191\n"
  "v_cvt_pk_bf16_f32 v242, v172, v173\n"
  "v_cvt_pk_bf16_f32 v243, v174, v175\n"
  "global_store_dwordx2 v238, v[242:243], s[46:47] sc1\n"
  "v_mov_b32 v224, 0\n"
  "v_mov_b32 v225, 0\n"
  "v_mov_b32 v226, 0\n"
  "L_stg27_%=:\n"
  "s_waitcnt lgkmcnt(4)\n"
  "v_mfma_f32_16x16x32_bf16 v[0:3], v[192:195], a[224:227], v[0:3]\n"
  "ds_read_b64_tr_b16 v[204:205], v209 offset:57344\n"
  "s_waitcnt lgkmcnt(3)\n"
  "v_mfma_f32_16x16x32_bf16 v[0:3], v[196:199], a[240:243], v[0:3]\n"
  "ds_read_b64_tr_b16 v[206:207], v209 offset:58368\n"
  "v_mfma_f32_16x16x32_bf16 v[4:7], v[196:199], a[244:247], v[4:7]\n"
  "v_mov_b32 v227, 0\n"
  "v_add_u32 v217, 0x8000, v232\n"
  "v_mfma_f32_16x16x32_bf16 v[4:7], v[192:195], a[228:231], v[4:7]\n"
  "s_add_u32 m0, s49, 32768\n"
  "s_add_u32 s87, s38, 65536\n"
  "v_mfma_f32_16x16x32_bf16 v[8:11], v[192:195], a[232:235], v[8:11]\n"
  "v_add_u32 v216, s87, v232\n"
  "v_mfma_f32_16x16x32_bf16 v[8:11], v[196:199], a[248:251], v[8:11]\n"
  "v_mov_b32 v218, 0xff800000\n"
  "v_mfma_f32_16x16x32_bf16 v[12:15], v[196:199], a[252:255], v[12:15]\n"
  "ds_read_b64_tr_b16 v[196:197], v210 offset:49152\n"
  "v_add_u32 v228, 174080, v232\n"
  "v_mfma_f32_16x16x32_bf16 v[12:15], v[192:195], a[236:239], v[12:15]\n"
  "ds_read_b64_tr_b16 v[198:199], v210 offset:50176\n"
  "v_lshrrev_b32 v251, 4, v233\n"
  "s_waitcnt lgkmcnt(4)\n"
  "v_mfma_f32_16x16x32_bf16 v[28:31], v[200:203], a[236:239], v[28:31]\n"
  "ds_read_b64_tr_b16 v[192:193], v210 offset:57344\n"
  "v_lshlrev_b32 v250, 1, v251\n"
  "s_waitcnt lgkmcnt(3)\n"
  "v_mfma_f32_16x16x32_bf16 v[28:31], v[204:207], a[252:255], v[28:31]\n"
  "ds_read_b64_tr_b16 v[194:195], v210 offset:58368\n"
  "v_and_b32 v229, 15, v233\n"
  "v_mfma_f32_16x16x32_bf16 v[24:27], v[204:207], a[248:251], v[24:27]\n"
  "v_xor_b32 v250, v250, v229\n"
  "v_lshlrev_b32 v251, 10, v251\n"
  "v_mfma_f32_16x16x32_bf16 v[24:27], v[200:203], a[232:235], v[24:27]\n"
  "v_lshl_add_u32 v229, v250, 4, v251\n"
  "v_mfma_f32_16x16x32_bf16 v[20:23], v[200:203], a[228:231], v[20:23]\n"
  "global_load_lds_dwordx4 v232, s[20:21] offset:0 sc1\n"
  "v_xor_b32 v230, 128, v229\n"
  "v_mfma_f32_16x16x32_bf16 v[20:23], v[204:207], a[244:247], v[20:23]\n"
  "v_add_u32 v229, 2048, v229\n"
  "v_mfma_f32_16x16x32_bf16 v[16:19], v[204:207], a[240:243], v[16:19]\n"
  "ds_read_b64_tr_b16 v[204:205], v211 offset:49152\n"
  "v_add_u32 v230, 5120, v230\n"
  "v_mfma_f32_16x16x32_bf16 v[16:19], v[200:203], a[224:227], v[16:19]\n"
  "ds_read_b64_tr_b16 v[206:207], v211 offset:50176\n"
  "s_waitcnt lgkmcnt(4)\n"
  "v_mfma_f32_16x16x32_bf16 v[32:35], v[196:199], a[224:227], v[32:35]\n"
  "ds_read_b64_tr_b16 v[200:201], v211 offset:57344\n"
  "s_waitcnt lgkmcnt(3)\n"
  "v_mfma_f32_16x16x32_bf16 v[32:35], v[192:195], a[240:243], v[32:35]\n"
  "ds_read_b64_tr_b16 v[202:203], v211 offset:58368\n"
  "v_mfma_f32_16x16x32_bf16 v[36:39], v[192:195], a[244:247], v[36:39]\n"
  "s_getpc_b64 s[26:27]\n"
  "L_pc2_%=:\n"
  "s_sub_u32 s26, s26, L_pc2_%=-L_start_%=\n"
  "s_subb_u32 s27, s27, 0\n"
  "v_mfma_f32_16x16x32_bf16 v[36:39], v[196:199], a[228:231], v[36:39]\n"
  "v_cvt_pk_bf16_f32 v244, v0, v1\n"
  "v_cvt_pk_bf16_f32 v245, v2, v3\n"
  "v_mfma_f32_16x16x32_bf16 v[40:43], v[196:199], a[232:235], v[40:43]\n"
  "v_cvt_pk_bf16_f32 v246, v16, v17\n"
  "v_cvt_pk_bf16_f32 v247, v18, v19\n"
  "v_mfma_f32_16x16x32_bf16 v[40:43], v[192:195], a[248:251], v[40:43]\n"
  "ds_write_b128 v254, v[244:247] offset:0\n"
  "v_mfma_f32_16x16x32_bf16 v[44:47], v[192:195], a[252:255], v[44:47]\n"
  "ds_read_b64_tr_b16 v[192:193], v212 offset:49152\n"
  "v_cvt_pk_bf16_f32 v248, v4, v5\n"
  "v_mfma_f32_16x16x32_bf16 v[44:47], v[196:199], a[236:239], v[44:47]\n"
  "ds_read_b64_tr_b16 v[194:195], v212 offset:50176\n"
  "v_cvt_pk_bf16_f32 v249, v6, v7\n"
  "s_waitcnt lgkmcnt(5)\n"
  "v_mfma_f32_16x16x32_bf16 v[60:63], v[204:207], a[236:239], v[60:63]\n"
  "ds_read_b64_tr_b16 v[196:197], v212 offset:57344\n"
  "v_cvt_pk_bf16_f32 v250, v20, v21\n"
  "s_waitcnt lgkmcnt(4)\n"
  "v_mfma_f32_16x16x32_bf16 v[60:63], v[200:203], a[252:255], v[60:63]\n"
  "ds_read_b64_tr_b16 v[198:199], v212 offset:58368\n"
  "v_cvt_pk_bf16_f32 v251, v22, v23\n"
  "v_mfma_f32_16x16x32_bf16 v[56:59], v[200:203], a[248:251], v[56:59]\n"
  "global_load_lds_dwordx4 v232, s[20:21] offset:1024 sc1\n"
  "v_mfma_f32_16x16x32_bf16 v[56:59], v[204:207], a[232:235], v[56:59]\n"
  "ds_write_b128 v254, v[248:251] offset:4096\n"
  "v_mfma_f32_16x16x32_bf16 v[52:55], v[204:207], a[228:231], v[52:55]\n"
  "v_cvt_pk_bf16_f32 v244, v8, v9\n"
  "v_cvt_pk_bf16_f32 v245, v10, v11\n"
  "v_mfma_f32_16x16x32_bf16 v[52:55], v[200:203], a[244:247], v[52:55]\n"
  "v_cvt_pk_bf16_f32 v246, v24, v25\n"
  "v_cvt_pk_bf16_f32 v247, v26, v27\n"
  "v_mfma_f32_16x16x32_bf16 v[48:51], v[200:203], a[240:243], v[48:51]\n"
  "ds_read_b64_tr_b16 v[200:201], v213 offset:49152\n"
  "v_mfma_f32_16x16x32_bf16 v[48:51], v[204:207], a[224:227], v[48:51]\n"
  "ds_read_b64_tr_b16 v[202:203], v213 offset:50176\n"
  "s_waitcnt lgkmcnt(5)\n"
  "v_mfma_f32_16x16x32_bf16 v[64:67], v[192:195], a[224:227], v[64:67]\n"
  "ds_read_b64_tr_b16 v[204:205], v213 offset:57344\n"
  "s_waitcnt lgkmcnt(4)\n"
  "v_mfma_f32_16x16x32_bf16 v[64:67], v[196:199], a[240:243], v[64:67]\n"
  "ds_read_b64_tr_b16 v[206:207], v213 offset:58368\n"
  "v_mfma_f32_16x16x32_bf16 v[68:71], v[196:199], a[244:247], v[68:71]\n"
  "ds_write_b128 v254, v[244:247] offset:8192\n"
  "v_mfma_f32_16x16x32_bf16 v[68:71], v[192:195], a[228:231], v[68:71]\n"
  "v_cvt_pk_bf16_f32 v248, v12, v13\n"
  "v_cvt_pk_bf16_f32 v249, v14, v15\n"
  "v_mfma_f32_16x16x32_bf16 v[72:75], v[192:195], a[232:235], v[72:75]\n"
  "v_cvt_pk_bf16_f32 v250, v28, v29\n"
  "v_cvt_pk_bf16_f32 v251, v30, v31\n"
  "v_mfma_f32_16x16x32_bf16 v[72:75], v[196:199], a[248:251], v[72:75]\n"
  "ds_write_b128 v254, v[248:251] offset:12288\n"
  "v_mfma_f32_16x16x32_bf16 v[76:79], v[196:199], a[252:255], v[76:79]\n"
  "ds_read_b64_tr_b16 v[196:197], v214 offset:49152\n"
  "v_cvt_pk_bf16_f32 v244, v32, v33\n"
  "v_mfma_f32_16x16x32_bf16 v[76:79], v[192:195], a[236:239], v[76:79]\n"
  "ds_read_b64_tr_b16 v[198:199], v214 offset:50176\n"
  "v_cvt_pk_bf16_f32 v245, v34, v35\n"
  "s_waitcnt lgkmcnt(6)\n"
  "v_mfma_f32_16x16x32_bf16 v[92:95], v[200:203], a[236:239], v[92:95]\n"
  "ds_read_b64_tr_b16 v[192:193], v214 offset:57344\n"
  "v_cvt_pk_bf16_f32 v246, v48, v49\n"
  "s_waitcnt lgkmcnt(5)\n"
  "v_mfma_f32_16x16x32_bf16 v[92:95], v[204:207], a[252:255], v[92:95]\n"
  "ds_read_b64_tr_b16 v[194:195], v214 offset:58368\n"
  "v_cvt_pk_bf16_f32 v247, v50, v51\n"
  "v_mfma_f32_16x16x32_bf16 v[88:91], v[204:207], a[248:251], v[88:91]\n"
  "global_load_lds_dwordx4 v232, s[20:21] offset:2048 sc1\n"
  "v_mfma_f32_16x16x32_bf16 v[88:91], v[200:203], a[232:235], v[88:91]\n"
  "ds_write_b128 v254, v[244:247] offset:1024\n"
  "v_mfma_f32_16x16x32_bf16 v[84:87], v[200:203], a[228:231], v[84:87]\n"
  "v_cvt_pk_bf16_f32 v248, v36, v37\n"
  "v_cvt_pk_bf16_f32 v249, v38, v39\n"
  "v_mfma_f32_16x16x32_bf16 v[84:87], v[204:207], a[244:247], v[84:87]\n"
  "v_cvt_pk_bf16_f32 v250, v52, v53\n"
  "v_cvt_pk_bf16_f32 v251, v54, v55\n"
  "v_mfma_f32_16x16x32_bf16 v[80:83], v[204:207], a[240:243], v[80:83]\n"
  "ds_read_b64_tr_b16 v[204:205], v215 offset:49152\n"
  "v_mfma_f32_16x16x32_bf16 v[80:83], v[200:203], a[224:227], v[80:83]\n"
  "ds_read_b64_tr_b16 v[206:207], v215 offset:50176\n"
  "s_waitcnt lgkmcnt(5)\n"
  "v_mfma_f32_16x16x32_bf16 v[96:99], v[196:199], a[224:227], v[96:99]\n"
  "ds_read_b64_tr_b16 v[200:201], v215 offset:57344\n"
  "s_waitcnt lgkmcnt(4)\n"
  "v_mfma_f32_16x16x32_bf16 v[96:99], v[192:195], a[240:243], v[96:99]\n"
  "ds_read_b64_tr_b16 v[202:203], v215 offset:58368\n"
  "v_mfma_f32_16x16x32_bf16 v[100:103], v[192:195], a[244:247], v[100:103]\n"
  "ds_write_b128 v254, v[248:251] offset:5120\n"
  "v_mfma_f32_16x16x32_bf16 v[100:103], v[196:199], a[228:231], v[100:103]\n"
  "v_cvt_pk_bf16_f32 v244, v40, v41\n"
  "v_cvt_pk_bf16_f32 v245, v42, v43\n"
  "v_mfma_f32_16x16x32_bf16 v[104:107], v[196:199], a[232:235], v[104:107]\n"
  "v_cvt_pk_bf16_f32 v246, v56, v57\n"
  "v_cvt_pk_bf16_f32 v247, v58, v59\n"
  "v_mfma_f32_16x16x32_bf16 v[104:107], v[192:195], a[248:251], v[104:107]\n"
  "global_load_lds_dwordx4 v232, s[20:21] offset:3072 sc1\n"
  "s_add_u32 s20, s20, 16384\n"
  "s_addc_u32 s21, s21, 0\n"
  "v_mfma_f32_16x16x32_bf16 v[108:111], v[192:195], a[252:255], v[108:111]\n"
  "ds_write_b128 v254, v[244:247] offset:9216\n"
  "v_mfma_f32_16x16x32_bf16 v[108:111], v[196:199], a[236:239], v[108:111]\n"
  "v_cvt_pk_bf16_f32 v248, v44, v45\n"
  "v_cvt_pk_bf16_f32 v249, v46, v47\n"
  "s_waitcnt lgkmcnt(4)\n"
  "v_mfma_f32_16x16x32_bf16 v[124:127], v[204:207], a[236:239], v[124:127]\n"
  "v_cvt_pk_bf16_f32 v250, v60, v61\n"
  "v_cvt_pk_bf16_f32 v251, v62, v63\n"
  "s_waitcnt lgkmcnt(2)\n"
  "v_mfma_f32_16x16x32_bf16 v[124:127], v[200:203], a[252:255], v[124:127]\n"
  "ds_write_b128 v254, v[248:251] offset:13312\n"
  "v_mfma_f32_16x16x32_bf16 v[120:123], v[200:203], a[248:251], v[120:123]\n"
  "v_cvt_pk_bf16_f32 v244, v64, v65\n"
  "v_cvt_pk_bf16_f32 v245, v66, v67\n"
  "v_mfma_f32_16x16x32_bf16 v[120:123], v[204:207], a[232:235], v[120:123]\n"
  "v_cvt_pk_bf16_f32 v246, v80, v81\n"
  "v_cvt_pk_bf16_f32 v247, v82, v83\n"
  "v_mfma_f32_16x16x32_bf16 v[116:119], v[204:207], a[228:231], v[116:119]\n"
  "ds_write_b128 v254, v[244:247] offset:2048\n"
  "v_mfma_f32_16x16x32_bf16 v[116:119], v[200:203], a[244:247], v[116:119]\n"
  "v_cvt_pk_bf16_f32 v248, v68, v69\n"
  "v_cvt_pk_bf16_f32 v249, v70, v71\n"
  "v_mfma_f32_16x16x32_bf16 v[112:115], v[200:203], a[240:243], v[112:115]\n"
  "v_cvt_pk_bf16_f32 v250, v84, v85\n"
  "v_cvt_pk_bf16_f32 v251, v86, v87\n"
  "v_mfma_f32_16x16x32_bf16 v[112:115], v[204:207], a[224:227], v[112:115]\n"
  "ds_write_b128 v254, v[248:251] offset:6144\n"
  "v_cvt_pk_bf16_f32 v244, v72, v73\n"
  "v_cvt_pk_bf16_f32 v245, v74, v75\n"
  "v_cvt_pk_bf16_f32 v246, v88, v89\n"
  "v_cvt_pk_bf16_f32 v247, v90, v91\n"
  "ds_write_b128 v254, v[244:247] offset:10240\n"
  "v_cvt_pk_bf16_f32 v248, v76, v77\n"
  "v_cvt_pk_bf16_f32 v249, v78, v79\n"
  "v_cvt_pk_bf16_f32 v250, v92, v93\n"
  "v_cvt_pk_bf16_f32 v251, v94, v95\n"
  "ds_write_b128 v254, v[248:251] offset:14336\n"
  "L_trans_%=:\n"
  "v_mov_b32 v235, 0x3f803f80\n"
  "v_accvgpr_write_b32 a192, v235\n"
  "v_accvgpr_write_b32 a193, v235\n"
  "v_accvgpr_write_b32 a194, v235\n"
  "v_accvgpr_write_b32 a195, v235\n"
  "v_cvt_pk_bf16_f32 v244, v96, v97\n"
  "v_cvt_pk_bf16_f32 v245, v98, v99\n"
  "v_cvt_pk_bf16_f32 v246, v112, v113\n"
  "v_cvt_pk_bf16_f32 v247, v114, v115\n"
  "v_mfma_f32_32x32x16_bf16 v[200:215], v[224:227], v[224:227], 0\n"
  "ds_write_b128 v254, v[244:247] offset:3072\n"
  "v_cvt_pk_bf16_f32 v248, v100, v101\n"
  "v_cvt_pk_bf16_f32 v249, v102, v103\n"
  "v_cvt_pk_bf16_f32 v250, v116, v117\n"
  "v_mfma_f32_32x32x16_bf16 v[0:15], v[224:227], v[224:227], 0\n"
  "v_cvt_pk_bf16_f32 v251, v118, v119\n"
  "ds_write_b128 v254, v[248:251] offset:7168\n"
  "v_cvt_pk_bf16_f32 v244, v104, v105\n"
  "v_cvt_pk_bf16_f32 v245, v106, v107\n"
  "v_mfma_f32_32x32x16_bf16 v[16:31], v[224:227], v[224:227], 0\n"
  "v_cvt_pk_bf16_f32 v246, v120, v121\n"
  "v_cvt_pk_bf16_f32 v247, v122, v123\n"
  "ds_write_b128 v254, v[244:247] offset:11264\n"
  "v_cvt_pk_bf16_f32 v248, v108, v109\n"
  "v_mfma_f32_32x32x16_bf16 v[32:47], v[224:227], v[224:227], 0\n"
  "v_cvt_pk_bf16_f32 v249, v110, v111\n"
  "v_cvt_pk_bf16_f32 v250, v124, v125\n"
  "v_cvt_pk_bf16_f32 v251, v126, v127\n"
  "ds_write_b128 v254, v[248:251] offset:15360\n"
  "v_mfma_f32_32x32x16_bf16 v[48:63], v[224:227], v[224:227], 0\n"
  "s_waitcnt vmcnt(0)\n"
  "s_waitcnt lgkmcnt(0)\n"
  "s_barrier\n"
  "ds_read_b128 a[128:131], v216 offset:0\n"
  "ds_read_b128 a[132:135], v216 offset:1024\n"
  "ds_read_b128 a[136:139], v216 offset:2048\n"
  "ds_read_b128 a[140:143], v216 offset:3072\n"
  "ds_read_b128 a[0:3], v254 offset:0\n"
  "ds_read_b128 a[16:19], v254 offset:4096\n"
  "ds_read_b128 a[32:35], v254 offset:8192\n"
  "ds_read_b128 a[48:51], v254 offset:12288\n"
  "ds_read_b128 a[4:7], v254 offset:1024\n"
  "ds_read_b128 a[20:23], v254 offset:5120\n"
  "ds_read_b128 a[36:39], v254 offset:9216\n"
  "ds_read_b128 a[52:55], v254 offset:13312\n"
  "ds_read_b128 a[8:11], v254 offset:2048\n"
  "ds_read_b128 a[24:27], v254 offset:6144\n"
  "ds_read_b128 a[40:43], v254 offset:10240\n"
  "ds_read_b128 a[56:59], v254 offset:14336\n"
  "ds_read_b128 a[12:15], v254 offset:3072\n"
  "ds_read_b128 a[28:31], v254 offset:7168\n"
  "ds_read_b128 a[44:47], v254 offset:11264\n"
  "ds_read_b128 a[60:63], v254 offset:15360\n"
  "v_mfma_f32_32x32x16_bf16 v[64:79], v[224:227], v[224:227], 0\n"
  "v_mfma_f32_32x32x16_bf16 v[80:95], v[224:227], v[224:227], 0\n"
  "v_mfma_f32_32x32x16_bf16 v[96:111], v[224:227], v[224:227], 0\n"
  "v_mfma_f32_32x32x16_bf16 v[112:127], v[224:227], v[224:227], 0\n"
  "s_xor_b32 s49, s49, 0x10000\n"
  "s_waitcnt lgkmcnt(15)\n"
  "v_mfma_f32_16x16x32_bf16 v[168:171], a[128:131], a[0:3], 0\n"
  "s_add_u32 m0, s49, 0\n"
  "s_waitcnt lgkmcnt(14)\n"
  "v_mfma_f32_16x16x32_bf16 v[172:175], a[128:131], a[16:19], 0\n"
  "global_load_lds_dwordx4 v232, s[20:21] offset:0 sc1\n"
  "s_waitcnt lgkmcnt(13)\n"
  "v_mfma_f32_16x16x32_bf16 v[176:179], a[128:131], a[32:35], 0\n"
  "global_load_lds_dwordx4 v232, s[20:21] offset:1024 sc1\n"
  "s_waitcnt lgkmcnt(12)\n"
  "v_mfma_f32_16x16x32_bf16 v[180:183], a[128:131], a[48:51], 0\n"
  "global_load_lds_dwordx4 v232, s[20:21] offset:2048 sc1\n"
  "s_waitcnt lgkmcnt(11)\n"
  "v_mfma_f32_16x16x32_bf16 v[168:171], a[132:135], a[4:7], v[168:171]\n"
  "ds_read_b128 a[144:147], v216 offset:4096\n"
  "global_load_lds_dwordx4 v232, s[20:21] offset:3072 sc1\n"
  "s_waitcnt lgkmcnt(11)\n"
  "v_mfma_f32_16x16x32_bf16 v[172:175], a[132:135], a[20:23], v[172:175]\n"
  "s_add_u32 s20, s20, 16384\n"
  "s_addc_u32 s21, s21, 0\n"
  "ds_read_b128 a[148:151], v216 offset:5120\n"
  "s_waitcnt lgkmcnt(11)\n"
  "v_mfma_f32_16x16x32_bf16 v[176:179], a[132:135], a[36:39], v[176:179]\n"
  "s_add_u32 m0, s49, 32768\n"
  "ds_read_b128 a[152:155], v216 offset:6144\n"
  "s_waitcnt lgkmcnt(11)\n"
  "v_mfma_f32_16x16x32_bf16 v[180:183], a[132:135], a[52:55], v[180:183]\n"
  "ds_read_b128 a[156:159], v216 offset:7168\n"
  "global_load_lds_dwordx4 v232, s[20:21] offset:0 sc1\n"
  "s_waitcnt lgkmcnt(11)\n"
  "v_mfma_f32_16x16x32_bf16 v[168:171], a[136:139], a[8:11], v[168:171]\n"
  "ds_read_b128 a[160:163], v216 offset:32768\n"
  "global_load_lds_dwordx4 v232, s[20:21] offset:1024 sc1\n"
  "s_waitcnt lgkmcnt(11)\n"
  "v_mfma_f32_16x16x32_bf16 v[172:175], a[136:139], a[24:27], v[172:175]\n"
  "ds_read_b128 a[164:167], v216 offset:33792\n"
  "global_load_lds_dwordx4 v232, s[20:21] offset:2048 sc1\n"
  "s_waitcnt lgkmcnt(11)\n"
  "v_mfma_f32_16x16x32_bf16 v[176:179], a[136:139], a[40:43], v[176:179]\n"
  "ds_read_b128 a[168:171], v216 offset:34816\n"
  "global_load_lds_dwordx4 v232, s[20:21] offset:3072 sc1\n"
  "s_waitcnt lgkmcnt(11)\n"
  "v_mfma_f32_16x16x32_bf16 v[180:183], a[136:139], a[56:59], v[180:183]\n"
  "s_add_u32 s20, s20, 16384\n"
  "s_addc_u32 s21, s21, 0\n"
  "ds_read_b128 a[172:175], v216 offset:35840\n"
  "s_waitcnt lgkmcnt(11)\n"
  "v_mfma_f32_16x16x32_bf16 v[168:171], a[140:143], a[12:15], v[168:171]\n"
  "s_xor_b32 s49, s49, 0x10000\n"
  "ds_read_b128 a[176:179], v216 offset:36864\n"
  "s_waitcnt lgkmcnt(11)\n"
  "v_mfma_f32_16x16x32_bf16 v[172:175], a[140:143], a[28:31], v[172:175]\n"
  "s_cmp_eq_u32 s34, 0\n"
  "s_cselect_b32 s36, 917504, 0\n"
  "ds_read_b128 a[180:183], v216 offset:37888\n"
  "s_waitcnt lgkmcnt(11)\n"
  "v_mfma_f32_16x16x32_bf16 v[176:179], a[140:143], a[44:47], v[176:179]\n"
  "s_add_u32 s20, s20, s36\n"
  "s_addc_u32 s21, s21, 0\n"
  "ds_read_b128 a[184:187], v216 offset:38912\n"
  "s_waitcnt lgkmcnt(11)\n"
  "v_mfma_f32_16x16x32_bf16 v[180:183], a[140:143], a[60:63], v[180:183]\n"
  "s_lshl_b32 s36, s35, 10\n"
  "s_add_u32 s36, s36, s38\n"
  "s_add_u32 s36, s36, 49152\n"
  "ds_read_b128 a[188:191], v216 offset:39936\n"
  "v_add_u32 v234, s36, v232\n"
  "ds_read_b128 a[64:67], v216 offset:8192\n"
  "ds_read_b128 a[96:99], v216 offset:40960\n"
  "ds_read_b128 a[100:103], v216 offset:41984\n"
  "ds_read_b128 a[68:71], v216 offset:9216\n"
  "ds_read_b128 a[72:75], v216 offset:10240\n"
  "ds_read_b128 a[104:107], v216 offset:43008\n"
  "ds_read_b128 a[108:111], v216 offset:44032\n"
  "ds_read_b128 a[76:79], v216 offset:11264\n"
  "v_max_f32 v168, v168, v169\n"
  "v_max3_f32 v168, v168, v170, v171\n"
  "v_max_f32 v172, v172, v173\n"
  "v_max3_f32 v172, v172, v174, v175\n"
  "v_max_f32 v176, v176, v177\n"
  "v_max3_f32 v176, v176, v178, v179\n"
  "v_max_f32 v180, v180, v181\n"
  "v_max3_f32 v180, v180, v182, v183\n"
  "v_mov_b32 v169, v168\n"
  "v_mov_b32 v173, v172\n"
  "v_mov_b32 v177, v176\n"
  "v_mov_b32 v181, v180\n"
  "v_permlane16_swap_b32 v168, v169\n"
  "v_permlane16_swap_b32 v172, v173\n"
  "v_permlane16_swap_b32 v176, v177\n"
  "v_permlane16_swap_b32 v180, v181\n"
  "v_max_f32 v168, v168, v169\n"
  "v_max_f32 v172, v172, v173\n"
  "v_max_f32 v176, v176, v177\n"
  "v_max_f32 v180, v180, v181\n"
  "v_mov_b32 v169, v168\n"
  "v_mov_b32 v173, v172\n"
  "v_mov_b32 v177, v176\n"
  "v_mov_b32 v181, v180\n"
  "v_permlane32_swap_b32 v168, v169\n"
  "v_permlane32_swap_b32 v172, v173\n"
  "v_permlane32_swap_b32 v176, v177\n"
  "v_permlane32_swap_b32 v180, v181\n"
  "v_max_f32 v168, v168, v169\n"
  "v_max_f32 v172, v172, v173\n"
  "v_max_f32 v176, v176, v177\n"
  "v_max_f32 v180, v180, v181\n"
  "v_sub_f32 v220, 0, v168\n"
  "v_sub_f32 v221, 0, v172\n"
  "v_sub_f32 v222, 0, v176\n"
  "v_sub_f32 v223, 0, v180\n"
  "ds_write_b128 v234, v[220:223]\n"
  "s_lshl_b32 s36, s34, 2\n"
  "s_add_u32 s36, s36, s35\n"
  "s_cmp_eq_u32 s36, 3\n"
  "s_cbranch_scc1 L_fill0d_%=\n"
  "v_mov_b32 v152, v220\n"
  "v_mov_b32 v153, v220\n"
  "v_mov_b32 v154, v220\n"
  "v_mov_b32 v155, v220\n"
  "s_nop 1\n"
  "v_mfma_f32_16x16x32_bf16 v[128:131], a[128:131], a[0:3], v[152:155]\n"
  "v_mov_b32 v156, v221\n"
  "v_mov_b32 v157, v221\n"
  "v_mfma_f32_16x16x32_bf16 v[128:131], a[132:135], a[4:7], v[128:131]\n"
  "v_mov_b32 v158, v221\n"
  "v_mov_b32 v159, v221\n"
  "v_mfma_f32_16x16x32_bf16 v[128:131], a[136:139], a[8:11], v[128:131]\n"
  "v_mov_b32 v160, v222\n"
  "v_mov_b32 v161, v222\n"
  "v_mfma_f32_16x16x32_bf16 v[128:131], a[140:143], a[12:15], v[128:131]\n"
  "v_mov_b32 v162, v222\n"
  "v_mov_b32 v163, v222\n"
  "s_waitcnt lgkmcnt(15)\n"
  "v_mfma_f32_16x16x32_bf16 v[132:135], a[156:159], a[12:15], v[152:155]\n"
  "v_mov_b32 v164, v223\n"
  "v_mov_b32 v165, v223\n"
  "v_mfma_f32_16x16x32_bf16 v[132:135], a[152:155], a[8:11], v[132:135]\n"
  "v_mov_b32 v166, v223\n"
  "v_mov_b32 v167, v223\n"
  "v_mfma_f32_16x16x32_bf16 v[132:135], a[148:151], a[4:7], v[132:135]\n"
  "v_exp_f32 v128, v128\n"
  "v_mfma_f32_16x16x32_bf16 v[132:135], a[144:147], a[0:3], v[132:135]\n"
  "v_exp_f32 v129, v129\n"
  "v_mfma_f32_16x16x32_bf16 v[136:139], a[160:163], a[0:3], v[152:155]\n"
  "v_exp_f32 v130, v130\n"
  "v_mfma_f32_16x16x32_bf16 v[136:139], a[164:167], a[4:7], v[136:139]\n"
  "v_exp_f32 v131, v131\n"
  "s_waitcnt lgkmcnt(14)\n"
  "v_mfma_f32_16x16x32_bf16 v[136:139], a[168:171], a[8:11], v[136:139]\n"
  "s_nop 0\n"
  "v_exp_f32 v132, v132\n"
  "s_waitcnt lgkmcnt(13)\n"
  "v_mfma_f32_16x16x32_bf16 v[136:139], a[172:175], a[12:15], v[136:139]\n"
  "v_exp_f32 v133, v133\n"
  "s_waitcnt lgkmcnt(9)\n"
  "v_mfma_f32_16x16x32_bf16 v[140:143], a[188:191], a[12:15], v[152:155]\n"
  "v_exp_f32 v134, v134\n"
  "v_mfma_f32_16x16x32_bf16 v[140:143], a[184:187], a[8:11], v[140:143]\n"
  "v_exp_f32 v135, v135\n"
  "v_mfma_f32_16x16x32_bf16 v[140:143], a[180:183], a[4:7], v[140:143]\n"
  "s_nop 0\n"
  "v_exp_f32 v136, v136\n"
  "v_mfma_f32_16x16x32_bf16 v[140:143], a[176:179], a[0:3], v[140:143]\n"
  "v_exp_f32 v137, v137\n"
  "v_exp_f32 v138, v138\n"
  "v_exp_f32 v139, v139\n"
  "s_nop 4\n"
  "v_exp_f32 v140, v140\n"
  "v_exp_f32 v141, v141\n"
  "v_exp_f32 v142, v142\n"
  "v_exp_f32 v143, v143\n"
  "v_cvt_pk_bf16_f32 v144, v128, v129\n"
  "v_cvt_pk_bf16_f32 v145, v130, v131\n"
  "v_cvt_pk_bf16_f32 v146, v132, v133\n"
  "v_cvt_pk_bf16_f32 v147, v134, v135\n"
  "v_cvt_pk_bf16_f32 v148, v136, v137\n"
  "v_cvt_pk_bf16_f32 v149, v138, v139\n"
  "v_cvt_pk_bf16_f32 v150, v140, v141\n"
  "v_cvt_pk_bf16_f32 v151, v142, v143\n"
  "v_cndmask_b32 v172, v221, v218, s[88:89]\n"
  "v_cndmask_b32 v173, v221, v218, s[90:91]\n"
  "v_cndmask_b32 v174, v221, v218, s[92:93]\n"
  "v_cndmask_b32 v175, v221, v218, s[94:95]\n"
  "v_cndmask_b32 v176, v222, v218, s[88:89]\n"
  "v_cndmask_b32 v177, v222, v218, s[90:91]\n"
  "v_cndmask_b32 v178, v222, v218, s[92:93]\n"
  "v_cndmask_b32 v179, v222, v218, s[94:95]\n"
  "v_cndmask_b32 v180, v223, v218, s[88:89]\n"
  "v_cndmask_b32 v181, v223, v218, s[90:91]\n"
  "v_cndmask_b32 v182, v223, v218, s[92:93]\n"
  "v_cndmask_b32 v183, v223, v218, s[94:95]\n"
  "v_cndmask_b32 v168, v220, v218, s[88:89]\n"
  "v_cndmask_b32 v169, v220, v218, s[90:91]\n"
  "v_cndmask_b32 v170, v220, v218, s[92:93]\n"
  "v_cndmask_b32 v171, v220, v218, s[94:95]\n"
  "s_branch L_fill1_%=\n"
  "L_fill0d_%=:\n"
  "v_cndmask_b32 v168, v220, v218, s[88:89]\n"
  "v_cndmask_b32 v169, v220, v218, s[90:91]\n"
  "v_cndmask_b32 v170, v220, v218, s[92:93]\n"
  "v_cndmask_b32 v171, v220, v218, s[94:95]\n"
  "s_nop 1\n"
  "v_mfma_f32_16x16x32_bf16 v[128:131], a[128:131], a[0:3], v[168:171]\n"
  "v_mov_b32 v156, v221\n"
  "v_mov_b32 v157, v221\n"
  "v_mfma_f32_16x16x32_bf16 v[128:131], a[132:135], a[4:7], v[128:131]\n"
  "v_mov_b32 v158, v221\n"
  "v_mov_b32 v159, v221\n"
  "v_mfma_f32_16x16x32_bf16 v[128:131], a[136:139], a[8:11], v[128:131]\n"
  "v_mov_b32 v160, v222\n"
  "v_mov_b32 v161, v222\n"
  "v_mfma_f32_16x16x32_bf16 v[128:131], a[140:143], a[12:15], v[128:131]\n"
  "s_nop 7\n"
  "v_exp_f32 v128, v128\n"
  "v_exp_f32 v129, v129\n"
  "v_exp_f32 v130, v130\n"
  "v_exp_f32 v131, v131\n"
  "v_cvt_pk_bf16_f32 v144, v128, v129\n"
  "v_cvt_pk_bf16_f32 v145, v130, v131\n"
  "v_mov_b32 v146, 0\n"
  "v_mov_b32 v147, 0\n"
  "v_mov_b32 v162, v222\n"
  "v_mov_b32 v163, v222\n"
  "v_mov_b32 v164, v223\n"
  "v_mov_b32 v165, v223\n"
  "v_mov_b32 v166, v223\n"
  "v_mov_b32 v167, v223\n"
  "v_cndmask_b32 v172, v221, v218, s[88:89]\n"
  "v_cndmask_b32 v173, v221, v218, s[90:91]\n"
  "v_cndmask_b32 v174, v221, v218, s[92:93]\n"
  "v_cndmask_b32 v175, v221, v218, s[94:95]\n"
  "v_cndmask_b32 v176, v222, v218, s[88:89]\n"
  "v_cndmask_b32 v177, v222, v218, s[90:91]\n"
  "v_cndmask_b32 v178, v222, v218, s[92:93]\n"
  "v_cndmask_b32 v179, v222, v218, s[94:95]\n"
  "v_cndmask_b32 v180, v223, v218, s[88:89]\n"
  "v_cndmask_b32 v181, v223, v218, s[90:91]\n"
  "v_cndmask_b32 v182, v223, v218, s[92:93]\n"
  "v_cndmask_b32 v183, v223, v218, s[94:95]\n"
  "v_mov_b32 v152, v220\n"
  "v_mov_b32 v153, v220\n"
  "v_mov_b32 v154, v220\n"
  "v_mov_b32 v155, v220\n"
  "L_fill1_%=:\n"
  "v_readlane_b32 s22, v252, 0\n"
  "s_mov_b32 s24, 0\n"
  "s_waitcnt lgkmcnt(0)\n"
  "s_barrier\n"
  "v_subrev_u32 v234, 2048, v234\n"
  "ds_read_b128 v[220:223], v234\n"
  "v_mov_b32 v236, s16\n"
  "v_mov_b32 v237, s17\n"
  "s_lshl_b32 s36, s86, 9\n"
  "v_mov_b32 v238, s36\n"
  "s_mov_b64 exec, 1\n"
  "global_store_dwordx2 v238, v[236:237], s[62:63] offset:128 sc1\n"
  "s_mov_b64 exec, -1\n"
  "s_add_u32 s22, s26, s22\n"
  "s_addc_u32 s23, s27, 0\n"
  "s_lshr_b32 s36, s86, 3\n"
  "s_lshl_b32 s36, s36, 2\n"
  "s_add_u32 s36, s36, s35\n"
  "s_lshl_b32 s36, s36, 9\n"
  "s_min_u32 s36, s36, L_end_%=-L_SFD_%=-384\n"
  "s_add_u32 s36, s36, L_SFD_%=-L_start_%=\n"
  "v_and_b32 v196, 3, v233\n"
  "v_lshlrev_b32 v196, 7, v196\n"
  "v_add_u32 v196, s36, v196\n"
  "s_lshl_b32 s36, s86, 2\n"
  "s_add_u32 s36, s36, s35\n"
  "s_lshl_b32 s37, s36, 12\n"
  "s_add_u32 s60, s74, s37\n"
  "s_addc_u32 s61, s75, 0\n"
  "v_and_b32 v240, 31, v233\n"
  "v_lshlrev_b32 v240, 7, v240\n"
  "global_load_dword v231, v240, s[60:61]\n"
  "s_min_u32 s36, s36, 343\n"
  "s_lshl_b32 s37, s36, 12\n"
  "s_add_u32 s60, s80, s37\n"
  "s_addc_u32 s61, s81, 0\n"
  "global_load_dword v231, v240, s[60:61]\n"
  "s_lshl_b32 s36, s57, 2\n"
  "s_add_u32 s36, s36, s58\n"
  "s_lshl_b32 s36, s36, 2\n"
  "s_add_u32 s36, s36, s56\n"
  "s_lshl_b32 s36, s36, 15\n"
  "s_mul_i32 s37, s35, 6144\n"
  "s_add_u32 s36, s36, s37\n"
  "s_add_u32 s36, s36, 75501568\n"
  "s_add_u32 s28, s84, s36\n"
  "s_addc_u32 s29, s85, 0\n"
  "v_mov_b32 v184, -1\n"
  "v_mov_b32 v185, -1\n"
  "v_mov_b32 v186, -1\n"
  "v_mov_b32 v187, -1\n"
  "global_store_dwordx4 v232, v[184:187], s[28:29] offset:-4096 sc1\n"
  "global_store_dwordx4 v232, v[184:187], s[28:29] offset:-3072 sc1\n"
  "s_setpc_b64 s[22:23]\n"
  "L_PF2_%=:\n"
  "v_mfma_f32_16x16x32_bf16 v[128:131], a[128:131], a[16:19], v[156:159]\n"
  "ds_read_b128 a[80:83], v216 offset:12288\n"
  "ds_read_b128 a[112:115], v216 offset:45056\n"
  "v_mfma_f32_16x16x32_bf16 v[128:131], a[132:135], a[20:23], v[128:131]\n"
  "ds_read_b128 a[116:119], v216 offset:46080\n"
  "ds_read_b128 a[84:87], v216 offset:13312\n"
  "v_mfma_f32_16x16x32_bf16 v[128:131], a[136:139], a[24:27], v[128:131]\n"
  "ds_read_b128 a[88:91], v216 offset:14336\n"
  "ds_read_b128 a[120:123], v216 offset:47104\n"
  "v_mfma_f32_16x16x32_bf16 v[128:131], a[140:143], a[28:31], v[128:131]\n"
  "ds_read_b128 a[124:127], v216 offset:48128\n"
  "ds_read_b128 a[92:95], v216 offset:15360\n"
  "v_mfma_f32_16x16x32_bf16 v[132:135], a[156:159], a[28:31], v[156:159]\n"
  "s_nop 1\n"
  "v_mfma_f32_16x16x32_bf16 v[132:135], a[152:155], a[24:27], v[132:135]\n"
  "s_nop 1\n"
  "v_xor_b32 v216, 0x10000, v216\n"
  "v_mfma_f32_16x16x32_bf16 v[132:135], a[148:151], a[20:23], v[132:135]\n"
  "s_add_u32 s24, s24, 1\n"
  "v_exp_f32 v128, v128\n"
  "v_mfma_f32_16x16x32_bf16 v[132:135], a[144:147], a[16:19], v[132:135]\n"
  "v_exp_f32 v129, v129\n"
  "v_mfma_f32_16x16x32_bf16 v[136:139], a[160:163], a[16:19], v[156:159]\n"
  "v_cvt_pk_bf16_f32 v188, v128, v129\n"
  "v_mfma_f32_16x16x32_bf16 v[136:139], a[164:167], a[20:23], v[136:139]\n"
  "v_exp_f32 v130, v130\n"
  "v_mfma_f32_16x16x32_bf16 v[136:139], a[168:171], a[24:27], v[136:139]\n"
  "v_mfma_f32_16x16x32_bf16 v[136:139], a[172:175], a[28:31], v[136:139]\n"
  "v_exp_f32 v131, v131\n"
  "v_mfma_f32_16x16x32_bf16 v[140:143], a[188:191], a[28:31], v[156:159]\n"
  "v_exp_f32 v132, v132\n"
  "v_mfma_f32_16x16x32_bf16 v[140:143], a[184:187], a[24:27], v[140:143]\n"
  "s_waitcnt lgkmcnt(0)\n"
  "s_barrier\n"
  "v_cvt_pk_bf16_f32 v189, v130, v131\n"
  "v_mfma_f32_16x16x32_bf16 v[140:143], a[180:183], a[20:23], v[140:143]\n"
  "v_exp_f32 v133, v133\n"
  "v_mfma_f32_16x16x32_bf16 v[140:143], a[176:179], a[16:19], v[140:143]\n"
  "v_cvt_pk_bf16_f32 v190, v132, v133\n"
  "v_mfma_f32_16x16x32_bf16 v[0:3], a[64:67], v[144:147], v[0:3]\n"
  "s_add_u32 m0, s49, 0\n"
  "v_exp_f32 v134, v134\n"
  "v_mfma_f32_16x16x32_bf16 v[0:3], a[96:99], v[148:151], v[0:3]\n"
  "v_mfma_f32_16x16x32_bf16 v[4:7], a[100:103], v[148:151], v[4:7]\n"
  "v_exp_f32 v135, v135\n"
  "v_mfma_f32_16x16x32_bf16 v[4:7], a[68:71], v[144:147], v[4:7]\n"
  "v_exp_f32 v136, v136\n"
  "v_mfma_f32_16x16x32_bf16 v[8:11], a[72:75], v[144:147], v[8:11]\n"
  "global_load_lds_dwordx4 v232, s[20:21] offset:0 sc1\n"
  "v_cvt_pk_bf16_f32 v191, v134, v135\n"
  "v_mfma_f32_16x16x32_bf16 v[8:11], a[104:107], v[148:151], v[8:11]\n"
  "v_exp_f32 v137, v137\n"
  "v_mfma_f32_16x16x32_bf16 v[12:15], a[108:111], v[148:151], v[12:15]\n"
  "global_load_lds_dwordx4 v232, s[20:21] offset:1024 sc1\n"
  "v_cvt_pk_bf16_f32 v192, v136, v137\n"
  "v_mfma_f32_16x16x32_bf16 v[12:15], a[76:79], v[144:147], v[12:15]\n"
  "v_exp_f32 v138, v138\n"
  "v_mfma_f32_16x16x32_bf16 v[16:19], a[80:83], v[144:147], v[16:19]\n"
  "v_mfma_f32_16x16x32_bf16 v[16:19], a[112:115], v[148:151], v[16:19]\n"
  "v_exp_f32 v139, v139\n"
  "v_mfma_f32_16x16x32_bf16 v[20:23], a[116:119], v[148:151], v[20:23]\n"
  "v_exp_f32 v140, v140\n"
  "v_mfma_f32_16x16x32_bf16 v[20:23], a[84:87], v[144:147], v[20:23]\n"
  "global_load_lds_dwordx4 v232, s[20:21] offset:2048 sc1\n"
  "v_cvt_pk_bf16_f32 v193, v138, v139\n"
  "v_mfma_f32_16x16x32_bf16 v[24:27], a[88:91], v[144:147], v[24:27]\n"
  "v_exp_f32 v141, v141\n"
  "v_mfma_f32_16x16x32_bf16 v[24:27], a[120:123], v[148:151], v[24:27]\n"
  "global_load_lds_dwordx4 v232, s[20:21] offset:3072 sc1\n"
  "v_cvt_pk_bf16_f32 v194, v140, v141\n"
  "v_mfma_f32_16x16x32_bf16 v[28:31], a[124:127], v[148:151], v[28:31]\n"
  "v_exp_f32 v142, v142\n"
  "v_mfma_f32_16x16x32_bf16 v[28:31], a[92:95], v[144:147], v[28:31]\n"
  "s_add_u32 m0, s49, 16384\n"
  "v_mfma_f32_16x16x32_bf16 v[200:203], a[192:195], v[144:147], v[200:203]\n"
  "v_exp_f32 v143, v143\n"
  "v_mfma_f32_16x16x32_bf16 v[200:203], a[192:195], v[148:151], v[200:203]\n"
  "v_cvt_pk_bf16_f32 v195, v142, v143\n"
  "v_mfma_f32_16x16x32_bf16 v[128:131], a[128:131], a[32:35], v[160:163]\n"
  "global_load_lds_dwordx4 v217, s[20:21] offset:0 sc1\n"
  "v_mfma_f32_16x16x32_bf16 v[128:131], a[132:135], a[36:39], v[128:131]\n"
  "v_mfma_f32_16x16x32_bf16 v[128:131], a[136:139], a[40:43], v[128:131]\n"
  "v_mfma_f32_16x16x32_bf16 v[128:131], a[140:143], a[44:47], v[128:131]\n"
  "s_nop 1\n"
  "global_load_lds_dwordx4 v217, s[20:21] offset:1024 sc1\n"
  "v_mfma_f32_16x16x32_bf16 v[132:135], a[156:159], a[44:47], v[160:163]\n"
  "s_nop 1\n"
  "v_mfma_f32_16x16x32_bf16 v[132:135], a[152:155], a[40:43], v[132:135]\n"
  "s_nop 1\n"
  "v_mfma_f32_16x16x32_bf16 v[132:135], a[148:151], a[36:39], v[132:135]\n"
  "v_exp_f32 v128, v128\n"
  "v_mfma_f32_16x16x32_bf16 v[132:135], a[144:147], a[32:35], v[132:135]\n"
  "v_exp_f32 v129, v129\n"
  "v_mfma_f32_16x16x32_bf16 v[136:139], a[160:163], a[32:35], v[160:163]\n"
  "global_load_lds_dwordx4 v217, s[20:21] offset:2048 sc1\n"
  "v_cvt_pk_bf16_f32 v144, v128, v129\n"
  "v_mfma_f32_16x16x32_bf16 v[136:139], a[164:167], a[36:39], v[136:139]\n"
  "v_exp_f32 v130, v130\n"
  "v_mfma_f32_16x16x32_bf16 v[136:139], a[168:171], a[40:43], v[136:139]\n"
  "global_load_lds_dwordx4 v217, s[20:21] offset:3072 sc1\n"
  "v_mfma_f32_16x16x32_bf16 v[136:139], a[172:175], a[44:47], v[136:139]\n"
  "v_exp_f32 v131, v131\n"
  "v_mfma_f32_16x16x32_bf16 v[140:143], a[188:191], a[44:47], v[160:163]\n"
  "v_exp_f32 v132, v132\n"
  "v_mfma_f32_16x16x32_bf16 v[140:143], a[184:187], a[40:43], v[140:143]\n"
  "s_add_u32 s20, s20, 16384\n"
  "s_addc_u32 s21, s21, 0\n"
  "v_cvt_pk_bf16_f32 v145, v130, v131\n"
  "v_mfma_f32_16x16x32_bf16 v[140:143], a[180:183], a[36:39], v[140:143]\n"
  "v_exp_f32 v133, v133\n"
  "v_mfma_f32_16x16x32_bf16 v[140:143], a[176:179], a[32:35], v[140:143]\n"
  "v_cvt_pk_bf16_f32 v146, v132, v133\n"
  "v_mfma_f32_16x16x32_bf16 v[32:35], a[64:67], v[188:191], v[32:35]\n"
  "s_add_u32 m0, s49, 32768\n"
  "v_exp_f32 v134, v134\n"
  "v_mfma_f32_16x16x32_bf16 v[32:35], a[96:99], v[192:195], v[32:35]\n"
  "v_mfma_f32_16x16x32_bf16 v[36:39], a[100:103], v[192:195], v[36:39]\n"
  "v_exp_f32 v135, v135\n"
  "v_mfma_f32_16x16x32_bf16 v[36:39], a[68:71], v[188:191], v[36:39]\n"
  "v_exp_f32 v136, v136\n"
  "v_mfma_f32_16x16x32_bf16 v[40:43], a[72:75], v[188:191], v[40:43]\n"
  "global_load_lds_dwordx4 v232, s[20:21] offset:0 sc1\n"
  "v_cvt_pk_bf16_f32 v147, v134, v135\n"
  "v_mfma_f32_16x16x32_bf16 v[40:43], a[104:107], v[192:195], v[40:43]\n"
  "v_exp_f32 v137, v137\n"
  "v_mfma_f32_16x16x32_bf16 v[44:47], a[108:111], v[192:195], v[44:47]\n"
  "global_load_lds_dwordx4 v232, s[20:21] offset:1024 sc1\n"
  "v_cvt_pk_bf16_f32 v148, v136, v137\n"
  "v_mfma_f32_16x16x32_bf16 v[44:47], a[76:79], v[188:191], v[44:47]\n"
  "v_exp_f32 v138, v138\n"
  "v_mfma_f32_16x16x32_bf16 v[48:51], a[80:83], v[188:191], v[48:51]\n"
  "v_mfma_f32_16x16x32_bf16 v[48:51], a[112:115], v[192:195], v[48:51]\n"
  "v_exp_f32 v139, v139\n"
  "v_mfma_f32_16x16x32_bf16 v[52:55], a[116:119], v[192:195], v[52:55]\n"
  "v_exp_f32 v140, v140\n"
  "v_mfma_f32_16x16x32_bf16 v[52:55], a[84:87], v[188:191], v[52:55]\n"
  "global_load_lds_dwordx4 v232, s[20:21] offset:2048 sc1\n"
  "v_cvt_pk_bf16_f32 v149, v138, v139\n"
  "v_mfma_f32_16x16x32_bf16 v[56:59], a[88:91], v[188:191], v[56:59]\n"
  "v_exp_f32 v141, v141\n"
  "v_mfma_f32_16x16x32_bf16 v[56:59], a[120:123], v[192:195], v[56:59]\n"
  "global_load_lds_dwordx4 v232, s[20:21] offset:3072 sc1\n"
  "v_cvt_pk_bf16_f32 v150, v140, v141\n"
  "v_mfma_f32_16x16x32_bf16 v[60:63], a[124:127], v[192:195], v[60:63]\n"
  "v_exp_f32 v142, v142\n"
  "v_mfma_f32_16x16x32_bf16 v[60:63], a[92:95], v[188:191], v[60:63]\n"
  "v_mfma_f32_16x16x32_bf16 v[204:207], a[192:195], v[188:191], v[204:207]\n"
  "s_add_u32 m0, s49, 49152\n"
  "v_exp_f32 v143, v143\n"
  "v_mfma_f32_16x16x32_bf16 v[204:207], a[192:195], v[192:195], v[204:207]\n"
  "s_waitcnt vmcnt(12)\n"
  "s_barrier\n"
  "v_cvt_pk_bf16_f32 v151, v142, v143\n"
  "v_mfma_f32_16x16x32_bf16 v[128:131], a[128:131], a[48:51], v[164:167]\n"
  "v_mfma_f32_16x16x32_bf16 v[128:131], a[132:135], a[52:55], v[128:131]\n"
  "ds_read_b128 a[128:131], v216 offset:0\n"
  "v_mfma_f32_16x16x32_bf16 v[128:131], a[136:139], a[56:59], v[128:131]\n"
  "ds_read_b128 a[132:135], v216 offset:1024\n"
  "v_mfma_f32_16x16x32_bf16 v[128:131], a[140:143], a[60:63], v[128:131]\n"
  "s_nop 1\n"
  "ds_read_b128 a[136:139], v216 offset:2048\n"
  "v_mfma_f32_16x16x32_bf16 v[132:135], a[156:159], a[60:63], v[164:167]\n"
  "s_nop 1\n"
  "ds_read_b128 a[140:143], v216 offset:3072\n"
  "v_mfma_f32_16x16x32_bf16 v[132:135], a[152:155], a[56:59], v[132:135]\n"
  "s_nop 1\n"
  "ds_read_b128 a[156:159], v216 offset:7168\n"
  "v_mfma_f32_16x16x32_bf16 v[132:135], a[148:151], a[52:55], v[132:135]\n"
  "v_exp_f32 v128, v128\n"
  "v_mfma_f32_16x16x32_bf16 v[132:135], a[144:147], a[48:51], v[132:135]\n"
  "v_exp_f32 v129, v129\n"
  "v_mfma_f32_16x16x32_bf16 v[136:139], a[160:163], a[48:51], v[164:167]\n"
  "ds_read_b128 a[152:155], v216 offset:6144\n"
  "v_cvt_pk_bf16_f32 v188, v128, v129\n"
  "v_mfma_f32_16x16x32_bf16 v[136:139], a[164:167], a[52:55], v[136:139]\n"
  "v_exp_f32 v130, v130\n"
  "v_mfma_f32_16x16x32_bf16 v[136:139], a[168:171], a[56:59], v[136:139]\n"
  "ds_read_b128 a[148:151], v216 offset:5120\n"
  "ds_read_b128 a[144:147], v216 offset:4096\n"
  "v_mfma_f32_16x16x32_bf16 v[136:139], a[172:175], a[60:63], v[136:139]\n"
  "v_exp_f32 v131, v131\n"
  "v_mfma_f32_16x16x32_bf16 v[140:143], a[188:191], a[60:63], v[164:167]\n"
  "v_exp_f32 v132, v132\n"
  "v_mfma_f32_16x16x32_bf16 v[140:143], a[184:187], a[56:59], v[140:143]\n"
  "ds_read_b128 a[160:163], v216 offset:32768\n"
  "v_cvt_pk_bf16_f32 v189, v130, v131\n"
  "v_mfma_f32_16x16x32_bf16 v[140:143], a[180:183], a[52:55], v[140:143]\n"
  "v_exp_f32 v133, v133\n"
  "v_mfma_f32_16x16x32_bf16 v[140:143], a[176:179], a[48:51], v[140:143]\n"
  "ds_read_b128 a[164:167], v216 offset:33792\n"
  "v_cvt_pk_bf16_f32 v190, v132, v133\n"
  "v_mfma_f32_16x16x32_bf16 v[64:67], a[64:67], v[144:147], v[64:67]\n"
  "v_exp_f32 v134, v134\n"
  "v_mfma_f32_16x16x32_bf16 v[64:67], a[96:99], v[148:151], v[64:67]\n"
  "ds_read_b128 a[168:171], v216 offset:34816\n"
  "ds_read_b128 a[172:175], v216 offset:35840\n"
  "v_mfma_f32_16x16x32_bf16 v[68:71], a[100:103], v[148:151], v[68:71]\n"
  "v_exp_f32 v135, v135\n"
  "v_mfma_f32_16x16x32_bf16 v[68:71], a[68:71], v[144:147], v[68:71]\n"
  "v_exp_f32 v136, v136\n"
  "v_mfma_f32_16x16x32_bf16 v[72:75], a[72:75], v[144:147], v[72:75]\n"
  "ds_read_b128 a[188:191], v216 offset:39936\n"
  "v_cvt_pk_bf16_f32 v191, v134, v135\n"
  "v_mfma_f32_16x16x32_bf16 v[72:75], a[104:107], v[148:151], v[72:75]\n"
  "v_exp_f32 v137, v137\n"
  "v_mfma_f32_16x16x32_bf16 v[76:79], a[108:111], v[148:151], v[76:79]\n"
  "ds_read_b128 a[184:187], v216 offset:38912\n"
  "v_cvt_pk_bf16_f32 v192, v136, v137\n"
  "v_mfma_f32_16x16x32_bf16 v[76:79], a[76:79], v[144:147], v[76:79]\n"
  "v_exp_f32 v138, v138\n"
  "v_mfma_f32_16x16x32_bf16 v[80:83], a[80:83], v[144:147], v[80:83]\n"
  "ds_read_b128 a[180:183], v216 offset:37888\n"
  "ds_read_b128 a[176:179], v216 offset:36864\n"
  "v_mfma_f32_16x16x32_bf16 v[80:83], a[112:115], v[148:151], v[80:83]\n"
  "v_exp_f32 v139, v139\n"
  "v_mfma_f32_16x16x32_bf16 v[84:87], a[116:119], v[148:151], v[84:87]\n"
  "v_exp_f32 v140, v140\n"
  "v_mfma_f32_16x16x32_bf16 v[84:87], a[84:87], v[144:147], v[84:87]\n"
  "v_cvt_pk_bf16_f32 v193, v138, v139\n"
  "v_mfma_f32_16x16x32_bf16 v[88:91], a[88:91], v[144:147], v[88:91]\n"
  "v_exp_f32 v141, v141\n"
  "v_mfma_f32_16x16x32_bf16 v[88:91], a[120:123], v[148:151], v[88:91]\n"
  "v_cvt_pk_bf16_f32 v194, v140, v141\n"
  "v_mfma_f32_16x16x32_bf16 v[92:95], a[124:127], v[148:151], v[92:95]\n"
  "v_exp_f32 v142, v142\n"
  "v_mfma_f32_16x16x32_bf16 v[92:95], a[92:95], v[144:147], v[92:95]\n"
  "v_mfma_f32_16x16x32_bf16 v[208:211], a[192:195], v[144:147], v[208:211]\n"
  "v_exp_f32 v143, v143\n"
  "v_mfma_f32_16x16x32_bf16 v[208:211], a[192:195], v[148:151], v[208:211]\n"
  "s_waitcnt lgkmcnt(0)\n"
  "v_cvt_pk_bf16_f32 v195, v142, v143\n"
  "v_mfma_f32_16x16x32_bf16 v[128:131], a[128:131], a[0:3], v[152:155]\n"
  "v_mfma_f32_16x16x32_bf16 v[128:131], a[132:135], a[4:7], v[128:131]\n"
  "global_load_dwordx2 a[244:245], v253, s[62:63] offset:128 sc1\n"
  "v_mfma_f32_16x16x32_bf16 v[128:131], a[136:139], a[8:11], v[128:131]\n"
  "global_load_lds_dwordx4 v217, s[20:21] offset:0 sc1\n"
  "v_mfma_f32_16x16x32_bf16 v[128:131], a[140:143], a[12:15], v[128:131]\n"
  "s_nop 1\n"
  "v_mfma_f32_16x16x32_bf16 v[132:135], a[156:159], a[12:15], v[152:155]\n"
  "s_nop 1\n"
  "v_mfma_f32_16x16x32_bf16 v[132:135], a[152:155], a[8:11], v[132:135]\n"
  "s_nop 1\n"
  "global_load_lds_dwordx4 v217, s[20:21] offset:1024 sc1\n"
  "v_mfma_f32_16x16x32_bf16 v[132:135], a[148:151], a[4:7], v[132:135]\n"
  "v_exp_f32 v128, v128\n"
  "v_mfma_f32_16x16x32_bf16 v[132:135], a[144:147], a[0:3], v[132:135]\n"
  "v_exp_f32 v129, v129\n"
  "v_mfma_f32_16x16x32_bf16 v[136:139], a[160:163], a[0:3], v[152:155]\n"
  "global_load_lds_dwordx4 v217, s[20:21] offset:2048 sc1\n"
  "v_cvt_pk_bf16_f32 v144, v128, v129\n"
  "v_mfma_f32_16x16x32_bf16 v[136:139], a[164:167], a[4:7], v[136:139]\n"
  "v_exp_f32 v130, v130\n"
  "v_mfma_f32_16x16x32_bf16 v[136:139], a[168:171], a[8:11], v[136:139]\n"
  "v_mfma_f32_16x16x32_bf16 v[136:139], a[172:175], a[12:15], v[136:139]\n"
  "v_exp_f32 v131, v131\n"
  "v_mfma_f32_16x16x32_bf16 v[140:143], a[188:191], a[12:15], v[152:155]\n"
  "v_exp_f32 v132, v132\n"
  "v_mfma_f32_16x16x32_bf16 v[140:143], a[184:187], a[8:11], v[140:143]\n"
  "global_load_lds_dwordx4 v217, s[20:21] offset:3072 sc1\n"
  "v_cvt_pk_bf16_f32 v145, v130, v131\n"
  "v_mfma_f32_16x16x32_bf16 v[140:143], a[180:183], a[4:7], v[140:143]\n"
  "v_exp_f32 v133, v133\n"
  "v_mfma_f32_16x16x32_bf16 v[140:143], a[176:179], a[0:3], v[140:143]\n"
  "s_sub_u32 s20, s20, 81920\n"
  "s_subb_u32 s21, s21, 0\n"
  "v_cvt_pk_bf16_f32 v146, v132, v133\n"
  "v_mfma_f32_16x16x32_bf16 v[96:99], a[64:67], v[188:191], v[96:99]\n"
  "v_exp_f32 v134, v134\n"
  "v_mfma_f32_16x16x32_bf16 v[96:99], a[96:99], v[192:195], v[96:99]\n"
  "ds_read_b128 a[64:67], v216 offset:8192\n"
  "v_mfma_f32_16x16x32_bf16 v[100:103], a[100:103], v[192:195], v[100:103]\n"
  "v_exp_f32 v135, v135\n"
  "v_mfma_f32_16x16x32_bf16 v[100:103], a[68:71], v[188:191], v[100:103]\n"
  "v_exp_f32 v136, v136\n"
  "v_mfma_f32_16x16x32_bf16 v[104:107], a[72:75], v[188:191], v[104:107]\n"
  "ds_read_b128 a[96:99], v216 offset:40960\n"
  "v_cvt_pk_bf16_f32 v147, v134, v135\n"
  "v_mfma_f32_16x16x32_bf16 v[104:107], a[104:107], v[192:195], v[104:107]\n"
  "v_exp_f32 v137, v137\n"
  "v_mfma_f32_16x16x32_bf16 v[108:111], a[108:111], v[192:195], v[108:111]\n"
  "ds_read_b128 a[100:103], v216 offset:41984\n"
  "v_cvt_pk_bf16_f32 v148, v136, v137\n"
  "v_mfma_f32_16x16x32_bf16 v[108:111], a[76:79], v[188:191], v[108:111]\n"
  "v_exp_f32 v138, v138\n"
  "v_mfma_f32_16x16x32_bf16 v[112:115], a[80:83], v[188:191], v[112:115]\n"
  "ds_read_b128 a[68:71], v216 offset:9216\n"
  "ds_read_b128 a[72:75], v216 offset:10240\n"
  "v_mfma_f32_16x16x32_bf16 v[112:115], a[112:115], v[192:195], v[112:115]\n"
  "v_exp_f32 v139, v139\n"
  "v_mfma_f32_16x16x32_bf16 v[116:119], a[116:119], v[192:195], v[116:119]\n"
  "v_exp_f32 v140, v140\n"
  "v_mfma_f32_16x16x32_bf16 v[116:119], a[84:87], v[188:191], v[116:119]\n"
  "ds_read_b128 a[104:107], v216 offset:43008\n"
  "v_cvt_pk_bf16_f32 v149, v138, v139\n"
  "v_mfma_f32_16x16x32_bf16 v[120:123], a[88:91], v[188:191], v[120:123]\n"
  "v_exp_f32 v141, v141\n"
  "v_mfma_f32_16x16x32_bf16 v[120:123], a[120:123], v[192:195], v[120:123]\n"
  "ds_read_b128 a[108:111], v216 offset:44032\n"
  "v_cvt_pk_bf16_f32 v150, v140, v141\n"
  "v_mfma_f32_16x16x32_bf16 v[124:127], a[124:127], v[192:195], v[124:127]\n"
  "v_exp_f32 v142, v142\n"
  "v_mfma_f32_16x16x32_bf16 v[124:127], a[92:95], v[188:191], v[124:127]\n"
  "s_xor_b32 s49, s49, 0x10000\n"
  "ds_read_b128 a[76:79], v216 offset:11264\n"
  "v_mfma_f32_16x16x32_bf16 v[212:215], a[192:195], v[188:191], v[212:215]\n"
  "v_exp_f32 v143, v143\n"
  "v_mfma_f32_16x16x32_bf16 v[212:215], a[192:195], v[192:195], v[212:215]\n"
  "v_cvt_pk_bf16_f32 v151, v142, v143\n"
  "L_PFF_%=:\n"
  "v_mfma_f32_16x16x32_bf16 v[128:131], a[128:131], a[16:19], v[156:159]\n"
  "ds_read_b128 a[80:83], v216 offset:12288\n"
  "ds_read_b128 a[112:115], v216 offset:45056\n"
  "v_mfma_f32_16x16x32_bf16 v[128:131], a[132:135], a[20:23], v[128:131]\n"
  "ds_read_b128 a[116:119], v216 offset:46080\n"
  "ds_read_b128 a[84:87], v216 offset:13312\n"
  "v_mfma_f32_16x16x32_bf16 v[128:131], a[136:139], a[24:27], v[128:131]\n"
  "ds_read_b128 a[88:91], v216 offset:14336\n"
  "ds_read_b128 a[120:123], v216 offset:47104\n"
  "v_mfma_f32_16x16x32_bf16 v[128:131], a[140:143], a[28:31], v[128:131]\n"
  "ds_read_b128 a[124:127], v216 offset:48128\n"
  "ds_read_b128 a[92:95], v216 offset:15360\n"
  "v_mfma_f32_16x16x32_bf16 v[132:135], a[156:159], a[28:31], v[156:159]\n"
  "s_nop 1\n"
  "v_mfma_f32_16x16x32_bf16 v[132:135], a[152:155], a[24:27], v[132:135]\n"
  "s_nop 1\n"
  "v_xor_b32 v216, 0x10000, v216\n"
  "v_mfma_f32_16x16x32_bf16 v[132:135], a[148:151], a[20:23], v[132:135]\n"
  "s_add_u32 s24, s24, 1\n"
  "v_exp_f32 v128, v128\n"
  "v_mfma_f32_16x16x32_bf16 v[132:135], a[144:147], a[16:19], v[132:135]\n"
  "v_exp_f32 v129, v129\n"
  "v_mfma_f32_16x16x32_bf16 v[136:139], a[160:163], a[16:19], v[156:159]\n"
  "v_cvt_pk_bf16_f32 v188, v128, v129\n"
  "v_readlane_b32 s22, v252, s24\n"
  "v_mfma_f32_16x16x32_bf16 v[136:139], a[164:167], a[20:23], v[136:139]\n"
  "v_exp_f32 v130, v130\n"
  "v_mfma_f32_16x16x32_bf16 v[136:139], a[168:171], a[24:27], v[136:139]\n"
  "s_add_u32 s22, s26, s22\n"
  "s_addc_u32 s23, s27, 0\n"
  "v_mfma_f32_16x16x32_bf16 v[136:139], a[172:175], a[28:31], v[136:139]\n"
  "v_exp_f32 v131, v131\n"
  "v_mfma_f32_16x16x32_bf16 v[140:143], a[188:191], a[28:31], v[156:159]\n"
  "v_exp_f32 v132, v132\n"
  "v_mfma_f32_16x16x32_bf16 v[140:143], a[184:187], a[24:27], v[140:143]\n"
  "s_waitcnt lgkmcnt(0)\n"
  "s_barrier\n"
  "v_cvt_pk_bf16_f32 v189, v130, v131\n"
  "v_mfma_f32_16x16x32_bf16 v[140:143], a[180:183], a[20:23], v[140:143]\n"
  "v_exp_f32 v133, v133\n"
  "v_mfma_f32_16x16x32_bf16 v[140:143], a[176:179], a[16:19], v[140:143]\n"
  "v_cvt_pk_bf16_f32 v190, v132, v133\n"
  "v_mfma_f32_16x16x32_bf16 v[0:3], a[64:67], v[144:147], v[0:3]\n"
  "s_add_u32 m0, s49, 0\n"
  "v_exp_f32 v134, v134\n"
  "v_mfma_f32_16x16x32_bf16 v[0:3], a[96:99], v[148:151], v[0:3]\n"
  "v_mfma_f32_16x16x32_bf16 v[4:7], a[100:103], v[148:151], v[4:7]\n"
  "v_exp_f32 v135, v135\n"
  "v_mfma_f32_16x16x32_bf16 v[4:7], a[68:71], v[144:147], v[4:7]\n"
  "v_exp_f32 v136, v136\n"
  "v_mfma_f32_16x16x32_bf16 v[8:11], a[72:75], v[144:147], v[8:11]\n"
  "global_load_lds_dwordx4 v232, s[20:21] offset:0 sc1\n"
  "v_cvt_pk_bf16_f32 v191, v134, v135\n"
  "v_mfma_f32_16x16x32_bf16 v[8:11], a[104:107], v[148:151], v[8:11]\n"
  "v_exp_f32 v137, v137\n"
  "v_mfma_f32_16x16x32_bf16 v[12:15], a[108:111], v[148:151], v[12:15]\n"
  "global_load_lds_dwordx4 v232, s[20:21] offset:1024 sc1\n"
  "v_cvt_pk_bf16_f32 v192, v136, v137\n"
  "v_mfma_f32_16x16x32_bf16 v[12:15], a[76:79], v[144:147], v[12:15]\n"
  "v_exp_f32 v138, v138\n"
  "v_mfma_f32_16x16x32_bf16 v[16:19], a[80:83], v[144:147], v[16:19]\n"
  "v_mfma_f32_16x16x32_bf16 v[16:19], a[112:115], v[148:151], v[16:19]\n"
  "v_exp_f32 v139, v139\n"
  "v_mfma_f32_16x16x32_bf16 v[20:23], a[116:119], v[148:151], v[20:23]\n"
  "v_exp_f32 v140, v140\n"
  "v_mfma_f32_16x16x32_bf16 v[20:23], a[84:87], v[144:147], v[20:23]\n"
  "global_load_lds_dwordx4 v232, s[20:21] offset:2048 sc1\n"
  "v_cvt_pk_bf16_f32 v193, v138, v139\n"
  "v_mfma_f32_16x16x32_bf16 v[24:27], a[88:91], v[144:147], v[24:27]\n"
  "v_exp_f32 v141, v141\n"
  "v_mfma_f32_16x16x32_bf16 v[24:27], a[120:123], v[148:151], v[24:27]\n"
  "global_load_lds_dwordx4 v232, s[20:21] offset:3072 sc1\n"
  "v_cvt_pk_bf16_f32 v194, v140, v141\n"
  "v_mfma_f32_16x16x32_bf16 v[28:31], a[124:127], v[148:151], v[28:31]\n"
  "v_exp_f32 v142, v142\n"
  "v_mfma_f32_16x16x32_bf16 v[28:31], a[92:95], v[144:147], v[28:31]\n"
  "s_add_u32 m0, s49, 16384\n"
  "v_mfma_f32_16x16x32_bf16 v[200:203], a[192:195], v[144:147], v[200:203]\n"
  "v_exp_f32 v143, v143\n"
  "v_mfma_f32_16x16x32_bf16 v[200:203], a[192:195], v[148:151], v[200:203]\n"
  "v_cvt_pk_bf16_f32 v195, v142, v143\n"
  "v_mfma_f32_16x16x32_bf16 v[128:131], a[128:131], a[32:35], v[160:163]\n"
  "global_load_lds_dwordx4 v217, s[20:21] offset:0 sc1\n"
  "v_mfma_f32_16x16x32_bf16 v[128:131], a[132:135], a[36:39], v[128:131]\n"
  "v_mfma_f32_16x16x32_bf16 v[128:131], a[136:139], a[40:43], v[128:131]\n"
  "v_mfma_f32_16x16x32_bf16 v[128:131], a[140:143], a[44:47], v[128:131]\n"
  "s_nop 1\n"
  "global_load_lds_dwordx4 v217, s[20:21] offset:1024 sc1\n"
  "v_mfma_f32_16x16x32_bf16 v[132:135], a[156:159], a[44:47], v[160:163]\n"
  "s_nop 1\n"
  "v_mfma_f32_16x16x32_bf16 v[132:135], a[152:155], a[40:43], v[132:135]\n"
  "s_nop 1\n"
  "v_mfma_f32_16x16x32_bf16 v[132:135], a[148:151], a[36:39], v[132:135]\n"
  "v_exp_f32 v128, v128\n"
  "v_mfma_f32_16x16x32_bf16 v[132:135], a[144:147], a[32:35], v[132:135]\n"
  "v_exp_f32 v129, v129\n"
  "v_mfma_f32_16x16x32_bf16 v[136:139], a[160:163], a[32:35], v[160:163]\n"
  "global_load_lds_dwordx4 v217, s[20:21] offset:2048 sc1\n"
  "v_cvt_pk_bf16_f32 v144, v128, v129\n"
  "v_mfma_f32_16x16x32_bf16 v[136:139], a[164:167], a[36:39], v[136:139]\n"
  "v_exp_f32 v130, v130\n"
  "v_mfma_f32_16x16x32_bf16 v[136:139], a[168:171], a[40:43], v[136:139]\n"
  "global_load_lds_dwordx4 v217, s[20:21] offset:3072 sc1\n"
  "v_mfma_f32_16x16x32_bf16 v[136:139], a[172:175], a[44:47], v[136:139]\n"
  "v_exp_f32 v131, v131\n"
  "v_mfma_f32_16x16x32_bf16 v[140:143], a[188:191], a[44:47], v[160:163]\n"
  "v_exp_f32 v132, v132\n"
  "v_mfma_f32_16x16x32_bf16 v[140:143], a[184:187], a[40:43], v[140:143]\n"
  "s_add_u32 s20, s20, 16384\n"
  "s_addc_u32 s21, s21, 0\n"
  "v_cvt_pk_bf16_f32 v145, v130, v131\n"
  "v_mfma_f32_16x16x32_bf16 v[140:143], a[180:183], a[36:39], v[140:143]\n"
  "v_exp_f32 v133, v133\n"
  "v_mfma_f32_16x16x32_bf16 v[140:143], a[176:179], a[32:35], v[140:143]\n"
  "v_cvt_pk_bf16_f32 v146, v132, v133\n"
  "v_mfma_f32_16x16x32_bf16 v[32:35], a[64:67], v[188:191], v[32:35]\n"
  "s_add_u32 m0, s49, 32768\n"
  "v_exp_f32 v134, v134\n"
  "v_mfma_f32_16x16x32_bf16 v[32:35], a[96:99], v[192:195], v[32:35]\n"
  "v_mfma_f32_16x16x32_bf16 v[36:39], a[100:103], v[192:195], v[36:39]\n"
  "v_exp_f32 v135, v135\n"
  "v_mfma_f32_16x16x32_bf16 v[36:39], a[68:71], v[188:191], v[36:39]\n"
  "v_exp_f32 v136, v136\n"
  "v_mfma_f32_16x16x32_bf16 v[40:43], a[72:75], v[188:191], v[40:43]\n"
  "global_load_lds_dwordx4 v232, s[20:21] offset:0 sc1\n"
  "v_cvt_pk_bf16_f32 v147, v134, v135\n"
  "v_mfma_f32_16x16x32_bf16 v[40:43], a[104:107], v[192:195], v[40:43]\n"
  "v_exp_f32 v137, v137\n"
  "v_mfma_f32_16x16x32_bf16 v[44:47], a[108:111], v[192:195], v[44:47]\n"
  "global_load_lds_dwordx4 v232, s[20:21] offset:1024 sc1\n"
  "v_cvt_pk_bf16_f32 v148, v136, v137\n"
  "v_mfma_f32_16x16x32_bf16 v[44:47], a[76:79], v[188:191], v[44:47]\n"
  "v_exp_f32 v138, v138\n"
  "v_mfma_f32_16x16x32_bf16 v[48:51], a[80:83], v[188:191], v[48:51]\n"
  "v_mfma_f32_16x16x32_bf16 v[48:51], a[112:115], v[192:195], v[48:51]\n"
  "v_exp_f32 v139, v139\n"
  "v_mfma_f32_16x16x32_bf16 v[52:55], a[116:119], v[192:195], v[52:55]\n"
  "v_exp_f32 v140, v140\n"
  "v_mfma_f32_16x16x32_bf16 v[52:55], a[84:87], v[188:191], v[52:55]\n"
  "global_load_lds_dwordx4 v232, s[20:21] offset:2048 sc1\n"
  "v_cvt_pk_bf16_f32 v149, v138, v139\n"
  "v_mfma_f32_16x16x32_bf16 v[56:59], a[88:91], v[188:191], v[56:59]\n"
  "v_exp_f32 v141, v141\n"
  "v_mfma_f32_16x16x32_bf16 v[56:59], a[120:123], v[192:195], v[56:59]\n"
  "global_load_lds_dwordx4 v232, s[20:21] offset:3072 sc1\n"
  "v_cvt_pk_bf16_f32 v150, v140, v141\n"
  "v_mfma_f32_16x16x32_bf16 v[60:63], a[124:127], v[192:195], v[60:63]\n"
  "v_exp_f32 v142, v142\n"
  "v_mfma_f32_16x16x32_bf16 v[60:63], a[92:95], v[188:191], v[60:63]\n"
  "v_mfma_f32_16x16x32_bf16 v[204:207], a[192:195], v[188:191], v[204:207]\n"
  "s_add_u32 m0, s49, 49152\n"
  "v_exp_f32 v143, v143\n"
  "v_mfma_f32_16x16x32_bf16 v[204:207], a[192:195], v[192:195], v[204:207]\n"
  "s_waitcnt vmcnt(12)\n"
  "s_barrier\n"
  "v_cvt_pk_bf16_f32 v151, v142, v143\n"
  "v_mfma_f32_16x16x32_bf16 v[128:131], a[128:131], a[48:51], v[164:167]\n"
  "v_mfma_f32_16x16x32_bf16 v[128:131], a[132:135], a[52:55], v[128:131]\n"
  "ds_read_b128 a[128:131], v216 offset:0\n"
  "v_mfma_f32_16x16x32_bf16 v[128:131], a[136:139], a[56:59], v[128:131]\n"
  "ds_read_b128 a[132:135], v216 offset:1024\n"
  "v_mfma_f32_16x16x32_bf16 v[128:131], a[140:143], a[60:63], v[128:131]\n"
  "s_nop 1\n"
  "ds_read_b128 a[136:139], v216 offset:2048\n"
  "v_mfma_f32_16x16x32_bf16 v[132:135], a[156:159], a[60:63], v[164:167]\n"
  "s_nop 1\n"
  "ds_read_b128 a[140:143], v216 offset:3072\n"
  "v_mfma_f32_16x16x32_bf16 v[132:135], a[152:155], a[56:59], v[132:135]\n"
  "s_nop 1\n"
  "ds_read_b128 a[156:159], v216 offset:7168\n"
  "v_mfma_f32_16x16x32_bf16 v[132:135], a[148:151], a[52:55], v[132:135]\n"
  "v_exp_f32 v128, v128\n"
  "v_mfma_f32_16x16x32_bf16 v[132:135], a[144:147], a[48:51], v[132:135]\n"
  "v_exp_f32 v129, v129\n"
  "v_mfma_f32_16x16x32_bf16 v[136:139], a[160:163], a[48:51], v[164:167]\n"
  "ds_read_b128 a[152:155], v216 offset:6144\n"
  "v_cvt_pk_bf16_f32 v188, v128, v129\n"
  "v_mfma_f32_16x16x32_bf16 v[136:139], a[164:167], a[52:55], v[136:139]\n"
  "v_exp_f32 v130, v130\n"
  "v_mfma_f32_16x16x32_bf16 v[136:139], a[168:171], a[56:59], v[136:139]\n"
  "ds_read_b128 a[148:151], v216 offset:5120\n"
  "ds_read_b128 a[144:147], v216 offset:4096\n"
  "v_mfma_f32_16x16x32_bf16 v[136:139], a[172:175], a[60:63], v[136:139]\n"
  "v_exp_f32 v131, v131\n"
  "v_mfma_f32_16x16x32_bf16 v[140:143], a[188:191], a[60:63], v[164:167]\n"
  "v_exp_f32 v132, v132\n"
  "v_mfma_f32_16x16x32_bf16 v[140:143], a[184:187], a[56:59], v[140:143]\n"
  "ds_read_b128 a[160:163], v216 offset:32768\n"
  "v_cvt_pk_bf16_f32 v189, v130, v131\n"
  "v_mfma_f32_16x16x32_bf16 v[140:143], a[180:183], a[52:55], v[140:143]\n"
  "v_exp_f32 v133, v133\n"
  "v_mfma_f32_16x16x32_bf16 v[140:143], a[176:179], a[48:51], v[140:143]\n"
  "ds_read_b128 a[164:167], v216 offset:33792\n"
  "v_cvt_pk_bf16_f32 v190, v132, v133\n"
  "v_mfma_f32_16x16x32_bf16 v[64:67], a[64:67], v[144:147], v[64:67]\n"
  "v_exp_f32 v134, v134\n"
  "v_mfma_f32_16x16x32_bf16 v[64:67], a[96:99], v[148:151], v[64:67]\n"
  "ds_read_b128 a[168:171], v216 offset:34816\n"
  "ds_read_b128 a[172:175], v216 offset:35840\n"
  "v_mfma_f32_16x16x32_bf16 v[68:71], a[100:103], v[148:151], v[68:71]\n"
  "v_exp_f32 v135, v135\n"
  "v_mfma_f32_16x16x32_bf16 v[68:71], a[68:71], v[144:147], v[68:71]\n"
  "v_exp_f32 v136, v136\n"
  "v_mfma_f32_16x16x32_bf16 v[72:75], a[72:75], v[144:147], v[72:75]\n"
  "ds_read_b128 a[188:191], v216 offset:39936\n"
  "v_cvt_pk_bf16_f32 v191, v134, v135\n"
  "v_mfma_f32_16x16x32_bf16 v[72:75], a[104:107], v[148:151], v[72:75]\n"
  "v_exp_f32 v137, v137\n"
  "v_mfma_f32_16x16x32_bf16 v[76:79], a[108:111], v[148:151], v[76:79]\n"
  "ds_read_b128 a[184:187], v216 offset:38912\n"
  "v_cvt_pk_bf16_f32 v192, v136, v137\n"
  "v_mfma_f32_16x16x32_bf16 v[76:79], a[76:79], v[144:147], v[76:79]\n"
  "v_exp_f32 v138, v138\n"
  "v_mfma_f32_16x16x32_bf16 v[80:83], a[80:83], v[144:147], v[80:83]\n"
  "ds_read_b128 a[180:183], v216 offset:37888\n"
  "ds_read_b128 a[176:179], v216 offset:36864\n"
  "v_mfma_f32_16x16x32_bf16 v[80:83], a[112:115], v[148:151], v[80:83]\n"
  "v_exp_f32 v139, v139\n"
  "v_mfma_f32_16x16x32_bf16 v[84:87], a[116:119], v[148:151], v[84:87]\n"
  "v_exp_f32 v140, v140\n"
  "v_mfma_f32_16x16x32_bf16 v[84:87], a[84:87], v[144:147], v[84:87]\n"
  "v_cvt_pk_bf16_f32 v193, v138, v139\n"
  "v_mfma_f32_16x16x32_bf16 v[88:91], a[88:91], v[144:147], v[88:91]\n"
  "v_exp_f32 v141, v141\n"
  "v_mfma_f32_16x16x32_bf16 v[88:91], a[120:123], v[148:151], v[88:91]\n"
  "v_cvt_pk_bf16_f32 v194, v140, v141\n"
  "v_mfma_f32_16x16x32_bf16 v[92:95], a[124:127], v[148:151], v[92:95]\n"
  "v_exp_f32 v142, v142\n"
  "v_mfma_f32_16x16x32_bf16 v[92:95], a[92:95], v[144:147], v[92:95]\n"
  "v_mfma_f32_16x16x32_bf16 v[208:211], a[192:195], v[144:147], v[208:211]\n"
  "v_exp_f32 v143, v143\n"
  "v_mfma_f32_16x16x32_bf16 v[208:211], a[192:195], v[148:151], v[208:211]\n"
  "s_waitcnt lgkmcnt(0)\n"
  "v_cvt_pk_bf16_f32 v195, v142, v143\n"
  "v_mfma_f32_16x16x32_bf16 v[128:131], a[128:131], a[0:3], v[152:155]\n"
  "v_mfma_f32_16x16x32_bf16 v[128:131], a[132:135], a[4:7], v[128:131]\n"
  "global_load_dwordx2 a[244:245], v253, s[62:63] offset:128 sc1\n"
  "v_mfma_f32_16x16x32_bf16 v[128:131], a[136:139], a[8:11], v[128:131]\n"
  "global_load_lds_dwordx4 v217, s[20:21] offset:0 sc1\n"
  "v_mfma_f32_16x16x32_bf16 v[128:131], a[140:143], a[12:15], v[128:131]\n"
  "s_nop 1\n"
  "global_load_dword v231, v196, s[26:27]\n"
  "v_mfma_f32_16x16x32_bf16 v[132:135], a[156:159], a[12:15], v[152:155]\n"
  "s_nop 1\n"
  "v_mfma_f32_16x16x32_bf16 v[132:135], a[152:155], a[8:11], v[132:135]\n"
  "s_nop 1\n"
  "global_load_lds_dwordx4 v217, s[20:21] offset:1024 sc1\n"
  "v_mfma_f32_16x16x32_bf16 v[132:135], a[148:151], a[4:7], v[132:135]\n"
  "v_exp_f32 v128, v128\n"
  "v_mfma_f32_16x16x32_bf16 v[132:135], a[144:147], a[0:3], v[132:135]\n"
  "v_exp_f32 v129, v129\n"
  "v_mfma_f32_16x16x32_bf16 v[136:139], a[160:163], a[0:3], v[152:155]\n"
  "global_load_lds_dwordx4 v217, s[20:21] offset:2048 sc1\n"
  "v_cvt_pk_bf16_f32 v144, v128, v129\n"
  "v_mfma_f32_16x16x32_bf16 v[136:139], a[164:167], a[4:7], v[136:139]\n"
  "v_exp_f32 v130, v130\n"
  "v_mfma_f32_16x16x32_bf16 v[136:139], a[168:171], a[8:11], v[136:139]\n"
  "v_mfma_f32_16x16x32_bf16 v[136:139], a[172:175], a[12:15], v[136:139]\n"
  "v_exp_f32 v131, v131\n"
  "v_mfma_f32_16x16x32_bf16 v[140:143], a[188:191], a[12:15], v[152:155]\n"
  "v_exp_f32 v132, v132\n"
  "v_mfma_f32_16x16x32_bf16 v[140:143], a[184:187], a[8:11], v[140:143]\n"
  "global_load_lds_dwordx4 v217, s[20:21] offset:3072 sc1\n"
  "v_cvt_pk_bf16_f32 v145, v130, v131\n"
  "v_mfma_f32_16x16x32_bf16 v[140:143], a[180:183], a[4:7], v[140:143]\n"
  "v_exp_f32 v133, v133\n"
  "v_mfma_f32_16x16x32_bf16 v[140:143], a[176:179], a[0:3], v[140:143]\n"
  "s_sub_u32 s20, s20, 81920\n"
  "s_subb_u32 s21, s21, 0\n"
  "v_cvt_pk_bf16_f32 v146, v132, v133\n"
  "v_mfma_f32_16x16x32_bf16 v[96:99], a[64:67], v[188:191], v[96:99]\n"
  "v_exp_f32 v134, v134\n"
  "v_mfma_f32_16x16x32_bf16 v[96:99], a[96:99], v[192:195], v[96:99]\n"
  "ds_read_b128 a[64:67], v216 offset:8192\n"
  "v_mfma_f32_16x16x32_bf16 v[100:103], a[100:103], v[192:195], v[100:103]\n"
  "v_exp_f32 v135, v135\n"
  "v_mfma_f32_16x16x32_bf16 v[100:103], a[68:71], v[188:191], v[100:103]\n"
  "v_exp_f32 v136, v136\n"
  "v_mfma_f32_16x16x32_bf16 v[104:107], a[72:75], v[188:191], v[104:107]\n"
  "ds_read_b128 a[96:99], v216 offset:40960\n"
  "v_cvt_pk_bf16_f32 v147, v134, v135\n"
  "v_mfma_f32_16x16x32_bf16 v[104:107], a[104:107], v[192:195], v[104:107]\n"
  "v_exp_f32 v137, v137\n"
  "v_mfma_f32_16x16x32_bf16 v[108:111], a[108:111], v[192:195], v[108:111]\n"
  "ds_read_b128 a[100:103], v216 offset:41984\n"
  "v_cvt_pk_bf16_f32 v148, v136, v137\n"
  "v_mfma_f32_16x16x32_bf16 v[108:111], a[76:79], v[188:191], v[108:111]\n"
  "v_exp_f32 v138, v138\n"
  "v_mfma_f32_16x16x32_bf16 v[112:115], a[80:83], v[188:191], v[112:115]\n"
  "ds_read_b128 a[68:71], v216 offset:9216\n"
  "ds_read_b128 a[72:75], v216 offset:10240\n"
  "v_mfma_f32_16x16x32_bf16 v[112:115], a[112:115], v[192:195], v[112:115]\n"
  "v_exp_f32 v139, v139\n"
  "v_mfma_f32_16x16x32_bf16 v[116:119], a[116:119], v[192:195], v[116:119]\n"
  "v_exp_f32 v140, v140\n"
  "v_mfma_f32_16x16x32_bf16 v[116:119], a[84:87], v[188:191], v[116:119]\n"
  "ds_read_b128 a[104:107], v216 offset:43008\n"
  "v_cvt_pk_bf16_f32 v149, v138, v139\n"
  "v_mfma_f32_16x16x32_bf16 v[120:123], a[88:91], v[188:191], v[120:123]\n"
  "v_exp_f32 v141, v141\n"
  "v_mfma_f32_16x16x32_bf16 v[120:123], a[120:123], v[192:195], v[120:123]\n"
  "ds_read_b128 a[108:111], v216 offset:44032\n"
  "v_cvt_pk_bf16_f32 v150, v140, v141\n"
  "v_mfma_f32_16x16x32_bf16 v[124:127], a[124:127], v[192:195], v[124:127]\n"
  "v_exp_f32 v142, v142\n"
  "v_mfma_f32_16x16x32_bf16 v[124:127], a[92:95], v[188:191], v[124:127]\n"
  "s_xor_b32 s49, s49, 0x10000\n"
  "ds_read_b128 a[76:79], v216 offset:11264\n"
  "v_mfma_f32_16x16x32_bf16 v[212:215], a[192:195], v[188:191], v[212:215]\n"
  "v_exp_f32 v143, v143\n"
  "v_mfma_f32_16x16x32_bf16 v[212:215], a[192:195], v[192:195], v[212:215]\n"
  "v_cvt_pk_bf16_f32 v151, v142, v143\n"
  "s_setpc_b64 s[22:23]\n"
  "L_SF2_%=:\n"
  "v_mfma_f32_16x16x32_bf16 v[128:131], a[128:131], a[16:19], v[156:159]\n"
  "ds_read_b128 a[80:83], v216 offset:12288\n"
  "ds_read_b128 a[112:115], v216 offset:45056\n"
  "v_mfma_f32_16x16x32_bf16 v[128:131], a[132:135], a[20:23], v[128:131]\n"
  "ds_read_b128 a[116:119], v216 offset:46080\n"
  "ds_read_b128 a[84:87], v216 offset:13312\n"
  "v_mfma_f32_16x16x32_bf16 v[128:131], a[136:139], a[24:27], v[128:131]\n"
  "ds_read_b128 a[88:91], v216 offset:14336\n"
  "ds_read_b128 a[120:123], v216 offset:47104\n"
  "v_mfma_f32_16x16x32_bf16 v[128:131], a[140:143], a[28:31], v[128:131]\n"
  "ds_read_b128 a[124:127], v216 offset:48128\n"
  "ds_read_b128 a[92:95], v216 offset:15360\n"
  "v_mfma_f32_16x16x32_bf16 v[132:135], a[156:159], a[28:31], v[156:159]\n"
  "s_nop 1\n"
  "v_mfma_f32_16x16x32_bf16 v[132:135], a[152:155], a[24:27], v[132:135]\n"
  "s_nop 1\n"
  "v_xor_b32 v216, 0x10000, v216\n"
  "v_mfma_f32_16x16x32_bf16 v[132:135], a[148:151], a[20:23], v[132:135]\n"
  "s_add_u32 s24, s24, 1\n"
  "v_exp_f32 v128, v128\n"
  "v_mfma_f32_16x16x32_bf16 v[132:135], a[144:147], a[16:19], v[132:135]\n"
  "v_exp_f32 v129, v129\n"
  "v_mfma_f32_16x16x32_bf16 v[136:139], a[160:163], a[16:19], v[156:159]\n"
  "v_cvt_pk_bf16_f32 v188, v128, v129\n"
  "v_mfma_f32_16x16x32_bf16 v[136:139], a[164:167], a[20:23], v[136:139]\n"
  "v_exp_f32 v130, v130\n"
  "v_mfma_f32_16x16x32_bf16 v[136:139], a[168:171], a[24:27], v[136:139]\n"
  "v_mfma_f32_16x16x32_bf16 v[136:139], a[172:175], a[28:31], v[136:139]\n"
  "v_exp_f32 v131, v131\n"
  "v_mfma_f32_16x16x32_bf16 v[140:143], a[188:191], a[28:31], v[156:159]\n"
  "v_exp_f32 v132, v132\n"
  "v_mfma_f32_16x16x32_bf16 v[140:143], a[184:187], a[24:27], v[140:143]\n"
  "s_waitcnt lgkmcnt(0)\n"
  "s_barrier\n"
  "v_cvt_pk_bf16_f32 v189, v130, v131\n"
  "v_mfma_f32_16x16x32_bf16 v[140:143], a[180:183], a[20:23], v[140:143]\n"
  "v_exp_f32 v133, v133\n"
  "v_mfma_f32_16x16x32_bf16 v[140:143], a[176:179], a[16:19], v[140:143]\n"
  "v_cvt_pk_bf16_f32 v190, v132, v133\n"
  "v_mfma_f32_16x16x32_bf16 v[0:3], a[64:67], v[144:147], v[0:3]\n"
  "s_add_u32 m0, s49, 0\n"
  "v_exp_f32 v134, v134\n"
  "v_mfma_f32_16x16x32_bf16 v[0:3], a[96:99], v[148:151], v[0:3]\n"
  "v_mfma_f32_16x16x32_bf16 v[4:7], a[100:103], v[148:151], v[4:7]\n"
  "v_exp_f32 v135, v135\n"
  "v_mfma_f32_16x16x32_bf16 v[4:7], a[68:71], v[144:147], v[4:7]\n"
  "v_exp_f32 v136, v136\n"
  "v_mfma_f32_16x16x32_bf16 v[8:11], a[72:75], v[144:147], v[8:11]\n"
  "v_cvt_pk_bf16_f32 v191, v134, v135\n"
  "v_mfma_f32_16x16x32_bf16 v[8:11], a[104:107], v[148:151], v[8:11]\n"
  "v_exp_f32 v137, v137\n"
  "v_mfma_f32_16x16x32_bf16 v[12:15], a[108:111], v[148:151], v[12:15]\n"
  "global_load_lds_dwordx4 v232, s[20:21] offset:0 sc1\n"
  "v_cvt_pk_bf16_f32 v192, v136, v137\n"
  "v_mfma_f32_16x16x32_bf16 v[12:15], a[76:79], v[144:147], v[12:15]\n"
  "v_exp_f32 v138, v138\n"
  "v_mfma_f32_16x16x32_bf16 v[16:19], a[80:83], v[144:147], v[16:19]\n"
  "v_mfma_f32_16x16x32_bf16 v[16:19], a[112:115], v[148:151], v[16:19]\n"
  "v_exp_f32 v139, v139\n"
  "v_mfma_f32_16x16x32_bf16 v[20:23], a[116:119], v[148:151], v[20:23]\n"
  "v_exp_f32 v140, v140\n"
  "v_mfma_f32_16x16x32_bf16 v[20:23], a[84:87], v[144:147], v[20:23]\n"
  "global_load_lds_dwordx4 v232, s[20:21] offset:1024 sc1\n"
  "v_cvt_pk_bf16_f32 v193, v138, v139\n"
  "v_mfma_f32_16x16x32_bf16 v[24:27], a[88:91], v[144:147], v[24:27]\n"
  "v_exp_f32 v141, v141\n"
  "v_mfma_f32_16x16x32_bf16 v[24:27], a[120:123], v[148:151], v[24:27]\n"
  "v_cvt_pk_bf16_f32 v194, v140, v141\n"
  "v_mfma_f32_16x16x32_bf16 v[28:31], a[124:127], v[148:151], v[28:31]\n"
  "v_exp_f32 v142, v142\n"
  "v_mfma_f32_16x16x32_bf16 v[28:31], a[92:95], v[144:147], v[28:31]\n"
  "global_load_lds_dwordx4 v232, s[20:21] offset:2048 sc1\n"
  "v_mfma_f32_16x16x32_bf16 v[200:203], a[192:195], v[144:147], v[200:203]\n"
  "v_exp_f32 v143, v143\n"
  "v_mfma_f32_16x16x32_bf16 v[200:203], a[192:195], v[148:151], v[200:203]\n"
  "v_cvt_pk_bf16_f32 v195, v142, v143\n"
  "v_mfma_f32_16x16x32_bf16 v[128:131], a[128:131], a[32:35], v[160:163]\n"
  "v_mfma_f32_16x16x32_bf16 v[128:131], a[132:135], a[36:39], v[128:131]\n"
  "v_mfma_f32_16x16x32_bf16 v[128:131], a[136:139], a[40:43], v[128:131]\n"
  "v_mfma_f32_16x16x32_bf16 v[128:131], a[140:143], a[44:47], v[128:131]\n"
  "s_nop 1\n"
  "global_load_lds_dwordx4 v232, s[20:21] offset:3072 sc1\n"
  "v_mfma_f32_16x16x32_bf16 v[132:135], a[156:159], a[44:47], v[160:163]\n"
  "s_nop 1\n"
  "v_mfma_f32_16x16x32_bf16 v[132:135], a[152:155], a[40:43], v[132:135]\n"
  "s_nop 1\n"
  "v_mfma_f32_16x16x32_bf16 v[132:135], a[148:151], a[36:39], v[132:135]\n"
  "v_exp_f32 v128, v128\n"
  "v_mfma_f32_16x16x32_bf16 v[132:135], a[144:147], a[32:35], v[132:135]\n"
  "v_exp_f32 v129, v129\n"
  "v_mfma_f32_16x16x32_bf16 v[136:139], a[160:163], a[32:35], v[160:163]\n"
  "s_add_u32 s20, s20, 16384\n"
  "s_addc_u32 s21, s21, 0\n"
  "v_cvt_pk_bf16_f32 v144, v128, v129\n"
  "v_mfma_f32_16x16x32_bf16 v[136:139], a[164:167], a[36:39], v[136:139]\n"
  "v_exp_f32 v130, v130\n"
  "v_mfma_f32_16x16x32_bf16 v[136:139], a[168:171], a[40:43], v[136:139]\n"
  "v_mfma_f32_16x16x32_bf16 v[136:139], a[172:175], a[44:47], v[136:139]\n"
  "v_exp_f32 v131, v131\n"
  "v_mfma_f32_16x16x32_bf16 v[140:143], a[188:191], a[44:47], v[160:163]\n"
  "v_exp_f32 v132, v132\n"
  "v_mfma_f32_16x16x32_bf16 v[140:143], a[184:187], a[40:43], v[140:143]\n"
  "s_add_u32 m0, s49, 32768\n"
  "v_cvt_pk_bf16_f32 v145, v130, v131\n"
  "v_mfma_f32_16x16x32_bf16 v[140:143], a[180:183], a[36:39], v[140:143]\n"
  "v_exp_f32 v133, v133\n"
  "v_mfma_f32_16x16x32_bf16 v[140:143], a[176:179], a[32:35], v[140:143]\n"
  "v_cvt_pk_bf16_f32 v146, v132, v133\n"
  "v_mfma_f32_16x16x32_bf16 v[32:35], a[64:67], v[188:191], v[32:35]\n"
  "v_exp_f32 v134, v134\n"
  "v_mfma_f32_16x16x32_bf16 v[32:35], a[96:99], v[192:195], v[32:35]\n"
  "v_mfma_f32_16x16x32_bf16 v[36:39], a[100:103], v[192:195], v[36:39]\n"
  "v_exp_f32 v135, v135\n"
  "v_mfma_f32_16x16x32_bf16 v[36:39], a[68:71], v[188:191], v[36:39]\n"
  "v_exp_f32 v136, v136\n"
  "v_mfma_f32_16x16x32_bf16 v[40:43], a[72:75], v[188:191], v[40:43]\n"
  "global_load_lds_dwordx4 v232, s[20:21] offset:0 sc1\n"
  "v_cvt_pk_bf16_f32 v147, v134, v135\n"
  "v_mfma_f32_16x16x32_bf16 v[40:43], a[104:107], v[192:195], v[40:43]\n"
  "v_exp_f32 v137, v137\n"
  "v_mfma_f32_16x16x32_bf16 v[44:47], a[108:111], v[192:195], v[44:47]\n"
  "v_cvt_pk_bf16_f32 v148, v136, v137\n"
  "v_mfma_f32_16x16x32_bf16 v[44:47], a[76:79], v[188:191], v[44:47]\n"
  "v_exp_f32 v138, v138\n"
  "v_mfma_f32_16x16x32_bf16 v[48:51], a[80:83], v[188:191], v[48:51]\n"
  "global_load_lds_dwordx4 v232, s[20:21] offset:1024 sc1\n"
  "v_mfma_f32_16x16x32_bf16 v[48:51], a[112:115], v[192:195], v[48:51]\n"
  "v_exp_f32 v139, v139\n"
  "v_mfma_f32_16x16x32_bf16 v[52:55], a[116:119], v[192:195], v[52:55]\n"
  "v_exp_f32 v140, v140\n"
  "v_mfma_f32_16x16x32_bf16 v[52:55], a[84:87], v[188:191], v[52:55]\n"
  "v_cvt_pk_bf16_f32 v149, v138, v139\n"
  "v_mfma_f32_16x16x32_bf16 v[56:59], a[88:91], v[188:191], v[56:59]\n"
  "v_exp_f32 v141, v141\n"
  "v_mfma_f32_16x16x32_bf16 v[56:59], a[120:123], v[192:195], v[56:59]\n"
  "global_load_lds_dwordx4 v232, s[20:21] offset:2048 sc1\n"
  "v_cvt_pk_bf16_f32 v150, v140, v141\n"
  "v_mfma_f32_16x16x32_bf16 v[60:63], a[124:127], v[192:195], v[60:63]\n"
  "v_exp_f32 v142, v142\n"
  "v_mfma_f32_16x16x32_bf16 v[60:63], a[92:95], v[188:191], v[60:63]\n"
  "v_mfma_f32_16x16x32_bf16 v[204:207], a[192:195], v[188:191], v[204:207]\n"
  "v_exp_f32 v143, v143\n"
  "v_mfma_f32_16x16x32_bf16 v[204:207], a[192:195], v[192:195], v[204:207]\n"
  "s_waitcnt vmcnt(8)\n"
  "s_barrier\n"
  "v_cvt_pk_bf16_f32 v151, v142, v143\n"
  "v_mfma_f32_16x16x32_bf16 v[128:131], a[128:131], a[48:51], v[164:167]\n"
  "v_mfma_f32_16x16x32_bf16 v[128:131], a[132:135], a[52:55], v[128:131]\n"
  "ds_read_b128 a[128:131], v216 offset:0\n"
  "v_mfma_f32_16x16x32_bf16 v[128:131], a[136:139], a[56:59], v[128:131]\n"
  "ds_read_b128 a[132:135], v216 offset:1024\n"
  "v_mfma_f32_16x16x32_bf16 v[128:131], a[140:143], a[60:63], v[128:131]\n"
  "s_nop 1\n"
  "ds_read_b128 a[136:139], v216 offset:2048\n"
  "v_mfma_f32_16x16x32_bf16 v[132:135], a[156:159], a[60:63], v[164:167]\n"
  "s_nop 1\n"
  "ds_read_b128 a[140:143], v216 offset:3072\n"
  "v_mfma_f32_16x16x32_bf16 v[132:135], a[152:155], a[56:59], v[132:135]\n"
  "s_nop 1\n"
  "ds_read_b128 a[156:159], v216 offset:7168\n"
  "v_mfma_f32_16x16x32_bf16 v[132:135], a[148:151], a[52:55], v[132:135]\n"
  "v_exp_f32 v128, v128\n"
  "v_mfma_f32_16x16x32_bf16 v[132:135], a[144:147], a[48:51], v[132:135]\n"
  "v_exp_f32 v129, v129\n"
  "v_mfma_f32_16x16x32_bf16 v[136:139], a[160:163], a[48:51], v[164:167]\n"
  "ds_read_b128 a[152:155], v216 offset:6144\n"
  "v_cvt_pk_bf16_f32 v188, v128, v129\n"
  "v_mfma_f32_16x16x32_bf16 v[136:139], a[164:167], a[52:55], v[136:139]\n"
  "v_exp_f32 v130, v130\n"
  "v_mfma_f32_16x16x32_bf16 v[136:139], a[168:171], a[56:59], v[136:139]\n"
  "ds_read_b128 a[148:151], v216 offset:5120\n"
  "ds_read_b128 a[144:147], v216 offset:4096\n"
  "v_mfma_f32_16x16x32_bf16 v[136:139], a[172:175], a[60:63], v[136:139]\n"
  "v_exp_f32 v131, v131\n"
  "v_mfma_f32_16x16x32_bf16 v[140:143], a[188:191], a[60:63], v[164:167]\n"
  "v_exp_f32 v132, v132\n"
  "v_mfma_f32_16x16x32_bf16 v[140:143], a[184:187], a[56:59], v[140:143]\n"
  "ds_read_b128 a[160:163], v216 offset:32768\n"
  "v_cvt_pk_bf16_f32 v189, v130, v131\n"
  "v_mfma_f32_16x16x32_bf16 v[140:143], a[180:183], a[52:55], v[140:143]\n"
  "v_exp_f32 v133, v133\n"
  "v_mfma_f32_16x16x32_bf16 v[140:143], a[176:179], a[48:51], v[140:143]\n"
  "ds_read_b128 a[164:167], v216 offset:33792\n"
  "v_cvt_pk_bf16_f32 v190, v132, v133\n"
  "v_mfma_f32_16x16x32_bf16 v[64:67], a[64:67], v[144:147], v[64:67]\n"
  "v_exp_f32 v134, v134\n"
  "v_mfma_f32_16x16x32_bf16 v[64:67], a[96:99], v[148:151], v[64:67]\n"
  "ds_read_b128 a[168:171], v216 offset:34816\n"
  "ds_read_b128 a[172:175], v216 offset:35840\n"
  "v_mfma_f32_16x16x32_bf16 v[68:71], a[100:103], v[148:151], v[68:71]\n"
  "v_exp_f32 v135, v135\n"
  "v_mfma_f32_16x16x32_bf16 v[68:71], a[68:71], v[144:147], v[68:71]\n"
  "v_exp_f32 v136, v136\n"
  "v_mfma_f32_16x16x32_bf16 v[72:75], a[72:75], v[144:147], v[72:75]\n"
  "ds_read_b128 a[188:191], v216 offset:39936\n"
  "v_cvt_pk_bf16_f32 v191, v134, v135\n"
  "v_mfma_f32_16x16x32_bf16 v[72:75], a[104:107], v[148:151], v[72:75]\n"
  "v_exp_f32 v137, v137\n"
  "v_mfma_f32_16x16x32_bf16 v[76:79], a[108:111], v[148:151], v[76:79]\n"
  "ds_read_b128 a[184:187], v216 offset:38912\n"
  "v_cvt_pk_bf16_f32 v192, v136, v137\n"
  "v_mfma_f32_16x16x32_bf16 v[76:79], a[76:79], v[144:147], v[76:79]\n"
  "v_exp_f32 v138, v138\n"
  "v_mfma_f32_16x16x32_bf16 v[80:83], a[80:83], v[144:147], v[80:83]\n"
  "ds_read_b128 a[180:183], v216 offset:37888\n"
  "ds_read_b128 a[176:179], v216 offset:36864\n"
  "v_mfma_f32_16x16x32_bf16 v[80:83], a[112:115], v[148:151], v[80:83]\n"
  "v_exp_f32 v139, v139\n"
  "v_mfma_f32_16x16x32_bf16 v[84:87], a[116:119], v[148:151], v[84:87]\n"
  "v_exp_f32 v140, v140\n"
  "v_mfma_f32_16x16x32_bf16 v[84:87], a[84:87], v[144:147], v[84:87]\n"
  "v_cvt_pk_bf16_f32 v193, v138, v139\n"
  "v_mfma_f32_16x16x32_bf16 v[88:91], a[88:91], v[144:147], v[88:91]\n"
  "v_exp_f32 v141, v141\n"
  "v_mfma_f32_16x16x32_bf16 v[88:91], a[120:123], v[148:151], v[88:91]\n"
  "v_cvt_pk_bf16_f32 v194, v140, v141\n"
  "v_mfma_f32_16x16x32_bf16 v[92:95], a[124:127], v[148:151], v[92:95]\n"
  "v_exp_f32 v142, v142\n"
  "v_mfma_f32_16x16x32_bf16 v[92:95], a[92:95], v[144:147], v[92:95]\n"
  "v_mfma_f32_16x16x32_bf16 v[208:211], a[192:195], v[144:147], v[208:211]\n"
  "v_exp_f32 v143, v143\n"
  "v_mfma_f32_16x16x32_bf16 v[208:211], a[192:195], v[148:151], v[208:211]\n"
  "s_waitcnt lgkmcnt(0)\n"
  "v_cvt_pk_bf16_f32 v195, v142, v143\n"
  "v_mfma_f32_16x16x32_bf16 v[128:131], a[128:131], a[0:3], v[152:155]\n"
  "v_mfma_f32_16x16x32_bf16 v[128:131], a[132:135], a[4:7], v[128:131]\n"
  "global_load_dwordx2 a[244:245], v253, s[62:63] offset:128 sc1\n"
  "v_mfma_f32_16x16x32_bf16 v[128:131], a[136:139], a[8:11], v[128:131]\n"
  "global_load_lds_dwordx4 v232, s[20:21] offset:3072 sc1\n"
  "v_mfma_f32_16x16x32_bf16 v[128:131], a[140:143], a[12:15], v[128:131]\n"
  "s_nop 1\n"
  "v_mfma_f32_16x16x32_bf16 v[132:135], a[156:159], a[12:15], v[152:155]\n"
  "s_nop 1\n"
  "v_mfma_f32_16x16x32_bf16 v[132:135], a[152:155], a[8:11], v[132:135]\n"
  "s_nop 1\n"
  "v_mfma_f32_16x16x32_bf16 v[132:135], a[148:151], a[4:7], v[132:135]\n"
  "v_exp_f32 v128, v128\n"
  "v_mfma_f32_16x16x32_bf16 v[132:135], a[144:147], a[0:3], v[132:135]\n"
  "v_exp_f32 v129, v129\n"
  "v_mfma_f32_16x16x32_bf16 v[136:139], a[160:163], a[0:3], v[152:155]\n"
  "s_cmp_eq_u32 s24, s31\n"
  "s_cselect_b32 s36, s25, 0x4000\n"
  "v_cvt_pk_bf16_f32 v144, v128, v129\n"
  "v_mfma_f32_16x16x32_bf16 v[136:139], a[164:167], a[4:7], v[136:139]\n"
  "v_exp_f32 v130, v130\n"
  "v_mfma_f32_16x16x32_bf16 v[136:139], a[168:171], a[8:11], v[136:139]\n"
  "v_mfma_f32_16x16x32_bf16 v[136:139], a[172:175], a[12:15], v[136:139]\n"
  "v_exp_f32 v131, v131\n"
  "v_mfma_f32_16x16x32_bf16 v[140:143], a[188:191], a[12:15], v[152:155]\n"
  "v_exp_f32 v132, v132\n"
  "v_mfma_f32_16x16x32_bf16 v[140:143], a[184:187], a[8:11], v[140:143]\n"
  "s_add_u32 s20, s20, s36\n"
  "s_addc_u32 s21, s21, 0\n"
  "v_cvt_pk_bf16_f32 v145, v130, v131\n"
  "v_mfma_f32_16x16x32_bf16 v[140:143], a[180:183], a[4:7], v[140:143]\n"
  "v_exp_f32 v133, v133\n"
  "v_mfma_f32_16x16x32_bf16 v[140:143], a[176:179], a[0:3], v[140:143]\n"
  "v_cvt_pk_bf16_f32 v146, v132, v133\n"
  "v_mfma_f32_16x16x32_bf16 v[96:99], a[64:67], v[188:191], v[96:99]\n"
  "v_exp_f32 v134, v134\n"
  "v_mfma_f32_16x16x32_bf16 v[96:99], a[96:99], v[192:195], v[96:99]\n"
  "ds_read_b128 a[64:67], v216 offset:8192\n"
  "v_mfma_f32_16x16x32_bf16 v[100:103], a[100:103], v[192:195], v[100:103]\n"
  "v_exp_f32 v135, v135\n"
  "v_mfma_f32_16x16x32_bf16 v[100:103], a[68:71], v[188:191], v[100:103]\n"
  "v_exp_f32 v136, v136\n"
  "v_mfma_f32_16x16x32_bf16 v[104:107], a[72:75], v[188:191], v[104:107]\n"
  "ds_read_b128 a[96:99], v216 offset:40960\n"
  "v_cvt_pk_bf16_f32 v147, v134, v135\n"
  "v_mfma_f32_16x16x32_bf16 v[104:107], a[104:107], v[192:195], v[104:107]\n"
  "v_exp_f32 v137, v137\n"
  "v_mfma_f32_16x16x32_bf16 v[108:111], a[108:111], v[192:195], v[108:111]\n"
  "ds_read_b128 a[100:103], v216 offset:41984\n"
  "v_cvt_pk_bf16_f32 v148, v136, v137\n"
  "v_mfma_f32_16x16x32_bf16 v[108:111], a[76:79], v[188:191], v[108:111]\n"
  "v_exp_f32 v138, v138\n"
  "v_mfma_f32_16x16x32_bf16 v[112:115], a[80:83], v[188:191], v[112:115]\n"
  "ds_read_b128 a[68:71], v216 offset:9216\n"
  "ds_read_b128 a[72:75], v216 offset:10240\n"
  "v_mfma_f32_16x16x32_bf16 v[112:115], a[112:115], v[192:195], v[112:115]\n"
  "v_exp_f32 v139, v139\n"
  "v_mfma_f32_16x16x32_bf16 v[116:119], a[116:119], v[192:195], v[116:119]\n"
  "v_exp_f32 v140, v140\n"
  "v_mfma_f32_16x16x32_bf16 v[116:119], a[84:87], v[188:191], v[116:119]\n"
  "ds_read_b128 a[104:107], v216 offset:43008\n"
  "v_cvt_pk_bf16_f32 v149, v138, v139\n"
  "v_mfma_f32_16x16x32_bf16 v[120:123], a[88:91], v[188:191], v[120:123]\n"
  "v_exp_f32 v141, v141\n"
  "v_mfma_f32_16x16x32_bf16 v[120:123], a[120:123], v[192:195], v[120:123]\n"
  "ds_read_b128 a[108:111], v216 offset:44032\n"
  "v_cvt_pk_bf16_f32 v150, v140, v141\n"
  "v_mfma_f32_16x16x32_bf16 v[124:127], a[124:127], v[192:195], v[124:127]\n"
  "v_exp_f32 v142, v142\n"
  "v_mfma_f32_16x16x32_bf16 v[124:127], a[92:95], v[188:191], v[124:127]\n"
  "s_xor_b32 s49, s49, 0x10000\n"
  "ds_read_b128 a[76:79], v216 offset:11264\n"
  "v_mfma_f32_16x16x32_bf16 v[212:215], a[192:195], v[188:191], v[212:215]\n"
  "v_exp_f32 v143, v143\n"
  "v_mfma_f32_16x16x32_bf16 v[212:215], a[192:195], v[192:195], v[212:215]\n"
  "v_cvt_pk_bf16_f32 v151, v142, v143\n"
  "L_SFF_%=:\n"
  "v_mfma_f32_16x16x32_bf16 v[128:131], a[128:131], a[16:19], v[156:159]\n"
  "ds_read_b128 a[80:83], v216 offset:12288\n"
  "ds_read_b128 a[112:115], v216 offset:45056\n"
  "v_mfma_f32_16x16x32_bf16 v[128:131], a[132:135], a[20:23], v[128:131]\n"
  "ds_read_b128 a[116:119], v216 offset:46080\n"
  "ds_read_b128 a[84:87], v216 offset:13312\n"
  "v_mfma_f32_16x16x32_bf16 v[128:131], a[136:139], a[24:27], v[128:131]\n"
  "ds_read_b128 a[88:91], v216 offset:14336\n"
  "ds_read_b128 a[120:123], v216 offset:47104\n"
  "v_mfma_f32_16x16x32_bf16 v[128:131], a[140:143], a[28:31], v[128:131]\n"
  "ds_read_b128 a[124:127], v216 offset:48128\n"
  "ds_read_b128 a[92:95], v216 offset:15360\n"
  "v_mfma_f32_16x16x32_bf16 v[132:135], a[156:159], a[28:31], v[156:159]\n"
  "s_nop 1\n"
  "v_mfma_f32_16x16x32_bf16 v[132:135], a[152:155], a[24:27], v[132:135]\n"
  "s_nop 1\n"
  "v_xor_b32 v216, 0x10000, v216\n"
  "v_mfma_f32_16x16x32_bf16 v[132:135], a[148:151], a[20:23], v[132:135]\n"
  "s_add_u32 s24, s24, 1\n"
  "v_exp_f32 v128, v128\n"
  "v_mfma_f32_16x16x32_bf16 v[132:135], a[144:147], a[16:19], v[132:135]\n"
  "v_exp_f32 v129, v129\n"
  "v_mfma_f32_16x16x32_bf16 v[136:139], a[160:163], a[16:19], v[156:159]\n"
  "v_cvt_pk_bf16_f32 v188, v128, v129\n"
  "v_readlane_b32 s22, v252, s24\n"
  "v_mfma_f32_16x16x32_bf16 v[136:139], a[164:167], a[20:23], v[136:139]\n"
  "v_exp_f32 v130, v130\n"
  "v_mfma_f32_16x16x32_bf16 v[136:139], a[168:171], a[24:27], v[136:139]\n"
  "s_add_u32 s22, s26, s22\n"
  "s_addc_u32 s23, s27, 0\n"
  "v_mfma_f32_16x16x32_bf16 v[136:139], a[172:175], a[28:31], v[136:139]\n"
  "v_exp_f32 v131, v131\n"
  "v_mfma_f32_16x16x32_bf16 v[140:143], a[188:191], a[28:31], v[156:159]\n"
  "v_exp_f32 v132, v132\n"
  "v_mfma_f32_16x16x32_bf16 v[140:143], a[184:187], a[24:27], v[140:143]\n"
  "s_waitcnt lgkmcnt(0)\n"
  "s_barrier\n"
  "v_cvt_pk_bf16_f32 v189, v130, v131\n"
  "v_mfma_f32_16x16x32_bf16 v[140:143], a[180:183], a[20:23], v[140:143]\n"
  "v_exp_f32 v133, v133\n"
  "v_mfma_f32_16x16x32_bf16 v[140:143], a[176:179], a[16:19], v[140:143]\n"
  "v_cvt_pk_bf16_f32 v190, v132, v133\n"
  "v_mfma_f32_16x16x32_bf16 v[0:3], a[64:67], v[144:147], v[0:3]\n"
  "s_add_u32 m0, s49, 0\n"
  "v_exp_f32 v134, v134\n"
  "v_mfma_f32_16x16x32_bf16 v[0:3], a[96:99], v[148:151], v[0:3]\n"
  "v_mfma_f32_16x16x32_bf16 v[4:7], a[100:103], v[148:151], v[4:7]\n"
  "v_exp_f32 v135, v135\n"
  "v_mfma_f32_16x16x32_bf16 v[4:7], a[68:71], v[144:147], v[4:7]\n"
  "v_exp_f32 v136, v136\n"
  "v_mfma_f32_16x16x32_bf16 v[8:11], a[72:75], v[144:147], v[8:11]\n"
  "v_cvt_pk_bf16_f32 v191, v134, v135\n"
  "v_mfma_f32_16x16x32_bf16 v[8:11], a[104:107], v[148:151], v[8:11]\n"
  "v_exp_f32 v137, v137\n"
  "v_mfma_f32_16x16x32_bf16 v[12:15], a[108:111], v[148:151], v[12:15]\n"
  "global_load_lds_dwordx4 v232, s[20:21] offset:0 sc1\n"
  "v_cvt_pk_bf16_f32 v192, v136, v137\n"
  "v_mfma_f32_16x16x32_bf16 v[12:15], a[76:79], v[144:147], v[12:15]\n"
  "v_exp_f32 v138, v138\n"
  "v_mfma_f32_16x16x32_bf16 v[16:19], a[80:83], v[144:147], v[16:19]\n"
  "v_mfma_f32_16x16x32_bf16 v[16:19], a[112:115], v[148:151], v[16:19]\n"
  "v_exp_f32 v139, v139\n"
  "v_mfma_f32_16x16x32_bf16 v[20:23], a[116:119], v[148:151], v[20:23]\n"
  "v_exp_f32 v140, v140\n"
  "v_mfma_f32_16x16x32_bf16 v[20:23], a[84:87], v[144:147], v[20:23]\n"
  "global_load_lds_dwordx4 v232, s[20:21] offset:1024 sc1\n"
  "v_cvt_pk_bf16_f32 v193, v138, v139\n"
  "v_mfma_f32_16x16x32_bf16 v[24:27], a[88:91], v[144:147], v[24:27]\n"
  "v_exp_f32 v141, v141\n"
  "v_mfma_f32_16x16x32_bf16 v[24:27], a[120:123], v[148:151], v[24:27]\n"
  "v_cvt_pk_bf16_f32 v194, v140, v141\n"
  "v_mfma_f32_16x16x32_bf16 v[28:31], a[124:127], v[148:151], v[28:31]\n"
  "v_exp_f32 v142, v142\n"
  "v_mfma_f32_16x16x32_bf16 v[28:31], a[92:95], v[144:147], v[28:31]\n"
  "global_load_lds_dwordx4 v232, s[20:21] offset:2048 sc1\n"
  "v_mfma_f32_16x16x32_bf16 v[200:203], a[192:195], v[144:147], v[200:203]\n"
  "v_exp_f32 v143, v143\n"
  "v_mfma_f32_16x16x32_bf16 v[200:203], a[192:195], v[148:151], v[200:203]\n"
  "v_cvt_pk_bf16_f32 v195, v142, v143\n"
  "v_mfma_f32_16x16x32_bf16 v[128:131], a[128:131], a[32:35], v[160:163]\n"
  "v_mfma_f32_16x16x32_bf16 v[128:131], a[132:135], a[36:39], v[128:131]\n"
  "v_mfma_f32_16x16x32_bf16 v[128:131], a[136:139], a[40:43], v[128:131]\n"
  "v_mfma_f32_16x16x32_bf16 v[128:131], a[140:143], a[44:47], v[128:131]\n"
  "s_nop 1\n"
  "global_load_lds_dwordx4 v232, s[20:21] offset:3072 sc1\n"
  "v_mfma_f32_16x16x32_bf16 v[132:135], a[156:159], a[44:47], v[160:163]\n"
  "s_nop 1\n"
  "v_mfma_f32_16x16x32_bf16 v[132:135], a[152:155], a[40:43], v[132:135]\n"
  "s_nop 1\n"
  "v_mfma_f32_16x16x32_bf16 v[132:135], a[148:151], a[36:39], v[132:135]\n"
  "v_exp_f32 v128, v128\n"
  "v_mfma_f32_16x16x32_bf16 v[132:135], a[144:147], a[32:35], v[132:135]\n"
  "v_exp_f32 v129, v129\n"
  "v_mfma_f32_16x16x32_bf16 v[136:139], a[160:163], a[32:35], v[160:163]\n"
  "s_add_u32 s20, s20, 16384\n"
  "s_addc_u32 s21, s21, 0\n"
  "v_cvt_pk_bf16_f32 v144, v128, v129\n"
  "v_mfma_f32_16x16x32_bf16 v[136:139], a[164:167], a[36:39], v[136:139]\n"
  "v_exp_f32 v130, v130\n"
  "v_mfma_f32_16x16x32_bf16 v[136:139], a[168:171], a[40:43], v[136:139]\n"
  "v_mfma_f32_16x16x32_bf16 v[136:139], a[172:175], a[44:47], v[136:139]\n"
  "v_exp_f32 v131, v131\n"
  "v_mfma_f32_16x16x32_bf16 v[140:143], a[188:191], a[44:47], v[160:163]\n"
  "v_exp_f32 v132, v132\n"
  "v_mfma_f32_16x16x32_bf16 v[140:143], a[184:187], a[40:43], v[140:143]\n"
  "s_add_u32 m0, s49, 32768\n"
  "v_cvt_pk_bf16_f32 v145, v130, v131\n"
  "v_mfma_f32_16x16x32_bf16 v[140:143], a[180:183], a[36:39], v[140:143]\n"
  "v_exp_f32 v133, v133\n"
  "v_mfma_f32_16x16x32_bf16 v[140:143], a[176:179], a[32:35], v[140:143]\n"
  "v_cvt_pk_bf16_f32 v146, v132, v133\n"
  "v_mfma_f32_16x16x32_bf16 v[32:35], a[64:67], v[188:191], v[32:35]\n"
  "v_exp_f32 v134, v134\n"
  "v_mfma_f32_16x16x32_bf16 v[32:35], a[96:99], v[192:195], v[32:35]\n"
  "v_mfma_f32_16x16x32_bf16 v[36:39], a[100:103], v[192:195], v[36:39]\n"
  "v_exp_f32 v135, v135\n"
  "v_mfma_f32_16x16x32_bf16 v[36:39], a[68:71], v[188:191], v[36:39]\n"
  "v_exp_f32 v136, v136\n"
  "v_mfma_f32_16x16x32_bf16 v[40:43], a[72:75], v[188:191], v[40:43]\n"
  "global_load_lds_dwordx4 v232, s[20:21] offset:0 sc1\n"
  "v_cvt_pk_bf16_f32 v147, v134, v135\n"
  "v_mfma_f32_16x16x32_bf16 v[40:43], a[104:107], v[192:195], v[40:43]\n"
  "v_exp_f32 v137, v137\n"
  "v_mfma_f32_16x16x32_bf16 v[44:47], a[108:111], v[192:195], v[44:47]\n"
  "v_cvt_pk_bf16_f32 v148, v136, v137\n"
  "v_mfma_f32_16x16x32_bf16 v[44:47], a[76:79], v[188:191], v[44:47]\n"
  "v_exp_f32 v138, v138\n"
  "v_mfma_f32_16x16x32_bf16 v[48:51], a[80:83], v[188:191], v[48:51]\n"
  "global_load_lds_dwordx4 v232, s[20:21] offset:1024 sc1\n"
  "v_mfma_f32_16x16x32_bf16 v[48:51], a[112:115], v[192:195], v[48:51]\n"
  "v_exp_f32 v139, v139\n"
  "v_mfma_f32_16x16x32_bf16 v[52:55], a[116:119], v[192:195], v[52:55]\n"
  "v_exp_f32 v140, v140\n"
  "v_mfma_f32_16x16x32_bf16 v[52:55], a[84:87], v[188:191], v[52:55]\n"
  "v_cvt_pk_bf16_f32 v149, v138, v139\n"
  "v_mfma_f32_16x16x32_bf16 v[56:59], a[88:91], v[188:191], v[56:59]\n"
  "v_exp_f32 v141, v141\n"
  "v_mfma_f32_16x16x32_bf16 v[56:59], a[120:123], v[192:195], v[56:59]\n"
  "global_load_lds_dwordx4 v232, s[20:21] offset:2048 sc1\n"
  "v_cvt_pk_bf16_f32 v150, v140, v141\n"
  "v_mfma_f32_16x16x32_bf16 v[60:63], a[124:127], v[192:195], v[60:63]\n"
  "v_exp_f32 v142, v142\n"
  "v_mfma_f32_16x16x32_bf16 v[60:63], a[92:95], v[188:191], v[60:63]\n"
  "v_mfma_f32_16x16x32_bf16 v[204:207], a[192:195], v[188:191], v[204:207]\n"
  "v_exp_f32 v143, v143\n"
  "v_mfma_f32_16x16x32_bf16 v[204:207], a[192:195], v[192:195], v[204:207]\n"
  "s_waitcnt vmcnt(7)\n"
  "s_barrier\n"
  "v_cvt_pk_bf16_f32 v151, v142, v143\n"
  "v_mfma_f32_16x16x32_bf16 v[128:131], a[128:131], a[48:51], v[164:167]\n"
  "v_mfma_f32_16x16x32_bf16 v[128:131], a[132:135], a[52:55], v[128:131]\n"
  "ds_read_b128 a[128:131], v216 offset:0\n"
  "v_mfma_f32_16x16x32_bf16 v[128:131], a[136:139], a[56:59], v[128:131]\n"
  "ds_read_b128 a[132:135], v216 offset:1024\n"
  "v_mfma_f32_16x16x32_bf16 v[128:131], a[140:143], a[60:63], v[128:131]\n"
  "s_nop 1\n"
  "ds_read_b128 a[136:139], v216 offset:2048\n"
  "v_mfma_f32_16x16x32_bf16 v[132:135], a[156:159], a[60:63], v[164:167]\n"
  "s_nop 1\n"
  "ds_read_b128 a[140:143], v216 offset:3072\n"
  "v_mfma_f32_16x16x32_bf16 v[132:135], a[152:155], a[56:59], v[132:135]\n"
  "s_nop 1\n"
  "ds_read_b128 a[156:159], v216 offset:7168\n"
  "v_mfma_f32_16x16x32_bf16 v[132:135], a[148:151], a[52:55], v[132:135]\n"
  "v_exp_f32 v128, v128\n"
  "v_mfma_f32_16x16x32_bf16 v[132:135], a[144:147], a[48:51], v[132:135]\n"
  "v_exp_f32 v129, v129\n"
  "v_mfma_f32_16x16x32_bf16 v[136:139], a[160:163], a[48:51], v[164:167]\n"
  "ds_read_b128 a[152:155], v216 offset:6144\n"
  "v_cvt_pk_bf16_f32 v188, v128, v129\n"
  "v_mfma_f32_16x16x32_bf16 v[136:139], a[164:167], a[52:55], v[136:139]\n"
  "v_exp_f32 v130, v130\n"
  "v_mfma_f32_16x16x32_bf16 v[136:139], a[168:171], a[56:59], v[136:139]\n"
  "ds_read_b128 a[148:151], v216 offset:5120\n"
  "ds_read_b128 a[144:147], v216 offset:4096\n"
  "v_mfma_f32_16x16x32_bf16 v[136:139], a[172:175], a[60:63], v[136:139]\n"
  "v_exp_f32 v131, v131\n"
  "v_mfma_f32_16x16x32_bf16 v[140:143], a[188:191], a[60:63], v[164:167]\n"
  "v_exp_f32 v132, v132\n"
  "v_mfma_f32_16x16x32_bf16 v[140:143], a[184:187], a[56:59], v[140:143]\n"
  "ds_read_b128 a[160:163], v216 offset:32768\n"
  "v_cvt_pk_bf16_f32 v189, v130, v131\n"
  "v_mfma_f32_16x16x32_bf16 v[140:143], a[180:183], a[52:55], v[140:143]\n"
  "v_exp_f32 v133, v133\n"
  "v_mfma_f32_16x16x32_bf16 v[140:143], a[176:179], a[48:51], v[140:143]\n"
  "ds_read_b128 a[164:167], v216 offset:33792\n"
  "v_cvt_pk_bf16_f32 v190, v132, v133\n"
  "v_mfma_f32_16x16x32_bf16 v[64:67], a[64:67], v[144:147], v[64:67]\n"
  "v_exp_f32 v134, v134\n"
  "v_mfma_f32_16x16x32_bf16 v[64:67], a[96:99], v[148:151], v[64:67]\n"
  "ds_read_b128 a[168:171], v216 offset:34816\n"
  "ds_read_b128 a[172:175], v216 offset:35840\n"
  "v_mfma_f32_16x16x32_bf16 v[68:71], a[100:103], v[148:151], v[68:71]\n"
  "v_exp_f32 v135, v135\n"
  "v_mfma_f32_16x16x32_bf16 v[68:71], a[68:71], v[144:147], v[68:71]\n"
  "v_exp_f32 v136, v136\n"
  "v_mfma_f32_16x16x32_bf16 v[72:75], a[72:75], v[144:147], v[72:75]\n"
  "ds_read_b128 a[188:191], v216 offset:39936\n"
  "v_cvt_pk_bf16_f32 v191, v134, v135\n"
  "v_mfma_f32_16x16x32_bf16 v[72:75], a[104:107], v[148:151], v[72:75]\n"
  "v_exp_f32 v137, v137\n"
  "v_mfma_f32_16x16x32_bf16 v[76:79], a[108:111], v[148:151], v[76:79]\n"
  "ds_read_b128 a[184:187], v216 offset:38912\n"
  "v_cvt_pk_bf16_f32 v192, v136, v137\n"
  "v_mfma_f32_16x16x32_bf16 v[76:79], a[76:79], v[144:147], v[76:79]\n"
  "v_exp_f32 v138, v138\n"
  "v_mfma_f32_16x16x32_bf16 v[80:83], a[80:83], v[144:147], v[80:83]\n"
  "ds_read_b128 a[180:183], v216 offset:37888\n"
  "ds_read_b128 a[176:179], v216 offset:36864\n"
  "v_mfma_f32_16x16x32_bf16 v[80:83], a[112:115], v[148:151], v[80:83]\n"
  "v_exp_f32 v139, v139\n"
  "v_mfma_f32_16x16x32_bf16 v[84:87], a[116:119], v[148:151], v[84:87]\n"
  "v_exp_f32 v140, v140\n"
  "v_mfma_f32_16x16x32_bf16 v[84:87], a[84:87], v[144:147], v[84:87]\n"
  "v_cvt_pk_bf16_f32 v193, v138, v139\n"
  "v_mfma_f32_16x16x32_bf16 v[88:91], a[88:91], v[144:147], v[88:91]\n"
  "v_exp_f32 v141, v141\n"
  "v_mfma_f32_16x16x32_bf16 v[88:91], a[120:123], v[148:151], v[88:91]\n"
  "v_cvt_pk_bf16_f32 v194, v140, v141\n"
  "v_mfma_f32_16x16x32_bf16 v[92:95], a[124:127], v[148:151], v[92:95]\n"
  "v_exp_f32 v142, v142\n"
  "v_mfma_f32_16x16x32_bf16 v[92:95], a[92:95], v[144:147], v[92:95]\n"
  "v_mfma_f32_16x16x32_bf16 v[208:211], a[192:195], v[144:147], v[208:211]\n"
  "v_exp_f32 v143, v143\n"
  "v_mfma_f32_16x16x32_bf16 v[208:211], a[192:195], v[148:151], v[208:211]\n"
  "s_waitcnt lgkmcnt(0)\n"
  "v_cvt_pk_bf16_f32 v195, v142, v143\n"
  "v_mfma_f32_16x16x32_bf16 v[128:131], a[128:131], a[0:3], v[152:155]\n"
  "v_mfma_f32_16x16x32_bf16 v[128:131], a[132:135], a[4:7], v[128:131]\n"
  "global_load_dwordx2 a[244:245], v253, s[62:63] offset:128 sc1\n"
  "v_mfma_f32_16x16x32_bf16 v[128:131], a[136:139], a[8:11], v[128:131]\n"
  "global_load_lds_dwordx4 v232, s[20:21] offset:3072 sc1\n"
  "v_mfma_f32_16x16x32_bf16 v[128:131], a[140:143], a[12:15], v[128:131]\n"
  "s_nop 1\n"
  "global_load_dword v231, v196, s[26:27]\n"
  "v_mfma_f32_16x16x32_bf16 v[132:135], a[156:159], a[12:15], v[152:155]\n"
  "s_nop 1\n"
  "v_mfma_f32_16x16x32_bf16 v[132:135], a[152:155], a[8:11], v[132:135]\n"
  "s_nop 1\n"
  "v_mfma_f32_16x16x32_bf16 v[132:135], a[148:151], a[4:7], v[132:135]\n"
  "v_exp_f32 v128, v128\n"
  "v_mfma_f32_16x16x32_bf16 v[132:135], a[144:147], a[0:3], v[132:135]\n"
  "v_exp_f32 v129, v129\n"
  "v_mfma_f32_16x16x32_bf16 v[136:139], a[160:163], a[0:3], v[152:155]\n"
  "s_cmp_eq_u32 s24, s31\n"
  "s_cselect_b32 s36, s25, 0x4000\n"
  "v_cvt_pk_bf16_f32 v144, v128, v129\n"
  "v_mfma_f32_16x16x32_bf16 v[136:139], a[164:167], a[4:7], v[136:139]\n"
  "v_exp_f32 v130, v130\n"
  "v_mfma_f32_16x16x32_bf16 v[136:139], a[168:171], a[8:11], v[136:139]\n"
  "v_mfma_f32_16x16x32_bf16 v[136:139], a[172:175], a[12:15], v[136:139]\n"
  "v_exp_f32 v131, v131\n"
  "v_mfma_f32_16x16x32_bf16 v[140:143], a[188:191], a[12:15], v[152:155]\n"
  "v_exp_f32 v132, v132\n"
  "v_mfma_f32_16x16x32_bf16 v[140:143], a[184:187], a[8:11], v[140:143]\n"
  "s_add_u32 s20, s20, s36\n"
  "s_addc_u32 s21, s21, 0\n"
  "v_cvt_pk_bf16_f32 v145, v130, v131\n"
  "v_mfma_f32_16x16x32_bf16 v[140:143], a[180:183], a[4:7], v[140:143]\n"
  "v_exp_f32 v133, v133\n"
  "v_mfma_f32_16x16x32_bf16 v[140:143], a[176:179], a[0:3], v[140:143]\n"
  "v_cvt_pk_bf16_f32 v146, v132, v133\n"
  "v_mfma_f32_16x16x32_bf16 v[96:99], a[64:67], v[188:191], v[96:99]\n"
  "v_exp_f32 v134, v134\n"
  "v_mfma_f32_16x16x32_bf16 v[96:99], a[96:99], v[192:195], v[96:99]\n"
  "ds_read_b128 a[64:67], v216 offset:8192\n"
  "v_mfma_f32_16x16x32_bf16 v[100:103], a[100:103], v[192:195], v[100:103]\n"
  "v_exp_f32 v135, v135\n"
  "v_mfma_f32_16x16x32_bf16 v[100:103], a[68:71], v[188:191], v[100:103]\n"
  "v_exp_f32 v136, v136\n"
  "v_mfma_f32_16x16x32_bf16 v[104:107], a[72:75], v[188:191], v[104:107]\n"
  "ds_read_b128 a[96:99], v216 offset:40960\n"
  "v_cvt_pk_bf16_f32 v147, v134, v135\n"
  "v_mfma_f32_16x16x32_bf16 v[104:107], a[104:107], v[192:195], v[104:107]\n"
  "v_exp_f32 v137, v137\n"
  "v_mfma_f32_16x16x32_bf16 v[108:111], a[108:111], v[192:195], v[108:111]\n"
  "ds_read_b128 a[100:103], v216 offset:41984\n"
  "v_cvt_pk_bf16_f32 v148, v136, v137\n"
  "v_mfma_f32_16x16x32_bf16 v[108:111], a[76:79], v[188:191], v[108:111]\n"
  "v_exp_f32 v138, v138\n"
  "v_mfma_f32_16x16x32_bf16 v[112:115], a[80:83], v[188:191], v[112:115]\n"
  "ds_read_b128 a[68:71], v216 offset:9216\n"
  "ds_read_b128 a[72:75], v216 offset:10240\n"
  "v_mfma_f32_16x16x32_bf16 v[112:115], a[112:115], v[192:195], v[112:115]\n"
  "v_exp_f32 v139, v139\n"
  "v_mfma_f32_16x16x32_bf16 v[116:119], a[116:119], v[192:195], v[116:119]\n"
  "v_exp_f32 v140, v140\n"
  "v_mfma_f32_16x16x32_bf16 v[116:119], a[84:87], v[188:191], v[116:119]\n"
  "ds_read_b128 a[104:107], v216 offset:43008\n"
  "v_cvt_pk_bf16_f32 v149, v138, v139\n"
  "v_mfma_f32_16x16x32_bf16 v[120:123], a[88:91], v[188:191], v[120:123]\n"
  "v_exp_f32 v141, v141\n"
  "v_mfma_f32_16x16x32_bf16 v[120:123], a[120:123], v[192:195], v[120:123]\n"
  "ds_read_b128 a[108:111], v216 offset:44032\n"
  "v_cvt_pk_bf16_f32 v150, v140, v141\n"
  "v_mfma_f32_16x16x32_bf16 v[124:127], a[124:127], v[192:195], v[124:127]\n"
  "v_exp_f32 v142, v142\n"
  "v_mfma_f32_16x16x32_bf16 v[124:127], a[92:95], v[188:191], v[124:127]\n"
  "s_xor_b32 s49, s49, 0x10000\n"
  "ds_read_b128 a[76:79], v216 offset:11264\n"
  "v_mfma_f32_16x16x32_bf16 v[212:215], a[192:195], v[188:191], v[212:215]\n"
  "v_exp_f32 v143, v143\n"
  "v_mfma_f32_16x16x32_bf16 v[212:215], a[192:195], v[192:195], v[212:215]\n"
  "v_cvt_pk_bf16_f32 v151, v142, v143\n"
  "s_setpc_b64 s[22:23]\n"
  "L_SFD_%=:\n"
  "v_mfma_f32_16x16x32_bf16 v[128:131], a[128:131], a[16:19], v[156:159]\n"
  "ds_read_b128 a[80:83], v216 offset:12288\n"
  "ds_read_b128 a[112:115], v216 offset:45056\n"
  "v_mfma_f32_16x16x32_bf16 v[128:131], a[132:135], a[20:23], v[128:131]\n"
  "ds_read_b128 a[116:119], v216 offset:46080\n"
  "ds_read_b128 a[84:87], v216 offset:13312\n"
  "v_mfma_f32_16x16x32_bf16 v[128:131], a[136:139], a[24:27], v[128:131]\n"
  "ds_read_b128 a[88:91], v216 offset:14336\n"
  "ds_read_b128 a[120:123], v216 offset:47104\n"
  "v_mfma_f32_16x16x32_bf16 v[128:131], a[140:143], a[28:31], v[128:131]\n"
  "ds_read_b128 a[124:127], v216 offset:48128\n"
  "ds_read_b128 a[92:95], v216 offset:15360\n"
  "v_mfma_f32_16x16x32_bf16 v[132:135], a[156:159], a[28:31], v[156:159]\n"
  "s_nop 1\n"
  "v_mfma_f32_16x16x32_bf16 v[132:135], a[152:155], a[24:27], v[132:135]\n"
  "s_nop 1\n"
  "v_xor_b32 v216, 0x10000, v216\n"
  "v_mfma_f32_16x16x32_bf16 v[132:135], a[148:151], a[20:23], v[132:135]\n"
  "s_add_u32 s24, s24, 1\n"
  "v_exp_f32 v128, v128\n"
  "v_mfma_f32_16x16x32_bf16 v[132:135], a[144:147], a[16:19], v[132:135]\n"
  "v_exp_f32 v129, v129\n"
  "v_mfma_f32_16x16x32_bf16 v[136:139], a[160:163], a[16:19], v[156:159]\n"
  "v_cvt_pk_bf16_f32 v188, v128, v129\n"
  "v_readlane_b32 s22, v252, s24\n"
  "v_mfma_f32_16x16x32_bf16 v[136:139], a[164:167], a[20:23], v[136:139]\n"
  "v_exp_f32 v130, v130\n"
  "v_mfma_f32_16x16x32_bf16 v[136:139], a[168:171], a[24:27], v[136:139]\n"
  "s_add_u32 s22, s26, s22\n"
  "s_addc_u32 s23, s27, 0\n"
  "v_mfma_f32_16x16x32_bf16 v[136:139], a[172:175], a[28:31], v[136:139]\n"
  "v_exp_f32 v131, v131\n"
  "v_mfma_f32_16x16x32_bf16 v[140:143], a[188:191], a[28:31], v[156:159]\n"
  "v_exp_f32 v132, v132\n"
  "v_mfma_f32_16x16x32_bf16 v[140:143], a[184:187], a[24:27], v[140:143]\n"
  "s_waitcnt lgkmcnt(0)\n"
  "s_barrier\n"
  "v_cvt_pk_bf16_f32 v189, v130, v131\n"
  "v_mfma_f32_16x16x32_bf16 v[140:143], a[180:183], a[20:23], v[140:143]\n"
  "v_exp_f32 v133, v133\n"
  "v_mfma_f32_16x16x32_bf16 v[140:143], a[176:179], a[16:19], v[140:143]\n"
  "v_cvt_pk_bf16_f32 v190, v132, v133\n"
  "v_mfma_f32_16x16x32_bf16 v[0:3], a[64:67], v[144:147], v[0:3]\n"
  "s_add_u32 m0, s49, 0\n"
  "v_exp_f32 v134, v134\n"
  "v_mfma_f32_16x16x32_bf16 v[0:3], a[96:99], v[148:151], v[0:3]\n"
  "v_mfma_f32_16x16x32_bf16 v[4:7], a[100:103], v[148:151], v[4:7]\n"
  "v_exp_f32 v135, v135\n"
  "v_mfma_f32_16x16x32_bf16 v[4:7], a[68:71], v[144:147], v[4:7]\n"
  "v_exp_f32 v136, v136\n"
  "v_mfma_f32_16x16x32_bf16 v[8:11], a[72:75], v[144:147], v[8:11]\n"
  "v_cvt_pk_bf16_f32 v191, v134, v135\n"
  "v_mfma_f32_16x16x32_bf16 v[8:11], a[104:107], v[148:151], v[8:11]\n"
  "v_exp_f32 v137, v137\n"
  "v_mfma_f32_16x16x32_bf16 v[12:15], a[108:111], v[148:151], v[12:15]\n"
  "global_load_lds_dwordx4 v232, s[20:21] offset:0 sc1\n"
  "v_cvt_pk_bf16_f32 v192, v136, v137\n"
  "v_mfma_f32_16x16x32_bf16 v[12:15], a[76:79], v[144:147], v[12:15]\n"
  "v_exp_f32 v138, v138\n"
  "v_mfma_f32_16x16x32_bf16 v[16:19], a[80:83], v[144:147], v[16:19]\n"
  "v_mfma_f32_16x16x32_bf16 v[16:19], a[112:115], v[148:151], v[16:19]\n"
  "v_exp_f32 v139, v139\n"
  "v_mfma_f32_16x16x32_bf16 v[20:23], a[116:119], v[148:151], v[20:23]\n"
  "v_exp_f32 v140, v140\n"
  "v_mfma_f32_16x16x32_bf16 v[20:23], a[84:87], v[144:147], v[20:23]\n"
  "v_cvt_pk_bf16_f32 v193, v138, v139\n"
  "v_mfma_f32_16x16x32_bf16 v[24:27], a[88:91], v[144:147], v[24:27]\n"
  "v_exp_f32 v141, v141\n"
  "v_mfma_f32_16x16x32_bf16 v[24:27], a[120:123], v[148:151], v[24:27]\n"
  "global_load_lds_dwordx4 v232, s[20:21] offset:1024 sc1\n"
  "v_cvt_pk_bf16_f32 v194, v140, v141\n"
  "v_mfma_f32_16x16x32_bf16 v[28:31], a[124:127], v[148:151], v[28:31]\n"
  "v_exp_f32 v142, v142\n"
  "v_mfma_f32_16x16x32_bf16 v[28:31], a[92:95], v[144:147], v[28:31]\n"
  "v_mfma_f32_16x16x32_bf16 v[200:203], a[192:195], v[144:147], v[200:203]\n"
  "v_exp_f32 v143, v143\n"
  "v_mfma_f32_16x16x32_bf16 v[200:203], a[192:195], v[148:151], v[200:203]\n"
  "v_cvt_pk_bf16_f32 v195, v142, v143\n"
  "v_mfma_f32_16x16x32_bf16 v[128:131], a[128:131], a[32:35], v[160:163]\n"
  "global_load_lds_dwordx4 v232, s[20:21] offset:2048 sc1\n"
  "v_mfma_f32_16x16x32_bf16 v[128:131], a[132:135], a[36:39], v[128:131]\n"
  "v_mfma_f32_16x16x32_bf16 v[128:131], a[136:139], a[40:43], v[128:131]\n"
  "v_mfma_f32_16x16x32_bf16 v[128:131], a[140:143], a[44:47], v[128:131]\n"
  "s_nop 1\n"
  "v_mfma_f32_16x16x32_bf16 v[132:135], a[156:159], a[44:47], v[160:163]\n"
  "s_nop 1\n"
  "v_mfma_f32_16x16x32_bf16 v[132:135], a[152:155], a[40:43], v[132:135]\n"
  "s_nop 1\n"
  "v_mfma_f32_16x16x32_bf16 v[132:135], a[148:151], a[36:39], v[132:135]\n"
  "v_exp_f32 v128, v128\n"
  "v_mfma_f32_16x16x32_bf16 v[132:135], a[144:147], a[32:35], v[132:135]\n"
  "v_exp_f32 v129, v129\n"
  "v_mfma_f32_16x16x32_bf16 v[136:139], a[160:163], a[32:35], v[160:163]\n"
  "global_load_lds_dwordx4 v232, s[20:21] offset:3072 sc1\n"
  "v_cvt_pk_bf16_f32 v144, v128, v129\n"
  "v_mfma_f32_16x16x32_bf16 v[136:139], a[164:167], a[36:39], v[136:139]\n"
  "v_exp_f32 v130, v130\n"
  "v_mfma_f32_16x16x32_bf16 v[136:139], a[168:171], a[40:43], v[136:139]\n"
  "v_mfma_f32_16x16x32_bf16 v[136:139], a[172:175], a[44:47], v[136:139]\n"
  "v_exp_f32 v131, v131\n"
  "v_mfma_f32_16x16x32_bf16 v[140:143], a[188:191], a[44:47], v[160:163]\n"
  "v_exp_f32 v132, v132\n"
  "v_mfma_f32_16x16x32_bf16 v[140:143], a[184:187], a[40:43], v[140:143]\n"
  "s_add_u32 s20, s20, 16384\n"
  "s_addc_u32 s21, s21, 0\n"
  "v_cvt_pk_bf16_f32 v145, v130, v131\n"
  "v_mfma_f32_16x16x32_bf16 v[140:143], a[180:183], a[36:39], v[140:143]\n"
  "v_exp_f32 v133, v133\n"
  "v_mfma_f32_16x16x32_bf16 v[140:143], a[176:179], a[32:35], v[140:143]\n"
  "v_cvt_pk_bf16_f32 v146, v132, v133\n"
  "v_mfma_f32_16x16x32_bf16 v[32:35], a[64:67], v[188:191], v[32:35]\n"
  "v_exp_f32 v134, v134\n"
  "v_mfma_f32_16x16x32_bf16 v[32:35], a[96:99], v[192:195], v[32:35]\n"
  "v_mfma_f32_16x16x32_bf16 v[36:39], a[100:103], v[192:195], v[36:39]\n"
  "s_add_u32 m0, s49, 32768\n"
  "v_exp_f32 v135, v135\n"
  "v_mfma_f32_16x16x32_bf16 v[36:39], a[68:71], v[188:191], v[36:39]\n"
  "v_exp_f32 v136, v136\n"
  "v_mfma_f32_16x16x32_bf16 v[40:43], a[72:75], v[188:191], v[40:43]\n"
  "v_cvt_pk_bf16_f32 v147, v134, v135\n"
  "v_mfma_f32_16x16x32_bf16 v[40:43], a[104:107], v[192:195], v[40:43]\n"
  "v_exp_f32 v137, v137\n"
  "v_mfma_f32_16x16x32_bf16 v[44:47], a[108:111], v[192:195], v[44:47]\n"
  "v_cvt_pk_bf16_f32 v148, v136, v137\n"
  "v_mfma_f32_16x16x32_bf16 v[44:47], a[76:79], v[188:191], v[44:47]\n"
  "v_exp_f32 v138, v138\n"
  "v_mfma_f32_16x16x32_bf16 v[48:51], a[80:83], v[188:191], v[48:51]\n"
  "global_load_lds_dwordx4 v232, s[20:21] offset:0 sc1\n"
  "v_mfma_f32_16x16x32_bf16 v[48:51], a[112:115], v[192:195], v[48:51]\n"
  "v_exp_f32 v139, v139\n"
  "v_mfma_f32_16x16x32_bf16 v[52:55], a[116:119], v[192:195], v[52:55]\n"
  "v_exp_f32 v140, v140\n"
  "v_mfma_f32_16x16x32_bf16 v[52:55], a[84:87], v[188:191], v[52:55]\n"
  "v_cvt_pk_bf16_f32 v149, v138, v139\n"
  "v_mfma_f32_16x16x32_bf16 v[56:59], a[88:91], v[188:191], v[56:59]\n"
  "v_exp_f32 v141, v141\n"
  "v_mfma_f32_16x16x32_bf16 v[56:59], a[120:123], v[192:195], v[56:59]\n"
  "v_cvt_pk_bf16_f32 v150, v140, v141\n"
  "v_mfma_f32_16x16x32_bf16 v[60:63], a[124:127], v[192:195], v[60:63]\n"
  "v_exp_f32 v142, v142\n"
  "v_mfma_f32_16x16x32_bf16 v[60:63], a[92:95], v[188:191], v[60:63]\n"
  "global_load_lds_dwordx4 v232, s[20:21] offset:1024 sc1\n"
  "v_mfma_f32_16x16x32_bf16 v[204:207], a[192:195], v[188:191], v[204:207]\n"
  "v_exp_f32 v143, v143\n"
  "v_mfma_f32_16x16x32_bf16 v[204:207], a[192:195], v[192:195], v[204:207]\n"
  "s_waitcnt vmcnt(7)\n"
  "s_barrier\n"
  "v_cvt_pk_bf16_f32 v151, v142, v143\n"
  "v_mfma_f32_16x16x32_bf16 v[128:131], a[128:131], a[48:51], v[164:167]\n"
  "v_mfma_f32_16x16x32_bf16 v[128:131], a[132:135], a[52:55], v[128:131]\n"
  "ds_read_b128 a[128:131], v216 offset:0\n"
  "v_mfma_f32_16x16x32_bf16 v[128:131], a[136:139], a[56:59], v[128:131]\n"
  "ds_read_b128 a[132:135], v216 offset:1024\n"
  "global_load_lds_dwordx4 v232, s[20:21] offset:2048 sc1\n"
  "v_mfma_f32_16x16x32_bf16 v[128:131], a[140:143], a[60:63], v[128:131]\n"
  "s_nop 1\n"
  "ds_read_b128 a[136:139], v216 offset:2048\n"
  "v_mfma_f32_16x16x32_bf16 v[132:135], a[156:159], a[60:63], v[164:167]\n"
  "s_nop 1\n"
  "ds_read_b128 a[140:143], v216 offset:3072\n"
  "v_mfma_f32_16x16x32_bf16 v[132:135], a[152:155], a[56:59], v[132:135]\n"
  "s_nop 1\n"
  "ds_read_b128 a[156:159], v216 offset:7168\n"
  "v_mfma_f32_16x16x32_bf16 v[132:135], a[148:151], a[52:55], v[132:135]\n"
  "v_exp_f32 v128, v128\n"
  "v_mfma_f32_16x16x32_bf16 v[132:135], a[144:147], a[48:51], v[132:135]\n"
  "v_exp_f32 v129, v129\n"
  "v_mfma_f32_16x16x32_bf16 v[136:139], a[160:163], a[48:51], v[164:167]\n"
  "ds_read_b128 a[152:155], v216 offset:6144\n"
  "v_cvt_pk_bf16_f32 v188, v128, v129\n"
  "v_mfma_f32_16x16x32_bf16 v[136:139], a[164:167], a[52:55], v[136:139]\n"
  "v_exp_f32 v130, v130\n"
  "v_mfma_f32_16x16x32_bf16 v[136:139], a[168:171], a[56:59], v[136:139]\n"
  "ds_read_b128 a[148:151], v216 offset:5120\n"
  "ds_read_b128 a[144:147], v216 offset:4096\n"
  "v_mfma_f32_16x16x32_bf16 v[136:139], a[172:175], a[60:63], v[136:139]\n"
  "v_exp_f32 v131, v131\n"
  "v_mfma_f32_16x16x32_bf16 v[140:143], a[188:191], a[60:63], v[164:167]\n"
  "v_exp_f32 v132, v132\n"
  "v_mfma_f32_16x16x32_bf16 v[140:143], a[184:187], a[56:59], v[140:143]\n"
  "ds_read_b128 a[160:163], v216 offset:32768\n"
  "v_cvt_pk_bf16_f32 v189, v130, v131\n"
  "v_mfma_f32_16x16x32_bf16 v[140:143], a[180:183], a[52:55], v[140:143]\n"
  "v_exp_f32 v133, v133\n"
  "v_mfma_f32_16x16x32_bf16 v[140:143], a[176:179], a[48:51], v[140:143]\n"
  "ds_read_b128 a[164:167], v216 offset:33792\n"
  "v_cvt_pk_bf16_f32 v190, v132, v133\n"
  "v_mfma_f32_16x16x32_bf16 v[64:67], a[64:67], v[144:147], v[64:67]\n"
  "v_exp_f32 v134, v134\n"
  "v_mfma_f32_16x16x32_bf16 v[64:67], a[96:99], v[148:151], v[64:67]\n"
  "ds_read_b128 a[168:171], v216 offset:34816\n"
  "ds_read_b128 a[172:175], v216 offset:35840\n"
  "v_mfma_f32_16x16x32_bf16 v[68:71], a[100:103], v[148:151], v[68:71]\n"
  "v_exp_f32 v135, v135\n"
  "v_mfma_f32_16x16x32_bf16 v[68:71], a[68:71], v[144:147], v[68:71]\n"
  "v_exp_f32 v136, v136\n"
  "v_mfma_f32_16x16x32_bf16 v[72:75], a[72:75], v[144:147], v[72:75]\n"
  "ds_read_b128 a[188:191], v216 offset:39936\n"
  "v_cvt_pk_bf16_f32 v191, v134, v135\n"
  "v_mfma_f32_16x16x32_bf16 v[72:75], a[104:107], v[148:151], v[72:75]\n"
  "v_exp_f32 v137, v137\n"
  "v_mfma_f32_16x16x32_bf16 v[76:79], a[108:111], v[148:151], v[76:79]\n"
  "ds_read_b128 a[184:187], v216 offset:38912\n"
  "v_cvt_pk_bf16_f32 v192, v136, v137\n"
  "v_mfma_f32_16x16x32_bf16 v[76:79], a[76:79], v[144:147], v[76:79]\n"
  "v_exp_f32 v138, v138\n"
  "v_mfma_f32_16x16x32_bf16 v[80:83], a[80:83], v[144:147], v[80:83]\n"
  "ds_read_b128 a[180:183], v216 offset:37888\n"
  "ds_read_b128 a[176:179], v216 offset:36864\n"
  "v_mfma_f32_16x16x32_bf16 v[80:83], a[112:115], v[148:151], v[80:83]\n"
  "v_exp_f32 v139, v139\n"
  "v_mfma_f32_16x16x32_bf16 v[84:87], a[116:119], v[148:151], v[84:87]\n"
  "v_exp_f32 v140, v140\n"
  "v_mfma_f32_16x16x32_bf16 v[84:87], a[84:87], v[144:147], v[84:87]\n"
  "global_load_lds_dwordx4 v232, s[20:21] offset:3072 sc1\n"
  "v_cvt_pk_bf16_f32 v193, v138, v139\n"
  "v_mfma_f32_16x16x32_bf16 v[88:91], a[88:91], v[144:147], v[88:91]\n"
  "v_exp_f32 v141, v141\n"
  "v_mfma_f32_16x16x32_bf16 v[88:91], a[120:123], v[148:151], v[88:91]\n"
  "s_cmp_eq_u32 s24, s31\n"
  "s_cselect_b32 s36, s25, 0x4000\n"
  "v_cvt_pk_bf16_f32 v194, v140, v141\n"
  "v_mfma_f32_16x16x32_bf16 v[92:95], a[124:127], v[148:151], v[92:95]\n"
  "v_exp_f32 v142, v142\n"
  "v_mfma_f32_16x16x32_bf16 v[92:95], a[92:95], v[144:147], v[92:95]\n"
  "s_add_u32 s20, s20, s36\n"
  "s_addc_u32 s21, s21, 0\n"
  "v_mfma_f32_16x16x32_bf16 v[208:211], a[192:195], v[144:147], v[208:211]\n"
  "v_exp_f32 v143, v143\n"
  "v_mfma_f32_16x16x32_bf16 v[208:211], a[192:195], v[148:151], v[208:211]\n"
  "s_waitcnt lgkmcnt(0)\n"
  "v_cvt_pk_bf16_f32 v195, v142, v143\n"
  "v_mfma_f32_16x16x32_bf16 v[128:131], a[128:131], a[0:3], v[168:171]\n"
  "v_mfma_f32_16x16x32_bf16 v[128:131], a[132:135], a[4:7], v[128:131]\n"
  "global_load_dwordx2 a[244:245], v253, s[62:63] offset:128 sc1\n"
  "v_mfma_f32_16x16x32_bf16 v[128:131], a[136:139], a[8:11], v[128:131]\n"
  "v_mfma_f32_16x16x32_bf16 v[128:131], a[140:143], a[12:15], v[128:131]\n"
  "s_nop 1\n"
  "v_mfma_f32_16x16x32_bf16 v[96:99], a[64:67], v[188:191], v[96:99]\n"
  "s_nop 1\n"
  "v_mfma_f32_16x16x32_bf16 v[96:99], a[96:99], v[192:195], v[96:99]\n"
  "s_nop 1\n"
  "ds_read_b128 a[64:67], v216 offset:8192\n"
  "v_mfma_f32_16x16x32_bf16 v[100:103], a[100:103], v[192:195], v[100:103]\n"
  "v_exp_f32 v128, v128\n"
  "v_mfma_f32_16x16x32_bf16 v[100:103], a[68:71], v[188:191], v[100:103]\n"
  "ds_read_b128 a[96:99], v216 offset:40960\n"
  "ds_read_b128 a[100:103], v216 offset:41984\n"
  "v_mfma_f32_16x16x32_bf16 v[104:107], a[72:75], v[188:191], v[104:107]\n"
  "ds_read_b128 a[68:71], v216 offset:9216\n"
  "v_mfma_f32_16x16x32_bf16 v[104:107], a[104:107], v[192:195], v[104:107]\n"
  "ds_read_b128 a[72:75], v216 offset:10240\n"
  "v_mfma_f32_16x16x32_bf16 v[108:111], a[108:111], v[192:195], v[108:111]\n"
  "v_exp_f32 v129, v129\n"
  "v_mfma_f32_16x16x32_bf16 v[108:111], a[76:79], v[188:191], v[108:111]\n"
  "ds_read_b128 a[104:107], v216 offset:43008\n"
  "v_cvt_pk_bf16_f32 v144, v128, v129\n"
  "v_mfma_f32_16x16x32_bf16 v[112:115], a[80:83], v[188:191], v[112:115]\n"
  "ds_read_b128 a[108:111], v216 offset:44032\n"
  "ds_read_b128 a[76:79], v216 offset:11264\n"
  "v_mfma_f32_16x16x32_bf16 v[112:115], a[112:115], v[192:195], v[112:115]\n"
  "v_mfma_f32_16x16x32_bf16 v[116:119], a[116:119], v[192:195], v[116:119]\n"
  "v_mfma_f32_16x16x32_bf16 v[116:119], a[84:87], v[188:191], v[116:119]\n"
  "v_exp_f32 v130, v130\n"
  "v_mfma_f32_16x16x32_bf16 v[120:123], a[88:91], v[188:191], v[120:123]\n"
  "v_mfma_f32_16x16x32_bf16 v[120:123], a[120:123], v[192:195], v[120:123]\n"
  "v_mfma_f32_16x16x32_bf16 v[124:127], a[124:127], v[192:195], v[124:127]\n"
  "v_mfma_f32_16x16x32_bf16 v[124:127], a[92:95], v[188:191], v[124:127]\n"
  "s_xor_b32 s49, s49, 0x10000\n"
  "v_mfma_f32_16x16x32_bf16 v[212:215], a[192:195], v[188:191], v[212:215]\n"
  "v_exp_f32 v131, v131\n"
  "v_mfma_f32_16x16x32_bf16 v[212:215], a[192:195], v[192:195], v[212:215]\n"
  "v_cvt_pk_bf16_f32 v145, v130, v131\n"
  "v_mov_b32 v146, 0\n"
  "v_mov_b32 v147, 0\n"
  "s_setpc_b64 s[22:23]\n"
  "L_PFD_%=:\n"
  "v_mfma_f32_16x16x32_bf16 v[128:131], a[128:131], a[16:19], v[156:159]\n"
  "ds_read_b128 a[80:83], v216 offset:12288\n"
  "ds_read_b128 a[112:115], v216 offset:45056\n"
  "v_mfma_f32_16x16x32_bf16 v[128:131], a[132:135], a[20:23], v[128:131]\n"
  "ds_read_b128 a[116:119], v216 offset:46080\n"
  "ds_read_b128 a[84:87], v216 offset:13312\n"
  "v_mfma_f32_16x16x32_bf16 v[128:131], a[136:139], a[24:27], v[128:131]\n"
  "ds_read_b128 a[88:91], v216 offset:14336\n"
  "ds_read_b128 a[120:123], v216 offset:47104\n"
  "v_mfma_f32_16x16x32_bf16 v[128:131], a[140:143], a[28:31], v[128:131]\n"
  "ds_read_b128 a[124:127], v216 offset:48128\n"
  "ds_read_b128 a[92:95], v216 offset:15360\n"
  "v_mfma_f32_16x16x32_bf16 v[132:135], a[156:159], a[28:31], v[156:159]\n"
  "s_nop 1\n"
  "v_mfma_f32_16x16x32_bf16 v[132:135], a[152:155], a[24:27], v[132:135]\n"
  "s_nop 1\n"
  "v_xor_b32 v216, 0x10000, v216\n"
  "v_mfma_f32_16x16x32_bf16 v[132:135], a[148:151], a[20:23], v[132:135]\n"
  "s_add_u32 s24, s24, 1\n"
  "v_exp_f32 v128, v128\n"
  "v_mfma_f32_16x16x32_bf16 v[132:135], a[144:147], a[16:19], v[132:135]\n"
  "v_exp_f32 v129, v129\n"
  "v_mfma_f32_16x16x32_bf16 v[136:139], a[160:163], a[16:19], v[156:159]\n"
  "v_cvt_pk_bf16_f32 v188, v128, v129\n"
  "v_readlane_b32 s22, v252, s24\n"
  "v_mfma_f32_16x16x32_bf16 v[136:139], a[164:167], a[20:23], v[136:139]\n"
  "v_exp_f32 v130, v130\n"
  "v_mfma_f32_16x16x32_bf16 v[136:139], a[168:171], a[24:27], v[136:139]\n"
  "s_add_u32 s22, s26, s22\n"
  "s_addc_u32 s23, s27, 0\n"
  "v_mfma_f32_16x16x32_bf16 v[136:139], a[172:175], a[28:31], v[136:139]\n"
  "v_exp_f32 v131, v131\n"
  "v_mfma_f32_16x16x32_bf16 v[140:143], a[188:191], a[28:31], v[156:159]\n"
  "v_exp_f32 v132, v132\n"
  "v_mfma_f32_16x16x32_bf16 v[140:143], a[184:187], a[24:27], v[140:143]\n"
  "s_waitcnt lgkmcnt(0)\n"
  "s_barrier\n"
  "v_cvt_pk_bf16_f32 v189, v130, v131\n"
  "v_mfma_f32_16x16x32_bf16 v[140:143], a[180:183], a[20:23], v[140:143]\n"
  "v_exp_f32 v133, v133\n"
  "v_mfma_f32_16x16x32_bf16 v[140:143], a[176:179], a[16:19], v[140:143]\n"
  "v_cvt_pk_bf16_f32 v190, v132, v133\n"
  "v_mfma_f32_16x16x32_bf16 v[0:3], a[64:67], v[144:147], v[0:3]\n"
  "s_add_u32 m0, s49, 0\n"
  "v_exp_f32 v134, v134\n"
  "v_mfma_f32_16x16x32_bf16 v[0:3], a[96:99], v[148:151], v[0:3]\n"
  "v_mfma_f32_16x16x32_bf16 v[4:7], a[100:103], v[148:151], v[4:7]\n"
  "v_exp_f32 v135, v135\n"
  "v_mfma_f32_16x16x32_bf16 v[4:7], a[68:71], v[144:147], v[4:7]\n"
  "v_exp_f32 v136, v136\n"
  "v_mfma_f32_16x16x32_bf16 v[8:11], a[72:75], v[144:147], v[8:11]\n"
  "global_load_lds_dwordx4 v232, s[20:21] offset:0 sc1\n"
  "v_cvt_pk_bf16_f32 v191, v134, v135\n"
  "v_mfma_f32_16x16x32_bf16 v[8:11], a[104:107], v[148:151], v[8:11]\n"
  "v_exp_f32 v137, v137\n"
  "v_mfma_f32_16x16x32_bf16 v[12:15], a[108:111], v[148:151], v[12:15]\n"
  "v_cvt_pk_bf16_f32 v192, v136, v137\n"
  "v_mfma_f32_16x16x32_bf16 v[12:15], a[76:79], v[144:147], v[12:15]\n"
  "v_exp_f32 v138, v138\n"
  "v_mfma_f32_16x16x32_bf16 v[16:19], a[80:83], v[144:147], v[16:19]\n"
  "global_load_lds_dwordx4 v232, s[20:21] offset:1024 sc1\n"
  "v_mfma_f32_16x16x32_bf16 v[16:19], a[112:115], v[148:151], v[16:19]\n"
  "v_exp_f32 v139, v139\n"
  "v_mfma_f32_16x16x32_bf16 v[20:23], a[116:119], v[148:151], v[20:23]\n"
  "v_exp_f32 v140, v140\n"
  "v_mfma_f32_16x16x32_bf16 v[20:23], a[84:87], v[144:147], v[20:23]\n"
  "global_load_lds_dwordx4 v232, s[20:21] offset:2048 sc1\n"
  "v_cvt_pk_bf16_f32 v193, v138, v139\n"
  "v_mfma_f32_16x16x32_bf16 v[24:27], a[88:91], v[144:147], v[24:27]\n"
  "v_exp_f32 v141, v141\n"
  "v_mfma_f32_16x16x32_bf16 v[24:27], a[120:123], v[148:151], v[24:27]\n"
  "v_cvt_pk_bf16_f32 v194, v140, v141\n"
  "v_mfma_f32_16x16x32_bf16 v[28:31], a[124:127], v[148:151], v[28:31]\n"
  "v_exp_f32 v142, v142\n"
  "v_mfma_f32_16x16x32_bf16 v[28:31], a[92:95], v[144:147], v[28:31]\n"
  "global_load_lds_dwordx4 v232, s[20:21] offset:3072 sc1\n"
  "v_mfma_f32_16x16x32_bf16 v[200:203], a[192:195], v[144:147], v[200:203]\n"
  "v_exp_f32 v143, v143\n"
  "v_mfma_f32_16x16x32_bf16 v[200:203], a[192:195], v[148:151], v[200:203]\n"
  "s_add_u32 m0, s49, 16384\n"
  "v_cvt_pk_bf16_f32 v195, v142, v143\n"
  "v_mfma_f32_16x16x32_bf16 v[128:131], a[128:131], a[32:35], v[160:163]\n"
  "v_mfma_f32_16x16x32_bf16 v[128:131], a[132:135], a[36:39], v[128:131]\n"
  "v_mfma_f32_16x16x32_bf16 v[128:131], a[136:139], a[40:43], v[128:131]\n"
  "v_mfma_f32_16x16x32_bf16 v[128:131], a[140:143], a[44:47], v[128:131]\n"
  "s_nop 1\n"
  "global_load_lds_dwordx4 v217, s[20:21] offset:0 sc1\n"
  "v_mfma_f32_16x16x32_bf16 v[132:135], a[156:159], a[44:47], v[160:163]\n"
  "s_nop 1\n"
  "v_mfma_f32_16x16x32_bf16 v[132:135], a[152:155], a[40:43], v[132:135]\n"
  "s_nop 1\n"
  "v_mfma_f32_16x16x32_bf16 v[132:135], a[148:151], a[36:39], v[132:135]\n"
  "v_exp_f32 v128, v128\n"
  "v_mfma_f32_16x16x32_bf16 v[132:135], a[144:147], a[32:35], v[132:135]\n"
  "v_exp_f32 v129, v129\n"
  "v_mfma_f32_16x16x32_bf16 v[136:139], a[160:163], a[32:35], v[160:163]\n"
  "global_load_lds_dwordx4 v217, s[20:21] offset:1024 sc1\n"
  "v_cvt_pk_bf16_f32 v144, v128, v129\n"
  "v_mfma_f32_16x16x32_bf16 v[136:139], a[164:167], a[36:39], v[136:139]\n"
  "v_exp_f32 v130, v130\n"
  "v_mfma_f32_16x16x32_bf16 v[136:139], a[168:171], a[40:43], v[136:139]\n"
  "global_load_lds_dwordx4 v217, s[20:21] offset:2048 sc1\n"
  "v_mfma_f32_16x16x32_bf16 v[136:139], a[172:175], a[44:47], v[136:139]\n"
  "v_exp_f32 v131, v131\n"
  "v_mfma_f32_16x16x32_bf16 v[140:143], a[188:191], a[44:47], v[160:163]\n"
  "v_exp_f32 v132, v132\n"
  "v_mfma_f32_16x16x32_bf16 v[140:143], a[184:187], a[40:43], v[140:143]\n"
  "global_load_lds_dwordx4 v217, s[20:21] offset:3072 sc1\n"
  "v_cvt_pk_bf16_f32 v145, v130, v131\n"
  "v_mfma_f32_16x16x32_bf16 v[140:143], a[180:183], a[36:39], v[140:143]\n"
  "v_exp_f32 v133, v133\n"
  "v_mfma_f32_16x16x32_bf16 v[140:143], a[176:179], a[32:35], v[140:143]\n"
  "v_cvt_pk_bf16_f32 v146, v132, v133\n"
  "v_mfma_f32_16x16x32_bf16 v[32:35], a[64:67], v[188:191], v[32:35]\n"
  "v_exp_f32 v134, v134\n"
  "v_mfma_f32_16x16x32_bf16 v[32:35], a[96:99], v[192:195], v[32:35]\n"
  "s_add_u32 s20, s20, 16384\n"
  "s_addc_u32 s21, s21, 0\n"
  "v_mfma_f32_16x16x32_bf16 v[36:39], a[100:103], v[192:195], v[36:39]\n"
  "v_exp_f32 v135, v135\n"
  "v_mfma_f32_16x16x32_bf16 v[36:39], a[68:71], v[188:191], v[36:39]\n"
  "v_exp_f32 v136, v136\n"
  "v_mfma_f32_16x16x32_bf16 v[40:43], a[72:75], v[188:191], v[40:43]\n"
  "v_cvt_pk_bf16_f32 v147, v134, v135\n"
  "v_mfma_f32_16x16x32_bf16 v[40:43], a[104:107], v[192:195], v[40:43]\n"
  "s_add_u32 m0, s49, 32768\n"
  "v_exp_f32 v137, v137\n"
  "v_mfma_f32_16x16x32_bf16 v[44:47], a[108:111], v[192:195], v[44:47]\n"
  "v_cvt_pk_bf16_f32 v148, v136, v137\n"
  "v_mfma_f32_16x16x32_bf16 v[44:47], a[76:79], v[188:191], v[44:47]\n"
  "v_exp_f32 v138, v138\n"
  "v_mfma_f32_16x16x32_bf16 v[48:51], a[80:83], v[188:191], v[48:51]\n"
  "global_load_lds_dwordx4 v232, s[20:21] offset:0 sc1\n"
  "v_mfma_f32_16x16x32_bf16 v[48:51], a[112:115], v[192:195], v[48:51]\n"
  "v_exp_f32 v139, v139\n"
  "v_mfma_f32_16x16x32_bf16 v[52:55], a[116:119], v[192:195], v[52:55]\n"
  "v_exp_f32 v140, v140\n"
  "v_mfma_f32_16x16x32_bf16 v[52:55], a[84:87], v[188:191], v[52:55]\n"
  "v_cvt_pk_bf16_f32 v149, v138, v139\n"
  "v_mfma_f32_16x16x32_bf16 v[56:59], a[88:91], v[188:191], v[56:59]\n"
  "v_exp_f32 v141, v141\n"
  "v_mfma_f32_16x16x32_bf16 v[56:59], a[120:123], v[192:195], v[56:59]\n"
  "global_load_lds_dwordx4 v232, s[20:21] offset:1024 sc1\n"
  "v_cvt_pk_bf16_f32 v150, v140, v141\n"
  "v_mfma_f32_16x16x32_bf16 v[60:63], a[124:127], v[192:195], v[60:63]\n"
  "v_exp_f32 v142, v142\n"
  "v_mfma_f32_16x16x32_bf16 v[60:63], a[92:95], v[188:191], v[60:63]\n"
  "global_load_lds_dwordx4 v232, s[20:21] offset:2048 sc1\n"
  "v_mfma_f32_16x16x32_bf16 v[204:207], a[192:195], v[188:191], v[204:207]\n"
  "v_exp_f32 v143, v143\n"
  "v_mfma_f32_16x16x32_bf16 v[204:207], a[192:195], v[192:195], v[204:207]\n"
  "s_waitcnt vmcnt(11)\n"
  "s_barrier\n"
  "v_cvt_pk_bf16_f32 v151, v142, v143\n"
  "v_mfma_f32_16x16x32_bf16 v[128:131], a[128:131], a[48:51], v[164:167]\n"
  "v_mfma_f32_16x16x32_bf16 v[128:131], a[132:135], a[52:55], v[128:131]\n"
  "ds_read_b128 a[128:131], v216 offset:0\n"
  "global_load_lds_dwordx4 v232, s[20:21] offset:3072 sc1\n"
  "v_mfma_f32_16x16x32_bf16 v[128:131], a[136:139], a[56:59], v[128:131]\n"
  "ds_read_b128 a[132:135], v216 offset:1024\n"
  "v_mfma_f32_16x16x32_bf16 v[128:131], a[140:143], a[60:63], v[128:131]\n"
  "s_nop 1\n"
  "ds_read_b128 a[136:139], v216 offset:2048\n"
  "v_mfma_f32_16x16x32_bf16 v[132:135], a[156:159], a[60:63], v[164:167]\n"
  "s_add_u32 m0, s49, 49152\n"
  "ds_read_b128 a[140:143], v216 offset:3072\n"
  "v_mfma_f32_16x16x32_bf16 v[132:135], a[152:155], a[56:59], v[132:135]\n"
  "s_nop 1\n"
  "ds_read_b128 a[156:159], v216 offset:7168\n"
  "v_mfma_f32_16x16x32_bf16 v[132:135], a[148:151], a[52:55], v[132:135]\n"
  "v_exp_f32 v128, v128\n"
  "v_mfma_f32_16x16x32_bf16 v[132:135], a[144:147], a[48:51], v[132:135]\n"
  "v_exp_f32 v129, v129\n"
  "v_mfma_f32_16x16x32_bf16 v[136:139], a[160:163], a[48:51], v[164:167]\n"
  "ds_read_b128 a[152:155], v216 offset:6144\n"
  "v_cvt_pk_bf16_f32 v188, v128, v129\n"
  "v_mfma_f32_16x16x32_bf16 v[136:139], a[164:167], a[52:55], v[136:139]\n"
  "v_exp_f32 v130, v130\n"
  "v_mfma_f32_16x16x32_bf16 v[136:139], a[168:171], a[56:59], v[136:139]\n"
  "ds_read_b128 a[148:151], v216 offset:5120\n"
  "ds_read_b128 a[144:147], v216 offset:4096\n"
  "v_mfma_f32_16x16x32_bf16 v[136:139], a[172:175], a[60:63], v[136:139]\n"
  "v_exp_f32 v131, v131\n"
  "v_mfma_f32_16x16x32_bf16 v[140:143], a[188:191], a[60:63], v[164:167]\n"
  "v_exp_f32 v132, v132\n"
  "v_mfma_f32_16x16x32_bf16 v[140:143], a[184:187], a[56:59], v[140:143]\n"
  "ds_read_b128 a[160:163], v216 offset:32768\n"
  "v_cvt_pk_bf16_f32 v189, v130, v131\n"
  "v_mfma_f32_16x16x32_bf16 v[140:143], a[180:183], a[52:55], v[140:143]\n"
  "v_exp_f32 v133, v133\n"
  "v_mfma_f32_16x16x32_bf16 v[140:143], a[176:179], a[48:51], v[140:143]\n"
  "ds_read_b128 a[164:167], v216 offset:33792\n"
  "v_cvt_pk_bf16_f32 v190, v132, v133\n"
  "v_mfma_f32_16x16x32_bf16 v[64:67], a[64:67], v[144:147], v[64:67]\n"
  "v_exp_f32 v134, v134\n"
  "v_mfma_f32_16x16x32_bf16 v[64:67], a[96:99], v[148:151], v[64:67]\n"
  "ds_read_b128 a[168:171], v216 offset:34816\n"
  "ds_read_b128 a[172:175], v216 offset:35840\n"
  "v_mfma_f32_16x16x32_bf16 v[68:71], a[100:103], v[148:151], v[68:71]\n"
  "v_exp_f32 v135, v135\n"
  "v_mfma_f32_16x16x32_bf16 v[68:71], a[68:71], v[144:147], v[68:71]\n"
  "v_exp_f32 v136, v136\n"
  "v_mfma_f32_16x16x32_bf16 v[72:75], a[72:75], v[144:147], v[72:75]\n"
  "ds_read_b128 a[188:191], v216 offset:39936\n"
  "v_cvt_pk_bf16_f32 v191, v134, v135\n"
  "v_mfma_f32_16x16x32_bf16 v[72:75], a[104:107], v[148:151], v[72:75]\n"
  "v_exp_f32 v137, v137\n"
  "v_mfma_f32_16x16x32_bf16 v[76:79], a[108:111], v[148:151], v[76:79]\n"
  "ds_read_b128 a[184:187], v216 offset:38912\n"
  "v_cvt_pk_bf16_f32 v192, v136, v137\n"
  "v_mfma_f32_16x16x32_bf16 v[76:79], a[76:79], v[144:147], v[76:79]\n"
  "v_exp_f32 v138, v138\n"
  "v_mfma_f32_16x16x32_bf16 v[80:83], a[80:83], v[144:147], v[80:83]\n"
  "ds_read_b128 a[180:183], v216 offset:37888\n"
  "ds_read_b128 a[176:179], v216 offset:36864\n"
  "v_mfma_f32_16x16x32_bf16 v[80:83], a[112:115], v[148:151], v[80:83]\n"
  "v_exp_f32 v139, v139\n"
  "v_mfma_f32_16x16x32_bf16 v[84:87], a[116:119], v[148:151], v[84:87]\n"
  "v_exp_f32 v140, v140\n"
  "v_mfma_f32_16x16x32_bf16 v[84:87], a[84:87], v[144:147], v[84:87]\n"
  "global_load_lds_dwordx4 v217, s[20:21] offset:0 sc1\n"
  "v_cvt_pk_bf16_f32 v193, v138, v139\n"
  "v_mfma_f32_16x16x32_bf16 v[88:91], a[88:91], v[144:147], v[88:91]\n"
  "v_exp_f32 v141, v141\n"
  "v_mfma_f32_16x16x32_bf16 v[88:91], a[120:123], v[148:151], v[88:91]\n"
  "global_load_lds_dwordx4 v217, s[20:21] offset:1024 sc1\n"
  "v_cvt_pk_bf16_f32 v194, v140, v141\n"
  "v_mfma_f32_16x16x32_bf16 v[92:95], a[124:127], v[148:151], v[92:95]\n"
  "v_exp_f32 v142, v142\n"
  "v_mfma_f32_16x16x32_bf16 v[92:95], a[92:95], v[144:147], v[92:95]\n"
  "global_load_lds_dwordx4 v217, s[20:21] offset:2048 sc1\n"
  "global_load_lds_dwordx4 v217, s[20:21] offset:3072 sc1\n"
  "v_mfma_f32_16x16x32_bf16 v[208:211], a[192:195], v[144:147], v[208:211]\n"
  "v_exp_f32 v143, v143\n"
  "v_mfma_f32_16x16x32_bf16 v[208:211], a[192:195], v[148:151], v[208:211]\n"
  "s_waitcnt lgkmcnt(0)\n"
  "v_cvt_pk_bf16_f32 v195, v142, v143\n"
  "v_mfma_f32_16x16x32_bf16 v[128:131], a[128:131], a[0:3], v[168:171]\n"
  "s_sub_u32 s20, s20, 81920\n"
  "s_subb_u32 s21, s21, 0\n"
  "v_mfma_f32_16x16x32_bf16 v[128:131], a[132:135], a[4:7], v[128:131]\n"
  "global_load_dwordx2 a[244:245], v253, s[62:63] offset:128 sc1\n"
  "v_mfma_f32_16x16x32_bf16 v[128:131], a[136:139], a[8:11], v[128:131]\n"
  "v_mfma_f32_16x16x32_bf16 v[128:131], a[140:143], a[12:15], v[128:131]\n"
  "s_nop 1\n"
  "v_mfma_f32_16x16x32_bf16 v[96:99], a[64:67], v[188:191], v[96:99]\n"
  "s_nop 1\n"
  "v_mfma_f32_16x16x32_bf16 v[96:99], a[96:99], v[192:195], v[96:99]\n"
  "s_nop 1\n"
  "ds_read_b128 a[64:67], v216 offset:8192\n"
  "v_mfma_f32_16x16x32_bf16 v[100:103], a[100:103], v[192:195], v[100:103]\n"
  "v_exp_f32 v128, v128\n"
  "v_mfma_f32_16x16x32_bf16 v[100:103], a[68:71], v[188:191], v[100:103]\n"
  "ds_read_b128 a[96:99], v216 offset:40960\n"
  "ds_read_b128 a[100:103], v216 offset:41984\n"
  "v_mfma_f32_16x16x32_bf16 v[104:107], a[72:75], v[188:191], v[104:107]\n"
  "ds_read_b128 a[68:71], v216 offset:9216\n"
  "v_mfma_f32_16x16x32_bf16 v[104:107], a[104:107], v[192:195], v[104:107]\n"
  "ds_read_b128 a[72:75], v216 offset:10240\n"
  "v_mfma_f32_16x16x32_bf16 v[108:111], a[108:111], v[192:195], v[108:111]\n"
  "v_exp_f32 v129, v129\n"
  "v_mfma_f32_16x16x32_bf16 v[108:111], a[76:79], v[188:191], v[108:111]\n"
  "ds_read_b128 a[104:107], v216 offset:43008\n"
  "v_cvt_pk_bf16_f32 v144, v128, v129\n"
  "v_mfma_f32_16x16x32_bf16 v[112:115], a[80:83], v[188:191], v[112:115]\n"
  "ds_read_b128 a[108:111], v216 offset:44032\n"
  "ds_read_b128 a[76:79], v216 offset:11264\n"
  "v_mfma_f32_16x16x32_bf16 v[112:115], a[112:115], v[192:195], v[112:115]\n"
  "v_mfma_f32_16x16x32_bf16 v[116:119], a[116:119], v[192:195], v[116:119]\n"
  "v_mfma_f32_16x16x32_bf16 v[116:119], a[84:87], v[188:191], v[116:119]\n"
  "v_exp_f32 v130, v130\n"
  "v_mfma_f32_16x16x32_bf16 v[120:123], a[88:91], v[188:191], v[120:123]\n"
  "v_mfma_f32_16x16x32_bf16 v[120:123], a[120:123], v[192:195], v[120:123]\n"
  "v_mfma_f32_16x16x32_bf16 v[124:127], a[124:127], v[192:195], v[124:127]\n"
  "v_mfma_f32_16x16x32_bf16 v[124:127], a[92:95], v[188:191], v[124:127]\n"
  "s_xor_b32 s49, s49, 0x10000\n"
  "v_mfma_f32_16x16x32_bf16 v[212:215], a[192:195], v[188:191], v[212:215]\n"
  "v_exp_f32 v131, v131\n"
  "v_mfma_f32_16x16x32_bf16 v[212:215], a[192:195], v[192:195], v[212:215]\n"
  "v_cvt_pk_bf16_f32 v145, v130, v131\n"
  "v_mov_b32 v146, 0\n"
  "v_mov_b32 v147, 0\n"
  "s_setpc_b64 s[22:23]\n"
  "L_PDX_%=:\n"
  "v_mfma_f32_16x16x32_bf16 v[128:131], a[128:131], a[16:19], v[156:159]\n"
  "ds_read_b128 a[80:83], v216 offset:12288\n"
  "ds_read_b128 a[112:115], v216 offset:45056\n"
  "v_mfma_f32_16x16x32_bf16 v[128:131], a[132:135], a[20:23], v[128:131]\n"
  "ds_read_b128 a[116:119], v216 offset:46080\n"
  "ds_read_b128 a[84:87], v216 offset:13312\n"
  "v_mfma_f32_16x16x32_bf16 v[128:131], a[136:139], a[24:27], v[128:131]\n"
  "ds_read_b128 a[88:91], v216 offset:14336\n"
  "ds_read_b128 a[120:123], v216 offset:47104\n"
  "v_mfma_f32_16x16x32_bf16 v[128:131], a[140:143], a[28:31], v[128:131]\n"
  "ds_read_b128 a[124:127], v216 offset:48128\n"
  "ds_read_b128 a[92:95], v216 offset:15360\n"
  "v_mfma_f32_16x16x32_bf16 v[132:135], a[156:159], a[28:31], v[172:175]\n"
  "s_nop 1\n"
  "v_mfma_f32_16x16x32_bf16 v[132:135], a[152:155], a[24:27], v[132:135]\n"
  "s_nop 1\n"
  "v_xor_b32 v216, 0x10000, v216\n"
  "v_mfma_f32_16x16x32_bf16 v[132:135], a[148:151], a[20:23], v[132:135]\n"
  "s_add_u32 s24, s24, 1\n"
  "v_exp_f32 v128, v128\n"
  "v_mfma_f32_16x16x32_bf16 v[132:135], a[144:147], a[16:19], v[132:135]\n"
  "v_exp_f32 v129, v129\n"
  "s_waitcnt lgkmcnt(15)\n"
  "v_mfma_f32_16x16x32_bf16 v[0:3], a[64:67], v[144:147], v[0:3]\n"
  "v_exp_f32 v130, v130\n"
  "s_waitcnt lgkmcnt(12)\n"
  "v_mfma_f32_16x16x32_bf16 v[4:7], a[68:71], v[144:147], v[4:7]\n"
  "v_exp_f32 v131, v131\n"
  "s_waitcnt lgkmcnt(11)\n"
  "v_mfma_f32_16x16x32_bf16 v[8:11], a[72:75], v[144:147], v[8:11]\n"
  "v_exp_f32 v132, v132\n"
  "s_waitcnt lgkmcnt(8)\n"
  "v_mfma_f32_16x16x32_bf16 v[12:15], a[76:79], v[144:147], v[12:15]\n"
  "s_waitcnt lgkmcnt(0)\n"
  "v_exp_f32 v133, v133\n"
  "v_mfma_f32_16x16x32_bf16 v[16:19], a[80:83], v[144:147], v[16:19]\n"
  "s_barrier\n"
  "v_exp_f32 v134, v134\n"
  "v_mfma_f32_16x16x32_bf16 v[20:23], a[84:87], v[144:147], v[20:23]\n"
  "v_exp_f32 v135, v135\n"
  "v_mfma_f32_16x16x32_bf16 v[24:27], a[88:91], v[144:147], v[24:27]\n"
  "v_cvt_pk_bf16_f32 v188, v128, v129\n"
  "v_cvt_pk_bf16_f32 v189, v130, v131\n"
  "v_mfma_f32_16x16x32_bf16 v[28:31], a[92:95], v[144:147], v[28:31]\n"
  "v_cvt_pk_bf16_f32 v190, v132, v133\n"
  "v_cvt_pk_bf16_f32 v191, v134, v135\n"
  "v_mfma_f32_16x16x32_bf16 v[200:203], a[192:195], v[144:147], v[200:203]\n"
  "v_readlane_b32 s22, v252, s24\n"
  "v_mfma_f32_16x16x32_bf16 v[128:131], a[128:131], a[32:35], v[160:163]\n"
  "s_add_u32 s22, s26, s22\n"
  "s_addc_u32 s23, s27, 0\n"
  "s_add_u32 m0, s49, 0\n"
  "v_mfma_f32_16x16x32_bf16 v[128:131], a[132:135], a[36:39], v[128:131]\n"
  "global_load_lds_dwordx4 v232, s[20:21] offset:0 sc1\n"
  "global_load_lds_dwordx4 v232, s[20:21] offset:1024 sc1\n"
  "v_mfma_f32_16x16x32_bf16 v[128:131], a[136:139], a[40:43], v[128:131]\n"
  "v_mfma_f32_16x16x32_bf16 v[128:131], a[140:143], a[44:47], v[128:131]\n"
  "s_nop 1\n"
  "v_mfma_f32_16x16x32_bf16 v[132:135], a[156:159], a[44:47], v[160:163]\n"
  "s_nop 1\n"
  "global_load_lds_dwordx4 v232, s[20:21] offset:2048 sc1\n"
  "v_mfma_f32_16x16x32_bf16 v[132:135], a[152:155], a[40:43], v[132:135]\n"
  "s_nop 1\n"
  "v_mfma_f32_16x16x32_bf16 v[132:135], a[148:151], a[36:39], v[132:135]\n"
  "v_exp_f32 v128, v128\n"
  "v_mfma_f32_16x16x32_bf16 v[132:135], a[144:147], a[32:35], v[132:135]\n"
  "v_exp_f32 v129, v129\n"
  "v_mfma_f32_16x16x32_bf16 v[136:139], a[160:163], a[32:35], v[176:179]\n"
  "v_exp_f32 v130, v130\n"
  "v_mfma_f32_16x16x32_bf16 v[136:139], a[164:167], a[36:39], v[136:139]\n"
  "v_exp_f32 v131, v131\n"
  "v_mfma_f32_16x16x32_bf16 v[136:139], a[168:171], a[40:43], v[136:139]\n"
  "s_nop 1\n"
  "v_exp_f32 v132, v132\n"
  "v_mfma_f32_16x16x32_bf16 v[136:139], a[172:175], a[44:47], v[136:139]\n"
  "v_exp_f32 v133, v133\n"
  "v_mfma_f32_16x16x32_bf16 v[32:35], a[64:67], v[188:191], v[32:35]\n"
  "v_cvt_pk_bf16_f32 v144, v128, v129\n"
  "v_cvt_pk_bf16_f32 v145, v130, v131\n"
  "v_mfma_f32_16x16x32_bf16 v[36:39], a[68:71], v[188:191], v[36:39]\n"
  "v_exp_f32 v134, v134\n"
  "v_mfma_f32_16x16x32_bf16 v[40:43], a[72:75], v[188:191], v[40:43]\n"
  "v_exp_f32 v135, v135\n"
  "v_mfma_f32_16x16x32_bf16 v[44:47], a[76:79], v[188:191], v[44:47]\n"
  "v_exp_f32 v136, v136\n"
  "v_mfma_f32_16x16x32_bf16 v[48:51], a[80:83], v[188:191], v[48:51]\n"
  "v_exp_f32 v137, v137\n"
  "v_mfma_f32_16x16x32_bf16 v[52:55], a[84:87], v[188:191], v[52:55]\n"
  "v_exp_f32 v138, v138\n"
  "v_mfma_f32_16x16x32_bf16 v[56:59], a[88:91], v[188:191], v[56:59]\n"
  "v_cvt_pk_bf16_f32 v146, v132, v133\n"
  "v_cvt_pk_bf16_f32 v147, v134, v135\n"
  "v_mfma_f32_16x16x32_bf16 v[60:63], a[92:95], v[188:191], v[60:63]\n"
  "v_exp_f32 v139, v139\n"
  "v_mfma_f32_16x16x32_bf16 v[204:207], a[192:195], v[188:191], v[204:207]\n"
  "s_waitcnt vmcnt(4)\n"
  "s_barrier\n"
  "v_cvt_pk_bf16_f32 v148, v136, v137\n"
  "v_cvt_pk_bf16_f32 v149, v138, v139\n"
  "v_mov_b32 v150, 0\n"
  "v_mov_b32 v151, 0\n"
  "v_mfma_f32_16x16x32_bf16 v[128:131], a[128:131], a[48:51], v[164:167]\n"
  "global_load_lds_dwordx4 v232, s[20:21] offset:3072 sc1\n"
  "s_add_u32 m0, s49, 16384\n"
  "s_nop 0\n"
  "global_load_lds_dwordx4 v217, s[20:21] offset:0 sc1\n"
  "v_mfma_f32_16x16x32_bf16 v[128:131], a[132:135], a[52:55], v[128:131]\n"
  "global_load_lds_dwordx4 v217, s[20:21] offset:1024 sc1\n"
  "global_load_lds_dwordx4 v217, s[20:21] offset:2048 sc1\n"
  "v_mfma_f32_16x16x32_bf16 v[128:131], a[136:139], a[56:59], v[128:131]\n"
  "global_load_lds_dwordx4 v217, s[20:21] offset:3072 sc1\n"
  "s_add_u32 s20, s20, 16384\n"
  "s_addc_u32 s21, s21, 0\n"
  "v_mfma_f32_16x16x32_bf16 v[128:131], a[140:143], a[60:63], v[128:131]\n"
  "s_add_u32 m0, s49, 32768\n"
  "s_nop 1\n"
  "v_mfma_f32_16x16x32_bf16 v[132:135], a[156:159], a[60:63], v[164:167]\n"
  "s_nop 1\n"
  "v_mfma_f32_16x16x32_bf16 v[132:135], a[152:155], a[56:59], v[132:135]\n"
  "s_nop 1\n"
  "global_load_lds_dwordx4 v232, s[20:21] offset:0 sc1\n"
  "v_mfma_f32_16x16x32_bf16 v[132:135], a[148:151], a[52:55], v[132:135]\n"
  "v_exp_f32 v128, v128\n"
  "v_mfma_f32_16x16x32_bf16 v[132:135], a[144:147], a[48:51], v[132:135]\n"
  "v_exp_f32 v129, v129\n"
  "v_mfma_f32_16x16x32_bf16 v[136:139], a[160:163], a[48:51], v[164:167]\n"
  "global_load_lds_dwordx4 v232, s[20:21] offset:1024 sc1\n"
  "v_cvt_pk_bf16_f32 v188, v128, v129\n"
  "v_mfma_f32_16x16x32_bf16 v[136:139], a[164:167], a[52:55], v[136:139]\n"
  "v_exp_f32 v130, v130\n"
  "v_mfma_f32_16x16x32_bf16 v[136:139], a[168:171], a[56:59], v[136:139]\n"
  "global_load_lds_dwordx4 v232, s[20:21] offset:2048 sc1\n"
  "v_mfma_f32_16x16x32_bf16 v[136:139], a[172:175], a[60:63], v[136:139]\n"
  "v_exp_f32 v131, v131\n"
  "v_mfma_f32_16x16x32_bf16 v[140:143], a[188:191], a[60:63], v[180:183]\n"
  "v_exp_f32 v132, v132\n"
  "v_mfma_f32_16x16x32_bf16 v[140:143], a[184:187], a[56:59], v[140:143]\n"
  "global_load_lds_dwordx4 v232, s[20:21] offset:3072 sc1\n"
  "v_cvt_pk_bf16_f32 v189, v130, v131\n"
  "v_mfma_f32_16x16x32_bf16 v[140:143], a[180:183], a[52:55], v[140:143]\n"
  "v_exp_f32 v133, v133\n"
  "v_mfma_f32_16x16x32_bf16 v[140:143], a[176:179], a[48:51], v[140:143]\n"
  "s_add_u32 m0, s49, 49152\n"
  "v_cvt_pk_bf16_f32 v190, v132, v133\n"
  "v_mfma_f32_16x16x32_bf16 v[64:67], a[64:67], v[144:147], v[64:67]\n"
  "v_exp_f32 v134, v134\n"
  "v_mfma_f32_16x16x32_bf16 v[64:67], a[96:99], v[148:151], v[64:67]\n"
  "global_load_lds_dwordx4 v217, s[20:21] offset:0 sc1\n"
  "v_mfma_f32_16x16x32_bf16 v[68:71], a[100:103], v[148:151], v[68:71]\n"
  "v_exp_f32 v135, v135\n"
  "v_mfma_f32_16x16x32_bf16 v[68:71], a[68:71], v[144:147], v[68:71]\n"
  "v_exp_f32 v136, v136\n"
  "v_mfma_f32_16x16x32_bf16 v[72:75], a[72:75], v[144:147], v[72:75]\n"
  "global_load_lds_dwordx4 v217, s[20:21] offset:1024 sc1\n"
  "v_cvt_pk_bf16_f32 v191, v134, v135\n"
  "v_mfma_f32_16x16x32_bf16 v[72:75], a[104:107], v[148:151], v[72:75]\n"
  "v_exp_f32 v137, v137\n"
  "v_mfma_f32_16x16x32_bf16 v[76:79], a[108:111], v[148:151], v[76:79]\n"
  "global_load_lds_dwordx4 v217, s[20:21] offset:2048 sc1\n"
  "v_cvt_pk_bf16_f32 v192, v136, v137\n"
  "v_mfma_f32_16x16x32_bf16 v[76:79], a[76:79], v[144:147], v[76:79]\n"
  "v_exp_f32 v138, v138\n"
  "v_mfma_f32_16x16x32_bf16 v[80:83], a[80:83], v[144:147], v[80:83]\n"
  "v_mfma_f32_16x16x32_bf16 v[80:83], a[112:115], v[148:151], v[80:83]\n"
  "v_exp_f32 v139, v139\n"
  "v_mfma_f32_16x16x32_bf16 v[84:87], a[116:119], v[148:151], v[84:87]\n"
  "v_exp_f32 v140, v140\n"
  "v_mfma_f32_16x16x32_bf16 v[84:87], a[84:87], v[144:147], v[84:87]\n"
  "global_load_lds_dwordx4 v217, s[20:21] offset:3072 sc1\n"
  "v_cvt_pk_bf16_f32 v193, v138, v139\n"
  "v_mfma_f32_16x16x32_bf16 v[88:91], a[88:91], v[144:147], v[88:91]\n"
  "v_exp_f32 v141, v141\n"
  "v_mfma_f32_16x16x32_bf16 v[88:91], a[120:123], v[148:151], v[88:91]\n"
  "s_sub_u32 s20, s20, 81920\n"
  "s_subb_u32 s21, s21, 0\n"
  "v_cvt_pk_bf16_f32 v194, v140, v141\n"
  "v_mfma_f32_16x16x32_bf16 v[92:95], a[124:127], v[148:151], v[92:95]\n"
  "v_exp_f32 v142, v142\n"
  "v_mfma_f32_16x16x32_bf16 v[92:95], a[92:95], v[144:147], v[92:95]\n"
  "v_mfma_f32_16x16x32_bf16 v[208:211], a[192:195], v[144:147], v[208:211]\n"
  "v_exp_f32 v143, v143\n"
  "v_mfma_f32_16x16x32_bf16 v[208:211], a[192:195], v[148:151], v[208:211]\n"
  "v_cvt_pk_bf16_f32 v195, v142, v143\n"
  "v_mfma_f32_16x16x32_bf16 v[96:99], a[64:67], v[188:191], v[96:99]\n"
  "s_nop 0\n"
  "v_mfma_f32_16x16x32_bf16 v[96:99], a[96:99], v[192:195], v[96:99]\n"
  "s_waitcnt vmcnt(16)\n"
  "global_load_dwordx2 a[244:245], v253, s[62:63] offset:128 sc1\n"
  "v_mfma_f32_16x16x32_bf16 v[100:103], a[100:103], v[192:195], v[100:103]\n"
  "v_mfma_f32_16x16x32_bf16 v[100:103], a[68:71], v[188:191], v[100:103]\n"
  "s_nop 1\n"
  "v_mfma_f32_16x16x32_bf16 v[104:107], a[72:75], v[188:191], v[104:107]\n"
  "s_nop 1\n"
  "v_mfma_f32_16x16x32_bf16 v[104:107], a[104:107], v[192:195], v[104:107]\n"
  "s_nop 1\n"
  "v_mfma_f32_16x16x32_bf16 v[108:111], a[108:111], v[192:195], v[108:111]\n"
  "v_mfma_f32_16x16x32_bf16 v[108:111], a[76:79], v[188:191], v[108:111]\n"
  "v_mfma_f32_16x16x32_bf16 v[112:115], a[80:83], v[188:191], v[112:115]\n"
  "v_mfma_f32_16x16x32_bf16 v[112:115], a[112:115], v[192:195], v[112:115]\n"
  "v_mfma_f32_16x16x32_bf16 v[116:119], a[116:119], v[192:195], v[116:119]\n"
  "v_mfma_f32_16x16x32_bf16 v[116:119], a[84:87], v[188:191], v[116:119]\n"
  "v_mfma_f32_16x16x32_bf16 v[120:123], a[88:91], v[188:191], v[120:123]\n"
  "v_mfma_f32_16x16x32_bf16 v[120:123], a[120:123], v[192:195], v[120:123]\n"
  "v_mfma_f32_16x16x32_bf16 v[124:127], a[124:127], v[192:195], v[124:127]\n"
  "v_mfma_f32_16x16x32_bf16 v[124:127], a[92:95], v[188:191], v[124:127]\n"
  "s_xor_b32 s49, s49, 0x10000\n"
  "v_mfma_f32_16x16x32_bf16 v[212:215], a[192:195], v[188:191], v[212:215]\n"
  "v_mfma_f32_16x16x32_bf16 v[212:215], a[192:195], v[192:195], v[212:215]\n"
  "s_setpc_b64 s[22:23]\n"
  "L_PDF_%=:\n"
  "v_mfma_f32_16x16x32_bf16 v[128:131], a[128:131], a[16:19], v[156:159]\n"
  "ds_read_b128 a[80:83], v216 offset:12288\n"
  "ds_read_b128 a[112:115], v216 offset:45056\n"
  "v_mfma_f32_16x16x32_bf16 v[128:131], a[132:135], a[20:23], v[128:131]\n"
  "ds_read_b128 a[116:119], v216 offset:46080\n"
  "ds_read_b128 a[84:87], v216 offset:13312\n"
  "v_mfma_f32_16x16x32_bf16 v[128:131], a[136:139], a[24:27], v[128:131]\n"
  "ds_read_b128 a[88:91], v216 offset:14336\n"
  "ds_read_b128 a[120:123], v216 offset:47104\n"
  "v_mfma_f32_16x16x32_bf16 v[128:131], a[140:143], a[28:31], v[128:131]\n"
  "ds_read_b128 a[124:127], v216 offset:48128\n"
  "ds_read_b128 a[92:95], v216 offset:15360\n"
  "v_mfma_f32_16x16x32_bf16 v[132:135], a[156:159], a[28:31], v[172:175]\n"
  "s_nop 1\n"
  "v_mfma_f32_16x16x32_bf16 v[132:135], a[152:155], a[24:27], v[132:135]\n"
  "s_nop 1\n"
  "v_xor_b32 v216, 0x10000, v216\n"
  "v_mfma_f32_16x16x32_bf16 v[132:135], a[148:151], a[20:23], v[132:135]\n"
  "s_add_u32 s24, s24, 1\n"
  "v_exp_f32 v128, v128\n"
  "v_mfma_f32_16x16x32_bf16 v[132:135], a[144:147], a[16:19], v[132:135]\n"
  "v_exp_f32 v129, v129\n"
  "s_waitcnt lgkmcnt(15)\n"
  "v_mfma_f32_16x16x32_bf16 v[0:3], a[64:67], v[144:147], v[0:3]\n"
  "v_exp_f32 v130, v130\n"
  "s_waitcnt lgkmcnt(12)\n"
  "v_mfma_f32_16x16x32_bf16 v[4:7], a[68:71], v[144:147], v[4:7]\n"
  "v_exp_f32 v131, v131\n"
  "s_waitcnt lgkmcnt(11)\n"
  "v_mfma_f32_16x16x32_bf16 v[8:11], a[72:75], v[144:147], v[8:11]\n"
  "v_exp_f32 v132, v132\n"
  "s_waitcnt lgkmcnt(8)\n"
  "v_mfma_f32_16x16x32_bf16 v[12:15], a[76:79], v[144:147], v[12:15]\n"
  "s_waitcnt lgkmcnt(0)\n"
  "v_exp_f32 v133, v133\n"
  "v_mfma_f32_16x16x32_bf16 v[16:19], a[80:83], v[144:147], v[16:19]\n"
  "s_barrier\n"
  "v_exp_f32 v134, v134\n"
  "v_mfma_f32_16x16x32_bf16 v[20:23], a[84:87], v[144:147], v[20:23]\n"
  "v_exp_f32 v135, v135\n"
  "v_mfma_f32_16x16x32_bf16 v[24:27], a[88:91], v[144:147], v[24:27]\n"
  "v_cvt_pk_bf16_f32 v188, v128, v129\n"
  "v_cvt_pk_bf16_f32 v189, v130, v131\n"
  "v_mfma_f32_16x16x32_bf16 v[28:31], a[92:95], v[144:147], v[28:31]\n"
  "v_cvt_pk_bf16_f32 v190, v132, v133\n"
  "v_cvt_pk_bf16_f32 v191, v134, v135\n"
  "v_mfma_f32_16x16x32_bf16 v[200:203], a[192:195], v[144:147], v[200:203]\n"
  "v_readlane_b32 s22, v252, s24\n"
  "v_mfma_f32_16x16x32_bf16 v[128:131], a[128:131], a[32:35], v[160:163]\n"
  "s_add_u32 s22, s26, s22\n"
  "s_addc_u32 s23, s27, 0\n"
  "s_add_u32 m0, s49, 0\n"
  "v_mfma_f32_16x16x32_bf16 v[128:131], a[132:135], a[36:39], v[128:131]\n"
  "global_load_lds_dwordx4 v232, s[20:21] offset:0 sc1\n"
  "v_mfma_f32_16x16x32_bf16 v[128:131], a[136:139], a[40:43], v[128:131]\n"
  "v_mfma_f32_16x16x32_bf16 v[128:131], a[140:143], a[44:47], v[128:131]\n"
  "s_nop 1\n"
  "global_load_lds_dwordx4 v232, s[20:21] offset:1024 sc1\n"
  "v_mfma_f32_16x16x32_bf16 v[132:135], a[156:159], a[44:47], v[160:163]\n"
  "s_nop 1\n"
  "v_mfma_f32_16x16x32_bf16 v[132:135], a[152:155], a[40:43], v[132:135]\n"
  "s_nop 1\n"
  "v_mfma_f32_16x16x32_bf16 v[132:135], a[148:151], a[36:39], v[132:135]\n"
  "v_exp_f32 v128, v128\n"
  "v_mfma_f32_16x16x32_bf16 v[132:135], a[144:147], a[32:35], v[132:135]\n"
  "v_exp_f32 v129, v129\n"
  "v_mfma_f32_16x16x32_bf16 v[136:139], a[160:163], a[32:35], v[176:179]\n"
  "v_exp_f32 v130, v130\n"
  "v_mfma_f32_16x16x32_bf16 v[136:139], a[164:167], a[36:39], v[136:139]\n"
  "v_exp_f32 v131, v131\n"
  "v_mfma_f32_16x16x32_bf16 v[136:139], a[168:171], a[40:43], v[136:139]\n"
  "s_nop 1\n"
  "v_exp_f32 v132, v132\n"
  "v_mfma_f32_16x16x32_bf16 v[136:139], a[172:175], a[44:47], v[136:139]\n"
  "v_exp_f32 v133, v133\n"
  "v_mfma_f32_16x16x32_bf16 v[32:35], a[64:67], v[188:191], v[32:35]\n"
  "v_cvt_pk_bf16_f32 v144, v128, v129\n"
  "v_cvt_pk_bf16_f32 v145, v130, v131\n"
  "v_mfma_f32_16x16x32_bf16 v[36:39], a[68:71], v[188:191], v[36:39]\n"
  "v_exp_f32 v134, v134\n"
  "v_mfma_f32_16x16x32_bf16 v[40:43], a[72:75], v[188:191], v[40:43]\n"
  "v_exp_f32 v135, v135\n"
  "v_mfma_f32_16x16x32_bf16 v[44:47], a[76:79], v[188:191], v[44:47]\n"
  "v_exp_f32 v136, v136\n"
  "v_mfma_f32_16x16x32_bf16 v[48:51], a[80:83], v[188:191], v[48:51]\n"
  "v_exp_f32 v137, v137\n"
  "v_mfma_f32_16x16x32_bf16 v[52:55], a[84:87], v[188:191], v[52:55]\n"
  "v_exp_f32 v138, v138\n"
  "v_mfma_f32_16x16x32_bf16 v[56:59], a[88:91], v[188:191], v[56:59]\n"
  "v_cvt_pk_bf16_f32 v146, v132, v133\n"
  "v_cvt_pk_bf16_f32 v147, v134, v135\n"
  "v_mfma_f32_16x16x32_bf16 v[60:63], a[92:95], v[188:191], v[60:63]\n"
  "v_exp_f32 v139, v139\n"
  "v_mfma_f32_16x16x32_bf16 v[204:207], a[192:195], v[188:191], v[204:207]\n"
  "s_waitcnt vmcnt(3)\n"
  "s_barrier\n"
  "v_cvt_pk_bf16_f32 v148, v136, v137\n"
  "v_cvt_pk_bf16_f32 v149, v138, v139\n"
  "v_mov_b32 v150, 0\n"
  "v_mov_b32 v151, 0\n"
  "v_mfma_f32_16x16x32_bf16 v[128:131], a[128:131], a[48:51], v[164:167]\n"
  "global_load_lds_dwordx4 v232, s[20:21] offset:2048 sc1\n"
  "global_load_lds_dwordx4 v232, s[20:21] offset:3072 sc1\n"
  "s_add_u32 m0, s49, 16384\n"
  "v_mfma_f32_16x16x32_bf16 v[128:131], a[132:135], a[52:55], v[128:131]\n"
  "ds_read_b128 a[128:131], v216 offset:0\n"
  "global_load_lds_dwordx4 v217, s[20:21] offset:0 sc1\n"
  "v_mfma_f32_16x16x32_bf16 v[128:131], a[136:139], a[56:59], v[128:131]\n"
  "ds_read_b128 a[132:135], v216 offset:1024\n"
  "global_load_lds_dwordx4 v217, s[20:21] offset:1024 sc1\n"
  "v_mfma_f32_16x16x32_bf16 v[128:131], a[140:143], a[60:63], v[128:131]\n"
  "ds_read_b128 a[136:139], v216 offset:2048\n"
  "global_load_lds_dwordx4 v217, s[20:21] offset:2048 sc1\n"
  "v_mfma_f32_16x16x32_bf16 v[132:135], a[156:159], a[60:63], v[164:167]\n"
  "ds_read_b128 a[140:143], v216 offset:3072\n"
  "global_load_lds_dwordx4 v217, s[20:21] offset:3072 sc1\n"
  "v_mfma_f32_16x16x32_bf16 v[132:135], a[152:155], a[56:59], v[132:135]\n"
  "s_nop 1\n"
  "ds_read_b128 a[156:159], v216 offset:7168\n"
  "v_mfma_f32_16x16x32_bf16 v[132:135], a[148:151], a[52:55], v[132:135]\n"
  "v_exp_f32 v128, v128\n"
  "v_mfma_f32_16x16x32_bf16 v[132:135], a[144:147], a[48:51], v[132:135]\n"
  "v_exp_f32 v129, v129\n"
  "v_mfma_f32_16x16x32_bf16 v[136:139], a[160:163], a[48:51], v[164:167]\n"
  "ds_read_b128 a[152:155], v216 offset:6144\n"
  "v_cvt_pk_bf16_f32 v188, v128, v129\n"
  "v_mfma_f32_16x16x32_bf16 v[136:139], a[164:167], a[52:55], v[136:139]\n"
  "v_exp_f32 v130, v130\n"
  "v_mfma_f32_16x16x32_bf16 v[136:139], a[168:171], a[56:59], v[136:139]\n"
  "ds_read_b128 a[148:151], v216 offset:5120\n"
  "ds_read_b128 a[144:147], v216 offset:4096\n"
  "v_mfma_f32_16x16x32_bf16 v[136:139], a[172:175], a[60:63], v[136:139]\n"
  "v_exp_f32 v131, v131\n"
  "v_mfma_f32_16x16x32_bf16 v[140:143], a[188:191], a[60:63], v[180:183]\n"
  "v_exp_f32 v132, v132\n"
  "v_mfma_f32_16x16x32_bf16 v[140:143], a[184:187], a[56:59], v[140:143]\n"
  "ds_read_b128 a[160:163], v216 offset:32768\n"
  "v_cvt_pk_bf16_f32 v189, v130, v131\n"
  "v_mfma_f32_16x16x32_bf16 v[140:143], a[180:183], a[52:55], v[140:143]\n"
  "v_exp_f32 v133, v133\n"
  "v_mfma_f32_16x16x32_bf16 v[140:143], a[176:179], a[48:51], v[140:143]\n"
  "ds_read_b128 a[164:167], v216 offset:33792\n"
  "v_cvt_pk_bf16_f32 v190, v132, v133\n"
  "v_mfma_f32_16x16x32_bf16 v[64:67], a[64:67], v[144:147], v[64:67]\n"
  "v_exp_f32 v134, v134\n"
  "v_mfma_f32_16x16x32_bf16 v[64:67], a[96:99], v[148:151], v[64:67]\n"
  "ds_read_b128 a[168:171], v216 offset:34816\n"
  "ds_read_b128 a[172:175], v216 offset:35840\n"
  "v_mfma_f32_16x16x32_bf16 v[68:71], a[100:103], v[148:151], v[68:71]\n"
  "v_exp_f32 v135, v135\n"
  "v_mfma_f32_16x16x32_bf16 v[68:71], a[68:71], v[144:147], v[68:71]\n"
  "v_exp_f32 v136, v136\n"
  "v_mfma_f32_16x16x32_bf16 v[72:75], a[72:75], v[144:147], v[72:75]\n"
  "ds_read_b128 a[188:191], v216 offset:39936\n"
  "v_cvt_pk_bf16_f32 v191, v134, v135\n"
  "v_mfma_f32_16x16x32_bf16 v[72:75], a[104:107], v[148:151], v[72:75]\n"
  "v_exp_f32 v137, v137\n"
  "v_mfma_f32_16x16x32_bf16 v[76:79], a[108:111], v[148:151], v[76:79]\n"
  "ds_read_b128 a[184:187], v216 offset:38912\n"
  "v_cvt_pk_bf16_f32 v192, v136, v137\n"
  "v_mfma_f32_16x16x32_bf16 v[76:79], a[76:79], v[144:147], v[76:79]\n"
  "v_exp_f32 v138, v138\n"
  "v_mfma_f32_16x16x32_bf16 v[80:83], a[80:83], v[144:147], v[80:83]\n"
  "ds_read_b128 a[180:183], v216 offset:37888\n"
  "ds_read_b128 a[176:179], v216 offset:36864\n"
  "v_mfma_f32_16x16x32_bf16 v[80:83], a[112:115], v[148:151], v[80:83]\n"
  "v_exp_f32 v139, v139\n"
  "v_mfma_f32_16x16x32_bf16 v[84:87], a[116:119], v[148:151], v[84:87]\n"
  "v_exp_f32 v140, v140\n"
  "v_mfma_f32_16x16x32_bf16 v[84:87], a[84:87], v[144:147], v[84:87]\n"
  "s_add_u32 s20, s20, 16384\n"
  "s_addc_u32 s21, s21, 0\n"
  "v_cvt_pk_bf16_f32 v193, v138, v139\n"
  "v_mfma_f32_16x16x32_bf16 v[88:91], a[88:91], v[144:147], v[88:91]\n"
  "s_add_u32 m0, s49, 32768\n"
  "v_exp_f32 v141, v141\n"
  "v_mfma_f32_16x16x32_bf16 v[88:91], a[120:123], v[148:151], v[88:91]\n"
  "global_load_lds_dwordx4 v232, s[20:21] offset:0 sc1\n"
  "v_cvt_pk_bf16_f32 v194, v140, v141\n"
  "v_mfma_f32_16x16x32_bf16 v[92:95], a[124:127], v[148:151], v[92:95]\n"
  "v_exp_f32 v142, v142\n"
  "v_mfma_f32_16x16x32_bf16 v[92:95], a[92:95], v[144:147], v[92:95]\n"
  "global_load_lds_dwordx4 v232, s[20:21] offset:1024 sc1\n"
  "global_load_lds_dwordx4 v232, s[20:21] offset:2048 sc1\n"
  "v_mfma_f32_16x16x32_bf16 v[208:211], a[192:195], v[144:147], v[208:211]\n"
  "v_exp_f32 v143, v143\n"
  "v_mfma_f32_16x16x32_bf16 v[208:211], a[192:195], v[148:151], v[208:211]\n"
  "s_waitcnt lgkmcnt(0)\n"
  "global_load_lds_dwordx4 v232, s[20:21] offset:3072 sc1\n"
  "v_cvt_pk_bf16_f32 v195, v142, v143\n"
  "v_mfma_f32_16x16x32_bf16 v[128:131], a[128:131], a[0:3], v[152:155]\n"
  "s_add_u32 m0, s49, 49152\n"
  "s_nop 0\n"
  "global_load_lds_dwordx4 v217, s[20:21] offset:0 sc1\n"
  "global_load_lds_dwordx4 v217, s[20:21] offset:1024 sc1\n"
  "v_mfma_f32_16x16x32_bf16 v[128:131], a[132:135], a[4:7], v[128:131]\n"
  "s_waitcnt vmcnt(14)\n"
  "global_load_dwordx2 a[244:245], v253, s[62:63] offset:128 sc1\n"
  "global_load_lds_dwordx4 v217, s[20:21] offset:2048 sc1\n"
  "v_mfma_f32_16x16x32_bf16 v[128:131], a[136:139], a[8:11], v[128:131]\n"
  "global_load_lds_dwordx4 v217, s[20:21] offset:3072 sc1\n"
  "v_mfma_f32_16x16x32_bf16 v[128:131], a[140:143], a[12:15], v[128:131]\n"
  "s_nop 1\n"
  "v_mfma_f32_16x16x32_bf16 v[132:135], a[156:159], a[12:15], v[152:155]\n"
  "s_nop 1\n"
  "v_mfma_f32_16x16x32_bf16 v[132:135], a[152:155], a[8:11], v[132:135]\n"
  "s_sub_u32 s20, s20, 81920\n"
  "s_subb_u32 s21, s21, 0\n"
  "v_mfma_f32_16x16x32_bf16 v[132:135], a[148:151], a[4:7], v[132:135]\n"
  "v_exp_f32 v128, v128\n"
  "v_mfma_f32_16x16x32_bf16 v[132:135], a[144:147], a[0:3], v[132:135]\n"
  "v_exp_f32 v129, v129\n"
  "v_mfma_f32_16x16x32_bf16 v[136:139], a[160:163], a[0:3], v[152:155]\n"
  "v_cvt_pk_bf16_f32 v144, v128, v129\n"
  "v_mfma_f32_16x16x32_bf16 v[136:139], a[164:167], a[4:7], v[136:139]\n"
  "v_exp_f32 v130, v130\n"
  "v_mfma_f32_16x16x32_bf16 v[136:139], a[168:171], a[8:11], v[136:139]\n"
  "v_mfma_f32_16x16x32_bf16 v[136:139], a[172:175], a[12:15], v[136:139]\n"
  "v_exp_f32 v131, v131\n"
  "v_mfma_f32_16x16x32_bf16 v[140:143], a[188:191], a[12:15], v[152:155]\n"
  "v_exp_f32 v132, v132\n"
  "v_mfma_f32_16x16x32_bf16 v[140:143], a[184:187], a[8:11], v[140:143]\n"
  "v_cvt_pk_bf16_f32 v145, v130, v131\n"
  "v_mfma_f32_16x16x32_bf16 v[140:143], a[180:183], a[4:7], v[140:143]\n"
  "v_exp_f32 v133, v133\n"
  "v_mfma_f32_16x16x32_bf16 v[140:143], a[176:179], a[0:3], v[140:143]\n"
  "v_cvt_pk_bf16_f32 v146, v132, v133\n"
  "v_mfma_f32_16x16x32_bf16 v[96:99], a[64:67], v[188:191], v[96:99]\n"
  "v_exp_f32 v134, v134\n"
  "v_mfma_f32_16x16x32_bf16 v[96:99], a[96:99], v[192:195], v[96:99]\n"
  "ds_read_b128 a[64:67], v216 offset:8192\n"
  "v_mfma_f32_16x16x32_bf16 v[100:103], a[100:103], v[192:195], v[100:103]\n"
  "v_exp_f32 v135, v135\n"
  "v_mfma_f32_16x16x32_bf16 v[100:103], a[68:71], v[188:191], v[100:103]\n"
  "v_exp_f32 v136, v136\n"
  "v_mfma_f32_16x16x32_bf16 v[104:107], a[72:75], v[188:191], v[104:107]\n"
  "ds_read_b128 a[96:99], v216 offset:40960\n"
  "v_cvt_pk_bf16_f32 v147, v134, v135\n"
  "v_mfma_f32_16x16x32_bf16 v[104:107], a[104:107], v[192:195], v[104:107]\n"
  "v_exp_f32 v137, v137\n"
  "v_mfma_f32_16x16x32_bf16 v[108:111], a[108:111], v[192:195], v[108:111]\n"
  "ds_read_b128 a[100:103], v216 offset:41984\n"
  "v_cvt_pk_bf16_f32 v148, v136, v137\n"
  "v_mfma_f32_16x16x32_bf16 v[108:111], a[76:79], v[188:191], v[108:111]\n"
  "v_exp_f32 v138, v138\n"
  "v_mfma_f32_16x16x32_bf16 v[112:115], a[80:83], v[188:191], v[112:115]\n"
  "ds_read_b128 a[68:71], v216 offset:9216\n"
  "ds_read_b128 a[72:75], v216 offset:10240\n"
  "v_mfma_f32_16x16x32_bf16 v[112:115], a[112:115], v[192:195], v[112:115]\n"
  "v_exp_f32 v139, v139\n"
  "v_mfma_f32_16x16x32_bf16 v[116:119], a[116:119], v[192:195], v[116:119]\n"
  "v_exp_f32 v140, v140\n"
  "v_mfma_f32_16x16x32_bf16 v[116:119], a[84:87], v[188:191], v[116:119]\n"
  "ds_read_b128 a[104:107], v216 offset:43008\n"
  "v_cvt_pk_bf16_f32 v149, v138, v139\n"
  "v_mfma_f32_16x16x32_bf16 v[120:123], a[88:91], v[188:191], v[120:123]\n"
  "v_exp_f32 v141, v141\n"
  "v_mfma_f32_16x16x32_bf16 v[120:123], a[120:123], v[192:195], v[120:123]\n"
  "ds_read_b128 a[108:111], v216 offset:44032\n"
  "v_cvt_pk_bf16_f32 v150, v140, v141\n"
  "v_mfma_f32_16x16x32_bf16 v[124:127], a[124:127], v[192:195], v[124:127]\n"
  "v_exp_f32 v142, v142\n"
  "v_mfma_f32_16x16x32_bf16 v[124:127], a[92:95], v[188:191], v[124:127]\n"
  "s_xor_b32 s49, s49, 0x10000\n"
  "ds_read_b128 a[76:79], v216 offset:11264\n"
  "v_mfma_f32_16x16x32_bf16 v[212:215], a[192:195], v[188:191], v[212:215]\n"
  "v_exp_f32 v143, v143\n"
  "v_mfma_f32_16x16x32_bf16 v[212:215], a[192:195], v[192:195], v[212:215]\n"
  "v_cvt_pk_bf16_f32 v151, v142, v143\n"
  "s_setpc_b64 s[22:23]\n"
  "L_AFF_%=:\n"
  "v_mfma_f32_16x16x32_bf16 v[128:131], a[128:131], a[16:19], v[156:159]\n"
  "ds_read_b128 a[80:83], v216 offset:12288\n"
  "ds_read_b128 a[112:115], v216 offset:45056\n"
  "v_mfma_f32_16x16x32_bf16 v[128:131], a[132:135], a[20:23], v[128:131]\n"
  "ds_read_b128 a[116:119], v216 offset:46080\n"
  "ds_read_b128 a[84:87], v216 offset:13312\n"
  "v_mfma_f32_16x16x32_bf16 v[128:131], a[136:139], a[24:27], v[128:131]\n"
  "ds_read_b128 a[88:91], v216 offset:14336\n"
  "ds_read_b128 a[120:123], v216 offset:47104\n"
  "v_mfma_f32_16x16x32_bf16 v[128:131], a[140:143], a[28:31], v[128:131]\n"
  "ds_read_b128 a[124:127], v216 offset:48128\n"
  "ds_read_b128 a[92:95], v216 offset:15360\n"
  "v_mfma_f32_16x16x32_bf16 v[132:135], a[156:159], a[28:31], v[156:159]\n"
  "s_nop 1\n"
  "v_mfma_f32_16x16x32_bf16 v[132:135], a[152:155], a[24:27], v[132:135]\n"
  "s_nop 1\n"
  "v_xor_b32 v216, 0x10000, v216\n"
  "v_mfma_f32_16x16x32_bf16 v[132:135], a[148:151], a[20:23], v[132:135]\n"
  "s_add_u32 s24, s24, 1\n"
  "v_exp_f32 v128, v128\n"
  "v_mfma_f32_16x16x32_bf16 v[132:135], a[144:147], a[16:19], v[132:135]\n"
  "v_exp_f32 v129, v129\n"
  "v_mfma_f32_16x16x32_bf16 v[136:139], a[160:163], a[16:19], v[156:159]\n"
  "v_cvt_pk_bf16_f32 v188, v128, v129\n"
  "s_waitcnt vmcnt(2)\n"
  "v_accvgpr_read_b32 v234, a244\n"
  "v_mfma_f32_16x16x32_bf16 v[136:139], a[164:167], a[20:23], v[136:139]\n"
  "v_exp_f32 v130, v130\n"
  "v_mfma_f32_16x16x32_bf16 v[136:139], a[168:171], a[24:27], v[136:139]\n"
  "v_accvgpr_read_b32 v235, a245\n"
  "v_readlane_b32 s22, v252, s24\n"
  "v_mfma_f32_16x16x32_bf16 v[136:139], a[172:175], a[28:31], v[136:139]\n"
  "v_exp_f32 v131, v131\n"
  "v_mfma_f32_16x16x32_bf16 v[140:143], a[188:191], a[28:31], v[156:159]\n"
  "v_exp_f32 v132, v132\n"
  "v_mfma_f32_16x16x32_bf16 v[140:143], a[184:187], a[24:27], v[140:143]\n"
  "s_waitcnt lgkmcnt(0)\n"
  "s_barrier\n"
  "v_cvt_pk_bf16_f32 v189, v130, v131\n"
  "v_mfma_f32_16x16x32_bf16 v[140:143], a[180:183], a[20:23], v[140:143]\n"
  "v_exp_f32 v133, v133\n"
  "v_mfma_f32_16x16x32_bf16 v[140:143], a[176:179], a[16:19], v[140:143]\n"
  "v_cvt_pk_bf16_f32 v190, v132, v133\n"
  "v_cmp_ne_u64 s[96:97], s[16:17], v[234:235]\n"
  "v_mfma_f32_16x16x32_bf16 v[0:3], a[64:67], v[144:147], v[0:3]\n"
  "v_exp_f32 v134, v134\n"
  "v_mfma_f32_16x16x32_bf16 v[0:3], a[96:99], v[148:151], v[0:3]\n"
  "s_add_u32 s22, s26, s22\n"
  "s_addc_u32 s23, s27, 0\n"
  "v_mfma_f32_16x16x32_bf16 v[4:7], a[100:103], v[148:151], v[4:7]\n"
  "v_exp_f32 v135, v135\n"
  "v_mfma_f32_16x16x32_bf16 v[4:7], a[68:71], v[144:147], v[4:7]\n"
  "v_exp_f32 v136, v136\n"
  "v_mfma_f32_16x16x32_bf16 v[8:11], a[72:75], v[144:147], v[8:11]\n"
  "v_cvt_pk_bf16_f32 v191, v134, v135\n"
  "v_mfma_f32_16x16x32_bf16 v[8:11], a[104:107], v[148:151], v[8:11]\n"
  "v_exp_f32 v137, v137\n"
  "v_mfma_f32_16x16x32_bf16 v[12:15], a[108:111], v[148:151], v[12:15]\n"
  "v_cvt_pk_bf16_f32 v192, v136, v137\n"
  "v_mfma_f32_16x16x32_bf16 v[12:15], a[76:79], v[144:147], v[12:15]\n"
  "v_exp_f32 v138, v138\n"
  "v_mfma_f32_16x16x32_bf16 v[16:19], a[80:83], v[144:147], v[16:19]\n"
  "v_mfma_f32_16x16x32_bf16 v[16:19], a[112:115], v[148:151], v[16:19]\n"
  "v_exp_f32 v139, v139\n"
  "v_mfma_f32_16x16x32_bf16 v[20:23], a[116:119], v[148:151], v[20:23]\n"
  "v_exp_f32 v140, v140\n"
  "v_mfma_f32_16x16x32_bf16 v[20:23], a[84:87], v[144:147], v[20:23]\n"
  "v_cvt_pk_bf16_f32 v193, v138, v139\n"
  "v_mfma_f32_16x16x32_bf16 v[24:27], a[88:91], v[144:147], v[24:27]\n"
  "v_exp_f32 v141, v141\n"
  "v_mfma_f32_16x16x32_bf16 v[24:27], a[120:123], v[148:151], v[24:27]\n"
  "v_cvt_pk_bf16_f32 v194, v140, v141\n"
  "v_mfma_f32_16x16x32_bf16 v[28:31], a[124:127], v[148:151], v[28:31]\n"
  "v_exp_f32 v142, v142\n"
  "v_mfma_f32_16x16x32_bf16 v[28:31], a[92:95], v[144:147], v[28:31]\n"
  "v_mfma_f32_16x16x32_bf16 v[200:203], a[192:195], v[144:147], v[200:203]\n"
  "v_exp_f32 v143, v143\n"
  "v_mfma_f32_16x16x32_bf16 v[200:203], a[192:195], v[148:151], v[200:203]\n"
  "v_cvt_pk_bf16_f32 v195, v142, v143\n"
  "s_cmp_lg_u64 s[96:97], 0\n"
  "s_cbranch_scc1 L_hAFF_retry_%=\n"
  "L_hAFF_ok_%=:\n"
  "v_mfma_f32_16x16x32_bf16 v[128:131], a[128:131], a[32:35], v[160:163]\n"
  "s_add_u32 m0, s48, 0\n"
  "v_mfma_f32_16x16x32_bf16 v[128:131], a[132:135], a[36:39], v[128:131]\n"
  "v_mfma_f32_16x16x32_bf16 v[128:131], a[136:139], a[40:43], v[128:131]\n"
  "v_mfma_f32_16x16x32_bf16 v[128:131], a[140:143], a[44:47], v[128:131]\n"
  "s_nop 1\n"
  "global_load_lds_dwordx4 v232, s[50:51] sc1\n"
  "v_mfma_f32_16x16x32_bf16 v[132:135], a[156:159], a[44:47], v[160:163]\n"
  "s_nop 1\n"
  "v_mfma_f32_16x16x32_bf16 v[132:135], a[152:155], a[40:43], v[132:135]\n"
  "s_nop 1\n"
  "v_mfma_f32_16x16x32_bf16 v[132:135], a[148:151], a[36:39], v[132:135]\n"
  "v_exp_f32 v128, v128\n"
  "v_mfma_f32_16x16x32_bf16 v[132:135], a[144:147], a[32:35], v[132:135]\n"
  "v_exp_f32 v129, v129\n"
  "v_mfma_f32_16x16x32_bf16 v[136:139], a[160:163], a[32:35], v[160:163]\n"
  "global_load_lds_dwordx4 v228, s[50:51] offset:1024 sc1\n"
  "v_cvt_pk_bf16_f32 v144, v128, v129\n"
  "v_mfma_f32_16x16x32_bf16 v[136:139], a[164:167], a[36:39], v[136:139]\n"
  "v_exp_f32 v130, v130\n"
  "v_mfma_f32_16x16x32_bf16 v[136:139], a[168:171], a[40:43], v[136:139]\n"
  "global_load_lds_dwordx4 v229, s[52:53] offset:2048\n"
  "v_mfma_f32_16x16x32_bf16 v[136:139], a[172:175], a[44:47], v[136:139]\n"
  "v_exp_f32 v131, v131\n"
  "v_mfma_f32_16x16x32_bf16 v[140:143], a[188:191], a[44:47], v[160:163]\n"
  "v_exp_f32 v132, v132\n"
  "v_mfma_f32_16x16x32_bf16 v[140:143], a[184:187], a[40:43], v[140:143]\n"
  "global_load_lds_dwordx4 v230, s[52:53] offset:3072\n"
  "v_cvt_pk_bf16_f32 v145, v130, v131\n"
  "v_mfma_f32_16x16x32_bf16 v[140:143], a[180:183], a[36:39], v[140:143]\n"
  "v_exp_f32 v133, v133\n"
  "v_mfma_f32_16x16x32_bf16 v[140:143], a[176:179], a[32:35], v[140:143]\n"
  "v_cvt_pk_bf16_f32 v146, v132, v133\n"
  "v_mfma_f32_16x16x32_bf16 v[32:35], a[64:67], v[188:191], v[32:35]\n"
  "v_exp_f32 v134, v134\n"
  "v_mfma_f32_16x16x32_bf16 v[32:35], a[96:99], v[192:195], v[32:35]\n"
  "s_add_u32 s50, s50, 1024\n"
  "s_addc_u32 s51, s51, 0\n"
  "v_mfma_f32_16x16x32_bf16 v[36:39], a[100:103], v[192:195], v[36:39]\n"
  "v_exp_f32 v135, v135\n"
  "v_mfma_f32_16x16x32_bf16 v[36:39], a[68:71], v[188:191], v[36:39]\n"
  "v_exp_f32 v136, v136\n"
  "v_mfma_f32_16x16x32_bf16 v[40:43], a[72:75], v[188:191], v[40:43]\n"
  "s_add_u32 s52, s52, 32768\n"
  "s_addc_u32 s53, s53, 0\n"
  "v_cvt_pk_bf16_f32 v147, v134, v135\n"
  "v_mfma_f32_16x16x32_bf16 v[40:43], a[104:107], v[192:195], v[40:43]\n"
  "v_exp_f32 v137, v137\n"
  "v_mfma_f32_16x16x32_bf16 v[44:47], a[108:111], v[192:195], v[44:47]\n"
  "v_cvt_pk_bf16_f32 v148, v136, v137\n"
  "v_mfma_f32_16x16x32_bf16 v[44:47], a[76:79], v[188:191], v[44:47]\n"
  "s_add_u32 m0, s48, 16384\n"
  "v_exp_f32 v138, v138\n"
  "v_mfma_f32_16x16x32_bf16 v[48:51], a[80:83], v[188:191], v[48:51]\n"
  "v_mfma_f32_16x16x32_bf16 v[48:51], a[112:115], v[192:195], v[48:51]\n"
  "v_exp_f32 v139, v139\n"
  "v_mfma_f32_16x16x32_bf16 v[52:55], a[116:119], v[192:195], v[52:55]\n"
  "v_exp_f32 v140, v140\n"
  "v_mfma_f32_16x16x32_bf16 v[52:55], a[84:87], v[188:191], v[52:55]\n"
  "global_load_lds_dwordx4 v232, s[50:51] sc1\n"
  "v_cvt_pk_bf16_f32 v149, v138, v139\n"
  "v_mfma_f32_16x16x32_bf16 v[56:59], a[88:91], v[188:191], v[56:59]\n"
  "v_exp_f32 v141, v141\n"
  "v_mfma_f32_16x16x32_bf16 v[56:59], a[120:123], v[192:195], v[56:59]\n"
  "global_load_lds_dwordx4 v228, s[50:51] offset:1024 sc1\n"
  "v_cvt_pk_bf16_f32 v150, v140, v141\n"
  "v_mfma_f32_16x16x32_bf16 v[60:63], a[124:127], v[192:195], v[60:63]\n"
  "v_exp_f32 v142, v142\n"
  "v_mfma_f32_16x16x32_bf16 v[60:63], a[92:95], v[188:191], v[60:63]\n"
  "v_mfma_f32_16x16x32_bf16 v[204:207], a[192:195], v[188:191], v[204:207]\n"
  "v_exp_f32 v143, v143\n"
  "v_mfma_f32_16x16x32_bf16 v[204:207], a[192:195], v[192:195], v[204:207]\n"
  "s_waitcnt vmcnt(6)\n"
  "s_barrier\n"
  "v_cvt_pk_bf16_f32 v151, v142, v143\n"
  "v_mfma_f32_16x16x32_bf16 v[128:131], a[128:131], a[48:51], v[164:167]\n"
  "global_load_lds_dwordx4 v229, s[52:53] offset:2048\n"
  "v_mfma_f32_16x16x32_bf16 v[128:131], a[132:135], a[52:55], v[128:131]\n"
  "ds_read_b128 a[128:131], v216 offset:0\n"
  "v_mfma_f32_16x16x32_bf16 v[128:131], a[136:139], a[56:59], v[128:131]\n"
  "ds_read_b128 a[132:135], v216 offset:1024\n"
  "global_load_lds_dwordx4 v230, s[52:53] offset:3072\n"
  "v_mfma_f32_16x16x32_bf16 v[128:131], a[140:143], a[60:63], v[128:131]\n"
  "s_nop 1\n"
  "ds_read_b128 a[136:139], v216 offset:2048\n"
  "v_mfma_f32_16x16x32_bf16 v[132:135], a[156:159], a[60:63], v[164:167]\n"
  "s_nop 1\n"
  "ds_read_b128 a[140:143], v216 offset:3072\n"
  "v_mfma_f32_16x16x32_bf16 v[132:135], a[152:155], a[56:59], v[132:135]\n"
  "s_add_u32 s50, s50, 1024\n"
  "s_addc_u32 s51, s51, 0\n"
  "ds_read_b128 a[156:159], v216 offset:7168\n"
  "v_mfma_f32_16x16x32_bf16 v[132:135], a[148:151], a[52:55], v[132:135]\n"
  "v_exp_f32 v128, v128\n"
  "v_mfma_f32_16x16x32_bf16 v[132:135], a[144:147], a[48:51], v[132:135]\n"
  "v_exp_f32 v129, v129\n"
  "v_mfma_f32_16x16x32_bf16 v[136:139], a[160:163], a[48:51], v[164:167]\n"
  "ds_read_b128 a[152:155], v216 offset:6144\n"
  "v_cvt_pk_bf16_f32 v188, v128, v129\n"
  "v_mfma_f32_16x16x32_bf16 v[136:139], a[164:167], a[52:55], v[136:139]\n"
  "v_exp_f32 v130, v130\n"
  "v_mfma_f32_16x16x32_bf16 v[136:139], a[168:171], a[56:59], v[136:139]\n"
  "ds_read_b128 a[148:151], v216 offset:5120\n"
  "ds_read_b128 a[144:147], v216 offset:4096\n"
  "v_mfma_f32_16x16x32_bf16 v[136:139], a[172:175], a[60:63], v[136:139]\n"
  "v_exp_f32 v131, v131\n"
  "v_mfma_f32_16x16x32_bf16 v[140:143], a[188:191], a[60:63], v[164:167]\n"
  "v_exp_f32 v132, v132\n"
  "v_mfma_f32_16x16x32_bf16 v[140:143], a[184:187], a[56:59], v[140:143]\n"
  "ds_read_b128 a[160:163], v216 offset:32768\n"
  "v_cvt_pk_bf16_f32 v189, v130, v131\n"
  "v_mfma_f32_16x16x32_bf16 v[140:143], a[180:183], a[52:55], v[140:143]\n"
  "v_exp_f32 v133, v133\n"
  "v_mfma_f32_16x16x32_bf16 v[140:143], a[176:179], a[48:51], v[140:143]\n"
  "ds_read_b128 a[164:167], v216 offset:33792\n"
  "v_cvt_pk_bf16_f32 v190, v132, v133\n"
  "v_mfma_f32_16x16x32_bf16 v[64:67], a[64:67], v[144:147], v[64:67]\n"
  "v_exp_f32 v134, v134\n"
  "v_mfma_f32_16x16x32_bf16 v[64:67], a[96:99], v[148:151], v[64:67]\n"
  "ds_read_b128 a[168:171], v216 offset:34816\n"
  "ds_read_b128 a[172:175], v216 offset:35840\n"
  "v_mfma_f32_16x16x32_bf16 v[68:71], a[100:103], v[148:151], v[68:71]\n"
  "v_exp_f32 v135, v135\n"
  "v_mfma_f32_16x16x32_bf16 v[68:71], a[68:71], v[144:147], v[68:71]\n"
  "v_exp_f32 v136, v136\n"
  "v_mfma_f32_16x16x32_bf16 v[72:75], a[72:75], v[144:147], v[72:75]\n"
  "ds_read_b128 a[188:191], v216 offset:39936\n"
  "v_cvt_pk_bf16_f32 v191, v134, v135\n"
  "v_mfma_f32_16x16x32_bf16 v[72:75], a[104:107], v[148:151], v[72:75]\n"
  "v_exp_f32 v137, v137\n"
  "v_mfma_f32_16x16x32_bf16 v[76:79], a[108:111], v[148:151], v[76:79]\n"
  "ds_read_b128 a[184:187], v216 offset:38912\n"
  "v_cvt_pk_bf16_f32 v192, v136, v137\n"
  "v_mfma_f32_16x16x32_bf16 v[76:79], a[76:79], v[144:147], v[76:79]\n"
  "v_exp_f32 v138, v138\n"
  "v_mfma_f32_16x16x32_bf16 v[80:83], a[80:83], v[144:147], v[80:83]\n"
  "ds_read_b128 a[180:183], v216 offset:37888\n"
  "ds_read_b128 a[176:179], v216 offset:36864\n"
  "v_mfma_f32_16x16x32_bf16 v[80:83], a[112:115], v[148:151], v[80:83]\n"
  "v_exp_f32 v139, v139\n"
  "v_mfma_f32_16x16x32_bf16 v[84:87], a[116:119], v[148:151], v[84:87]\n"
  "v_exp_f32 v140, v140\n"
  "v_mfma_f32_16x16x32_bf16 v[84:87], a[84:87], v[144:147], v[84:87]\n"
  "s_add_u32 s52, s52, 32768\n"
  "s_addc_u32 s53, s53, 0\n"
  "v_cvt_pk_bf16_f32 v193, v138, v139\n"
  "v_mfma_f32_16x16x32_bf16 v[88:91], a[88:91], v[144:147], v[88:91]\n"
  "s_add_u32 m0, s48, 32768\n"
  "v_exp_f32 v141, v141\n"
  "v_mfma_f32_16x16x32_bf16 v[88:91], a[120:123], v[148:151], v[88:91]\n"
  "global_load_lds_dwordx4 v232, s[50:51] sc1\n"
  "v_cvt_pk_bf16_f32 v194, v140, v141\n"
  "v_mfma_f32_16x16x32_bf16 v[92:95], a[124:127], v[148:151], v[92:95]\n"
  "v_exp_f32 v142, v142\n"
  "v_mfma_f32_16x16x32_bf16 v[92:95], a[92:95], v[144:147], v[92:95]\n"
  "global_load_lds_dwordx4 v228, s[50:51] offset:1024 sc1\n"
  "global_load_lds_dwordx4 v229, s[52:53] offset:2048\n"
  "v_mfma_f32_16x16x32_bf16 v[208:211], a[192:195], v[144:147], v[208:211]\n"
  "v_exp_f32 v143, v143\n"
  "v_mfma_f32_16x16x32_bf16 v[208:211], a[192:195], v[148:151], v[208:211]\n"
  "s_waitcnt lgkmcnt(0)\n"
  "global_load_lds_dwordx4 v230, s[52:53] offset:3072\n"
  "v_cvt_pk_bf16_f32 v195, v142, v143\n"
  "v_mfma_f32_16x16x32_bf16 v[128:131], a[128:131], a[0:3], v[152:155]\n"
  "s_add_u32 s50, s50, 1024\n"
  "s_addc_u32 s51, s51, 0\n"
  "v_mfma_f32_16x16x32_bf16 v[128:131], a[132:135], a[4:7], v[128:131]\n"
  "s_add_u32 s52, s52, 32768\n"
  "s_addc_u32 s53, s53, 0\n"
  "global_load_dword v231, v196, s[26:27]\n"
  "v_mfma_f32_16x16x32_bf16 v[128:131], a[136:139], a[8:11], v[128:131]\n"
  "global_store_dwordx4 v232, v[184:187], s[28:29] offset:-2048 sc1\n"
  "v_mfma_f32_16x16x32_bf16 v[128:131], a[140:143], a[12:15], v[128:131]\n"
  "s_nop 1\n"
  "v_mfma_f32_16x16x32_bf16 v[132:135], a[156:159], a[12:15], v[152:155]\n"
  "s_add_u32 m0, s48, 49152\n"
  "s_nop 1\n"
  "v_mfma_f32_16x16x32_bf16 v[132:135], a[152:155], a[8:11], v[132:135]\n"
  "s_nop 1\n"
  "v_mfma_f32_16x16x32_bf16 v[132:135], a[148:151], a[4:7], v[132:135]\n"
  "v_exp_f32 v128, v128\n"
  "v_mfma_f32_16x16x32_bf16 v[132:135], a[144:147], a[0:3], v[132:135]\n"
  "v_exp_f32 v129, v129\n"
  "v_mfma_f32_16x16x32_bf16 v[136:139], a[160:163], a[0:3], v[152:155]\n"
  "global_load_lds_dwordx4 v232, s[50:51] sc1\n"
  "v_cvt_pk_bf16_f32 v144, v128, v129\n"
  "v_mfma_f32_16x16x32_bf16 v[136:139], a[164:167], a[4:7], v[136:139]\n"
  "v_exp_f32 v130, v130\n"
  "v_mfma_f32_16x16x32_bf16 v[136:139], a[168:171], a[8:11], v[136:139]\n"
  "v_mfma_f32_16x16x32_bf16 v[136:139], a[172:175], a[12:15], v[136:139]\n"
  "v_exp_f32 v131, v131\n"
  "v_mfma_f32_16x16x32_bf16 v[140:143], a[188:191], a[12:15], v[152:155]\n"
  "v_exp_f32 v132, v132\n"
  "v_mfma_f32_16x16x32_bf16 v[140:143], a[184:187], a[8:11], v[140:143]\n"
  "global_load_lds_dwordx4 v228, s[50:51] offset:1024 sc1\n"
  "v_cvt_pk_bf16_f32 v145, v130, v131\n"
  "v_mfma_f32_16x16x32_bf16 v[140:143], a[180:183], a[4:7], v[140:143]\n"
  "v_exp_f32 v133, v133\n"
  "v_mfma_f32_16x16x32_bf16 v[140:143], a[176:179], a[0:3], v[140:143]\n"
  "global_load_lds_dwordx4 v229, s[52:53] offset:2048\n"
  "v_cvt_pk_bf16_f32 v146, v132, v133\n"
  "v_mfma_f32_16x16x32_bf16 v[96:99], a[64:67], v[188:191], v[96:99]\n"
  "v_exp_f32 v134, v134\n"
  "v_mfma_f32_16x16x32_bf16 v[96:99], a[96:99], v[192:195], v[96:99]\n"
  "ds_read_b128 a[64:67], v216 offset:8192\n"
  "v_mfma_f32_16x16x32_bf16 v[100:103], a[100:103], v[192:195], v[100:103]\n"
  "v_exp_f32 v135, v135\n"
  "v_mfma_f32_16x16x32_bf16 v[100:103], a[68:71], v[188:191], v[100:103]\n"
  "v_exp_f32 v136, v136\n"
  "v_mfma_f32_16x16x32_bf16 v[104:107], a[72:75], v[188:191], v[104:107]\n"
  "ds_read_b128 a[96:99], v216 offset:40960\n"
  "v_cvt_pk_bf16_f32 v147, v134, v135\n"
  "v_mfma_f32_16x16x32_bf16 v[104:107], a[104:107], v[192:195], v[104:107]\n"
  "v_exp_f32 v137, v137\n"
  "v_mfma_f32_16x16x32_bf16 v[108:111], a[108:111], v[192:195], v[108:111]\n"
  "ds_read_b128 a[100:103], v216 offset:41984\n"
  "v_cvt_pk_bf16_f32 v148, v136, v137\n"
  "v_mfma_f32_16x16x32_bf16 v[108:111], a[76:79], v[188:191], v[108:111]\n"
  "v_exp_f32 v138, v138\n"
  "v_mfma_f32_16x16x32_bf16 v[112:115], a[80:83], v[188:191], v[112:115]\n"
  "ds_read_b128 a[68:71], v216 offset:9216\n"
  "ds_read_b128 a[72:75], v216 offset:10240\n"
  "v_mfma_f32_16x16x32_bf16 v[112:115], a[112:115], v[192:195], v[112:115]\n"
  "v_exp_f32 v139, v139\n"
  "v_mfma_f32_16x16x32_bf16 v[116:119], a[116:119], v[192:195], v[116:119]\n"
  "v_exp_f32 v140, v140\n"
  "v_mfma_f32_16x16x32_bf16 v[116:119], a[84:87], v[188:191], v[116:119]\n"
  "ds_read_b128 a[104:107], v216 offset:43008\n"
  "v_cvt_pk_bf16_f32 v149, v138, v139\n"
  "v_mfma_f32_16x16x32_bf16 v[120:123], a[88:91], v[188:191], v[120:123]\n"
  "v_exp_f32 v141, v141\n"
  "v_mfma_f32_16x16x32_bf16 v[120:123], a[120:123], v[192:195], v[120:123]\n"
  "ds_read_b128 a[108:111], v216 offset:44032\n"
  "v_cvt_pk_bf16_f32 v150, v140, v141\n"
  "v_mfma_f32_16x16x32_bf16 v[124:127], a[124:127], v[192:195], v[124:127]\n"
  "v_exp_f32 v142, v142\n"
  "v_mfma_f32_16x16x32_bf16 v[124:127], a[92:95], v[188:191], v[124:127]\n"
  "ds_read_b128 a[76:79], v216 offset:11264\n"
  "global_load_lds_dwordx4 v230, s[52:53] offset:3072\n"
  "v_mfma_f32_16x16x32_bf16 v[212:215], a[192:195], v[188:191], v[212:215]\n"
  "v_exp_f32 v143, v143\n"
  "v_mfma_f32_16x16x32_bf16 v[212:215], a[192:195], v[192:195], v[212:215]\n"
  "s_add_u32 s50, s50, 1024\n"
  "s_addc_u32 s51, s51, 0\n"
  "s_add_u32 s52, s52, 32768\n"
  "s_addc_u32 s53, s53, 0\n"
  "s_xor_b32 s49, s49, 0x10000\n"
  "v_cvt_pk_bf16_f32 v151, v142, v143\n"
  "s_setpc_b64 s[22:23]\n"
  "L_AFD_%=:\n"
  "v_mfma_f32_16x16x32_bf16 v[128:131], a[128:131], a[16:19], v[156:159]\n"
  "ds_read_b128 a[80:83], v216 offset:12288\n"
  "ds_read_b128 a[112:115], v216 offset:45056\n"
  "v_mfma_f32_16x16x32_bf16 v[128:131], a[132:135], a[20:23], v[128:131]\n"
  "ds_read_b128 a[116:119], v216 offset:46080\n"
  "ds_read_b128 a[84:87], v216 offset:13312\n"
  "v_mfma_f32_16x16x32_bf16 v[128:131], a[136:139], a[24:27], v[128:131]\n"
  "ds_read_b128 a[88:91], v216 offset:14336\n"
  "ds_read_b128 a[120:123], v216 offset:47104\n"
  "v_mfma_f32_16x16x32_bf16 v[128:131], a[140:143], a[28:31], v[128:131]\n"
  "ds_read_b128 a[124:127], v216 offset:48128\n"
  "ds_read_b128 a[92:95], v216 offset:15360\n"
  "v_mfma_f32_16x16x32_bf16 v[132:135], a[156:159], a[28:31], v[156:159]\n"
  "s_nop 1\n"
  "v_mfma_f32_16x16x32_bf16 v[132:135], a[152:155], a[24:27], v[132:135]\n"
  "s_nop 1\n"
  "v_xor_b32 v216, 0x10000, v216\n"
  "v_mfma_f32_16x16x32_bf16 v[132:135], a[148:151], a[20:23], v[132:135]\n"
  "s_add_u32 s24, s24, 1\n"
  "v_exp_f32 v128, v128\n"
  "v_mfma_f32_16x16x32_bf16 v[132:135], a[144:147], a[16:19], v[132:135]\n"
  "v_exp_f32 v129, v129\n"
  "v_mfma_f32_16x16x32_bf16 v[136:139], a[160:163], a[16:19], v[156:159]\n"
  "v_cvt_pk_bf16_f32 v188, v128, v129\n"
  "s_waitcnt vmcnt(5)\n"
  "v_accvgpr_read_b32 v234, a244\n"
  "v_mfma_f32_16x16x32_bf16 v[136:139], a[164:167], a[20:23], v[136:139]\n"
  "v_exp_f32 v130, v130\n"
  "v_mfma_f32_16x16x32_bf16 v[136:139], a[168:171], a[24:27], v[136:139]\n"
  "v_accvgpr_read_b32 v235, a245\n"
  "v_readlane_b32 s22, v252, s24\n"
  "v_mfma_f32_16x16x32_bf16 v[136:139], a[172:175], a[28:31], v[136:139]\n"
  "v_exp_f32 v131, v131\n"
  "v_mfma_f32_16x16x32_bf16 v[140:143], a[188:191], a[28:31], v[156:159]\n"
  "v_exp_f32 v132, v132\n"
  "v_mfma_f32_16x16x32_bf16 v[140:143], a[184:187], a[24:27], v[140:143]\n"
  "s_waitcnt lgkmcnt(0)\n"
  "s_barrier\n"
  "v_cvt_pk_bf16_f32 v189, v130, v131\n"
  "v_mfma_f32_16x16x32_bf16 v[140:143], a[180:183], a[20:23], v[140:143]\n"
  "v_exp_f32 v133, v133\n"
  "v_mfma_f32_16x16x32_bf16 v[140:143], a[176:179], a[16:19], v[140:143]\n"
  "v_cvt_pk_bf16_f32 v190, v132, v133\n"
  "v_cmp_ne_u64 s[96:97], s[16:17], v[234:235]\n"
  "v_mfma_f32_16x16x32_bf16 v[0:3], a[64:67], v[144:147], v[0:3]\n"
  "v_exp_f32 v134, v134\n"
  "v_mfma_f32_16x16x32_bf16 v[0:3], a[96:99], v[148:151], v[0:3]\n"
  "s_add_u32 s22, s26, s22\n"
  "s_addc_u32 s23, s27, 0\n"
  "v_mfma_f32_16x16x32_bf16 v[4:7], a[100:103], v[148:151], v[4:7]\n"
  "v_exp_f32 v135, v135\n"
  "v_mfma_f32_16x16x32_bf16 v[4:7], a[68:71], v[144:147], v[4:7]\n"
  "v_exp_f32 v136, v136\n"
  "v_mfma_f32_16x16x32_bf16 v[8:11], a[72:75], v[144:147], v[8:11]\n"
  "v_cvt_pk_bf16_f32 v191, v134, v135\n"
  "v_mfma_f32_16x16x32_bf16 v[8:11], a[104:107], v[148:151], v[8:11]\n"
  "v_exp_f32 v137, v137\n"
  "v_mfma_f32_16x16x32_bf16 v[12:15], a[108:111], v[148:151], v[12:15]\n"
  "v_cvt_pk_bf16_f32 v192, v136, v137\n"
  "v_mfma_f32_16x16x32_bf16 v[12:15], a[76:79], v[144:147], v[12:15]\n"
  "v_exp_f32 v138, v138\n"
  "v_mfma_f32_16x16x32_bf16 v[16:19], a[80:83], v[144:147], v[16:19]\n"
  "v_mfma_f32_16x16x32_bf16 v[16:19], a[112:115], v[148:151], v[16:19]\n"
  "v_exp_f32 v139, v139\n"
  "v_mfma_f32_16x16x32_bf16 v[20:23], a[116:119], v[148:151], v[20:23]\n"
  "v_exp_f32 v140, v140\n"
  "v_mfma_f32_16x16x32_bf16 v[20:23], a[84:87], v[144:147], v[20:23]\n"
  "v_cvt_pk_bf16_f32 v193, v138, v139\n"
  "v_mfma_f32_16x16x32_bf16 v[24:27], a[88:91], v[144:147], v[24:27]\n"
  "v_exp_f32 v141, v141\n"
  "v_mfma_f32_16x16x32_bf16 v[24:27], a[120:123], v[148:151], v[24:27]\n"
  "v_cvt_pk_bf16_f32 v194, v140, v141\n"
  "v_mfma_f32_16x16x32_bf16 v[28:31], a[124:127], v[148:151], v[28:31]\n"
  "v_exp_f32 v142, v142\n"
  "v_mfma_f32_16x16x32_bf16 v[28:31], a[92:95], v[144:147], v[28:31]\n"
  "v_mfma_f32_16x16x32_bf16 v[200:203], a[192:195], v[144:147], v[200:203]\n"
  "v_exp_f32 v143, v143\n"
  "v_mfma_f32_16x16x32_bf16 v[200:203], a[192:195], v[148:151], v[200:203]\n"
  "v_cvt_pk_bf16_f32 v195, v142, v143\n"
  "s_cmp_lg_u64 s[96:97], 0\n"
  "s_cbranch_scc1 L_hAFD_retry_%=\n"
  "L_hAFD_ok_%=:\n"
  "v_mfma_f32_16x16x32_bf16 v[128:131], a[128:131], a[32:35], v[160:163]\n"
  "s_add_u32 m0, s48, 0\n"
  "v_mfma_f32_16x16x32_bf16 v[128:131], a[132:135], a[36:39], v[128:131]\n"
  "v_mfma_f32_16x16x32_bf16 v[128:131], a[136:139], a[40:43], v[128:131]\n"
  "global_load_lds_dwordx4 v232, s[50:51] sc1\n"
  "v_mfma_f32_16x16x32_bf16 v[128:131], a[140:143], a[44:47], v[128:131]\n"
  "s_nop 1\n"
  "v_mfma_f32_16x16x32_bf16 v[132:135], a[156:159], a[44:47], v[160:163]\n"
  "s_nop 1\n"
  "v_mfma_f32_16x16x32_bf16 v[132:135], a[152:155], a[40:43], v[132:135]\n"
  "s_nop 1\n"
  "global_load_lds_dwordx4 v228, s[50:51] offset:1024 sc1\n"
  "v_mfma_f32_16x16x32_bf16 v[132:135], a[148:151], a[36:39], v[132:135]\n"
  "v_exp_f32 v128, v128\n"
  "v_mfma_f32_16x16x32_bf16 v[132:135], a[144:147], a[32:35], v[132:135]\n"
  "v_exp_f32 v129, v129\n"
  "v_mfma_f32_16x16x32_bf16 v[136:139], a[160:163], a[32:35], v[160:163]\n"
  "global_load_lds_dwordx4 v229, s[52:53] offset:2048\n"
  "v_cvt_pk_bf16_f32 v144, v128, v129\n"
  "v_mfma_f32_16x16x32_bf16 v[136:139], a[164:167], a[36:39], v[136:139]\n"
  "v_exp_f32 v130, v130\n"
  "v_mfma_f32_16x16x32_bf16 v[136:139], a[168:171], a[40:43], v[136:139]\n"
  "v_mfma_f32_16x16x32_bf16 v[136:139], a[172:175], a[44:47], v[136:139]\n"
  "v_exp_f32 v131, v131\n"
  "v_mfma_f32_16x16x32_bf16 v[140:143], a[188:191], a[44:47], v[160:163]\n"
  "v_exp_f32 v132, v132\n"
  "v_mfma_f32_16x16x32_bf16 v[140:143], a[184:187], a[40:43], v[140:143]\n"
  "global_load_lds_dwordx4 v230, s[52:53] offset:3072\n"
  "v_cvt_pk_bf16_f32 v145, v130, v131\n"
  "v_mfma_f32_16x16x32_bf16 v[140:143], a[180:183], a[36:39], v[140:143]\n"
  "v_exp_f32 v133, v133\n"
  "v_mfma_f32_16x16x32_bf16 v[140:143], a[176:179], a[32:35], v[140:143]\n"
  "s_add_u32 s50, s50, 1024\n"
  "s_addc_u32 s51, s51, 0\n"
  "v_cvt_pk_bf16_f32 v146, v132, v133\n"
  "v_mfma_f32_16x16x32_bf16 v[32:35], a[64:67], v[188:191], v[32:35]\n"
  "v_exp_f32 v134, v134\n"
  "v_mfma_f32_16x16x32_bf16 v[32:35], a[96:99], v[192:195], v[32:35]\n"
  "s_add_u32 s52, s52, 32768\n"
  "s_addc_u32 s53, s53, 0\n"
  "v_mfma_f32_16x16x32_bf16 v[36:39], a[100:103], v[192:195], v[36:39]\n"
  "v_exp_f32 v135, v135\n"
  "v_mfma_f32_16x16x32_bf16 v[36:39], a[68:71], v[188:191], v[36:39]\n"
  "v_exp_f32 v136, v136\n"
  "v_mfma_f32_16x16x32_bf16 v[40:43], a[72:75], v[188:191], v[40:43]\n"
  "s_add_u32 m0, s48, 16384\n"
  "v_cvt_pk_bf16_f32 v147, v134, v135\n"
  "v_mfma_f32_16x16x32_bf16 v[40:43], a[104:107], v[192:195], v[40:43]\n"
  "v_exp_f32 v137, v137\n"
  "v_mfma_f32_16x16x32_bf16 v[44:47], a[108:111], v[192:195], v[44:47]\n"
  "v_cvt_pk_bf16_f32 v148, v136, v137\n"
  "v_mfma_f32_16x16x32_bf16 v[44:47], a[76:79], v[188:191], v[44:47]\n"
  "v_exp_f32 v138, v138\n"
  "v_mfma_f32_16x16x32_bf16 v[48:51], a[80:83], v[188:191], v[48:51]\n"
  "global_load_lds_dwordx4 v232, s[50:51] sc1\n"
  "v_mfma_f32_16x16x32_bf16 v[48:51], a[112:115], v[192:195], v[48:51]\n"
  "v_exp_f32 v139, v139\n"
  "v_mfma_f32_16x16x32_bf16 v[52:55], a[116:119], v[192:195], v[52:55]\n"
  "v_exp_f32 v140, v140\n"
  "v_mfma_f32_16x16x32_bf16 v[52:55], a[84:87], v[188:191], v[52:55]\n"
  "global_load_lds_dwordx4 v228, s[50:51] offset:1024 sc1\n"
  "v_cvt_pk_bf16_f32 v149, v138, v139\n"
  "v_mfma_f32_16x16x32_bf16 v[56:59], a[88:91], v[188:191], v[56:59]\n"
  "v_exp_f32 v141, v141\n"
  "v_mfma_f32_16x16x32_bf16 v[56:59], a[120:123], v[192:195], v[56:59]\n"
  "global_load_lds_dwordx4 v229, s[52:53] offset:2048\n"
  "v_cvt_pk_bf16_f32 v150, v140, v141\n"
  "v_mfma_f32_16x16x32_bf16 v[60:63], a[124:127], v[192:195], v[60:63]\n"
  "v_exp_f32 v142, v142\n"
  "v_mfma_f32_16x16x32_bf16 v[60:63], a[92:95], v[188:191], v[60:63]\n"
  "global_load_lds_dwordx4 v230, s[52:53] offset:3072\n"
  "v_mfma_f32_16x16x32_bf16 v[204:207], a[192:195], v[188:191], v[204:207]\n"
  "v_exp_f32 v143, v143\n"
  "v_mfma_f32_16x16x32_bf16 v[204:207], a[192:195], v[192:195], v[204:207]\n"
  "s_waitcnt vmcnt(8)\n"
  "s_barrier\n"
  "v_cvt_pk_bf16_f32 v151, v142, v143\n"
  "v_mfma_f32_16x16x32_bf16 v[128:131], a[128:131], a[48:51], v[164:167]\n"
  "s_add_u32 s50, s50, 1024\n"
  "s_addc_u32 s51, s51, 0\n"
  "v_mfma_f32_16x16x32_bf16 v[128:131], a[132:135], a[52:55], v[128:131]\n"
  "ds_read_b128 a[128:131], v216 offset:0\n"
  "v_mfma_f32_16x16x32_bf16 v[128:131], a[136:139], a[56:59], v[128:131]\n"
  "ds_read_b128 a[132:135], v216 offset:1024\n"
  "v_mfma_f32_16x16x32_bf16 v[128:131], a[140:143], a[60:63], v[128:131]\n"
  "s_add_u32 s52, s52, 32768\n"
  "s_addc_u32 s53, s53, 0\n"
  "ds_read_b128 a[136:139], v216 offset:2048\n"
  "v_mfma_f32_16x16x32_bf16 v[132:135], a[156:159], a[60:63], v[164:167]\n"
  "s_nop 1\n"
  "ds_read_b128 a[140:143], v216 offset:3072\n"
  "v_mfma_f32_16x16x32_bf16 v[132:135], a[152:155], a[56:59], v[132:135]\n"
  "s_nop 1\n"
  "ds_read_b128 a[156:159], v216 offset:7168\n"
  "v_mfma_f32_16x16x32_bf16 v[132:135], a[148:151], a[52:55], v[132:135]\n"
  "s_add_u32 m0, s48, 32768\n"
  "v_exp_f32 v128, v128\n"
  "v_mfma_f32_16x16x32_bf16 v[132:135], a[144:147], a[48:51], v[132:135]\n"
  "v_exp_f32 v129, v129\n"
  "v_mfma_f32_16x16x32_bf16 v[136:139], a[160:163], a[48:51], v[164:167]\n"
  "ds_read_b128 a[152:155], v216 offset:6144\n"
  "v_cvt_pk_bf16_f32 v188, v128, v129\n"
  "v_mfma_f32_16x16x32_bf16 v[136:139], a[164:167], a[52:55], v[136:139]\n"
  "v_exp_f32 v130, v130\n"
  "v_mfma_f32_16x16x32_bf16 v[136:139], a[168:171], a[56:59], v[136:139]\n"
  "ds_read_b128 a[148:151], v216 offset:5120\n"
  "ds_read_b128 a[144:147], v216 offset:4096\n"
  "v_mfma_f32_16x16x32_bf16 v[136:139], a[172:175], a[60:63], v[136:139]\n"
  "v_exp_f32 v131, v131\n"
  "v_mfma_f32_16x16x32_bf16 v[140:143], a[188:191], a[60:63], v[164:167]\n"
  "v_exp_f32 v132, v132\n"
  "v_mfma_f32_16x16x32_bf16 v[140:143], a[184:187], a[56:59], v[140:143]\n"
  "ds_read_b128 a[160:163], v216 offset:32768\n"
  "v_cvt_pk_bf16_f32 v189, v130, v131\n"
  "v_mfma_f32_16x16x32_bf16 v[140:143], a[180:183], a[52:55], v[140:143]\n"
  "v_exp_f32 v133, v133\n"
  "v_mfma_f32_16x16x32_bf16 v[140:143], a[176:179], a[48:51], v[140:143]\n"
  "ds_read_b128 a[164:167], v216 offset:33792\n"
  "v_cvt_pk_bf16_f32 v190, v132, v133\n"
  "v_mfma_f32_16x16x32_bf16 v[64:67], a[64:67], v[144:147], v[64:67]\n"
  "v_exp_f32 v134, v134\n"
  "v_mfma_f32_16x16x32_bf16 v[64:67], a[96:99], v[148:151], v[64:67]\n"
  "ds_read_b128 a[168:171], v216 offset:34816\n"
  "ds_read_b128 a[172:175], v216 offset:35840\n"
  "v_mfma_f32_16x16x32_bf16 v[68:71], a[100:103], v[148:151], v[68:71]\n"
  "v_exp_f32 v135, v135\n"
  "v_mfma_f32_16x16x32_bf16 v[68:71], a[68:71], v[144:147], v[68:71]\n"
  "v_exp_f32 v136, v136\n"
  "v_mfma_f32_16x16x32_bf16 v[72:75], a[72:75], v[144:147], v[72:75]\n"
  "ds_read_b128 a[188:191], v216 offset:39936\n"
  "v_cvt_pk_bf16_f32 v191, v134, v135\n"
  "v_mfma_f32_16x16x32_bf16 v[72:75], a[104:107], v[148:151], v[72:75]\n"
  "v_exp_f32 v137, v137\n"
  "v_mfma_f32_16x16x32_bf16 v[76:79], a[108:111], v[148:151], v[76:79]\n"
  "ds_read_b128 a[184:187], v216 offset:38912\n"
  "v_cvt_pk_bf16_f32 v192, v136, v137\n"
  "v_mfma_f32_16x16x32_bf16 v[76:79], a[76:79], v[144:147], v[76:79]\n"
  "v_exp_f32 v138, v138\n"
  "v_mfma_f32_16x16x32_bf16 v[80:83], a[80:83], v[144:147], v[80:83]\n"
  "ds_read_b128 a[180:183], v216 offset:37888\n"
  "ds_read_b128 a[176:179], v216 offset:36864\n"
  "v_mfma_f32_16x16x32_bf16 v[80:83], a[112:115], v[148:151], v[80:83]\n"
  "v_exp_f32 v139, v139\n"
  "v_mfma_f32_16x16x32_bf16 v[84:87], a[116:119], v[148:151], v[84:87]\n"
  "v_exp_f32 v140, v140\n"
  "v_mfma_f32_16x16x32_bf16 v[84:87], a[84:87], v[144:147], v[84:87]\n"
  "global_load_lds_dwordx4 v232, s[50:51] sc1\n"
  "v_cvt_pk_bf16_f32 v193, v138, v139\n"
  "v_mfma_f32_16x16x32_bf16 v[88:91], a[88:91], v[144:147], v[88:91]\n"
  "v_exp_f32 v141, v141\n"
  "v_mfma_f32_16x16x32_bf16 v[88:91], a[120:123], v[148:151], v[88:91]\n"
  "global_load_lds_dwordx4 v228, s[50:51] offset:1024 sc1\n"
  "v_cvt_pk_bf16_f32 v194, v140, v141\n"
  "v_mfma_f32_16x16x32_bf16 v[92:95], a[124:127], v[148:151], v[92:95]\n"
  "v_exp_f32 v142, v142\n"
  "v_mfma_f32_16x16x32_bf16 v[92:95], a[92:95], v[144:147], v[92:95]\n"
  "global_load_lds_dwordx4 v229, s[52:53] offset:2048\n"
  "global_load_lds_dwordx4 v230, s[52:53] offset:3072\n"
  "v_mfma_f32_16x16x32_bf16 v[208:211], a[192:195], v[144:147], v[208:211]\n"
  "v_exp_f32 v143, v143\n"
  "v_mfma_f32_16x16x32_bf16 v[208:211], a[192:195], v[148:151], v[208:211]\n"
  "s_waitcnt lgkmcnt(0)\n"
  "v_cvt_pk_bf16_f32 v195, v142, v143\n"
  "v_mfma_f32_16x16x32_bf16 v[128:131], a[128:131], a[0:3], v[168:171]\n"
  "s_add_u32 s50, s50, 1024\n"
  "s_addc_u32 s51, s51, 0\n"
  "v_mfma_f32_16x16x32_bf16 v[128:131], a[132:135], a[4:7], v[128:131]\n"
  "s_add_u32 s52, s52, 32768\n"
  "s_addc_u32 s53, s53, 0\n"
  "global_store_dwordx4 v232, v[184:187], s[28:29] offset:-2048 sc1\n"
  "v_mfma_f32_16x16x32_bf16 v[128:131], a[136:139], a[8:11], v[128:131]\n"
  "s_add_u32 m0, s48, 49152\n"
  "s_nop 0\n"
  "global_load_lds_dwordx4 v232, s[50:51] sc1\n"
  "global_load_lds_dwordx4 v228, s[50:51] offset:1024 sc1\n"
  "v_mfma_f32_16x16x32_bf16 v[128:131], a[140:143], a[12:15], v[128:131]\n"
  "s_nop 1\n"
  "v_mfma_f32_16x16x32_bf16 v[96:99], a[64:67], v[188:191], v[96:99]\n"
  "s_nop 1\n"
  "global_load_lds_dwordx4 v229, s[52:53] offset:2048\n"
  "v_mfma_f32_16x16x32_bf16 v[96:99], a[96:99], v[192:195], v[96:99]\n"
  "s_nop 1\n"
  "ds_read_b128 a[64:67], v216 offset:8192\n"
  "v_mfma_f32_16x16x32_bf16 v[100:103], a[100:103], v[192:195], v[100:103]\n"
  "v_exp_f32 v128, v128\n"
  "v_mfma_f32_16x16x32_bf16 v[100:103], a[68:71], v[188:191], v[100:103]\n"
  "ds_read_b128 a[96:99], v216 offset:40960\n"
  "ds_read_b128 a[100:103], v216 offset:41984\n"
  "v_mfma_f32_16x16x32_bf16 v[104:107], a[72:75], v[188:191], v[104:107]\n"
  "ds_read_b128 a[68:71], v216 offset:9216\n"
  "global_load_lds_dwordx4 v230, s[52:53] offset:3072\n"
  "v_mfma_f32_16x16x32_bf16 v[104:107], a[104:107], v[192:195], v[104:107]\n"
  "ds_read_b128 a[72:75], v216 offset:10240\n"
  "v_mfma_f32_16x16x32_bf16 v[108:111], a[108:111], v[192:195], v[108:111]\n"
  "v_exp_f32 v129, v129\n"
  "v_mfma_f32_16x16x32_bf16 v[108:111], a[76:79], v[188:191], v[108:111]\n"
  "ds_read_b128 a[104:107], v216 offset:43008\n"
  "v_cvt_pk_bf16_f32 v144, v128, v129\n"
  "v_mfma_f32_16x16x32_bf16 v[112:115], a[80:83], v[188:191], v[112:115]\n"
  "ds_read_b128 a[108:111], v216 offset:44032\n"
  "ds_read_b128 a[76:79], v216 offset:11264\n"
  "v_mfma_f32_16x16x32_bf16 v[112:115], a[112:115], v[192:195], v[112:115]\n"
  "s_add_u32 s50, s50, 1024\n"
  "s_addc_u32 s51, s51, 0\n"
  "v_mfma_f32_16x16x32_bf16 v[116:119], a[116:119], v[192:195], v[116:119]\n"
  "s_add_u32 s52, s52, 32768\n"
  "s_addc_u32 s53, s53, 0\n"
  "v_mfma_f32_16x16x32_bf16 v[116:119], a[84:87], v[188:191], v[116:119]\n"
  "v_exp_f32 v130, v130\n"
  "v_mfma_f32_16x16x32_bf16 v[120:123], a[88:91], v[188:191], v[120:123]\n"
  "v_mfma_f32_16x16x32_bf16 v[120:123], a[120:123], v[192:195], v[120:123]\n"
  "v_mfma_f32_16x16x32_bf16 v[124:127], a[124:127], v[192:195], v[124:127]\n"
  "v_mfma_f32_16x16x32_bf16 v[124:127], a[92:95], v[188:191], v[124:127]\n"
  "s_xor_b32 s49, s49, 0x10000\n"
  "v_mfma_f32_16x16x32_bf16 v[212:215], a[192:195], v[188:191], v[212:215]\n"
  "v_exp_f32 v131, v131\n"
  "v_mfma_f32_16x16x32_bf16 v[212:215], a[192:195], v[192:195], v[212:215]\n"
  "v_cvt_pk_bf16_f32 v145, v130, v131\n"
  "v_mov_b32 v146, 0\n"
  "v_mov_b32 v147, 0\n"
  "s_setpc_b64 s[22:23]\n"
  "L_ADX_%=:\n"
  "v_mfma_f32_16x16x32_bf16 v[128:131], a[128:131], a[16:19], v[156:159]\n"
  "ds_read_b128 a[80:83], v216 offset:12288\n"
  "ds_read_b128 a[112:115], v216 offset:45056\n"
  "v_mfma_f32_16x16x32_bf16 v[128:131], a[132:135], a[20:23], v[128:131]\n"
  "ds_read_b128 a[116:119], v216 offset:46080\n"
  "ds_read_b128 a[84:87], v216 offset:13312\n"
  "v_mfma_f32_16x16x32_bf16 v[128:131], a[136:139], a[24:27], v[128:131]\n"
  "ds_read_b128 a[88:91], v216 offset:14336\n"
  "ds_read_b128 a[120:123], v216 offset:47104\n"
  "v_mfma_f32_16x16x32_bf16 v[128:131], a[140:143], a[28:31], v[128:131]\n"
  "ds_read_b128 a[124:127], v216 offset:48128\n"
  "ds_read_b128 a[92:95], v216 offset:15360\n"
  "v_mfma_f32_16x16x32_bf16 v[132:135], a[156:159], a[28:31], v[172:175]\n"
  "s_nop 1\n"
  "v_mfma_f32_16x16x32_bf16 v[132:135], a[152:155], a[24:27], v[132:135]\n"
  "s_nop 1\n"
  "v_xor_b32 v216, 0x10000, v216\n"
  "v_mfma_f32_16x16x32_bf16 v[132:135], a[148:151], a[20:23], v[132:135]\n"
  "s_add_u32 s24, s24, 1\n"
  "v_exp_f32 v128, v128\n"
  "v_mfma_f32_16x16x32_bf16 v[132:135], a[144:147], a[16:19], v[132:135]\n"
  "v_exp_f32 v129, v129\n"
  "s_waitcnt lgkmcnt(15)\n"
  "v_mfma_f32_16x16x32_bf16 v[0:3], a[64:67], v[144:147], v[0:3]\n"
  "v_exp_f32 v130, v130\n"
  "s_waitcnt lgkmcnt(12)\n"
  "v_mfma_f32_16x16x32_bf16 v[4:7], a[68:71], v[144:147], v[4:7]\n"
  "v_exp_f32 v131, v131\n"
  "s_waitcnt lgkmcnt(11)\n"
  "v_mfma_f32_16x16x32_bf16 v[8:11], a[72:75], v[144:147], v[8:11]\n"
  "v_exp_f32 v132, v132\n"
  "s_waitcnt lgkmcnt(8)\n"
  "v_mfma_f32_16x16x32_bf16 v[12:15], a[76:79], v[144:147], v[12:15]\n"
  "s_waitcnt lgkmcnt(0)\n"
  "v_exp_f32 v133, v133\n"
  "v_mfma_f32_16x16x32_bf16 v[16:19], a[80:83], v[144:147], v[16:19]\n"
  "s_barrier\n"
  "v_exp_f32 v134, v134\n"
  "v_mfma_f32_16x16x32_bf16 v[20:23], a[84:87], v[144:147], v[20:23]\n"
  "v_exp_f32 v135, v135\n"
  "v_mfma_f32_16x16x32_bf16 v[24:27], a[88:91], v[144:147], v[24:27]\n"
  "v_cvt_pk_bf16_f32 v188, v128, v129\n"
  "v_cvt_pk_bf16_f32 v189, v130, v131\n"
  "v_mfma_f32_16x16x32_bf16 v[28:31], a[92:95], v[144:147], v[28:31]\n"
  "v_cvt_pk_bf16_f32 v190, v132, v133\n"
  "v_cvt_pk_bf16_f32 v191, v134, v135\n"
  "s_waitcnt vmcnt(0)\n"
  "v_accvgpr_read_b32 v234, a244\n"
  "v_accvgpr_read_b32 v235, a245\n"
  "v_cmp_ne_u64 s[96:97], s[16:17], v[234:235]\n"
  "v_mfma_f32_16x16x32_bf16 v[200:203], a[192:195], v[144:147], v[200:203]\n"
  "v_readlane_b32 s22, v252, s24\n"
  "s_cmp_lg_u64 s[96:97], 0\n"
  "s_cbranch_scc1 L_hADX_retry_%=\n"
  "L_hADX_ok_%=:\n"
  "v_mfma_f32_16x16x32_bf16 v[128:131], a[128:131], a[32:35], v[160:163]\n"
  "s_add_u32 s22, s26, s22\n"
  "s_addc_u32 s23, s27, 0\n"
  "s_add_u32 m0, s48, 0\n"
  "v_mfma_f32_16x16x32_bf16 v[128:131], a[132:135], a[36:39], v[128:131]\n"
  "v_mfma_f32_16x16x32_bf16 v[128:131], a[136:139], a[40:43], v[128:131]\n"
  "global_load_lds_dwordx4 v232, s[50:51] sc1\n"
  "v_mfma_f32_16x16x32_bf16 v[128:131], a[140:143], a[44:47], v[128:131]\n"
  "s_nop 1\n"
  "v_mfma_f32_16x16x32_bf16 v[132:135], a[156:159], a[44:47], v[160:163]\n"
  "s_nop 1\n"
  "global_load_lds_dwordx4 v228, s[50:51] offset:1024 sc1\n"
  "v_mfma_f32_16x16x32_bf16 v[132:135], a[152:155], a[40:43], v[132:135]\n"
  "s_nop 1\n"
  "v_mfma_f32_16x16x32_bf16 v[132:135], a[148:151], a[36:39], v[132:135]\n"
  "v_exp_f32 v128, v128\n"
  "v_mfma_f32_16x16x32_bf16 v[132:135], a[144:147], a[32:35], v[132:135]\n"
  "v_exp_f32 v129, v129\n"
  "v_mfma_f32_16x16x32_bf16 v[136:139], a[160:163], a[32:35], v[176:179]\n"
  "v_exp_f32 v130, v130\n"
  "v_mfma_f32_16x16x32_bf16 v[136:139], a[164:167], a[36:39], v[136:139]\n"
  "v_exp_f32 v131, v131\n"
  "v_mfma_f32_16x16x32_bf16 v[136:139], a[168:171], a[40:43], v[136:139]\n"
  "s_nop 1\n"
  "v_exp_f32 v132, v132\n"
  "v_mfma_f32_16x16x32_bf16 v[136:139], a[172:175], a[44:47], v[136:139]\n"
  "v_exp_f32 v133, v133\n"
  "v_mfma_f32_16x16x32_bf16 v[32:35], a[64:67], v[188:191], v[32:35]\n"
  "v_cvt_pk_bf16_f32 v144, v128, v129\n"
  "v_cvt_pk_bf16_f32 v145, v130, v131\n"
  "v_mfma_f32_16x16x32_bf16 v[36:39], a[68:71], v[188:191], v[36:39]\n"
  "v_exp_f32 v134, v134\n"
  "v_mfma_f32_16x16x32_bf16 v[40:43], a[72:75], v[188:191], v[40:43]\n"
  "v_exp_f32 v135, v135\n"
  "v_mfma_f32_16x16x32_bf16 v[44:47], a[76:79], v[188:191], v[44:47]\n"
  "v_exp_f32 v136, v136\n"
  "v_mfma_f32_16x16x32_bf16 v[48:51], a[80:83], v[188:191], v[48:51]\n"
  "v_exp_f32 v137, v137\n"
  "v_mfma_f32_16x16x32_bf16 v[52:55], a[84:87], v[188:191], v[52:55]\n"
  "v_exp_f32 v138, v138\n"
  "v_mfma_f32_16x16x32_bf16 v[56:59], a[88:91], v[188:191], v[56:59]\n"
  "v_cvt_pk_bf16_f32 v146, v132, v133\n"
  "v_cvt_pk_bf16_f32 v147, v134, v135\n"
  "v_mfma_f32_16x16x32_bf16 v[60:63], a[92:95], v[188:191], v[60:63]\n"
  "v_exp_f32 v139, v139\n"
  "v_mfma_f32_16x16x32_bf16 v[204:207], a[192:195], v[188:191], v[204:207]\n"
  "s_barrier\n"
  "v_cvt_pk_bf16_f32 v148, v136, v137\n"
  "v_cvt_pk_bf16_f32 v149, v138, v139\n"
  "v_mov_b32 v150, 0\n"
  "v_mov_b32 v151, 0\n"
  "v_mfma_f32_16x16x32_bf16 v[128:131], a[128:131], a[48:51], v[164:167]\n"
  "global_load_lds_dwordx4 v229, s[52:53] offset:2048\n"
  "global_load_lds_dwordx4 v230, s[52:53] offset:3072\n"
  "v_mfma_f32_16x16x32_bf16 v[128:131], a[132:135], a[52:55], v[128:131]\n"
  "s_add_u32 s50, s50, 1024\n"
  "s_addc_u32 s51, s51, 0\n"
  "v_mfma_f32_16x16x32_bf16 v[128:131], a[136:139], a[56:59], v[128:131]\n"
  "s_add_u32 s52, s52, 32768\n"
  "s_addc_u32 s53, s53, 0\n"
  "s_add_u32 m0, s48, 16384\n"
  "v_mfma_f32_16x16x32_bf16 v[128:131], a[140:143], a[60:63], v[128:131]\n"
  "global_load_lds_dwordx4 v232, s[50:51] sc1\n"
  "global_load_lds_dwordx4 v228, s[50:51] offset:1024 sc1\n"
  "v_mfma_f32_16x16x32_bf16 v[132:135], a[156:159], a[60:63], v[164:167]\n"
  "global_load_lds_dwordx4 v229, s[52:53] offset:2048\n"
  "global_load_lds_dwordx4 v230, s[52:53] offset:3072\n"
  "v_mfma_f32_16x16x32_bf16 v[132:135], a[152:155], a[56:59], v[132:135]\n"
  "s_nop 1\n"
  "v_mfma_f32_16x16x32_bf16 v[132:135], a[148:151], a[52:55], v[132:135]\n"
  "v_exp_f32 v128, v128\n"
  "v_mfma_f32_16x16x32_bf16 v[132:135], a[144:147], a[48:51], v[132:135]\n"
  "v_exp_f32 v129, v129\n"
  "v_mfma_f32_16x16x32_bf16 v[136:139], a[160:163], a[48:51], v[164:167]\n"
  "s_add_u32 s50, s50, 1024\n"
  "s_addc_u32 s51, s51, 0\n"
  "v_cvt_pk_bf16_f32 v188, v128, v129\n"
  "v_mfma_f32_16x16x32_bf16 v[136:139], a[164:167], a[52:55], v[136:139]\n"
  "v_exp_f32 v130, v130\n"
  "v_mfma_f32_16x16x32_bf16 v[136:139], a[168:171], a[56:59], v[136:139]\n"
  "s_add_u32 s52, s52, 32768\n"
  "s_addc_u32 s53, s53, 0\n"
  "v_mfma_f32_16x16x32_bf16 v[136:139], a[172:175], a[60:63], v[136:139]\n"
  "s_add_u32 m0, s48, 32768\n"
  "v_exp_f32 v131, v131\n"
  "v_mfma_f32_16x16x32_bf16 v[140:143], a[188:191], a[60:63], v[180:183]\n"
  "v_exp_f32 v132, v132\n"
  "v_mfma_f32_16x16x32_bf16 v[140:143], a[184:187], a[56:59], v[140:143]\n"
  "global_load_lds_dwordx4 v232, s[50:51] sc1\n"
  "v_cvt_pk_bf16_f32 v189, v130, v131\n"
  "v_mfma_f32_16x16x32_bf16 v[140:143], a[180:183], a[52:55], v[140:143]\n"
  "v_exp_f32 v133, v133\n"
  "v_mfma_f32_16x16x32_bf16 v[140:143], a[176:179], a[48:51], v[140:143]\n"
  "global_load_lds_dwordx4 v228, s[50:51] offset:1024 sc1\n"
  "v_cvt_pk_bf16_f32 v190, v132, v133\n"
  "v_mfma_f32_16x16x32_bf16 v[64:67], a[64:67], v[144:147], v[64:67]\n"
  "v_exp_f32 v134, v134\n"
  "v_mfma_f32_16x16x32_bf16 v[64:67], a[96:99], v[148:151], v[64:67]\n"
  "v_mfma_f32_16x16x32_bf16 v[68:71], a[100:103], v[148:151], v[68:71]\n"
  "v_exp_f32 v135, v135\n"
  "v_mfma_f32_16x16x32_bf16 v[68:71], a[68:71], v[144:147], v[68:71]\n"
  "v_exp_f32 v136, v136\n"
  "v_mfma_f32_16x16x32_bf16 v[72:75], a[72:75], v[144:147], v[72:75]\n"
  "global_load_lds_dwordx4 v229, s[52:53] offset:2048\n"
  "v_cvt_pk_bf16_f32 v191, v134, v135\n"
  "v_mfma_f32_16x16x32_bf16 v[72:75], a[104:107], v[148:151], v[72:75]\n"
  "v_exp_f32 v137, v137\n"
  "v_mfma_f32_16x16x32_bf16 v[76:79], a[108:111], v[148:151], v[76:79]\n"
  "global_load_lds_dwordx4 v230, s[52:53] offset:3072\n"
  "v_cvt_pk_bf16_f32 v192, v136, v137\n"
  "v_mfma_f32_16x16x32_bf16 v[76:79], a[76:79], v[144:147], v[76:79]\n"
  "v_exp_f32 v138, v138\n"
  "v_mfma_f32_16x16x32_bf16 v[80:83], a[80:83], v[144:147], v[80:83]\n"
  "s_add_u32 s50, s50, 1024\n"
  "s_addc_u32 s51, s51, 0\n"
  "v_mfma_f32_16x16x32_bf16 v[80:83], a[112:115], v[148:151], v[80:83]\n"
  "v_exp_f32 v139, v139\n"
  "v_mfma_f32_16x16x32_bf16 v[84:87], a[116:119], v[148:151], v[84:87]\n"
  "v_exp_f32 v140, v140\n"
  "v_mfma_f32_16x16x32_bf16 v[84:87], a[84:87], v[144:147], v[84:87]\n"
  "s_add_u32 s52, s52, 32768\n"
  "s_addc_u32 s53, s53, 0\n"
  "v_cvt_pk_bf16_f32 v193, v138, v139\n"
  "v_mfma_f32_16x16x32_bf16 v[88:91], a[88:91], v[144:147], v[88:91]\n"
  "v_exp_f32 v141, v141\n"
  "v_mfma_f32_16x16x32_bf16 v[88:91], a[120:123], v[148:151], v[88:91]\n"
  "v_cvt_pk_bf16_f32 v194, v140, v141\n"
  "v_mfma_f32_16x16x32_bf16 v[92:95], a[124:127], v[148:151], v[92:95]\n"
  "v_exp_f32 v142, v142\n"
  "v_mfma_f32_16x16x32_bf16 v[92:95], a[92:95], v[144:147], v[92:95]\n"
  "global_store_dwordx4 v232, v[184:187], s[28:29] offset:-2048 sc1\n"
  "s_add_u32 m0, s48, 49152\n"
  "v_mfma_f32_16x16x32_bf16 v[208:211], a[192:195], v[144:147], v[208:211]\n"
  "v_exp_f32 v143, v143\n"
  "v_mfma_f32_16x16x32_bf16 v[208:211], a[192:195], v[148:151], v[208:211]\n"
  "global_load_lds_dwordx4 v232, s[50:51] sc1\n"
  "v_cvt_pk_bf16_f32 v195, v142, v143\n"
  "v_mfma_f32_16x16x32_bf16 v[96:99], a[64:67], v[188:191], v[96:99]\n"
  "global_load_lds_dwordx4 v228, s[50:51] offset:1024 sc1\n"
  "v_mfma_f32_16x16x32_bf16 v[96:99], a[96:99], v[192:195], v[96:99]\n"
  "v_mfma_f32_16x16x32_bf16 v[100:103], a[100:103], v[192:195], v[100:103]\n"
  "global_load_lds_dwordx4 v229, s[52:53] offset:2048\n"
  "v_mfma_f32_16x16x32_bf16 v[100:103], a[68:71], v[188:191], v[100:103]\n"
  "s_nop 1\n"
  "v_mfma_f32_16x16x32_bf16 v[104:107], a[72:75], v[188:191], v[104:107]\n"
  "s_nop 1\n"
  "v_mfma_f32_16x16x32_bf16 v[104:107], a[104:107], v[192:195], v[104:107]\n"
  "s_nop 1\n"
  "global_load_lds_dwordx4 v230, s[52:53] offset:3072\n"
  "v_mfma_f32_16x16x32_bf16 v[108:111], a[108:111], v[192:195], v[108:111]\n"
  "v_mfma_f32_16x16x32_bf16 v[108:111], a[76:79], v[188:191], v[108:111]\n"
  "s_add_u32 s50, s50, 1024\n"
  "s_addc_u32 s51, s51, 0\n"
  "v_mfma_f32_16x16x32_bf16 v[112:115], a[80:83], v[188:191], v[112:115]\n"
  "v_mfma_f32_16x16x32_bf16 v[112:115], a[112:115], v[192:195], v[112:115]\n"
  "s_add_u32 s52, s52, 32768\n"
  "s_addc_u32 s53, s53, 0\n"
  "v_mfma_f32_16x16x32_bf16 v[116:119], a[116:119], v[192:195], v[116:119]\n"
  "v_mfma_f32_16x16x32_bf16 v[116:119], a[84:87], v[188:191], v[116:119]\n"
  "v_mfma_f32_16x16x32_bf16 v[120:123], a[88:91], v[188:191], v[120:123]\n"
  "v_mfma_f32_16x16x32_bf16 v[120:123], a[120:123], v[192:195], v[120:123]\n"
  "v_mfma_f32_16x16x32_bf16 v[124:127], a[124:127], v[192:195], v[124:127]\n"
  "v_mfma_f32_16x16x32_bf16 v[124:127], a[92:95], v[188:191], v[124:127]\n"
  "s_xor_b32 s49, s49, 0x10000\n"
  "v_mfma_f32_16x16x32_bf16 v[212:215], a[192:195], v[188:191], v[212:215]\n"
  "v_mfma_f32_16x16x32_bf16 v[212:215], a[192:195], v[192:195], v[212:215]\n"
  "s_setpc_b64 s[22:23]\n"
  "L_BFX_%=:\n"
  "v_mfma_f32_16x16x32_bf16 v[128:131], a[128:131], a[16:19], v[156:159]\n"
  "ds_read_b128 a[80:83], v216 offset:12288\n"
  "ds_read_b128 a[112:115], v216 offset:45056\n"
  "v_mfma_f32_16x16x32_bf16 v[128:131], a[132:135], a[20:23], v[128:131]\n"
  "ds_read_b128 a[116:119], v216 offset:46080\n"
  "ds_read_b128 a[84:87], v216 offset:13312\n"
  "v_mfma_f32_16x16x32_bf16 v[128:131], a[136:139], a[24:27], v[128:131]\n"
  "ds_read_b128 a[88:91], v216 offset:14336\n"
  "ds_read_b128 a[120:123], v216 offset:47104\n"
  "v_mfma_f32_16x16x32_bf16 v[128:131], a[140:143], a[28:31], v[128:131]\n"
  "ds_read_b128 a[124:127], v216 offset:48128\n"
  "ds_read_b128 a[92:95], v216 offset:15360\n"
  "v_mfma_f32_16x16x32_bf16 v[132:135], a[156:159], a[28:31], v[156:159]\n"
  "s_nop 1\n"
  "v_mfma_f32_16x16x32_bf16 v[132:135], a[152:155], a[24:27], v[132:135]\n"
  "s_nop 1\n"
  "v_xor_b32 v216, 0x10000, v216\n"
  "v_mfma_f32_16x16x32_bf16 v[132:135], a[148:151], a[20:23], v[132:135]\n"
  "s_add_u32 s24, s24, 1\n"
  "v_exp_f32 v128, v128\n"
  "v_mfma_f32_16x16x32_bf16 v[132:135], a[144:147], a[16:19], v[132:135]\n"
  "v_exp_f32 v129, v129\n"
  "v_mfma_f32_16x16x32_bf16 v[136:139], a[160:163], a[16:19], v[156:159]\n"
  "v_cvt_pk_bf16_f32 v188, v128, v129\n"
  "v_readlane_b32 s22, v252, s24\n"
  "v_mfma_f32_16x16x32_bf16 v[136:139], a[164:167], a[20:23], v[136:139]\n"
  "v_exp_f32 v130, v130\n"
  "v_mfma_f32_16x16x32_bf16 v[136:139], a[168:171], a[24:27], v[136:139]\n"
  "s_add_u32 s22, s26, s22\n"
  "s_addc_u32 s23, s27, 0\n"
  "v_mfma_f32_16x16x32_bf16 v[136:139], a[172:175], a[28:31], v[136:139]\n"
  "v_exp_f32 v131, v131\n"
  "v_mfma_f32_16x16x32_bf16 v[140:143], a[188:191], a[28:31], v[156:159]\n"
  "v_exp_f32 v132, v132\n"
  "v_mfma_f32_16x16x32_bf16 v[140:143], a[184:187], a[24:27], v[140:143]\n"
  "s_waitcnt lgkmcnt(0)\n"
  "s_barrier\n"
  "v_cvt_pk_bf16_f32 v189, v130, v131\n"
  "v_mfma_f32_16x16x32_bf16 v[140:143], a[180:183], a[20:23], v[140:143]\n"
  "v_exp_f32 v133, v133\n"
  "v_mfma_f32_16x16x32_bf16 v[140:143], a[176:179], a[16:19], v[140:143]\n"
  "v_cvt_pk_bf16_f32 v190, v132, v133\n"
  "v_mfma_f32_16x16x32_bf16 v[0:3], a[64:67], v[144:147], v[0:3]\n"
  "s_add_u32 m0, s48, 65536\n"
  "v_exp_f32 v134, v134\n"
  "v_mfma_f32_16x16x32_bf16 v[0:3], a[96:99], v[148:151], v[0:3]\n"
  "v_mfma_f32_16x16x32_bf16 v[4:7], a[100:103], v[148:151], v[4:7]\n"
  "v_exp_f32 v135, v135\n"
  "v_mfma_f32_16x16x32_bf16 v[4:7], a[68:71], v[144:147], v[4:7]\n"
  "v_exp_f32 v136, v136\n"
  "v_mfma_f32_16x16x32_bf16 v[8:11], a[72:75], v[144:147], v[8:11]\n"
  "global_load_lds_dwordx4 v232, s[50:51] sc1\n"
  "v_cvt_pk_bf16_f32 v191, v134, v135\n"
  "v_mfma_f32_16x16x32_bf16 v[8:11], a[104:107], v[148:151], v[8:11]\n"
  "v_exp_f32 v137, v137\n"
  "v_mfma_f32_16x16x32_bf16 v[12:15], a[108:111], v[148:151], v[12:15]\n"
  "v_cvt_pk_bf16_f32 v192, v136, v137\n"
  "v_mfma_f32_16x16x32_bf16 v[12:15], a[76:79], v[144:147], v[12:15]\n"
  "v_exp_f32 v138, v138\n"
  "v_mfma_f32_16x16x32_bf16 v[16:19], a[80:83], v[144:147], v[16:19]\n"
  "global_load_lds_dwordx4 v228, s[50:51] offset:1024 sc1\n"
  "v_mfma_f32_16x16x32_bf16 v[16:19], a[112:115], v[148:151], v[16:19]\n"
  "v_exp_f32 v139, v139\n"
  "v_mfma_f32_16x16x32_bf16 v[20:23], a[116:119], v[148:151], v[20:23]\n"
  "v_exp_f32 v140, v140\n"
  "v_mfma_f32_16x16x32_bf16 v[20:23], a[84:87], v[144:147], v[20:23]\n"
  "v_cvt_pk_bf16_f32 v193, v138, v139\n"
  "v_mfma_f32_16x16x32_bf16 v[24:27], a[88:91], v[144:147], v[24:27]\n"
  "v_exp_f32 v141, v141\n"
  "v_mfma_f32_16x16x32_bf16 v[24:27], a[120:123], v[148:151], v[24:27]\n"
  "global_load_lds_dwordx4 v229, s[52:53] offset:2048\n"
  "v_cvt_pk_bf16_f32 v194, v140, v141\n"
  "v_mfma_f32_16x16x32_bf16 v[28:31], a[124:127], v[148:151], v[28:31]\n"
  "v_exp_f32 v142, v142\n"
  "v_mfma_f32_16x16x32_bf16 v[28:31], a[92:95], v[144:147], v[28:31]\n"
  "v_mfma_f32_16x16x32_bf16 v[200:203], a[192:195], v[144:147], v[200:203]\n"
  "v_exp_f32 v143, v143\n"
  "v_mfma_f32_16x16x32_bf16 v[200:203], a[192:195], v[148:151], v[200:203]\n"
  "global_load_lds_dwordx4 v230, s[52:53] offset:3072\n"
  "v_cvt_pk_bf16_f32 v195, v142, v143\n"
  "v_mfma_f32_16x16x32_bf16 v[128:131], a[128:131], a[32:35], v[160:163]\n"
  "v_mfma_f32_16x16x32_bf16 v[128:131], a[132:135], a[36:39], v[128:131]\n"
  "v_mfma_f32_16x16x32_bf16 v[128:131], a[136:139], a[40:43], v[128:131]\n"
  "s_add_u32 s50, s50, 1024\n"
  "s_addc_u32 s51, s51, 0\n"
  "v_mfma_f32_16x16x32_bf16 v[128:131], a[140:143], a[44:47], v[128:131]\n"
  "s_nop 1\n"
  "v_mfma_f32_16x16x32_bf16 v[132:135], a[156:159], a[44:47], v[160:163]\n"
  "s_nop 1\n"
  "v_mfma_f32_16x16x32_bf16 v[132:135], a[152:155], a[40:43], v[132:135]\n"
  "s_nop 1\n"
  "v_mfma_f32_16x16x32_bf16 v[132:135], a[148:151], a[36:39], v[132:135]\n"
  "v_exp_f32 v128, v128\n"
  "v_mfma_f32_16x16x32_bf16 v[132:135], a[144:147], a[32:35], v[132:135]\n"
  "v_exp_f32 v129, v129\n"
  "v_mfma_f32_16x16x32_bf16 v[136:139], a[160:163], a[32:35], v[160:163]\n"
  "s_add_u32 s52, s52, 32768\n"
  "s_addc_u32 s53, s53, 0\n"
  "v_cvt_pk_bf16_f32 v144, v128, v129\n"
  "v_mfma_f32_16x16x32_bf16 v[136:139], a[164:167], a[36:39], v[136:139]\n"
  "v_exp_f32 v130, v130\n"
  "v_mfma_f32_16x16x32_bf16 v[136:139], a[168:171], a[40:43], v[136:139]\n"
  "s_add_u32 m0, s48, 81920\n"
  "v_mfma_f32_16x16x32_bf16 v[136:139], a[172:175], a[44:47], v[136:139]\n"
  "v_exp_f32 v131, v131\n"
  "v_mfma_f32_16x16x32_bf16 v[140:143], a[188:191], a[44:47], v[160:163]\n"
  "v_exp_f32 v132, v132\n"
  "v_mfma_f32_16x16x32_bf16 v[140:143], a[184:187], a[40:43], v[140:143]\n"
  "v_cvt_pk_bf16_f32 v145, v130, v131\n"
  "v_mfma_f32_16x16x32_bf16 v[140:143], a[180:183], a[36:39], v[140:143]\n"
  "v_exp_f32 v133, v133\n"
  "v_mfma_f32_16x16x32_bf16 v[140:143], a[176:179], a[32:35], v[140:143]\n"
  "global_load_lds_dwordx4 v232, s[50:51] sc1\n"
  "v_cvt_pk_bf16_f32 v146, v132, v133\n"
  "v_mfma_f32_16x16x32_bf16 v[32:35], a[64:67], v[188:191], v[32:35]\n"
  "v_exp_f32 v134, v134\n"
  "v_mfma_f32_16x16x32_bf16 v[32:35], a[96:99], v[192:195], v[32:35]\n"
  "v_mfma_f32_16x16x32_bf16 v[36:39], a[100:103], v[192:195], v[36:39]\n"
  "v_exp_f32 v135, v135\n"
  "v_mfma_f32_16x16x32_bf16 v[36:39], a[68:71], v[188:191], v[36:39]\n"
  "v_exp_f32 v136, v136\n"
  "v_mfma_f32_16x16x32_bf16 v[40:43], a[72:75], v[188:191], v[40:43]\n"
  "global_load_lds_dwordx4 v228, s[50:51] offset:1024 sc1\n"
  "v_cvt_pk_bf16_f32 v147, v134, v135\n"
  "v_mfma_f32_16x16x32_bf16 v[40:43], a[104:107], v[192:195], v[40:43]\n"
  "v_exp_f32 v137, v137\n"
  "v_mfma_f32_16x16x32_bf16 v[44:47], a[108:111], v[192:195], v[44:47]\n"
  "global_load_lds_dwordx4 v229, s[52:53] offset:2048\n"
  "v_cvt_pk_bf16_f32 v148, v136, v137\n"
  "v_mfma_f32_16x16x32_bf16 v[44:47], a[76:79], v[188:191], v[44:47]\n"
  "v_exp_f32 v138, v138\n"
  "v_mfma_f32_16x16x32_bf16 v[48:51], a[80:83], v[188:191], v[48:51]\n"
  "v_mfma_f32_16x16x32_bf16 v[48:51], a[112:115], v[192:195], v[48:51]\n"
  "v_exp_f32 v139, v139\n"
  "v_mfma_f32_16x16x32_bf16 v[52:55], a[116:119], v[192:195], v[52:55]\n"
  "v_exp_f32 v140, v140\n"
  "v_mfma_f32_16x16x32_bf16 v[52:55], a[84:87], v[188:191], v[52:55]\n"
  "global_load_lds_dwordx4 v230, s[52:53] offset:3072\n"
  "v_cvt_pk_bf16_f32 v149, v138, v139\n"
  "v_mfma_f32_16x16x32_bf16 v[56:59], a[88:91], v[188:191], v[56:59]\n"
  "v_exp_f32 v141, v141\n"
  "v_mfma_f32_16x16x32_bf16 v[56:59], a[120:123], v[192:195], v[56:59]\n"
  "v_cvt_pk_bf16_f32 v150, v140, v141\n"
  "v_mfma_f32_16x16x32_bf16 v[60:63], a[124:127], v[192:195], v[60:63]\n"
  "v_exp_f32 v142, v142\n"
  "v_mfma_f32_16x16x32_bf16 v[60:63], a[92:95], v[188:191], v[60:63]\n"
  "s_add_u32 s50, s50, 1024\n"
  "s_addc_u32 s51, s51, 0\n"
  "v_mfma_f32_16x16x32_bf16 v[204:207], a[192:195], v[188:191], v[204:207]\n"
  "v_exp_f32 v143, v143\n"
  "v_mfma_f32_16x16x32_bf16 v[204:207], a[192:195], v[192:195], v[204:207]\n"
  "s_barrier\n"
  "v_cvt_pk_bf16_f32 v151, v142, v143\n"
  "v_mfma_f32_16x16x32_bf16 v[128:131], a[128:131], a[48:51], v[164:167]\n"
  "v_mfma_f32_16x16x32_bf16 v[128:131], a[132:135], a[52:55], v[128:131]\n"
  "s_add_u32 s52, s52, 32768\n"
  "s_addc_u32 s53, s53, 0\n"
  "v_mfma_f32_16x16x32_bf16 v[128:131], a[136:139], a[56:59], v[128:131]\n"
  "v_mfma_f32_16x16x32_bf16 v[128:131], a[140:143], a[60:63], v[128:131]\n"
  "s_nop 1\n"
  "v_mfma_f32_16x16x32_bf16 v[132:135], a[156:159], a[60:63], v[164:167]\n"
  "s_nop 1\n"
  "v_mfma_f32_16x16x32_bf16 v[132:135], a[152:155], a[56:59], v[132:135]\n"
  "s_nop 1\n"
  "global_store_dwordx4 v232, v[184:187], s[28:29] offset:-1024 sc1\n"
  "v_mfma_f32_16x16x32_bf16 v[132:135], a[148:151], a[52:55], v[132:135]\n"
  "v_exp_f32 v128, v128\n"
  "v_mfma_f32_16x16x32_bf16 v[132:135], a[144:147], a[48:51], v[132:135]\n"
  "v_exp_f32 v129, v129\n"
  "v_mfma_f32_16x16x32_bf16 v[136:139], a[160:163], a[48:51], v[164:167]\n"
  "v_cvt_pk_bf16_f32 v188, v128, v129\n"
  "v_mfma_f32_16x16x32_bf16 v[136:139], a[164:167], a[52:55], v[136:139]\n"
  "v_exp_f32 v130, v130\n"
  "v_mfma_f32_16x16x32_bf16 v[136:139], a[168:171], a[56:59], v[136:139]\n"
  "global_store_dwordx4 v232, v[184:187], s[28:29] offset:0 sc1\n"
  "v_mfma_f32_16x16x32_bf16 v[136:139], a[172:175], a[60:63], v[136:139]\n"
  "v_exp_f32 v131, v131\n"
  "v_mfma_f32_16x16x32_bf16 v[140:143], a[188:191], a[60:63], v[164:167]\n"
  "v_exp_f32 v132, v132\n"
  "v_mfma_f32_16x16x32_bf16 v[140:143], a[184:187], a[56:59], v[140:143]\n"
  "s_add_u32 m0, s48, 98304\n"
  "v_cvt_pk_bf16_f32 v189, v130, v131\n"
  "v_mfma_f32_16x16x32_bf16 v[140:143], a[180:183], a[52:55], v[140:143]\n"
  "v_exp_f32 v133, v133\n"
  "v_mfma_f32_16x16x32_bf16 v[140:143], a[176:179], a[48:51], v[140:143]\n"
  "v_cvt_pk_bf16_f32 v190, v132, v133\n"
  "v_mfma_f32_16x16x32_bf16 v[64:67], a[64:67], v[144:147], v[64:67]\n"
  "v_exp_f32 v134, v134\n"
  "v_mfma_f32_16x16x32_bf16 v[64:67], a[96:99], v[148:151], v[64:67]\n"
  "global_load_lds_dwordx4 v232, s[50:51] sc1\n"
  "v_mfma_f32_16x16x32_bf16 v[68:71], a[100:103], v[148:151], v[68:71]\n"
  "v_exp_f32 v135, v135\n"
  "v_mfma_f32_16x16x32_bf16 v[68:71], a[68:71], v[144:147], v[68:71]\n"
  "v_exp_f32 v136, v136\n"
  "v_mfma_f32_16x16x32_bf16 v[72:75], a[72:75], v[144:147], v[72:75]\n"
  "v_cvt_pk_bf16_f32 v191, v134, v135\n"
  "v_mfma_f32_16x16x32_bf16 v[72:75], a[104:107], v[148:151], v[72:75]\n"
  "v_exp_f32 v137, v137\n"
  "v_mfma_f32_16x16x32_bf16 v[76:79], a[108:111], v[148:151], v[76:79]\n"
  "global_load_lds_dwordx4 v228, s[50:51] offset:1024 sc1\n"
  "v_cvt_pk_bf16_f32 v192, v136, v137\n"
  "v_mfma_f32_16x16x32_bf16 v[76:79], a[76:79], v[144:147], v[76:79]\n"
  "v_exp_f32 v138, v138\n"
  "v_mfma_f32_16x16x32_bf16 v[80:83], a[80:83], v[144:147], v[80:83]\n"
  "v_mfma_f32_16x16x32_bf16 v[80:83], a[112:115], v[148:151], v[80:83]\n"
  "v_exp_f32 v139, v139\n"
  "v_mfma_f32_16x16x32_bf16 v[84:87], a[116:119], v[148:151], v[84:87]\n"
  "v_exp_f32 v140, v140\n"
  "v_mfma_f32_16x16x32_bf16 v[84:87], a[84:87], v[144:147], v[84:87]\n"
  "global_load_lds_dwordx4 v229, s[52:53] offset:2048\n"
  "v_cvt_pk_bf16_f32 v193, v138, v139\n"
  "v_mfma_f32_16x16x32_bf16 v[88:91], a[88:91], v[144:147], v[88:91]\n"
  "v_exp_f32 v141, v141\n"
  "v_mfma_f32_16x16x32_bf16 v[88:91], a[120:123], v[148:151], v[88:91]\n"
  "global_load_lds_dwordx4 v230, s[52:53] offset:3072\n"
  "v_cvt_pk_bf16_f32 v194, v140, v141\n"
  "v_mfma_f32_16x16x32_bf16 v[92:95], a[124:127], v[148:151], v[92:95]\n"
  "v_exp_f32 v142, v142\n"
  "v_mfma_f32_16x16x32_bf16 v[92:95], a[92:95], v[144:147], v[92:95]\n"
  "v_mfma_f32_16x16x32_bf16 v[208:211], a[192:195], v[144:147], v[208:211]\n"
  "v_exp_f32 v143, v143\n"
  "v_mfma_f32_16x16x32_bf16 v[208:211], a[192:195], v[148:151], v[208:211]\n"
  "s_add_u32 s50, s50, 1024\n"
  "s_addc_u32 s51, s51, 0\n"
  "v_cvt_pk_bf16_f32 v195, v142, v143\n"
  "v_mfma_f32_16x16x32_bf16 v[96:99], a[64:67], v[188:191], v[96:99]\n"
  "s_nop 0\n"
  "v_mfma_f32_16x16x32_bf16 v[96:99], a[96:99], v[192:195], v[96:99]\n"
  "v_mfma_f32_16x16x32_bf16 v[100:103], a[100:103], v[192:195], v[100:103]\n"
  "v_mfma_f32_16x16x32_bf16 v[100:103], a[68:71], v[188:191], v[100:103]\n"
  "s_add_u32 s52, s52, 32768\n"
  "s_addc_u32 s53, s53, 0\n"
  "v_mfma_f32_16x16x32_bf16 v[104:107], a[72:75], v[188:191], v[104:107]\n"
  "s_nop 1\n"
  "v_mfma_f32_16x16x32_bf16 v[104:107], a[104:107], v[192:195], v[104:107]\n"
  "s_nop 1\n"
  "v_mfma_f32_16x16x32_bf16 v[108:111], a[108:111], v[192:195], v[108:111]\n"
  "v_mfma_f32_16x16x32_bf16 v[108:111], a[76:79], v[188:191], v[108:111]\n"
  "global_store_dwordx4 v232, v[184:187], s[28:29] offset:1024 sc1\n"
  "v_mfma_f32_16x16x32_bf16 v[112:115], a[80:83], v[188:191], v[112:115]\n"
  "v_mfma_f32_16x16x32_bf16 v[112:115], a[112:115], v[192:195], v[112:115]\n"
  "v_mfma_f32_16x16x32_bf16 v[116:119], a[116:119], v[192:195], v[116:119]\n"
  "v_mfma_f32_16x16x32_bf16 v[116:119], a[84:87], v[188:191], v[116:119]\n"
  "v_mfma_f32_16x16x32_bf16 v[120:123], a[88:91], v[188:191], v[120:123]\n"
  "v_mfma_f32_16x16x32_bf16 v[120:123], a[120:123], v[192:195], v[120:123]\n"
  "v_mfma_f32_16x16x32_bf16 v[124:127], a[124:127], v[192:195], v[124:127]\n"
  "v_mfma_f32_16x16x32_bf16 v[124:127], a[92:95], v[188:191], v[124:127]\n"
  "s_xor_b32 s49, s49, 0x10000\n"
  "v_mfma_f32_16x16x32_bf16 v[212:215], a[192:195], v[188:191], v[212:215]\n"
  "v_mfma_f32_16x16x32_bf16 v[212:215], a[192:195], v[192:195], v[212:215]\n"
  "s_setpc_b64 s[22:23]\n"
  "L_BDX_%=:\n"
  "v_mfma_f32_16x16x32_bf16 v[128:131], a[128:131], a[16:19], v[156:159]\n"
  "ds_read_b128 a[80:83], v216 offset:12288\n"
  "ds_read_b128 a[112:115], v216 offset:45056\n"
  "v_mfma_f32_16x16x32_bf16 v[128:131], a[132:135], a[20:23], v[128:131]\n"
  "ds_read_b128 a[116:119], v216 offset:46080\n"
  "ds_read_b128 a[84:87], v216 offset:13312\n"
  "v_mfma_f32_16x16x32_bf16 v[128:131], a[136:139], a[24:27], v[128:131]\n"
  "ds_read_b128 a[88:91], v216 offset:14336\n"
  "ds_read_b128 a[120:123], v216 offset:47104\n"
  "v_mfma_f32_16x16x32_bf16 v[128:131], a[140:143], a[28:31], v[128:131]\n"
  "ds_read_b128 a[124:127], v216 offset:48128\n"
  "ds_read_b128 a[92:95], v216 offset:15360\n"
  "v_mfma_f32_16x16x32_bf16 v[132:135], a[156:159], a[28:31], v[172:175]\n"
  "s_nop 1\n"
  "v_mfma_f32_16x16x32_bf16 v[132:135], a[152:155], a[24:27], v[132:135]\n"
  "s_nop 1\n"
  "v_xor_b32 v216, 0x10000, v216\n"
  "v_mfma_f32_16x16x32_bf16 v[132:135], a[148:151], a[20:23], v[132:135]\n"
  "s_add_u32 s24, s24, 1\n"
  "v_exp_f32 v128, v128\n"
  "v_mfma_f32_16x16x32_bf16 v[132:135], a[144:147], a[16:19], v[132:135]\n"
  "v_exp_f32 v129, v129\n"
  "s_waitcnt lgkmcnt(15)\n"
  "v_mfma_f32_16x16x32_bf16 v[0:3], a[64:67], v[144:147], v[0:3]\n"
  "v_exp_f32 v130, v130\n"
  "s_waitcnt lgkmcnt(12)\n"
  "v_mfma_f32_16x16x32_bf16 v[4:7], a[68:71], v[144:147], v[4:7]\n"
  "v_exp_f32 v131, v131\n"
  "s_waitcnt lgkmcnt(11)\n"
  "v_mfma_f32_16x16x32_bf16 v[8:11], a[72:75], v[144:147], v[8:11]\n"
  "v_exp_f32 v132, v132\n"
  "s_waitcnt lgkmcnt(8)\n"
  "v_mfma_f32_16x16x32_bf16 v[12:15], a[76:79], v[144:147], v[12:15]\n"
  "s_waitcnt lgkmcnt(0)\n"
  "v_exp_f32 v133, v133\n"
  "v_mfma_f32_16x16x32_bf16 v[16:19], a[80:83], v[144:147], v[16:19]\n"
  "s_barrier\n"
  "v_exp_f32 v134, v134\n"
  "v_mfma_f32_16x16x32_bf16 v[20:23], a[84:87], v[144:147], v[20:23]\n"
  "v_exp_f32 v135, v135\n"
  "v_mfma_f32_16x16x32_bf16 v[24:27], a[88:91], v[144:147], v[24:27]\n"
  "v_cvt_pk_bf16_f32 v188, v128, v129\n"
  "v_cvt_pk_bf16_f32 v189, v130, v131\n"
  "v_mfma_f32_16x16x32_bf16 v[28:31], a[92:95], v[144:147], v[28:31]\n"
  "v_cvt_pk_bf16_f32 v190, v132, v133\n"
  "v_cvt_pk_bf16_f32 v191, v134, v135\n"
  "v_mfma_f32_16x16x32_bf16 v[200:203], a[192:195], v[144:147], v[200:203]\n"
  "v_readlane_b32 s22, v252, s24\n"
  "v_mfma_f32_16x16x32_bf16 v[128:131], a[128:131], a[32:35], v[160:163]\n"
  "s_add_u32 s22, s26, s22\n"
  "s_addc_u32 s23, s27, 0\n"
  "s_add_u32 m0, s48, 65536\n"
  "v_mfma_f32_16x16x32_bf16 v[128:131], a[132:135], a[36:39], v[128:131]\n"
  "global_load_lds_dwordx4 v232, s[50:51] sc1\n"
  "v_mfma_f32_16x16x32_bf16 v[128:131], a[136:139], a[40:43], v[128:131]\n"
  "global_load_lds_dwordx4 v228, s[50:51] offset:1024 sc1\n"
  "v_mfma_f32_16x16x32_bf16 v[128:131], a[140:143], a[44:47], v[128:131]\n"
  "s_nop 1\n"
  "v_mfma_f32_16x16x32_bf16 v[132:135], a[156:159], a[44:47], v[160:163]\n"
  "s_nop 1\n"
  "v_mfma_f32_16x16x32_bf16 v[132:135], a[152:155], a[40:43], v[132:135]\n"
  "s_nop 1\n"
  "global_load_lds_dwordx4 v229, s[52:53] offset:2048\n"
  "v_mfma_f32_16x16x32_bf16 v[132:135], a[148:151], a[36:39], v[132:135]\n"
  "v_exp_f32 v128, v128\n"
  "v_mfma_f32_16x16x32_bf16 v[132:135], a[144:147], a[32:35], v[132:135]\n"
  "v_exp_f32 v129, v129\n"
  "v_mfma_f32_16x16x32_bf16 v[136:139], a[160:163], a[32:35], v[176:179]\n"
  "v_exp_f32 v130, v130\n"
  "v_mfma_f32_16x16x32_bf16 v[136:139], a[164:167], a[36:39], v[136:139]\n"
  "v_exp_f32 v131, v131\n"
  "v_mfma_f32_16x16x32_bf16 v[136:139], a[168:171], a[40:43], v[136:139]\n"
  "s_nop 1\n"
  "v_exp_f32 v132, v132\n"
  "v_mfma_f32_16x16x32_bf16 v[136:139], a[172:175], a[44:47], v[136:139]\n"
  "v_exp_f32 v133, v133\n"
  "v_mfma_f32_16x16x32_bf16 v[32:35], a[64:67], v[188:191], v[32:35]\n"
  "v_cvt_pk_bf16_f32 v144, v128, v129\n"
  "v_cvt_pk_bf16_f32 v145, v130, v131\n"
  "v_mfma_f32_16x16x32_bf16 v[36:39], a[68:71], v[188:191], v[36:39]\n"
  "v_exp_f32 v134, v134\n"
  "v_mfma_f32_16x16x32_bf16 v[40:43], a[72:75], v[188:191], v[40:43]\n"
  "v_exp_f32 v135, v135\n"
  "v_mfma_f32_16x16x32_bf16 v[44:47], a[76:79], v[188:191], v[44:47]\n"
  "v_exp_f32 v136, v136\n"
  "v_mfma_f32_16x16x32_bf16 v[48:51], a[80:83], v[188:191], v[48:51]\n"
  "v_exp_f32 v137, v137\n"
  "v_mfma_f32_16x16x32_bf16 v[52:55], a[84:87], v[188:191], v[52:55]\n"
  "v_exp_f32 v138, v138\n"
  "v_mfma_f32_16x16x32_bf16 v[56:59], a[88:91], v[188:191], v[56:59]\n"
  "v_cvt_pk_bf16_f32 v146, v132, v133\n"
  "v_cvt_pk_bf16_f32 v147, v134, v135\n"
  "v_mfma_f32_16x16x32_bf16 v[60:63], a[92:95], v[188:191], v[60:63]\n"
  "v_exp_f32 v139, v139\n"
  "v_mfma_f32_16x16x32_bf16 v[204:207], a[192:195], v[188:191], v[204:207]\n"
  "s_barrier\n"
  "v_cvt_pk_bf16_f32 v148, v136, v137\n"
  "v_cvt_pk_bf16_f32 v149, v138, v139\n"
  "v_mov_b32 v150, 0\n"
  "v_mov_b32 v151, 0\n"
  "v_mfma_f32_16x16x32_bf16 v[128:131], a[128:131], a[48:51], v[164:167]\n"
  "global_load_lds_dwordx4 v230, s[52:53] offset:3072\n"
  "s_add_u32 s50, s50, 1024\n"
  "s_addc_u32 s51, s51, 0\n"
  "v_mfma_f32_16x16x32_bf16 v[128:131], a[132:135], a[52:55], v[128:131]\n"
  "s_add_u32 s52, s52, 32768\n"
  "s_addc_u32 s53, s53, 0\n"
  "s_add_u32 m0, s48, 81920\n"
  "v_mfma_f32_16x16x32_bf16 v[128:131], a[136:139], a[56:59], v[128:131]\n"
  "global_load_lds_dwordx4 v232, s[50:51] sc1\n"
  "global_load_lds_dwordx4 v228, s[50:51] offset:1024 sc1\n"
  "v_mfma_f32_16x16x32_bf16 v[128:131], a[140:143], a[60:63], v[128:131]\n"
  "s_nop 1\n"
  "v_mfma_f32_16x16x32_bf16 v[132:135], a[156:159], a[60:63], v[164:167]\n"
  "s_nop 1\n"
  "v_mfma_f32_16x16x32_bf16 v[132:135], a[152:155], a[56:59], v[132:135]\n"
  "s_nop 1\n"
  "global_load_lds_dwordx4 v229, s[52:53] offset:2048\n"
  "v_mfma_f32_16x16x32_bf16 v[132:135], a[148:151], a[52:55], v[132:135]\n"
  "v_exp_f32 v128, v128\n"
  "v_mfma_f32_16x16x32_bf16 v[132:135], a[144:147], a[48:51], v[132:135]\n"
  "v_exp_f32 v129, v129\n"
  "v_mfma_f32_16x16x32_bf16 v[136:139], a[160:163], a[48:51], v[164:167]\n"
  "global_load_lds_dwordx4 v230, s[52:53] offset:3072\n"
  "v_cvt_pk_bf16_f32 v188, v128, v129\n"
  "v_mfma_f32_16x16x32_bf16 v[136:139], a[164:167], a[52:55], v[136:139]\n"
  "v_exp_f32 v130, v130\n"
  "v_mfma_f32_16x16x32_bf16 v[136:139], a[168:171], a[56:59], v[136:139]\n"
  "v_mfma_f32_16x16x32_bf16 v[136:139], a[172:175], a[60:63], v[136:139]\n"
  "v_exp_f32 v131, v131\n"
  "v_mfma_f32_16x16x32_bf16 v[140:143], a[188:191], a[60:63], v[180:183]\n"
  "v_exp_f32 v132, v132\n"
  "v_mfma_f32_16x16x32_bf16 v[140:143], a[184:187], a[56:59], v[140:143]\n"
  "s_add_u32 s50, s50, 1024\n"
  "s_addc_u32 s51, s51, 0\n"
  "v_cvt_pk_bf16_f32 v189, v130, v131\n"
  "v_mfma_f32_16x16x32_bf16 v[140:143], a[180:183], a[52:55], v[140:143]\n"
  "v_exp_f32 v133, v133\n"
  "v_mfma_f32_16x16x32_bf16 v[140:143], a[176:179], a[48:51], v[140:143]\n"
  "s_add_u32 s52, s52, 32768\n"
  "s_addc_u32 s53, s53, 0\n"
  "v_cvt_pk_bf16_f32 v190, v132, v133\n"
  "v_mfma_f32_16x16x32_bf16 v[64:67], a[64:67], v[144:147], v[64:67]\n"
  "v_exp_f32 v134, v134\n"
  "v_mfma_f32_16x16x32_bf16 v[64:67], a[96:99], v[148:151], v[64:67]\n"
  "global_store_dwordx4 v232, v[184:187], s[28:29] offset:-1024 sc1\n"
  "v_mfma_f32_16x16x32_bf16 v[68:71], a[100:103], v[148:151], v[68:71]\n"
  "v_exp_f32 v135, v135\n"
  "v_mfma_f32_16x16x32_bf16 v[68:71], a[68:71], v[144:147], v[68:71]\n"
  "v_exp_f32 v136, v136\n"
  "v_mfma_f32_16x16x32_bf16 v[72:75], a[72:75], v[144:147], v[72:75]\n"
  "v_cvt_pk_bf16_f32 v191, v134, v135\n"
  "v_mfma_f32_16x16x32_bf16 v[72:75], a[104:107], v[148:151], v[72:75]\n"
  "v_exp_f32 v137, v137\n"
  "v_mfma_f32_16x16x32_bf16 v[76:79], a[108:111], v[148:151], v[76:79]\n"
  "v_cvt_pk_bf16_f32 v192, v136, v137\n"
  "v_mfma_f32_16x16x32_bf16 v[76:79], a[76:79], v[144:147], v[76:79]\n"
  "v_exp_f32 v138, v138\n"
  "v_mfma_f32_16x16x32_bf16 v[80:83], a[80:83], v[144:147], v[80:83]\n"
  "global_store_dwordx4 v232, v[184:187], s[28:29] offset:0 sc1\n"
  "s_add_u32 m0, s48, 98304\n"
  "v_mfma_f32_16x16x32_bf16 v[80:83], a[112:115], v[148:151], v[80:83]\n"
  "v_exp_f32 v139, v139\n"
  "v_mfma_f32_16x16x32_bf16 v[84:87], a[116:119], v[148:151], v[84:87]\n"
  "v_exp_f32 v140, v140\n"
  "v_mfma_f32_16x16x32_bf16 v[84:87], a[84:87], v[144:147], v[84:87]\n"
  "global_load_lds_dwordx4 v232, s[50:51] sc1\n"
  "v_cvt_pk_bf16_f32 v193, v138, v139\n"
  "v_mfma_f32_16x16x32_bf16 v[88:91], a[88:91], v[144:147], v[88:91]\n"
  "v_exp_f32 v141, v141\n"
  "v_mfma_f32_16x16x32_bf16 v[88:91], a[120:123], v[148:151], v[88:91]\n"
  "global_load_lds_dwordx4 v228, s[50:51] offset:1024 sc1\n"
  "v_cvt_pk_bf16_f32 v194, v140, v141\n"
  "v_mfma_f32_16x16x32_bf16 v[92:95], a[124:127], v[148:151], v[92:95]\n"
  "v_exp_f32 v142, v142\n"
  "v_mfma_f32_16x16x32_bf16 v[92:95], a[92:95], v[144:147], v[92:95]\n"
  "global_load_lds_dwordx4 v229, s[52:53] offset:2048\n"
  "v_mfma_f32_16x16x32_bf16 v[208:211], a[192:195], v[144:147], v[208:211]\n"
  "v_exp_f32 v143, v143\n"
  "v_mfma_f32_16x16x32_bf16 v[208:211], a[192:195], v[148:151], v[208:211]\n"
  "v_cvt_pk_bf16_f32 v195, v142, v143\n"
  "v_mfma_f32_16x16x32_bf16 v[96:99], a[64:67], v[188:191], v[96:99]\n"
  "global_load_lds_dwordx4 v230, s[52:53] offset:3072\n"
  "v_mfma_f32_16x16x32_bf16 v[96:99], a[96:99], v[192:195], v[96:99]\n"
  "v_mfma_f32_16x16x32_bf16 v[100:103], a[100:103], v[192:195], v[100:103]\n"
  "v_mfma_f32_16x16x32_bf16 v[100:103], a[68:71], v[188:191], v[100:103]\n"
  "s_add_u32 s50, s50, 1024\n"
  "s_addc_u32 s51, s51, 0\n"
  "v_mfma_f32_16x16x32_bf16 v[104:107], a[72:75], v[188:191], v[104:107]\n"
  "s_nop 1\n"
  "v_mfma_f32_16x16x32_bf16 v[104:107], a[104:107], v[192:195], v[104:107]\n"
  "s_nop 1\n"
  "v_mfma_f32_16x16x32_bf16 v[108:111], a[108:111], v[192:195], v[108:111]\n"
  "s_add_u32 s52, s52, 32768\n"
  "s_addc_u32 s53, s53, 0\n"
  "v_mfma_f32_16x16x32_bf16 v[108:111], a[76:79], v[188:191], v[108:111]\n"
  "v_mfma_f32_16x16x32_bf16 v[112:115], a[80:83], v[188:191], v[112:115]\n"
  "v_mfma_f32_16x16x32_bf16 v[112:115], a[112:115], v[192:195], v[112:115]\n"
  "global_store_dwordx4 v232, v[184:187], s[28:29] offset:1024 sc1\n"
  "v_mfma_f32_16x16x32_bf16 v[116:119], a[116:119], v[192:195], v[116:119]\n"
  "v_mfma_f32_16x16x32_bf16 v[116:119], a[84:87], v[188:191], v[116:119]\n"
  "v_mfma_f32_16x16x32_bf16 v[120:123], a[88:91], v[188:191], v[120:123]\n"
  "v_mfma_f32_16x16x32_bf16 v[120:123], a[120:123], v[192:195], v[120:123]\n"
  "v_mfma_f32_16x16x32_bf16 v[124:127], a[124:127], v[192:195], v[124:127]\n"
  "v_mfma_f32_16x16x32_bf16 v[124:127], a[92:95], v[188:191], v[124:127]\n"
  "s_xor_b32 s49, s49, 0x10000\n"
  "v_mfma_f32_16x16x32_bf16 v[212:215], a[192:195], v[188:191], v[212:215]\n"
  "v_mfma_f32_16x16x32_bf16 v[212:215], a[192:195], v[192:195], v[212:215]\n"
  "s_setpc_b64 s[22:23]\n"
  "L_IP3_%=:\n"
  "s_mov_b32 s36, 0x1f800000\n"
  "s_mov_b32 s37, 0x5f800000\n"
  "s_mov_b64 s[60:61], 0\n"
  "v_cmp_nle_f32 vcc, s36, v200\n"
  "v_cmp_nge_f32 s[96:97], s37, v200\n"
  "s_or_b64 s[60:61], s[60:61], vcc\n"
  "s_or_b64 s[60:61], s[60:61], s[96:97]\n"
  "v_cmp_nle_f32 vcc, s36, v204\n"
  "v_cmp_nge_f32 s[96:97], s37, v204\n"
  "s_or_b64 s[60:61], s[60:61], vcc\n"
  "s_or_b64 s[60:61], s[60:61], s[96:97]\n"
  "v_cmp_nle_f32 vcc, s36, v208\n"
  "v_cmp_nge_f32 s[96:97], s37, v208\n"
  "s_or_b64 s[60:61], s[60:61], vcc\n"
  "s_or_b64 s[60:61], s[60:61], s[96:97]\n"
  "v_cmp_nle_f32 vcc, s36, v212\n"
  "v_cmp_nge_f32 s[96:97], s37, v212\n"
  "s_or_b64 s[60:61], s[60:61], vcc\n"
  "s_or_b64 s[60:61], s[60:61], s[96:97]\n"
  "s_cbranch_scc0 L_IP3_go_%=\n"
  "s_call_b64 s[18:19], L_slow0_%=\n"
  "s_call_b64 s[18:19], L_slow1_%=\n"
  "s_call_b64 s[18:19], L_slow2_%=\n"
  "s_call_b64 s[18:19], L_slow3_%=\n"
  "L_IP3_go_%=:\n"
  "s_waitcnt lgkmcnt(0)\n"
  "s_barrier\n"
  "s_and_b32 s36, s35, 1\n"
  "s_lshl_b32 s36, s36, 14\n"
  "s_add_u32 s36, s36, 131072\n"
  "s_add_u32 s36, s36, s38\n"
  "v_add_u32 v234, s36, v232\n"
  "ds_read_b128 a[0:3], v234 offset:0\n"
  "ds_read_b128 a[4:7], v234 offset:1024\n"
  "ds_read_b128 a[8:11], v234 offset:2048\n"
  "ds_read_b128 a[12:15], v234 offset:3072\n"
  "ds_read_b128 a[16:19], v234 offset:4096\n"
  "ds_read_b128 a[20:23], v234 offset:5120\n"
  "ds_read_b128 a[24:27], v234 offset:6144\n"
  "ds_read_b128 a[28:31], v234 offset:7168\n"
  "ds_read_b128 a[32:35], v234 offset:8192\n"
  "ds_read_b128 a[36:39], v234 offset:9216\n"
  "ds_read_b128 a[40:43], v234 offset:10240\n"
  "ds_read_b128 a[44:47], v234 offset:11264\n"
  "ds_read_b128 a[48:51], v234 offset:12288\n"
  "ds_read_b128 a[52:55], v234 offset:13312\n"
  "ds_read_b128 a[56:59], v234 offset:14336\n"
  "ds_read_b128 a[60:63], v234 offset:15360\n"
  "s_lshl_b32 s36, s39, 12\n"
  "v_add_u32 v219, s36, v232\n"
  "v_rcp_f32 v188, v200\n"
  "v_rcp_f32 v190, v204\n"
  "v_rcp_f32 v192, v208\n"
  "v_rcp_f32 v194, v212\n"
  "v_pk_mul_f32 v[0:1], v[0:1], v[188:189] op_sel_hi:[1,0]\n"
  "v_pk_mul_f32 v[2:3], v[2:3], v[188:189] op_sel_hi:[1,0]\n"
  "v_pk_mul_f32 v[4:5], v[4:5], v[188:189] op_sel_hi:[1,0]\n"
  "v_pk_mul_f32 v[6:7], v[6:7], v[188:189] op_sel_hi:[1,0]\n"
  "v_cvt_pk_bf16_f32 v236, v0, v1\n"
  "v_cvt_pk_bf16_f32 v237, v2, v3\n"
  "v_cvt_pk_bf16_f32 v238, v4, v5\n"
  "v_cvt_pk_bf16_f32 v239, v6, v7\n"
  "global_store_dwordx4 v219, v[236:239], s[40:41] offset:0 sc1\n"
  "v_mov_b32 v152, v220\n"
  "v_mov_b32 v153, v220\n"
  "s_add_u32 s24, s24, 1\n"
  "v_readlane_b32 s22, v252, s24\n"
  "v_mfma_f32_32x32x16_bf16 v[200:215], v[224:227], v[224:227], 0\n"
  "v_pk_mul_f32 v[8:9], v[8:9], v[188:189] op_sel_hi:[1,0]\n"
  "v_pk_mul_f32 v[10:11], v[10:11], v[188:189] op_sel_hi:[1,0]\n"
  "v_pk_mul_f32 v[12:13], v[12:13], v[188:189] op_sel_hi:[1,0]\n"
  "v_pk_mul_f32 v[14:15], v[14:15], v[188:189] op_sel_hi:[1,0]\n"
  "v_cvt_pk_bf16_f32 v240, v8, v9\n"
  "v_cvt_pk_bf16_f32 v241, v10, v11\n"
  "v_cvt_pk_bf16_f32 v242, v12, v13\n"
  "v_cvt_pk_bf16_f32 v243, v14, v15\n"
  "global_store_dwordx4 v219, v[240:243], s[40:41] offset:1024 sc1\n"
  "v_mov_b32 v154, v220\n"
  "v_mov_b32 v155, v220\n"
  "s_add_u32 s22, s26, s22\n"
  "s_addc_u32 s23, s27, 0\n"
  "s_add_u32 m0, s49, 0\n"
  "v_mfma_f32_32x32x16_bf16 v[0:15], v[224:227], v[224:227], 0\n"
  "v_pk_mul_f32 v[16:17], v[16:17], v[188:189] op_sel_hi:[1,0]\n"
  "v_pk_mul_f32 v[18:19], v[18:19], v[188:189] op_sel_hi:[1,0]\n"
  "v_pk_mul_f32 v[20:21], v[20:21], v[188:189] op_sel_hi:[1,0]\n"
  "v_pk_mul_f32 v[22:23], v[22:23], v[188:189] op_sel_hi:[1,0]\n"
  "v_cvt_pk_bf16_f32 v244, v16, v17\n"
  "v_cvt_pk_bf16_f32 v245, v18, v19\n"
  "v_cvt_pk_bf16_f32 v246, v20, v21\n"
  "v_cvt_pk_bf16_f32 v247, v22, v23\n"
  "global_store_dwordx4 v219, v[244:247], s[40:41] offset:2048 sc1\n"
  "v_mov_b32 v156, v221\n"
  "v_mov_b32 v157, v221\n"
  "global_load_lds_dwordx4 v232, s[20:21] offset:0 sc1\n"
  "global_load_lds_dwordx4 v232, s[20:21] offset:1024 sc1\n"
  "v_pk_mul_f32 v[24:25], v[24:25], v[188:189] op_sel_hi:[1,0]\n"
  "v_pk_mul_f32 v[26:27], v[26:27], v[188:189] op_sel_hi:[1,0]\n"
  "v_pk_mul_f32 v[28:29], v[28:29], v[188:189] op_sel_hi:[1,0]\n"
  "v_pk_mul_f32 v[30:31], v[30:31], v[188:189] op_sel_hi:[1,0]\n"
  "v_cvt_pk_bf16_f32 v248, v24, v25\n"
  "v_cvt_pk_bf16_f32 v249, v26, v27\n"
  "v_cvt_pk_bf16_f32 v250, v28, v29\n"
  "v_cvt_pk_bf16_f32 v251, v30, v31\n"
  "global_store_dwordx4 v219, v[248:251], s[40:41] offset:3072 sc1\n"
  "v_mov_b32 v158, v221\n"
  "v_mov_b32 v159, v221\n"
  "global_load_lds_dwordx4 v232, s[20:21] offset:2048 sc1\n"
  "global_load_lds_dwordx4 v232, s[20:21] offset:3072 sc1\n"
  "v_mfma_f32_32x32x16_bf16 v[16:31], v[224:227], v[224:227], 0\n"
  "v_pk_mul_f32 v[32:33], v[32:33], v[190:191] op_sel_hi:[1,0]\n"
  "v_pk_mul_f32 v[34:35], v[34:35], v[190:191] op_sel_hi:[1,0]\n"
  "v_pk_mul_f32 v[36:37], v[36:37], v[190:191] op_sel_hi:[1,0]\n"
  "v_pk_mul_f32 v[38:39], v[38:39], v[190:191] op_sel_hi:[1,0]\n"
  "v_cvt_pk_bf16_f32 v236, v32, v33\n"
  "v_cvt_pk_bf16_f32 v237, v34, v35\n"
  "v_cvt_pk_bf16_f32 v238, v36, v37\n"
  "v_cvt_pk_bf16_f32 v239, v38, v39\n"
  "global_store_dwordx4 v219, v[236:239], s[42:43] offset:0 sc1\n"
  "v_mov_b32 v160, v222\n"
  "v_mov_b32 v161, v222\n"
  "s_add_u32 m0, s49, 16384\n"
  "s_nop 0\n"
  "global_load_lds_dwordx4 v217, s[20:21] offset:0 sc1\n"
  "v_pk_mul_f32 v[40:41], v[40:41], v[190:191] op_sel_hi:[1,0]\n"
  "v_pk_mul_f32 v[42:43], v[42:43], v[190:191] op_sel_hi:[1,0]\n"
  "v_pk_mul_f32 v[44:45], v[44:45], v[190:191] op_sel_hi:[1,0]\n"
  "v_pk_mul_f32 v[46:47], v[46:47], v[190:191] op_sel_hi:[1,0]\n"
  "v_cvt_pk_bf16_f32 v240, v40, v41\n"
  "v_cvt_pk_bf16_f32 v241, v42, v43\n"
  "v_cvt_pk_bf16_f32 v242, v44, v45\n"
  "v_cvt_pk_bf16_f32 v243, v46, v47\n"
  "global_store_dwordx4 v219, v[240:243], s[42:43] offset:1024 sc1\n"
  "v_mov_b32 v162, v222\n"
  "v_mov_b32 v163, v222\n"
  "global_load_lds_dwordx4 v217, s[20:21] offset:1024 sc1\n"
  "global_load_lds_dwordx4 v217, s[20:21] offset:2048 sc1\n"
  "v_mfma_f32_32x32x16_bf16 v[32:47], v[224:227], v[224:227], 0\n"
  "v_pk_mul_f32 v[48:49], v[48:49], v[190:191] op_sel_hi:[1,0]\n"
  "v_pk_mul_f32 v[50:51], v[50:51], v[190:191] op_sel_hi:[1,0]\n"
  "v_pk_mul_f32 v[52:53], v[52:53], v[190:191] op_sel_hi:[1,0]\n"
  "v_pk_mul_f32 v[54:55], v[54:55], v[190:191] op_sel_hi:[1,0]\n"
  "v_cvt_pk_bf16_f32 v244, v48, v49\n"
  "v_cvt_pk_bf16_f32 v245, v50, v51\n"
  "v_cvt_pk_bf16_f32 v246, v52, v53\n"
  "v_cvt_pk_bf16_f32 v247, v54, v55\n"
  "global_store_dwordx4 v219, v[244:247], s[42:43] offset:2048 sc1\n"
  "v_mov_b32 v164, v223\n"
  "v_mov_b32 v165, v223\n"
  "global_load_lds_dwordx4 v217, s[20:21] offset:3072 sc1\n"
  "s_add_u32 s20, s20, 16384\n"
  "s_addc_u32 s21, s21, 0\n"
  "v_xor_b32 v216, 0x10000, v216\n"
  "v_add_u32 v216, 0x4000, v216\n"
  "s_waitcnt vmcnt(16)\n"
  "s_waitcnt lgkmcnt(0)\n"
  "s_barrier\n"
  "ds_read_b128 a[128:131], v216 offset:0\n"
  "ds_read_b128 a[132:135], v216 offset:1024\n"
  "ds_read_b128 a[136:139], v216 offset:2048\n"
  "ds_read_b128 a[140:143], v216 offset:3072\n"
  "ds_read_b128 a[144:147], v216 offset:4096\n"
  "ds_read_b128 a[148:151], v216 offset:5120\n"
  "ds_read_b128 a[152:155], v216 offset:6144\n"
  "ds_read_b128 a[156:159], v216 offset:7168\n"
  "ds_read_b128 a[160:163], v216 offset:32768\n"
  "ds_read_b128 a[164:167], v216 offset:33792\n"
  "ds_read_b128 a[168:171], v216 offset:34816\n"
  "ds_read_b128 a[172:175], v216 offset:35840\n"
  "ds_read_b128 a[176:179], v216 offset:36864\n"
  "ds_read_b128 a[180:183], v216 offset:37888\n"
  "ds_read_b128 a[184:187], v216 offset:38912\n"
  "ds_read_b128 a[188:191], v216 offset:39936\n"
  "ds_read_b128 a[64:67], v216 offset:8192\n"
  "ds_read_b128 a[96:99], v216 offset:40960\n"
  "ds_read_b128 a[100:103], v216 offset:41984\n"
  "ds_read_b128 a[68:71], v216 offset:9216\n"
  "ds_read_b128 a[72:75], v216 offset:10240\n"
  "ds_read_b128 a[104:107], v216 offset:43008\n"
  "ds_read_b128 a[108:111], v216 offset:44032\n"
  "ds_read_b128 a[76:79], v216 offset:11264\n"
  "v_pk_mul_f32 v[56:57], v[56:57], v[190:191] op_sel_hi:[1,0]\n"
  "v_pk_mul_f32 v[58:59], v[58:59], v[190:191] op_sel_hi:[1,0]\n"
  "v_pk_mul_f32 v[60:61], v[60:61], v[190:191] op_sel_hi:[1,0]\n"
  "v_pk_mul_f32 v[62:63], v[62:63], v[190:191] op_sel_hi:[1,0]\n"
  "v_cvt_pk_bf16_f32 v248, v56, v57\n"
  "v_cvt_pk_bf16_f32 v249, v58, v59\n"
  "v_cvt_pk_bf16_f32 v250, v60, v61\n"
  "v_cvt_pk_bf16_f32 v251, v62, v63\n"
  "global_store_dwordx4 v219, v[248:251], s[42:43] offset:3072 sc1\n"
  "v_mov_b32 v166, v223\n"
  "v_mov_b32 v167, v223\n"
  "s_add_u32 m0, s49, 32768\n"
  "s_nop 0\n"
  "global_load_lds_dwordx4 v232, s[20:21] offset:0 sc1\n"
  "v_mfma_f32_32x32x16_bf16 v[48:63], v[224:227], v[224:227], 0\n"
  "v_pk_mul_f32 v[64:65], v[64:65], v[192:193] op_sel_hi:[1,0]\n"
  "v_pk_mul_f32 v[66:67], v[66:67], v[192:193] op_sel_hi:[1,0]\n"
  "v_pk_mul_f32 v[68:69], v[68:69], v[192:193] op_sel_hi:[1,0]\n"
  "v_pk_mul_f32 v[70:71], v[70:71], v[192:193] op_sel_hi:[1,0]\n"
  "v_cvt_pk_bf16_f32 v236, v64, v65\n"
  "v_cvt_pk_bf16_f32 v237, v66, v67\n"
  "v_cvt_pk_bf16_f32 v238, v68, v69\n"
  "v_cvt_pk_bf16_f32 v239, v70, v71\n"
  "global_store_dwordx4 v219, v[236:239], s[44:45] offset:0 sc1\n"
  "v_cndmask_b32 v168, v220, v218, s[88:89]\n"
  "v_cndmask_b32 v169, v220, v218, s[90:91]\n"
  "global_load_lds_dwordx4 v232, s[20:21] offset:1024 sc1\n"
  "global_load_lds_dwordx4 v232, s[20:21] offset:2048 sc1\n"
  "v_pk_mul_f32 v[72:73], v[72:73], v[192:193] op_sel_hi:[1,0]\n"
  "v_pk_mul_f32 v[74:75], v[74:75], v[192:193] op_sel_hi:[1,0]\n"
  "v_pk_mul_f32 v[76:77], v[76:77], v[192:193] op_sel_hi:[1,0]\n"
  "v_pk_mul_f32 v[78:79], v[78:79], v[192:193] op_sel_hi:[1,0]\n"
  "v_cvt_pk_bf16_f32 v240, v72, v73\n"
  "v_cvt_pk_bf16_f32 v241, v74, v75\n"
  "v_cvt_pk_bf16_f32 v242, v76, v77\n"
  "v_cvt_pk_bf16_f32 v243, v78, v79\n"
  "global_store_dwordx4 v219, v[240:243], s[44:45] offset:1024 sc1\n"
  "v_cndmask_b32 v170, v220, v218, s[92:93]\n"
  "v_cndmask_b32 v171, v220, v218, s[94:95]\n"
  "global_load_lds_dwordx4 v232, s[20:21] offset:3072 sc1\n"
  "s_add_u32 m0, s49, 49152\n"
  "v_mfma_f32_32x32x16_bf16 v[64:79], v[224:227], v[224:227], 0\n"
  "v_pk_mul_f32 v[80:81], v[80:81], v[192:193] op_sel_hi:[1,0]\n"
  "v_pk_mul_f32 v[82:83], v[82:83], v[192:193] op_sel_hi:[1,0]\n"
  "v_pk_mul_f32 v[84:85], v[84:85], v[192:193] op_sel_hi:[1,0]\n"
  "v_pk_mul_f32 v[86:87], v[86:87], v[192:193] op_sel_hi:[1,0]\n"
  "v_cvt_pk_bf16_f32 v244, v80, v81\n"
  "v_cvt_pk_bf16_f32 v245, v82, v83\n"
  "v_cvt_pk_bf16_f32 v246, v84, v85\n"
  "v_cvt_pk_bf16_f32 v247, v86, v87\n"
  "global_store_dwordx4 v219, v[244:247], s[44:45] offset:2048 sc1\n"
  "v_cndmask_b32 v172, v221, v218, s[88:89]\n"
  "v_cndmask_b32 v173, v221, v218, s[90:91]\n"
  "global_load_lds_dwordx4 v217, s[20:21] offset:0 sc1\n"
  "global_load_lds_dwordx4 v217, s[20:21] offset:1024 sc1\n"
  "v_pk_mul_f32 v[88:89], v[88:89], v[192:193] op_sel_hi:[1,0]\n"
  "v_pk_mul_f32 v[90:91], v[90:91], v[192:193] op_sel_hi:[1,0]\n"
  "v_pk_mul_f32 v[92:93], v[92:93], v[192:193] op_sel_hi:[1,0]\n"
  "v_pk_mul_f32 v[94:95], v[94:95], v[192:193] op_sel_hi:[1,0]\n"
  "v_cvt_pk_bf16_f32 v248, v88, v89\n"
  "v_cvt_pk_bf16_f32 v249, v90, v91\n"
  "v_cvt_pk_bf16_f32 v250, v92, v93\n"
  "v_cvt_pk_bf16_f32 v251, v94, v95\n"
  "global_store_dwordx4 v219, v[248:251], s[44:45] offset:3072 sc1\n"
  "v_cndmask_b32 v174, v221, v218, s[92:93]\n"
  "v_cndmask_b32 v175, v221, v218, s[94:95]\n"
  "global_load_lds_dwordx4 v217, s[20:21] offset:2048 sc1\n"
  "global_load_lds_dwordx4 v217, s[20:21] offset:3072 sc1\n"
  "v_mfma_f32_32x32x16_bf16 v[80:95], v[224:227], v[224:227], 0\n"
  "v_pk_mul_f32 v[96:97], v[96:97], v[194:195] op_sel_hi:[1,0]\n"
  "v_pk_mul_f32 v[98:99], v[98:99], v[194:195] op_sel_hi:[1,0]\n"
  "v_pk_mul_f32 v[100:101], v[100:101], v[194:195] op_sel_hi:[1,0]\n"
  "v_pk_mul_f32 v[102:103], v[102:103], v[194:195] op_sel_hi:[1,0]\n"
  "v_cvt_pk_bf16_f32 v236, v96, v97\n"
  "v_cvt_pk_bf16_f32 v237, v98, v99\n"
  "v_cvt_pk_bf16_f32 v238, v100, v101\n"
  "v_cvt_pk_bf16_f32 v239, v102, v103\n"
  "global_store_dwordx4 v219, v[236:239], s[46:47] offset:0 sc1\n"
  "v_cndmask_b32 v176, v222, v218, s[88:89]\n"
  "v_cndmask_b32 v177, v222, v218, s[90:91]\n"
  "s_sub_u32 s20, s20, 81920\n"
  "s_subb_u32 s21, s21, 0\n"
  "v_pk_mul_f32 v[104:105], v[104:105], v[194:195] op_sel_hi:[1,0]\n"
  "v_pk_mul_f32 v[106:107], v[106:107], v[194:195] op_sel_hi:[1,0]\n"
  "v_pk_mul_f32 v[108:109], v[108:109], v[194:195] op_sel_hi:[1,0]\n"
  "v_pk_mul_f32 v[110:111], v[110:111], v[194:195] op_sel_hi:[1,0]\n"
  "v_cvt_pk_bf16_f32 v240, v104, v105\n"
  "v_cvt_pk_bf16_f32 v241, v106, v107\n"
  "v_cvt_pk_bf16_f32 v242, v108, v109\n"
  "v_cvt_pk_bf16_f32 v243, v110, v111\n"
  "global_store_dwordx4 v219, v[240:243], s[46:47] offset:1024 sc1\n"
  "v_cndmask_b32 v178, v222, v218, s[92:93]\n"
  "v_cndmask_b32 v179, v222, v218, s[94:95]\n"
  "v_mfma_f32_32x32x16_bf16 v[96:111], v[224:227], v[224:227], 0\n"
  "v_pk_mul_f32 v[112:113], v[112:113], v[194:195] op_sel_hi:[1,0]\n"
  "v_pk_mul_f32 v[114:115], v[114:115], v[194:195] op_sel_hi:[1,0]\n"
  "v_pk_mul_f32 v[116:117], v[116:117], v[194:195] op_sel_hi:[1,0]\n"
  "v_pk_mul_f32 v[118:119], v[118:119], v[194:195] op_sel_hi:[1,0]\n"
  "v_cvt_pk_bf16_f32 v244, v112, v113\n"
  "v_cvt_pk_bf16_f32 v245, v114, v115\n"
  "v_cvt_pk_bf16_f32 v246, v116, v117\n"
  "v_cvt_pk_bf16_f32 v247, v118, v119\n"
  "global_store_dwordx4 v219, v[244:247], s[46:47] offset:2048 sc1\n"
  "v_cndmask_b32 v180, v223, v218, s[88:89]\n"
  "v_cndmask_b32 v181, v223, v218, s[90:91]\n"
  "v_pk_mul_f32 v[120:121], v[120:121], v[194:195] op_sel_hi:[1,0]\n"
  "v_pk_mul_f32 v[122:123], v[122:123], v[194:195] op_sel_hi:[1,0]\n"
  "v_pk_mul_f32 v[124:125], v[124:125], v[194:195] op_sel_hi:[1,0]\n"
  "v_pk_mul_f32 v[126:127], v[126:127], v[194:195] op_sel_hi:[1,0]\n"
  "v_cvt_pk_bf16_f32 v248, v120, v121\n"
  "v_cvt_pk_bf16_f32 v249, v122, v123\n"
  "v_cvt_pk_bf16_f32 v250, v124, v125\n"
  "v_cvt_pk_bf16_f32 v251, v126, v127\n"
  "global_store_dwordx4 v219, v[248:251], s[46:47] offset:3072 sc1\n"
  "v_cndmask_b32 v182, v223, v218, s[92:93]\n"
  "v_cndmask_b32 v183, v223, v218, s[94:95]\n"
  "v_mfma_f32_32x32x16_bf16 v[112:127], v[224:227], v[224:227], 0\n"
  "s_waitcnt lgkmcnt(15)\n"
  "v_mfma_f32_16x16x32_bf16 v[128:131], a[128:131], a[0:3], v[168:171]\n"
  "v_mfma_f32_16x16x32_bf16 v[128:131], a[132:135], a[4:7], v[128:131]\n"
  "v_mfma_f32_16x16x32_bf16 v[128:131], a[136:139], a[8:11], v[128:131]\n"
  "v_mfma_f32_16x16x32_bf16 v[128:131], a[140:143], a[12:15], v[128:131]\n"
  "s_nop 7\n"
  "v_exp_f32 v128, v128\n"
  "v_exp_f32 v129, v129\n"
  "v_exp_f32 v130, v130\n"
  "v_exp_f32 v131, v131\n"
  "v_cvt_pk_bf16_f32 v144, v128, v129\n"
  "v_cvt_pk_bf16_f32 v145, v130, v131\n"
  "v_mov_b32 v146, 0\n"
  "v_mov_b32 v147, 0\n"
  "s_xor_b32 s49, s49, 0x10000\n"
  "s_setpc_b64 s[22:23]\n"
  "L_IP2_%=:\n"
  "s_mov_b32 s36, 0x1f800000\n"
  "s_mov_b32 s37, 0x5f800000\n"
  "s_mov_b64 s[60:61], 0\n"
  "v_cmp_nle_f32 vcc, s36, v200\n"
  "v_cmp_nge_f32 s[96:97], s37, v200\n"
  "s_or_b64 s[60:61], s[60:61], vcc\n"
  "s_or_b64 s[60:61], s[60:61], s[96:97]\n"
  "v_cmp_nle_f32 vcc, s36, v204\n"
  "v_cmp_nge_f32 s[96:97], s37, v204\n"
  "s_or_b64 s[60:61], s[60:61], vcc\n"
  "s_or_b64 s[60:61], s[60:61], s[96:97]\n"
  "v_cmp_nle_f32 vcc, s36, v208\n"
  "v_cmp_nge_f32 s[96:97], s37, v208\n"
  "s_or_b64 s[60:61], s[60:61], vcc\n"
  "s_or_b64 s[60:61], s[60:61], s[96:97]\n"
  "v_cmp_nle_f32 vcc, s36, v212\n"
  "v_cmp_nge_f32 s[96:97], s37, v212\n"
  "s_or_b64 s[60:61], s[60:61], vcc\n"
  "s_or_b64 s[60:61], s[60:61], s[96:97]\n"
  "s_cbranch_scc0 L_IP2_go_%=\n"
  "s_call_b64 s[18:19], L_slow0_%=\n"
  "s_call_b64 s[18:19], L_slow1_%=\n"
  "s_call_b64 s[18:19], L_slow2_%=\n"
  "s_call_b64 s[18:19], L_slow3_%=\n"
  "L_IP2_go_%=:\n"
  "s_waitcnt lgkmcnt(0)\n"
  "s_barrier\n"
  "s_and_b32 s36, s35, 1\n"
  "s_lshl_b32 s36, s36, 14\n"
  "s_add_u32 s36, s36, 131072\n"
  "s_add_u32 s36, s36, s38\n"
  "v_add_u32 v234, s36, v232\n"
  "ds_read_b128 a[0:3], v234 offset:0\n"
  "ds_read_b128 a[4:7], v234 offset:1024\n"
  "ds_read_b128 a[8:11], v234 offset:2048\n"
  "ds_read_b128 a[12:15], v234 offset:3072\n"
  "ds_read_b128 a[16:19], v234 offset:4096\n"
  "ds_read_b128 a[20:23], v234 offset:5120\n"
  "ds_read_b128 a[24:27], v234 offset:6144\n"
  "ds_read_b128 a[28:31], v234 offset:7168\n"
  "ds_read_b128 a[32:35], v234 offset:8192\n"
  "ds_read_b128 a[36:39], v234 offset:9216\n"
  "ds_read_b128 a[40:43], v234 offset:10240\n"
  "ds_read_b128 a[44:47], v234 offset:11264\n"
  "ds_read_b128 a[48:51], v234 offset:12288\n"
  "ds_read_b128 a[52:55], v234 offset:13312\n"
  "ds_read_b128 a[56:59], v234 offset:14336\n"
  "ds_read_b128 a[60:63], v234 offset:15360\n"
  "s_lshl_b32 s36, s39, 12\n"
  "v_add_u32 v219, s36, v232\n"
  "v_rcp_f32 v188, v200\n"
  "v_rcp_f32 v190, v204\n"
  "v_rcp_f32 v192, v208\n"
  "v_rcp_f32 v194, v212\n"
  "v_pk_mul_f32 v[0:1], v[0:1], v[188:189] op_sel_hi:[1,0]\n"
  "v_pk_mul_f32 v[2:3], v[2:3], v[188:189] op_sel_hi:[1,0]\n"
  "v_pk_mul_f32 v[4:5], v[4:5], v[188:189] op_sel_hi:[1,0]\n"
  "v_pk_mul_f32 v[6:7], v[6:7], v[188:189] op_sel_hi:[1,0]\n"
  "v_cvt_pk_bf16_f32 v236, v0, v1\n"
  "v_cvt_pk_bf16_f32 v237, v2, v3\n"
  "v_cvt_pk_bf16_f32 v238, v4, v5\n"
  "v_cvt_pk_bf16_f32 v239, v6, v7\n"
  "global_store_dwordx4 v219, v[236:239], s[40:41] offset:0 sc1\n"
  "v_mov_b32 v152, v220\n"
  "v_mov_b32 v153, v220\n"
  "s_add_u32 s24, s24, 1\n"
  "v_readlane_b32 s22, v252, s24\n"
  "v_mfma_f32_32x32x16_bf16 v[200:215], v[224:227], v[224:227], 0\n"
  "v_pk_mul_f32 v[8:9], v[8:9], v[188:189] op_sel_hi:[1,0]\n"
  "v_pk_mul_f32 v[10:11], v[10:11], v[188:189] op_sel_hi:[1,0]\n"
  "v_pk_mul_f32 v[12:13], v[12:13], v[188:189] op_sel_hi:[1,0]\n"
  "v_pk_mul_f32 v[14:15], v[14:15], v[188:189] op_sel_hi:[1,0]\n"
  "v_cvt_pk_bf16_f32 v240, v8, v9\n"
  "v_cvt_pk_bf16_f32 v241, v10, v11\n"
  "v_cvt_pk_bf16_f32 v242, v12, v13\n"
  "v_cvt_pk_bf16_f32 v243, v14, v15\n"
  "global_store_dwordx4 v219, v[240:243], s[40:41] offset:1024 sc1\n"
  "v_mov_b32 v154, v220\n"
  "v_mov_b32 v155, v220\n"
  "s_add_u32 s22, s26, s22\n"
  "s_addc_u32 s23, s27, 0\n"
  "s_add_u32 m0, s49, 0\n"
  "v_mfma_f32_32x32x16_bf16 v[0:15], v[224:227], v[224:227], 0\n"
  "v_pk_mul_f32 v[16:17], v[16:17], v[188:189] op_sel_hi:[1,0]\n"
  "v_pk_mul_f32 v[18:19], v[18:19], v[188:189] op_sel_hi:[1,0]\n"
  "v_pk_mul_f32 v[20:21], v[20:21], v[188:189] op_sel_hi:[1,0]\n"
  "v_pk_mul_f32 v[22:23], v[22:23], v[188:189] op_sel_hi:[1,0]\n"
  "v_cvt_pk_bf16_f32 v244, v16, v17\n"
  "v_cvt_pk_bf16_f32 v245, v18, v19\n"
  "v_cvt_pk_bf16_f32 v246, v20, v21\n"
  "v_cvt_pk_bf16_f32 v247, v22, v23\n"
  "global_store_dwordx4 v219, v[244:247], s[40:41] offset:2048 sc1\n"
  "v_mov_b32 v156, v221\n"
  "v_mov_b32 v157, v221\n"
  "global_load_lds_dwordx4 v232, s[20:21] offset:0 sc1\n"
  "global_load_lds_dwordx4 v232, s[20:21] offset:1024 sc1\n"
  "v_pk_mul_f32 v[24:25], v[24:25], v[188:189] op_sel_hi:[1,0]\n"
  "v_pk_mul_f32 v[26:27], v[26:27], v[188:189] op_sel_hi:[1,0]\n"
  "v_pk_mul_f32 v[28:29], v[28:29], v[188:189] op_sel_hi:[1,0]\n"
  "v_pk_mul_f32 v[30:31], v[30:31], v[188:189] op_sel_hi:[1,0]\n"
  "v_cvt_pk_bf16_f32 v248, v24, v25\n"
  "v_cvt_pk_bf16_f32 v249, v26, v27\n"
  "v_cvt_pk_bf16_f32 v250, v28, v29\n"
  "v_cvt_pk_bf16_f32 v251, v30, v31\n"
  "global_store_dwordx4 v219, v[248:251], s[40:41] offset:3072 sc1\n"
  "v_mov_b32 v158, v221\n"
  "v_mov_b32 v159, v221\n"
  "global_load_lds_dwordx4 v232, s[20:21] offset:2048 sc1\n"
  "global_load_lds_dwordx4 v232, s[20:21] offset:3072 sc1\n"
  "v_mfma_f32_32x32x16_bf16 v[16:31], v[224:227], v[224:227], 0\n"
  "v_pk_mul_f32 v[32:33], v[32:33], v[190:191] op_sel_hi:[1,0]\n"
  "v_pk_mul_f32 v[34:35], v[34:35], v[190:191] op_sel_hi:[1,0]\n"
  "v_pk_mul_f32 v[36:37], v[36:37], v[190:191] op_sel_hi:[1,0]\n"
  "v_pk_mul_f32 v[38:39], v[38:39], v[190:191] op_sel_hi:[1,0]\n"
  "v_cvt_pk_bf16_f32 v236, v32, v33\n"
  "v_cvt_pk_bf16_f32 v237, v34, v35\n"
  "v_cvt_pk_bf16_f32 v238, v36, v37\n"
  "v_cvt_pk_bf16_f32 v239, v38, v39\n"
  "global_store_dwordx4 v219, v[236:239], s[42:43] offset:0 sc1\n"
  "v_mov_b32 v160, v222\n"
  "v_mov_b32 v161, v222\n"
  "s_add_u32 m0, s49, 16384\n"
  "s_nop 0\n"
  "global_load_lds_dwordx4 v217, s[20:21] offset:0 sc1\n"
  "v_pk_mul_f32 v[40:41], v[40:41], v[190:191] op_sel_hi:[1,0]\n"
  "v_pk_mul_f32 v[42:43], v[42:43], v[190:191] op_sel_hi:[1,0]\n"
  "v_pk_mul_f32 v[44:45], v[44:45], v[190:191] op_sel_hi:[1,0]\n"
  "v_pk_mul_f32 v[46:47], v[46:47], v[190:191] op_sel_hi:[1,0]\n"
  "v_cvt_pk_bf16_f32 v240, v40, v41\n"
  "v_cvt_pk_bf16_f32 v241, v42, v43\n"
  "v_cvt_pk_bf16_f32 v242, v44, v45\n"
  "v_cvt_pk_bf16_f32 v243, v46, v47\n"
  "global_store_dwordx4 v219, v[240:243], s[42:43] offset:1024 sc1\n"
  "v_mov_b32 v162, v222\n"
  "v_mov_b32 v163, v222\n"
  "global_load_lds_dwordx4 v217, s[20:21] offset:1024 sc1\n"
  "global_load_lds_dwordx4 v217, s[20:21] offset:2048 sc1\n"
  "v_mfma_f32_32x32x16_bf16 v[32:47], v[224:227], v[224:227], 0\n"
  "v_pk_mul_f32 v[48:49], v[48:49], v[190:191] op_sel_hi:[1,0]\n"
  "v_pk_mul_f32 v[50:51], v[50:51], v[190:191] op_sel_hi:[1,0]\n"
  "v_pk_mul_f32 v[52:53], v[52:53], v[190:191] op_sel_hi:[1,0]\n"
  "v_pk_mul_f32 v[54:55], v[54:55], v[190:191] op_sel_hi:[1,0]\n"
  "v_cvt_pk_bf16_f32 v244, v48, v49\n"
  "v_cvt_pk_bf16_f32 v245, v50, v51\n"
  "v_cvt_pk_bf16_f32 v246, v52, v53\n"
  "v_cvt_pk_bf16_f32 v247, v54, v55\n"
  "global_store_dwordx4 v219, v[244:247], s[42:43] offset:2048 sc1\n"
  "v_mov_b32 v164, v223\n"
  "v_mov_b32 v165, v223\n"
  "global_load_lds_dwordx4 v217, s[20:21] offset:3072 sc1\n"
  "s_add_u32 s20, s20, 16384\n"
  "s_addc_u32 s21, s21, 0\n"
  "v_xor_b32 v216, 0x10000, v216\n"
  "v_add_u32 v216, 0x4000, v216\n"
  "s_waitcnt vmcnt(16)\n"
  "s_waitcnt lgkmcnt(0)\n"
  "s_barrier\n"
  "ds_read_b128 a[128:131], v216 offset:0\n"
  "ds_read_b128 a[132:135], v216 offset:1024\n"
  "ds_read_b128 a[136:139], v216 offset:2048\n"
  "ds_read_b128 a[140:143], v216 offset:3072\n"
  "ds_read_b128 a[144:147], v216 offset:4096\n"
  "ds_read_b128 a[148:151], v216 offset:5120\n"
  "ds_read_b128 a[152:155], v216 offset:6144\n"
  "ds_read_b128 a[156:159], v216 offset:7168\n"
  "ds_read_b128 a[160:163], v216 offset:32768\n"
  "ds_read_b128 a[164:167], v216 offset:33792\n"
  "ds_read_b128 a[168:171], v216 offset:34816\n"
  "ds_read_b128 a[172:175], v216 offset:35840\n"
  "ds_read_b128 a[176:179], v216 offset:36864\n"
  "ds_read_b128 a[180:183], v216 offset:37888\n"
  "ds_read_b128 a[184:187], v216 offset:38912\n"
  "ds_read_b128 a[188:191], v216 offset:39936\n"
  "ds_read_b128 a[64:67], v216 offset:8192\n"
  "ds_read_b128 a[96:99], v216 offset:40960\n"
  "ds_read_b128 a[100:103], v216 offset:41984\n"
  "ds_read_b128 a[68:71], v216 offset:9216\n"
  "ds_read_b128 a[72:75], v216 offset:10240\n"
  "ds_read_b128 a[104:107], v216 offset:43008\n"
  "ds_read_b128 a[108:111], v216 offset:44032\n"
  "ds_read_b128 a[76:79], v216 offset:11264\n"
  "v_pk_mul_f32 v[56:57], v[56:57], v[190:191] op_sel_hi:[1,0]\n"
  "v_pk_mul_f32 v[58:59], v[58:59], v[190:191] op_sel_hi:[1,0]\n"
  "v_pk_mul_f32 v[60:61], v[60:61], v[190:191] op_sel_hi:[1,0]\n"
  "v_pk_mul_f32 v[62:63], v[62:63], v[190:191] op_sel_hi:[1,0]\n"
  "v_cvt_pk_bf16_f32 v248, v56, v57\n"
  "v_cvt_pk_bf16_f32 v249, v58, v59\n"
  "v_cvt_pk_bf16_f32 v250, v60, v61\n"
  "v_cvt_pk_bf16_f32 v251, v62, v63\n"
  "global_store_dwordx4 v219, v[248:251], s[42:43] offset:3072 sc1\n"
  "v_mov_b32 v166, v223\n"
  "v_mov_b32 v167, v223\n"
  "s_add_u32 m0, s49, 32768\n"
  "s_nop 0\n"
  "global_load_lds_dwordx4 v232, s[20:21] offset:0 sc1\n"
  "v_mfma_f32_32x32x16_bf16 v[48:63], v[224:227], v[224:227], 0\n"
  "v_pk_mul_f32 v[64:65], v[64:65], v[192:193] op_sel_hi:[1,0]\n"
  "v_pk_mul_f32 v[66:67], v[66:67], v[192:193] op_sel_hi:[1,0]\n"
  "v_pk_mul_f32 v[68:69], v[68:69], v[192:193] op_sel_hi:[1,0]\n"
  "v_pk_mul_f32 v[70:71], v[70:71], v[192:193] op_sel_hi:[1,0]\n"
  "v_cvt_pk_bf16_f32 v236, v64, v65\n"
  "v_cvt_pk_bf16_f32 v237, v66, v67\n"
  "v_cvt_pk_bf16_f32 v238, v68, v69\n"
  "v_cvt_pk_bf16_f32 v239, v70, v71\n"
  "global_store_dwordx4 v219, v[236:239], s[44:45] offset:0 sc1\n"
  "v_cndmask_b32 v168, v220, v218, s[88:89]\n"
  "v_cndmask_b32 v169, v220, v218, s[90:91]\n"
  "global_load_lds_dwordx4 v232, s[20:21] offset:1024 sc1\n"
  "global_load_lds_dwordx4 v232, s[20:21] offset:2048 sc1\n"
  "v_pk_mul_f32 v[72:73], v[72:73], v[192:193] op_sel_hi:[1,0]\n"
  "v_pk_mul_f32 v[74:75], v[74:75], v[192:193] op_sel_hi:[1,0]\n"
  "v_pk_mul_f32 v[76:77], v[76:77], v[192:193] op_sel_hi:[1,0]\n"
  "v_pk_mul_f32 v[78:79], v[78:79], v[192:193] op_sel_hi:[1,0]\n"
  "v_cvt_pk_bf16_f32 v240, v72, v73\n"
  "v_cvt_pk_bf16_f32 v241, v74, v75\n"
  "v_cvt_pk_bf16_f32 v242, v76, v77\n"
  "v_cvt_pk_bf16_f32 v243, v78, v79\n"
  "global_store_dwordx4 v219, v[240:243], s[44:45] offset:1024 sc1\n"
  "v_cndmask_b32 v170, v220, v218, s[92:93]\n"
  "v_cndmask_b32 v171, v220, v218, s[94:95]\n"
  "global_load_lds_dwordx4 v232, s[20:21] offset:3072 sc1\n"
  "s_add_u32 m0, s49, 49152\n"
  "v_mfma_f32_32x32x16_bf16 v[64:79], v[224:227], v[224:227], 0\n"
  "v_pk_mul_f32 v[80:81], v[80:81], v[192:193] op_sel_hi:[1,0]\n"
  "v_pk_mul_f32 v[82:83], v[82:83], v[192:193] op_sel_hi:[1,0]\n"
  "v_pk_mul_f32 v[84:85], v[84:85], v[192:193] op_sel_hi:[1,0]\n"
  "v_pk_mul_f32 v[86:87], v[86:87], v[192:193] op_sel_hi:[1,0]\n"
  "v_cvt_pk_bf16_f32 v244, v80, v81\n"
  "v_cvt_pk_bf16_f32 v245, v82, v83\n"
  "v_cvt_pk_bf16_f32 v246, v84, v85\n"
  "v_cvt_pk_bf16_f32 v247, v86, v87\n"
  "global_store_dwordx4 v219, v[244:247], s[44:45] offset:2048 sc1\n"
  "v_cndmask_b32 v172, v221, v218, s[88:89]\n"
  "v_cndmask_b32 v173, v221, v218, s[90:91]\n"
  "global_load_lds_dwordx4 v217, s[20:21] offset:0 sc1\n"
  "global_load_lds_dwordx4 v217, s[20:21] offset:1024 sc1\n"
  "v_pk_mul_f32 v[88:89], v[88:89], v[192:193] op_sel_hi:[1,0]\n"
  "v_pk_mul_f32 v[90:91], v[90:91], v[192:193] op_sel_hi:[1,0]\n"
  "v_pk_mul_f32 v[92:93], v[92:93], v[192:193] op_sel_hi:[1,0]\n"
  "v_pk_mul_f32 v[94:95], v[94:95], v[192:193] op_sel_hi:[1,0]\n"
  "v_cvt_pk_bf16_f32 v248, v88, v89\n"
  "v_cvt_pk_bf16_f32 v249, v90, v91\n"
  "v_cvt_pk_bf16_f32 v250, v92, v93\n"
  "v_cvt_pk_bf16_f32 v251, v94, v95\n"
  "global_store_dwordx4 v219, v[248:251], s[44:45] offset:3072 sc1\n"
  "v_cndmask_b32 v174, v221, v218, s[92:93]\n"
  "v_cndmask_b32 v175, v221, v218, s[94:95]\n"
  "global_load_lds_dwordx4 v217, s[20:21] offset:2048 sc1\n"
  "global_load_lds_dwordx4 v217, s[20:21] offset:3072 sc1\n"
  "v_mfma_f32_32x32x16_bf16 v[80:95], v[224:227], v[224:227], 0\n"
  "v_pk_mul_f32 v[96:97], v[96:97], v[194:195] op_sel_hi:[1,0]\n"
  "v_pk_mul_f32 v[98:99], v[98:99], v[194:195] op_sel_hi:[1,0]\n"
  "v_pk_mul_f32 v[100:101], v[100:101], v[194:195] op_sel_hi:[1,0]\n"
  "v_pk_mul_f32 v[102:103], v[102:103], v[194:195] op_sel_hi:[1,0]\n"
  "v_cvt_pk_bf16_f32 v236, v96, v97\n"
  "v_cvt_pk_bf16_f32 v237, v98, v99\n"
  "v_cvt_pk_bf16_f32 v238, v100, v101\n"
  "v_cvt_pk_bf16_f32 v239, v102, v103\n"
  "global_store_dwordx4 v219, v[236:239], s[46:47] offset:0 sc1\n"
  "v_cndmask_b32 v176, v222, v218, s[88:89]\n"
  "v_cndmask_b32 v177, v222, v218, s[90:91]\n"
  "s_sub_u32 s20, s20, 81920\n"
  "s_subb_u32 s21, s21, 0\n"
  "v_pk_mul_f32 v[104:105], v[104:105], v[194:195] op_sel_hi:[1,0]\n"
  "v_pk_mul_f32 v[106:107], v[106:107], v[194:195] op_sel_hi:[1,0]\n"
  "v_pk_mul_f32 v[108:109], v[108:109], v[194:195] op_sel_hi:[1,0]\n"
  "v_pk_mul_f32 v[110:111], v[110:111], v[194:195] op_sel_hi:[1,0]\n"
  "v_cvt_pk_bf16_f32 v240, v104, v105\n"
  "v_cvt_pk_bf16_f32 v241, v106, v107\n"
  "v_cvt_pk_bf16_f32 v242, v108, v109\n"
  "v_cvt_pk_bf16_f32 v243, v110, v111\n"
  "global_store_dwordx4 v219, v[240:243], s[46:47] offset:1024 sc1\n"
  "v_cndmask_b32 v178, v222, v218, s[92:93]\n"
  "v_cndmask_b32 v179, v222, v218, s[94:95]\n"
  "v_mfma_f32_32x32x16_bf16 v[96:111], v[224:227], v[224:227], 0\n"
  "v_pk_mul_f32 v[112:113], v[112:113], v[194:195] op_sel_hi:[1,0]\n"
  "v_pk_mul_f32 v[114:115], v[114:115], v[194:195] op_sel_hi:[1,0]\n"
  "v_pk_mul_f32 v[116:117], v[116:117], v[194:195] op_sel_hi:[1,0]\n"
  "v_pk_mul_f32 v[118:119], v[118:119], v[194:195] op_sel_hi:[1,0]\n"
  "v_cvt_pk_bf16_f32 v244, v112, v113\n"
  "v_cvt_pk_bf16_f32 v245, v114, v115\n"
  "v_cvt_pk_bf16_f32 v246, v116, v117\n"
  "v_cvt_pk_bf16_f32 v247, v118, v119\n"
  "global_store_dwordx4 v219, v[244:247], s[46:47] offset:2048 sc1\n"
  "v_cndmask_b32 v180, v223, v218, s[88:89]\n"
  "v_cndmask_b32 v181, v223, v218, s[90:91]\n"
  "v_pk_mul_f32 v[120:121], v[120:121], v[194:195] op_sel_hi:[1,0]\n"
  "v_pk_mul_f32 v[122:123], v[122:123], v[194:195] op_sel_hi:[1,0]\n"
  "v_pk_mul_f32 v[124:125], v[124:125], v[194:195] op_sel_hi:[1,0]\n"
  "v_pk_mul_f32 v[126:127], v[126:127], v[194:195] op_sel_hi:[1,0]\n"
  "v_cvt_pk_bf16_f32 v248, v120, v121\n"
  "v_cvt_pk_bf16_f32 v249, v122, v123\n"
  "v_cvt_pk_bf16_f32 v250, v124, v125\n"
  "v_cvt_pk_bf16_f32 v251, v126, v127\n"
  "global_store_dwordx4 v219, v[248:251], s[46:47] offset:3072 sc1\n"
  "v_cndmask_b32 v182, v223, v218, s[92:93]\n"
  "v_cndmask_b32 v183, v223, v218, s[94:95]\n"
  "v_mfma_f32_32x32x16_bf16 v[112:127], v[224:227], v[224:227], 0\n"
  "s_waitcnt lgkmcnt(15)\n"
  "v_mfma_f32_16x16x32_bf16 v[128:131], a[128:131], a[0:3], v[152:155]\n"
  "v_mfma_f32_16x16x32_bf16 v[128:131], a[132:135], a[4:7], v[128:131]\n"
  "v_mfma_f32_16x16x32_bf16 v[128:131], a[136:139], a[8:11], v[128:131]\n"
  "v_mfma_f32_16x16x32_bf16 v[128:131], a[140:143], a[12:15], v[128:131]\n"
  "v_mfma_f32_16x16x32_bf16 v[132:135], a[156:159], a[12:15], v[152:155]\n"
  "v_mfma_f32_16x16x32_bf16 v[132:135], a[152:155], a[8:11], v[132:135]\n"
  "v_mfma_f32_16x16x32_bf16 v[132:135], a[148:151], a[4:7], v[132:135]\n"
  "s_nop 4\n"
  "v_exp_f32 v128, v128\n"
  "v_mfma_f32_16x16x32_bf16 v[132:135], a[144:147], a[0:3], v[132:135]\n"
  "v_exp_f32 v129, v129\n"
  "v_mfma_f32_16x16x32_bf16 v[136:139], a[160:163], a[0:3], v[152:155]\n"
  "v_exp_f32 v130, v130\n"
  "s_waitcnt lgkmcnt(14)\n"
  "v_mfma_f32_16x16x32_bf16 v[136:139], a[164:167], a[4:7], v[136:139]\n"
  "v_exp_f32 v131, v131\n"
  "s_waitcnt lgkmcnt(13)\n"
  "v_mfma_f32_16x16x32_bf16 v[136:139], a[168:171], a[8:11], v[136:139]\n"
  "v_exp_f32 v132, v132\n"
  "s_waitcnt lgkmcnt(12)\n"
  "v_mfma_f32_16x16x32_bf16 v[136:139], a[172:175], a[12:15], v[136:139]\n"
  "v_exp_f32 v133, v133\n"
  "s_waitcnt lgkmcnt(8)\n"
  "v_mfma_f32_16x16x32_bf16 v[140:143], a[188:191], a[12:15], v[152:155]\n"
  "v_exp_f32 v134, v134\n"
  "v_mfma_f32_16x16x32_bf16 v[140:143], a[184:187], a[8:11], v[140:143]\n"
  "v_exp_f32 v135, v135\n"
  "v_mfma_f32_16x16x32_bf16 v[140:143], a[180:183], a[4:7], v[140:143]\n"
  "s_nop 0\n"
  "v_exp_f32 v136, v136\n"
  "v_mfma_f32_16x16x32_bf16 v[140:143], a[176:179], a[0:3], v[140:143]\n"
  "v_exp_f32 v137, v137\n"
  "v_exp_f32 v138, v138\n"
  "v_exp_f32 v139, v139\n"
  "s_nop 4\n"
  "v_exp_f32 v140, v140\n"
  "v_exp_f32 v141, v141\n"
  "v_exp_f32 v142, v142\n"
  "v_exp_f32 v143, v143\n"
  "v_cvt_pk_bf16_f32 v144, v128, v129\n"
  "v_cvt_pk_bf16_f32 v145, v130, v131\n"
  "v_cvt_pk_bf16_f32 v146, v132, v133\n"
  "v_cvt_pk_bf16_f32 v147, v134, v135\n"
  "v_cvt_pk_bf16_f32 v148, v136, v137\n"
  "v_cvt_pk_bf16_f32 v149, v138, v139\n"
  "v_cvt_pk_bf16_f32 v150, v140, v141\n"
  "v_cvt_pk_bf16_f32 v151, v142, v143\n"
  "s_xor_b32 s49, s49, 0x10000\n"
  "s_setpc_b64 s[22:23]\n"
  "L_IA_%=:\n"
  "s_waitcnt vmcnt(0)\n"
  "v_accvgpr_read_b32 v234, a244\n"
  "v_accvgpr_read_b32 v235, a245\n"
  "v_cmp_ne_u64 s[96:97], s[16:17], v[234:235]\n"
  "s_nop 0\n"
  "s_cmp_lg_u64 s[96:97], 0\n"
  "s_cbranch_scc1 L_hIA_retry_%=\n"
  "L_hIA_ok_%=:\n"
  "s_mov_b32 s36, 0x1f800000\n"
  "s_mov_b32 s37, 0x5f800000\n"
  "s_mov_b64 s[60:61], 0\n"
  "v_cmp_nle_f32 vcc, s36, v200\n"
  "v_cmp_nge_f32 s[96:97], s37, v200\n"
  "s_or_b64 s[60:61], s[60:61], vcc\n"
  "s_or_b64 s[60:61], s[60:61], s[96:97]\n"
  "v_cmp_nle_f32 vcc, s36, v204\n"
  "v_cmp_nge_f32 s[96:97], s37, v204\n"
  "s_or_b64 s[60:61], s[60:61], vcc\n"
  "s_or_b64 s[60:61], s[60:61], s[96:97]\n"
  "v_cmp_nle_f32 vcc, s36, v208\n"
  "v_cmp_nge_f32 s[96:97], s37, v208\n"
  "s_or_b64 s[60:61], s[60:61], vcc\n"
  "s_or_b64 s[60:61], s[60:61], s[96:97]\n"
  "v_cmp_nle_f32 vcc, s36, v212\n"
  "v_cmp_nge_f32 s[96:97], s37, v212\n"
  "s_or_b64 s[60:61], s[60:61], vcc\n"
  "s_or_b64 s[60:61], s[60:61], s[96:97]\n"
  "s_cbranch_scc0 L_IA_go_%=\n"
  "s_call_b64 s[18:19], L_slow0_%=\n"
  "s_call_b64 s[18:19], L_slow1_%=\n"
  "s_call_b64 s[18:19], L_slow2_%=\n"
  "s_call_b64 s[18:19], L_slow3_%=\n"
  "L_IA_go_%=:\n"
  "s_waitcnt lgkmcnt(0)\n"
  "s_barrier\n"
  "s_and_b32 s36, s35, 1\n"
  "s_lshl_b32 s36, s36, 14\n"
  "s_add_u32 s36, s36, 131072\n"
  "s_add_u32 s36, s36, s38\n"
  "v_add_u32 v234, s36, v232\n"
  "ds_read_b128 a[0:3], v234 offset:0\n"
  "ds_read_b128 a[4:7], v234 offset:1024\n"
  "ds_read_b128 a[8:11], v234 offset:2048\n"
  "ds_read_b128 a[12:15], v234 offset:3072\n"
  "ds_read_b128 a[16:19], v234 offset:4096\n"
  "ds_read_b128 a[20:23], v234 offset:5120\n"
  "ds_read_b128 a[24:27], v234 offset:6144\n"
  "ds_read_b128 a[28:31], v234 offset:7168\n"
  "ds_read_b128 a[32:35], v234 offset:8192\n"
  "ds_read_b128 a[36:39], v234 offset:9216\n"
  "ds_read_b128 a[40:43], v234 offset:10240\n"
  "ds_read_b128 a[44:47], v234 offset:11264\n"
  "ds_read_b128 a[48:51], v234 offset:12288\n"
  "ds_read_b128 a[52:55], v234 offset:13312\n"
  "ds_read_b128 a[56:59], v234 offset:14336\n"
  "ds_read_b128 a[60:63], v234 offset:15360\n"
  "s_lshl_b32 s36, s39, 12\n"
  "v_add_u32 v219, s36, v232\n"
  "v_rcp_f32 v188, v200\n"
  "v_rcp_f32 v190, v204\n"
  "v_rcp_f32 v192, v208\n"
  "v_rcp_f32 v194, v212\n"
  "v_pk_mul_f32 v[0:1], v[0:1], v[188:189] op_sel_hi:[1,0]\n"
  "v_pk_mul_f32 v[2:3], v[2:3], v[188:189] op_sel_hi:[1,0]\n"
  "v_pk_mul_f32 v[4:5], v[4:5], v[188:189] op_sel_hi:[1,0]\n"
  "v_pk_mul_f32 v[6:7], v[6:7], v[188:189] op_sel_hi:[1,0]\n"
  "v_cvt_pk_bf16_f32 v236, v0, v1\n"
  "v_cvt_pk_bf16_f32 v237, v2, v3\n"
  "v_cvt_pk_bf16_f32 v238, v4, v5\n"
  "v_cvt_pk_bf16_f32 v239, v6, v7\n"
  "global_store_dwordx4 v219, v[236:239], s[40:41] offset:0 sc1\n"
  "v_mov_b32 v152, v220\n"
  "v_mov_b32 v153, v220\n"
  "s_add_u32 s24, s24, 1\n"
  "v_readlane_b32 s22, v252, s24\n"
  "v_mfma_f32_32x32x16_bf16 v[200:215], v[224:227], v[224:227], 0\n"
  "v_pk_mul_f32 v[8:9], v[8:9], v[188:189] op_sel_hi:[1,0]\n"
  "v_pk_mul_f32 v[10:11], v[10:11], v[188:189] op_sel_hi:[1,0]\n"
  "v_pk_mul_f32 v[12:13], v[12:13], v[188:189] op_sel_hi:[1,0]\n"
  "v_pk_mul_f32 v[14:15], v[14:15], v[188:189] op_sel_hi:[1,0]\n"
  "v_cvt_pk_bf16_f32 v240, v8, v9\n"
  "v_cvt_pk_bf16_f32 v241, v10, v11\n"
  "v_cvt_pk_bf16_f32 v242, v12, v13\n"
  "v_cvt_pk_bf16_f32 v243, v14, v15\n"
  "global_store_dwordx4 v219, v[240:243], s[40:41] offset:1024 sc1\n"
  "v_mov_b32 v154, v220\n"
  "v_mov_b32 v155, v220\n"
  "s_add_u32 s22, s26, s22\n"
  "s_addc_u32 s23, s27, 0\n"
  "s_add_u32 m0, s48, 0\n"
  "v_mfma_f32_32x32x16_bf16 v[0:15], v[224:227], v[224:227], 0\n"
  "v_pk_mul_f32 v[16:17], v[16:17], v[188:189] op_sel_hi:[1,0]\n"
  "v_pk_mul_f32 v[18:19], v[18:19], v[188:189] op_sel_hi:[1,0]\n"
  "v_pk_mul_f32 v[20:21], v[20:21], v[188:189] op_sel_hi:[1,0]\n"
  "v_pk_mul_f32 v[22:23], v[22:23], v[188:189] op_sel_hi:[1,0]\n"
  "v_cvt_pk_bf16_f32 v244, v16, v17\n"
  "v_cvt_pk_bf16_f32 v245, v18, v19\n"
  "v_cvt_pk_bf16_f32 v246, v20, v21\n"
  "v_cvt_pk_bf16_f32 v247, v22, v23\n"
  "global_store_dwordx4 v219, v[244:247], s[40:41] offset:2048 sc1\n"
  "v_mov_b32 v156, v221\n"
  "v_mov_b32 v157, v221\n"
  "global_load_lds_dwordx4 v232, s[50:51] sc1\n"
  "global_load_lds_dwordx4 v228, s[50:51] offset:1024 sc1\n"
  "v_pk_mul_f32 v[24:25], v[24:25], v[188:189] op_sel_hi:[1,0]\n"
  "v_pk_mul_f32 v[26:27], v[26:27], v[188:189] op_sel_hi:[1,0]\n"
  "v_pk_mul_f32 v[28:29], v[28:29], v[188:189] op_sel_hi:[1,0]\n"
  "v_pk_mul_f32 v[30:31], v[30:31], v[188:189] op_sel_hi:[1,0]\n"
  "v_cvt_pk_bf16_f32 v248, v24, v25\n"
  "v_cvt_pk_bf16_f32 v249, v26, v27\n"
  "v_cvt_pk_bf16_f32 v250, v28, v29\n"
  "v_cvt_pk_bf16_f32 v251, v30, v31\n"
  "global_store_dwordx4 v219, v[248:251], s[40:41] offset:3072 sc1\n"
  "v_mov_b32 v158, v221\n"
  "v_mov_b32 v159, v221\n"
  "global_load_lds_dwordx4 v229, s[52:53] offset:2048\n"
  "global_load_lds_dwordx4 v230, s[52:53] offset:3072\n"
  "v_mfma_f32_32x32x16_bf16 v[16:31], v[224:227], v[224:227], 0\n"
  "v_pk_mul_f32 v[32:33], v[32:33], v[190:191] op_sel_hi:[1,0]\n"
  "v_pk_mul_f32 v[34:35], v[34:35], v[190:191] op_sel_hi:[1,0]\n"
  "v_pk_mul_f32 v[36:37], v[36:37], v[190:191] op_sel_hi:[1,0]\n"
  "v_pk_mul_f32 v[38:39], v[38:39], v[190:191] op_sel_hi:[1,0]\n"
  "v_cvt_pk_bf16_f32 v236, v32, v33\n"
  "v_cvt_pk_bf16_f32 v237, v34, v35\n"
  "v_cvt_pk_bf16_f32 v238, v36, v37\n"
  "v_cvt_pk_bf16_f32 v239, v38, v39\n"
  "global_store_dwordx4 v219, v[236:239], s[42:43] offset:0 sc1\n"
  "v_mov_b32 v160, v222\n"
  "v_mov_b32 v161, v222\n"
  "s_add_u32 s50, s50, 1024\n"
  "s_addc_u32 s51, s51, 0\n"
  "s_add_u32 s52, s52, 32768\n"
  "s_addc_u32 s53, s53, 0\n"
  "v_pk_mul_f32 v[40:41], v[40:41], v[190:191] op_sel_hi:[1,0]\n"
  "v_pk_mul_f32 v[42:43], v[42:43], v[190:191] op_sel_hi:[1,0]\n"
  "v_pk_mul_f32 v[44:45], v[44:45], v[190:191] op_sel_hi:[1,0]\n"
  "v_pk_mul_f32 v[46:47], v[46:47], v[190:191] op_sel_hi:[1,0]\n"
  "v_cvt_pk_bf16_f32 v240, v40, v41\n"
  "v_cvt_pk_bf16_f32 v241, v42, v43\n"
  "v_cvt_pk_bf16_f32 v242, v44, v45\n"
  "v_cvt_pk_bf16_f32 v243, v46, v47\n"
  "global_store_dwordx4 v219, v[240:243], s[42:43] offset:1024 sc1\n"
  "v_mov_b32 v162, v222\n"
  "v_mov_b32 v163, v222\n"
  "s_add_u32 m0, s48, 16384\n"
  "s_nop 0\n"
  "global_load_lds_dwordx4 v232, s[50:51] sc1\n"
  "v_mfma_f32_32x32x16_bf16 v[32:47], v[224:227], v[224:227], 0\n"
  "v_pk_mul_f32 v[48:49], v[48:49], v[190:191] op_sel_hi:[1,0]\n"
  "v_pk_mul_f32 v[50:51], v[50:51], v[190:191] op_sel_hi:[1,0]\n"
  "v_pk_mul_f32 v[52:53], v[52:53], v[190:191] op_sel_hi:[1,0]\n"
  "v_pk_mul_f32 v[54:55], v[54:55], v[190:191] op_sel_hi:[1,0]\n"
  "v_cvt_pk_bf16_f32 v244, v48, v49\n"
  "v_cvt_pk_bf16_f32 v245, v50, v51\n"
  "v_cvt_pk_bf16_f32 v246, v52, v53\n"
  "v_cvt_pk_bf16_f32 v247, v54, v55\n"
  "global_store_dwordx4 v219, v[244:247], s[42:43] offset:2048 sc1\n"
  "v_mov_b32 v164, v223\n"
  "v_mov_b32 v165, v223\n"
  "global_load_lds_dwordx4 v228, s[50:51] offset:1024 sc1\n"
  "global_load_lds_dwordx4 v229, s[52:53] offset:2048\n"
  "global_load_lds_dwordx4 v230, s[52:53] offset:3072\n"
  "s_add_u32 s50, s50, 1024\n"
  "s_addc_u32 s51, s51, 0\n"
  "s_add_u32 s52, s52, 32768\n"
  "s_addc_u32 s53, s53, 0\n"
  "v_xor_b32 v216, 0x10000, v216\n"
  "v_add_u32 v216, 0x4000, v216\n"
  "s_waitcnt lgkmcnt(0)\n"
  "s_barrier\n"
  "ds_read_b128 a[128:131], v216 offset:0\n"
  "ds_read_b128 a[132:135], v216 offset:1024\n"
  "ds_read_b128 a[136:139], v216 offset:2048\n"
  "ds_read_b128 a[140:143], v216 offset:3072\n"
  "ds_read_b128 a[144:147], v216 offset:4096\n"
  "ds_read_b128 a[148:151], v216 offset:5120\n"
  "ds_read_b128 a[152:155], v216 offset:6144\n"
  "ds_read_b128 a[156:159], v216 offset:7168\n"
  "ds_read_b128 a[160:163], v216 offset:32768\n"
  "ds_read_b128 a[164:167], v216 offset:33792\n"
  "ds_read_b128 a[168:171], v216 offset:34816\n"
  "ds_read_b128 a[172:175], v216 offset:35840\n"
  "ds_read_b128 a[176:179], v216 offset:36864\n"
  "ds_read_b128 a[180:183], v216 offset:37888\n"
  "ds_read_b128 a[184:187], v216 offset:38912\n"
  "ds_read_b128 a[188:191], v216 offset:39936\n"
  "ds_read_b128 a[64:67], v216 offset:8192\n"
  "ds_read_b128 a[96:99], v216 offset:40960\n"
  "ds_read_b128 a[100:103], v216 offset:41984\n"
  "ds_read_b128 a[68:71], v216 offset:9216\n"
  "ds_read_b128 a[72:75], v216 offset:10240\n"
  "ds_read_b128 a[104:107], v216 offset:43008\n"
  "ds_read_b128 a[108:111], v216 offset:44032\n"
  "ds_read_b128 a[76:79], v216 offset:11264\n"
  "v_pk_mul_f32 v[56:57], v[56:57], v[190:191] op_sel_hi:[1,0]\n"
  "v_pk_mul_f32 v[58:59], v[58:59], v[190:191] op_sel_hi:[1,0]\n"
  "v_pk_mul_f32 v[60:61], v[60:61], v[190:191] op_sel_hi:[1,0]\n"
  "v_pk_mul_f32 v[62:63], v[62:63], v[190:191] op_sel_hi:[1,0]\n"
  "v_cvt_pk_bf16_f32 v248, v56, v57\n"
  "v_cvt_pk_bf16_f32 v249, v58, v59\n"
  "v_cvt_pk_bf16_f32 v250, v60, v61\n"
  "v_cvt_pk_bf16_f32 v251, v62, v63\n"
  "global_store_dwordx4 v219, v[248:251], s[42:43] offset:3072 sc1\n"
  "v_mov_b32 v166, v223\n"
  "v_mov_b32 v167, v223\n"
  "s_add_u32 m0, s48, 32768\n"
  "s_nop 0\n"
  "global_load_lds_dwordx4 v232, s[50:51] sc1\n"
  "v_mfma_f32_32x32x16_bf16 v[48:63], v[224:227], v[224:227], 0\n"
  "v_pk_mul_f32 v[64:65], v[64:65], v[192:193] op_sel_hi:[1,0]\n"
  "v_pk_mul_f32 v[66:67], v[66:67], v[192:193] op_sel_hi:[1,0]\n"
  "v_pk_mul_f32 v[68:69], v[68:69], v[192:193] op_sel_hi:[1,0]\n"
  "v_pk_mul_f32 v[70:71], v[70:71], v[192:193] op_sel_hi:[1,0]\n"
  "v_cvt_pk_bf16_f32 v236, v64, v65\n"
  "v_cvt_pk_bf16_f32 v237, v66, v67\n"
  "v_cvt_pk_bf16_f32 v238, v68, v69\n"
  "v_cvt_pk_bf16_f32 v239, v70, v71\n"
  "global_store_dwordx4 v219, v[236:239], s[44:45] offset:0 sc1\n"
  "v_cndmask_b32 v168, v220, v218, s[88:89]\n"
  "v_cndmask_b32 v169, v220, v218, s[90:91]\n"
  "global_load_lds_dwordx4 v228, s[50:51] offset:1024 sc1\n"
  "global_load_lds_dwordx4 v229, s[52:53] offset:2048\n"
  "v_pk_mul_f32 v[72:73], v[72:73], v[192:193] op_sel_hi:[1,0]\n"
  "v_pk_mul_f32 v[74:75], v[74:75], v[192:193] op_sel_hi:[1,0]\n"
  "v_pk_mul_f32 v[76:77], v[76:77], v[192:193] op_sel_hi:[1,0]\n"
  "v_pk_mul_f32 v[78:79], v[78:79], v[192:193] op_sel_hi:[1,0]\n"
  "v_cvt_pk_bf16_f32 v240, v72, v73\n"
  "v_cvt_pk_bf16_f32 v241, v74, v75\n"
  "v_cvt_pk_bf16_f32 v242, v76, v77\n"
  "v_cvt_pk_bf16_f32 v243, v78, v79\n"
  "global_store_dwordx4 v219, v[240:243], s[44:45] offset:1024 sc1\n"
  "v_cndmask_b32 v170, v220, v218, s[92:93]\n"
  "v_cndmask_b32 v171, v220, v218, s[94:95]\n"
  "global_load_lds_dwordx4 v230, s[52:53] offset:3072\n"
  "s_add_u32 s50, s50, 1024\n"
  "s_addc_u32 s51, s51, 0\n"
  "v_mfma_f32_32x32x16_bf16 v[64:79], v[224:227], v[224:227], 0\n"
  "v_pk_mul_f32 v[80:81], v[80:81], v[192:193] op_sel_hi:[1,0]\n"
  "v_pk_mul_f32 v[82:83], v[82:83], v[192:193] op_sel_hi:[1,0]\n"
  "v_pk_mul_f32 v[84:85], v[84:85], v[192:193] op_sel_hi:[1,0]\n"
  "v_pk_mul_f32 v[86:87], v[86:87], v[192:193] op_sel_hi:[1,0]\n"
  "v_cvt_pk_bf16_f32 v244, v80, v81\n"
  "v_cvt_pk_bf16_f32 v245, v82, v83\n"
  "v_cvt_pk_bf16_f32 v246, v84, v85\n"
  "v_cvt_pk_bf16_f32 v247, v86, v87\n"
  "global_store_dwordx4 v219, v[244:247], s[44:45] offset:2048 sc1\n"
  "v_cndmask_b32 v172, v221, v218, s[88:89]\n"
  "v_cndmask_b32 v173, v221, v218, s[90:91]\n"
  "s_add_u32 s52, s52, 32768\n"
  "s_addc_u32 s53, s53, 0\n"
  "global_store_dwordx4 v232, v[184:187], s[28:29] offset:-2048 sc1\n"
  "v_pk_mul_f32 v[88:89], v[88:89], v[192:193] op_sel_hi:[1,0]\n"
  "v_pk_mul_f32 v[90:91], v[90:91], v[192:193] op_sel_hi:[1,0]\n"
  "v_pk_mul_f32 v[92:93], v[92:93], v[192:193] op_sel_hi:[1,0]\n"
  "v_pk_mul_f32 v[94:95], v[94:95], v[192:193] op_sel_hi:[1,0]\n"
  "v_cvt_pk_bf16_f32 v248, v88, v89\n"
  "v_cvt_pk_bf16_f32 v249, v90, v91\n"
  "v_cvt_pk_bf16_f32 v250, v92, v93\n"
  "v_cvt_pk_bf16_f32 v251, v94, v95\n"
  "global_store_dwordx4 v219, v[248:251], s[44:45] offset:3072 sc1\n"
  "v_cndmask_b32 v174, v221, v218, s[92:93]\n"
  "v_cndmask_b32 v175, v221, v218, s[94:95]\n"
  "s_add_u32 m0, s48, 49152\n"
  "s_nop 0\n"
  "global_load_lds_dwordx4 v232, s[50:51] sc1\n"
  "v_mfma_f32_32x32x16_bf16 v[80:95], v[224:227], v[224:227], 0\n"
  "v_pk_mul_f32 v[96:97], v[96:97], v[194:195] op_sel_hi:[1,0]\n"
  "v_pk_mul_f32 v[98:99], v[98:99], v[194:195] op_sel_hi:[1,0]\n"
  "v_pk_mul_f32 v[100:101], v[100:101], v[194:195] op_sel_hi:[1,0]\n"
  "v_pk_mul_f32 v[102:103], v[102:103], v[194:195] op_sel_hi:[1,0]\n"
  "v_cvt_pk_bf16_f32 v236, v96, v97\n"
  "v_cvt_pk_bf16_f32 v237, v98, v99\n"
  "v_cvt_pk_bf16_f32 v238, v100, v101\n"
  "v_cvt_pk_bf16_f32 v239, v102, v103\n"
  "global_store_dwordx4 v219, v[236:239], s[46:47] offset:0 sc1\n"
  "v_cndmask_b32 v176, v222, v218, s[88:89]\n"
  "v_cndmask_b32 v177, v222, v218, s[90:91]\n"
  "global_load_lds_dwordx4 v228, s[50:51] offset:1024 sc1\n"
  "global_load_lds_dwordx4 v229, s[52:53] offset:2048\n"
  "v_pk_mul_f32 v[104:105], v[104:105], v[194:195] op_sel_hi:[1,0]\n"
  "v_pk_mul_f32 v[106:107], v[106:107], v[194:195] op_sel_hi:[1,0]\n"
  "v_pk_mul_f32 v[108:109], v[108:109], v[194:195] op_sel_hi:[1,0]\n"
  "v_pk_mul_f32 v[110:111], v[110:111], v[194:195] op_sel_hi:[1,0]\n"
  "v_cvt_pk_bf16_f32 v240, v104, v105\n"
  "v_cvt_pk_bf16_f32 v241, v106, v107\n"
  "v_cvt_pk_bf16_f32 v242, v108, v109\n"
  "v_cvt_pk_bf16_f32 v243, v110, v111\n"
  "global_store_dwordx4 v219, v[240:243], s[46:47] offset:1024 sc1\n"
  "v_cndmask_b32 v178, v222, v218, s[92:93]\n"
  "v_cndmask_b32 v179, v222, v218, s[94:95]\n"
  "global_load_lds_dwordx4 v230, s[52:53] offset:3072\n"
  "s_add_u32 s50, s50, 1024\n"
  "s_addc_u32 s51, s51, 0\n"
  "v_mfma_f32_32x32x16_bf16 v[96:111], v[224:227], v[224:227], 0\n"
  "v_pk_mul_f32 v[112:113], v[112:113], v[194:195] op_sel_hi:[1,0]\n"
  "v_pk_mul_f32 v[114:115], v[114:115], v[194:195] op_sel_hi:[1,0]\n"
  "v_pk_mul_f32 v[116:117], v[116:117], v[194:195] op_sel_hi:[1,0]\n"
  "v_pk_mul_f32 v[118:119], v[118:119], v[194:195] op_sel_hi:[1,0]\n"
  "v_cvt_pk_bf16_f32 v244, v112, v113\n"
  "v_cvt_pk_bf16_f32 v245, v114, v115\n"
  "v_cvt_pk_bf16_f32 v246, v116, v117\n"
  "v_cvt_pk_bf16_f32 v247, v118, v119\n"
  "global_store_dwordx4 v219, v[244:247], s[46:47] offset:2048 sc1\n"
  "v_cndmask_b32 v180, v223, v218, s[88:89]\n"
  "v_cndmask_b32 v181, v223, v218, s[90:91]\n"
  "s_add_u32 s52, s52, 32768\n"
  "s_addc_u32 s53, s53, 0\n"
  "v_pk_mul_f32 v[120:121], v[120:121], v[194:195] op_sel_hi:[1,0]\n"
  "v_pk_mul_f32 v[122:123], v[122:123], v[194:195] op_sel_hi:[1,0]\n"
  "v_pk_mul_f32 v[124:125], v[124:125], v[194:195] op_sel_hi:[1,0]\n"
  "v_pk_mul_f32 v[126:127], v[126:127], v[194:195] op_sel_hi:[1,0]\n"
  "v_cvt_pk_bf16_f32 v248, v120, v121\n"
  "v_cvt_pk_bf16_f32 v249, v122, v123\n"
  "v_cvt_pk_bf16_f32 v250, v124, v125\n"
  "v_cvt_pk_bf16_f32 v251, v126, v127\n"
  "global_store_dwordx4 v219, v[248:251], s[46:47] offset:3072 sc1\n"
  "v_cndmask_b32 v182, v223, v218, s[92:93]\n"
  "v_cndmask_b32 v183, v223, v218, s[94:95]\n"
  "v_mfma_f32_32x32x16_bf16 v[112:127], v[224:227], v[224:227], 0\n"
  "s_waitcnt lgkmcnt(15)\n"
  "v_mfma_f32_16x16x32_bf16 v[128:131], a[128:131], a[0:3], v[168:171]\n"
  "v_mfma_f32_16x16x32_bf16 v[128:131], a[132:135], a[4:7], v[128:131]\n"
  "v_mfma_f32_16x16x32_bf16 v[128:131], a[136:139], a[8:11], v[128:131]\n"
  "v_mfma_f32_16x16x32_bf16 v[128:131], a[140:143], a[12:15], v[128:131]\n"
  "s_nop 7\n"
  "v_exp_f32 v128, v128\n"
  "v_exp_f32 v129, v129\n"
  "v_exp_f32 v130, v130\n"
  "v_exp_f32 v131, v131\n"
  "v_cvt_pk_bf16_f32 v144, v128, v129\n"
  "v_cvt_pk_bf16_f32 v145, v130, v131\n"
  "v_mov_b32 v146, 0\n"
  "v_mov_b32 v147, 0\n"
  "s_xor_b32 s49, s49, 0x10000\n"
  "s_setpc_b64 s[22:23]\n"
  "L_IB_%=:\n"
  "s_mov_b32 s36, 0x1f800000\n"
  "s_mov_b32 s37, 0x5f800000\n"
  "s_mov_b64 s[60:61], 0\n"
  "v_cmp_nle_f32 vcc, s36, v200\n"
  "v_cmp_nge_f32 s[96:97], s37, v200\n"
  "s_or_b64 s[60:61], s[60:61], vcc\n"
  "s_or_b64 s[60:61], s[60:61], s[96:97]\n"
  "v_cmp_nle_f32 vcc, s36, v204\n"
  "v_cmp_nge_f32 s[96:97], s37, v204\n"
  "s_or_b64 s[60:61], s[60:61], vcc\n"
  "s_or_b64 s[60:61], s[60:61], s[96:97]\n"
  "v_cmp_nle_f32 vcc, s36, v208\n"
  "v_cmp_nge_f32 s[96:97], s37, v208\n"
  "s_or_b64 s[60:61], s[60:61], vcc\n"
  "s_or_b64 s[60:61], s[60:61], s[96:97]\n"
  "v_cmp_nle_f32 vcc, s36, v212\n"
  "v_cmp_nge_f32 s[96:97], s37, v212\n"
  "s_or_b64 s[60:61], s[60:61], vcc\n"
  "s_or_b64 s[60:61], s[60:61], s[96:97]\n"
  "s_cbranch_scc0 L_IB_go_%=\n"
  "s_call_b64 s[18:19], L_slow0_%=\n"
  "s_call_b64 s[18:19], L_slow1_%=\n"
  "s_call_b64 s[18:19], L_slow2_%=\n"
  "s_call_b64 s[18:19], L_slow3_%=\n"
  "L_IB_go_%=:\n"
  "s_waitcnt lgkmcnt(0)\n"
  "s_barrier\n"
  "s_and_b32 s36, s35, 1\n"
  "s_lshl_b32 s36, s36, 14\n"
  "s_add_u32 s36, s36, 131072\n"
  "s_add_u32 s36, s36, s38\n"
  "v_add_u32 v234, s36, v232\n"
  "ds_read_b128 a[0:3], v234 offset:0\n"
  "ds_read_b128 a[4:7], v234 offset:1024\n"
  "ds_read_b128 a[8:11], v234 offset:2048\n"
  "ds_read_b128 a[12:15], v234 offset:3072\n"
  "ds_read_b128 a[16:19], v234 offset:4096\n"
  "ds_read_b128 a[20:23], v234 offset:5120\n"
  "ds_read_b128 a[24:27], v234 offset:6144\n"
  "ds_read_b128 a[28:31], v234 offset:7168\n"
  "ds_read_b128 a[32:35], v234 offset:8192\n"
  "ds_read_b128 a[36:39], v234 offset:9216\n"
  "ds_read_b128 a[40:43], v234 offset:10240\n"
  "ds_read_b128 a[44:47], v234 offset:11264\n"
  "ds_read_b128 a[48:51], v234 offset:12288\n"
  "ds_read_b128 a[52:55], v234 offset:13312\n"
  "ds_read_b128 a[56:59], v234 offset:14336\n"
  "ds_read_b128 a[60:63], v234 offset:15360\n"
  "s_lshl_b32 s36, s39, 12\n"
  "v_add_u32 v219, s36, v232\n"
  "v_rcp_f32 v188, v200\n"
  "v_rcp_f32 v190, v204\n"
  "v_rcp_f32 v192, v208\n"
  "v_rcp_f32 v194, v212\n"
  "v_pk_mul_f32 v[0:1], v[0:1], v[188:189] op_sel_hi:[1,0]\n"
  "v_pk_mul_f32 v[2:3], v[2:3], v[188:189] op_sel_hi:[1,0]\n"
  "v_pk_mul_f32 v[4:5], v[4:5], v[188:189] op_sel_hi:[1,0]\n"
  "v_pk_mul_f32 v[6:7], v[6:7], v[188:189] op_sel_hi:[1,0]\n"
  "v_cvt_pk_bf16_f32 v236, v0, v1\n"
  "v_cvt_pk_bf16_f32 v237, v2, v3\n"
  "v_cvt_pk_bf16_f32 v238, v4, v5\n"
  "v_cvt_pk_bf16_f32 v239, v6, v7\n"
  "global_store_dwordx4 v219, v[236:239], s[40:41] offset:0 sc1\n"
  "s_add_u32 s24, s24, 1\n"
  "v_readlane_b32 s22, v252, s24\n"
  "v_mfma_f32_32x32x16_bf16 v[200:215], v[224:227], v[224:227], 0\n"
  "v_pk_mul_f32 v[8:9], v[8:9], v[188:189] op_sel_hi:[1,0]\n"
  "v_pk_mul_f32 v[10:11], v[10:11], v[188:189] op_sel_hi:[1,0]\n"
  "v_pk_mul_f32 v[12:13], v[12:13], v[188:189] op_sel_hi:[1,0]\n"
  "v_pk_mul_f32 v[14:15], v[14:15], v[188:189] op_sel_hi:[1,0]\n"
  "v_cvt_pk_bf16_f32 v240, v8, v9\n"
  "v_cvt_pk_bf16_f32 v241, v10, v11\n"
  "v_cvt_pk_bf16_f32 v242, v12, v13\n"
  "v_cvt_pk_bf16_f32 v243, v14, v15\n"
  "global_store_dwordx4 v219, v[240:243], s[40:41] offset:1024 sc1\n"
  "s_add_u32 s22, s26, s22\n"
  "s_addc_u32 s23, s27, 0\n"
  "s_add_u32 m0, s48, 65536\n"
  "v_mfma_f32_32x32x16_bf16 v[0:15], v[224:227], v[224:227], 0\n"
  "v_pk_mul_f32 v[16:17], v[16:17], v[188:189] op_sel_hi:[1,0]\n"
  "v_pk_mul_f32 v[18:19], v[18:19], v[188:189] op_sel_hi:[1,0]\n"
  "v_pk_mul_f32 v[20:21], v[20:21], v[188:189] op_sel_hi:[1,0]\n"
  "v_pk_mul_f32 v[22:23], v[22:23], v[188:189] op_sel_hi:[1,0]\n"
  "v_cvt_pk_bf16_f32 v244, v16, v17\n"
  "v_cvt_pk_bf16_f32 v245, v18, v19\n"
  "v_cvt_pk_bf16_f32 v246, v20, v21\n"
  "v_cvt_pk_bf16_f32 v247, v22, v23\n"
  "global_store_dwordx4 v219, v[244:247], s[40:41] offset:2048 sc1\n"
  "global_load_lds_dwordx4 v232, s[50:51] sc1\n"
  "global_load_lds_dwordx4 v228, s[50:51] offset:1024 sc1\n"
  "v_pk_mul_f32 v[24:25], v[24:25], v[188:189] op_sel_hi:[1,0]\n"
  "v_pk_mul_f32 v[26:27], v[26:27], v[188:189] op_sel_hi:[1,0]\n"
  "v_pk_mul_f32 v[28:29], v[28:29], v[188:189] op_sel_hi:[1,0]\n"
  "v_pk_mul_f32 v[30:31], v[30:31], v[188:189] op_sel_hi:[1,0]\n"
  "v_cvt_pk_bf16_f32 v248, v24, v25\n"
  "v_cvt_pk_bf16_f32 v249, v26, v27\n"
  "v_cvt_pk_bf16_f32 v250, v28, v29\n"
  "v_cvt_pk_bf16_f32 v251, v30, v31\n"
  "global_store_dwordx4 v219, v[248:251], s[40:41] offset:3072 sc1\n"
  "global_load_lds_dwordx4 v229, s[52:53] offset:2048\n"
  "global_load_lds_dwordx4 v230, s[52:53] offset:3072\n"
  "v_mfma_f32_32x32x16_bf16 v[16:31], v[224:227], v[224:227], 0\n"
  "v_pk_mul_f32 v[32:33], v[32:33], v[190:191] op_sel_hi:[1,0]\n"
  "v_pk_mul_f32 v[34:35], v[34:35], v[190:191] op_sel_hi:[1,0]\n"
  "v_pk_mul_f32 v[36:37], v[36:37], v[190:191] op_sel_hi:[1,0]\n"
  "v_pk_mul_f32 v[38:39], v[38:39], v[190:191] op_sel_hi:[1,0]\n"
  "v_cvt_pk_bf16_f32 v236, v32, v33\n"
  "v_cvt_pk_bf16_f32 v237, v34, v35\n"
  "v_cvt_pk_bf16_f32 v238, v36, v37\n"
  "v_cvt_pk_bf16_f32 v239, v38, v39\n"
  "global_store_dwordx4 v219, v[236:239], s[42:43] offset:0 sc1\n"
  "s_add_u32 s50, s50, 1024\n"
  "s_addc_u32 s51, s51, 0\n"
  "s_add_u32 s52, s52, 32768\n"
  "s_addc_u32 s53, s53, 0\n"
  "v_pk_mul_f32 v[40:41], v[40:41], v[190:191] op_sel_hi:[1,0]\n"
  "v_pk_mul_f32 v[42:43], v[42:43], v[190:191] op_sel_hi:[1,0]\n"
  "v_pk_mul_f32 v[44:45], v[44:45], v[190:191] op_sel_hi:[1,0]\n"
  "v_pk_mul_f32 v[46:47], v[46:47], v[190:191] op_sel_hi:[1,0]\n"
  "v_cvt_pk_bf16_f32 v240, v40, v41\n"
  "v_cvt_pk_bf16_f32 v241, v42, v43\n"
  "v_cvt_pk_bf16_f32 v242, v44, v45\n"
  "v_cvt_pk_bf16_f32 v243, v46, v47\n"
  "global_store_dwordx4 v219, v[240:243], s[42:43] offset:1024 sc1\n"
  "s_add_u32 m0, s48, 81920\n"
  "s_nop 0\n"
  "global_load_lds_dwordx4 v232, s[50:51] sc1\n"
  "v_mfma_f32_32x32x16_bf16 v[32:47], v[224:227], v[224:227], 0\n"
  "v_pk_mul_f32 v[48:49], v[48:49], v[190:191] op_sel_hi:[1,0]\n"
  "v_pk_mul_f32 v[50:51], v[50:51], v[190:191] op_sel_hi:[1,0]\n"
  "v_pk_mul_f32 v[52:53], v[52:53], v[190:191] op_sel_hi:[1,0]\n"
  "v_pk_mul_f32 v[54:55], v[54:55], v[190:191] op_sel_hi:[1,0]\n"
  "v_cvt_pk_bf16_f32 v244, v48, v49\n"
  "v_cvt_pk_bf16_f32 v245, v50, v51\n"
  "v_cvt_pk_bf16_f32 v246, v52, v53\n"
  "v_cvt_pk_bf16_f32 v247, v54, v55\n"
  "global_store_dwordx4 v219, v[244:247], s[42:43] offset:2048 sc1\n"
  "global_load_lds_dwordx4 v228, s[50:51] offset:1024 sc1\n"
  "global_load_lds_dwordx4 v229, s[52:53] offset:2048\n"
  "global_load_lds_dwordx4 v230, s[52:53] offset:3072\n"
  "s_add_u32 s50, s50, 1024\n"
  "s_addc_u32 s51, s51, 0\n"
  "s_add_u32 s52, s52, 32768\n"
  "s_addc_u32 s53, s53, 0\n"
  "global_store_dwordx4 v232, v[184:187], s[28:29] offset:-1024 sc1\n"
  "global_store_dwordx4 v232, v[184:187], s[28:29] offset:0 sc1\n"
  "v_xor_b32 v216, 0x10000, v216\n"
  "v_add_u32 v216, 0x4000, v216\n"
  "s_waitcnt lgkmcnt(0)\n"
  "s_barrier\n"
  "v_pk_mul_f32 v[56:57], v[56:57], v[190:191] op_sel_hi:[1,0]\n"
  "v_pk_mul_f32 v[58:59], v[58:59], v[190:191] op_sel_hi:[1,0]\n"
  "v_pk_mul_f32 v[60:61], v[60:61], v[190:191] op_sel_hi:[1,0]\n"
  "v_pk_mul_f32 v[62:63], v[62:63], v[190:191] op_sel_hi:[1,0]\n"
  "v_cvt_pk_bf16_f32 v248, v56, v57\n"
  "v_cvt_pk_bf16_f32 v249, v58, v59\n"
  "v_cvt_pk_bf16_f32 v250, v60, v61\n"
  "v_cvt_pk_bf16_f32 v251, v62, v63\n"
  "global_store_dwordx4 v219, v[248:251], s[42:43] offset:3072 sc1\n"
  "s_add_u32 m0, s48, 98304\n"
  "s_nop 0\n"
  "global_load_lds_dwordx4 v232, s[50:51] sc1\n"
  "v_mfma_f32_32x32x16_bf16 v[48:63], v[224:227], v[224:227], 0\n"
  "v_pk_mul_f32 v[64:65], v[64:65], v[192:193] op_sel_hi:[1,0]\n"
  "v_pk_mul_f32 v[66:67], v[66:67], v[192:193] op_sel_hi:[1,0]\n"
  "v_pk_mul_f32 v[68:69], v[68:69], v[192:193] op_sel_hi:[1,0]\n"
  "v_pk_mul_f32 v[70:71], v[70:71], v[192:193] op_sel_hi:[1,0]\n"
  "v_cvt_pk_bf16_f32 v236, v64, v65\n"
  "v_cvt_pk_bf16_f32 v237, v66, v67\n"
  "v_cvt_pk_bf16_f32 v238, v68, v69\n"
  "v_cvt_pk_bf16_f32 v239, v70, v71\n"
  "global_store_dwordx4 v219, v[236:239], s[44:45] offset:0 sc1\n"
  "global_load_lds_dwordx4 v228, s[50:51] offset:1024 sc1\n"
  "global_load_lds_dwordx4 v229, s[52:53] offset:2048\n"
  "v_pk_mul_f32 v[72:73], v[72:73], v[192:193] op_sel_hi:[1,0]\n"
  "v_pk_mul_f32 v[74:75], v[74:75], v[192:193] op_sel_hi:[1,0]\n"
  "v_pk_mul_f32 v[76:77], v[76:77], v[192:193] op_sel_hi:[1,0]\n"
  "v_pk_mul_f32 v[78:79], v[78:79], v[192:193] op_sel_hi:[1,0]\n"
  "v_cvt_pk_bf16_f32 v240, v72, v73\n"
  "v_cvt_pk_bf16_f32 v241, v74, v75\n"
  "v_cvt_pk_bf16_f32 v242, v76, v77\n"
  "v_cvt_pk_bf16_f32 v243, v78, v79\n"
  "global_store_dwordx4 v219, v[240:243], s[44:45] offset:1024 sc1\n"
  "global_load_lds_dwordx4 v230, s[52:53] offset:3072\n"
  "s_add_u32 s50, s50, 1024\n"
  "s_addc_u32 s51, s51, 0\n"
  "v_mfma_f32_32x32x16_bf16 v[64:79], v[224:227], v[224:227], 0\n"
  "v_pk_mul_f32 v[80:81], v[80:81], v[192:193] op_sel_hi:[1,0]\n"
  "v_pk_mul_f32 v[82:83], v[82:83], v[192:193] op_sel_hi:[1,0]\n"
  "v_pk_mul_f32 v[84:85], v[84:85], v[192:193] op_sel_hi:[1,0]\n"
  "v_pk_mul_f32 v[86:87], v[86:87], v[192:193] op_sel_hi:[1,0]\n"
  "v_cvt_pk_bf16_f32 v244, v80, v81\n"
  "v_cvt_pk_bf16_f32 v245, v82, v83\n"
  "v_cvt_pk_bf16_f32 v246, v84, v85\n"
  "v_cvt_pk_bf16_f32 v247, v86, v87\n"
  "global_store_dwordx4 v219, v[244:247], s[44:45] offset:2048 sc1\n"
  "s_add_u32 s52, s52, 32768\n"
  "s_addc_u32 s53, s53, 0\n"
  "global_store_dwordx4 v232, v[184:187], s[28:29] offset:1024 sc1\n"
  "v_pk_mul_f32 v[88:89], v[88:89], v[192:193] op_sel_hi:[1,0]\n"
  "v_pk_mul_f32 v[90:91], v[90:91], v[192:193] op_sel_hi:[1,0]\n"
  "v_pk_mul_f32 v[92:93], v[92:93], v[192:193] op_sel_hi:[1,0]\n"
  "v_pk_mul_f32 v[94:95], v[94:95], v[192:193] op_sel_hi:[1,0]\n"
  "v_cvt_pk_bf16_f32 v248, v88, v89\n"
  "v_cvt_pk_bf16_f32 v249, v90, v91\n"
  "v_cvt_pk_bf16_f32 v250, v92, v93\n"
  "v_cvt_pk_bf16_f32 v251, v94, v95\n"
  "global_store_dwordx4 v219, v[248:251], s[44:45] offset:3072 sc1\n"
  "v_mfma_f32_32x32x16_bf16 v[80:95], v[224:227], v[224:227], 0\n"
  "v_pk_mul_f32 v[96:97], v[96:97], v[194:195] op_sel_hi:[1,0]\n"
  "v_pk_mul_f32 v[98:99], v[98:99], v[194:195] op_sel_hi:[1,0]\n"
  "v_pk_mul_f32 v[100:101], v[100:101], v[194:195] op_sel_hi:[1,0]\n"
  "v_pk_mul_f32 v[102:103], v[102:103], v[194:195] op_sel_hi:[1,0]\n"
  "v_cvt_pk_bf16_f32 v236, v96, v97\n"
  "v_cvt_pk_bf16_f32 v237, v98, v99\n"
  "v_cvt_pk_bf16_f32 v238, v100, v101\n"
  "v_cvt_pk_bf16_f32 v239, v102, v103\n"
  "global_store_dwordx4 v219, v[236:239], s[46:47] offset:0 sc1\n"
  "v_pk_mul_f32 v[104:105], v[104:105], v[194:195] op_sel_hi:[1,0]\n"
  "v_pk_mul_f32 v[106:107], v[106:107], v[194:195] op_sel_hi:[1,0]\n"
  "v_pk_mul_f32 v[108:109], v[108:109], v[194:195] op_sel_hi:[1,0]\n"
  "v_pk_mul_f32 v[110:111], v[110:111], v[194:195] op_sel_hi:[1,0]\n"
  "v_cvt_pk_bf16_f32 v240, v104, v105\n"
  "v_cvt_pk_bf16_f32 v241, v106, v107\n"
  "v_cvt_pk_bf16_f32 v242, v108, v109\n"
  "v_cvt_pk_bf16_f32 v243, v110, v111\n"
  "global_store_dwordx4 v219, v[240:243], s[46:47] offset:1024 sc1\n"
  "v_mfma_f32_32x32x16_bf16 v[96:111], v[224:227], v[224:227], 0\n"
  "v_pk_mul_f32 v[112:113], v[112:113], v[194:195] op_sel_hi:[1,0]\n"
  "v_pk_mul_f32 v[114:115], v[114:115], v[194:195] op_sel_hi:[1,0]\n"
  "v_pk_mul_f32 v[116:117], v[116:117], v[194:195] op_sel_hi:[1,0]\n"
  "v_pk_mul_f32 v[118:119], v[118:119], v[194:195] op_sel_hi:[1,0]\n"
  "v_cvt_pk_bf16_f32 v244, v112, v113\n"
  "v_cvt_pk_bf16_f32 v245, v114, v115\n"
  "v_cvt_pk_bf16_f32 v246, v116, v117\n"
  "v_cvt_pk_bf16_f32 v247, v118, v119\n"
  "global_store_dwordx4 v219, v[244:247], s[46:47] offset:2048 sc1\n"
  "v_pk_mul_f32 v[120:121], v[120:121], v[194:195] op_sel_hi:[1,0]\n"
  "v_pk_mul_f32 v[122:123], v[122:123], v[194:195] op_sel_hi:[1,0]\n"
  "v_pk_mul_f32 v[124:125], v[124:125], v[194:195] op_sel_hi:[1,0]\n"
  "v_pk_mul_f32 v[126:127], v[126:127], v[194:195] op_sel_hi:[1,0]\n"
  "v_cvt_pk_bf16_f32 v248, v120, v121\n"
  "v_cvt_pk_bf16_f32 v249, v122, v123\n"
  "v_cvt_pk_bf16_f32 v250, v124, v125\n"
  "v_cvt_pk_bf16_f32 v251, v126, v127\n"
  "global_store_dwordx4 v219, v[248:251], s[46:47] offset:3072 sc1\n"
  "v_mfma_f32_32x32x16_bf16 v[112:127], v[224:227], v[224:227], 0\n"
  "s_xor_b32 s49, s49, 0x10000\n"
  "s_setpc_b64 s[22:23]\n"
  "L_final_%=:\n"
  "s_lshl_b32 s36, s34, 3\n"
  "s_sub_u32 s54, 120, s36\n"
  "s_and_b32 s36, s35, 1\n"
  "s_lshl_b32 s36, s36, 2\n"
  "s_add_u32 s54, s54, s36\n"
  "s_mul_i32 s36, s54, 175104\n"
  "s_add_u32 s36, s36, 35651584\n"
  "s_add_u32 s40, s84, s36\n"
  "s_addc_u32 s41, s85, 0\n"
  "s_add_u32 s42, s40, 175104\n"
  "s_addc_u32 s43, s41, 0\n"
  "s_add_u32 s44, s42, 175104\n"
  "s_addc_u32 s45, s43, 0\n"
  "s_add_u32 s46, s44, 175104\n"
  "s_addc_u32 s47, s45, 0\n"
  "s_mul_i32 s36, s35, 8704\n"
  "s_add_u32 s36, s36, s38\n"
  "s_add_u32 s36, s36, 114688\n"
  "v_add_u32 v234, s36, v232\n"
  "s_xor_b32 s37, s35, 2\n"
  "s_mul_i32 s37, s37, 8704\n"
  "s_add_u32 s37, s37, s38\n"
  "s_add_u32 s37, s37, 114688\n"
  "v_add_u32 v235, s37, v232\n"
  "v_lshrrev_b32 v219, 1, v232\n"
  "v_sub_u32 v238, v234, v219\n"
  "v_sub_u32 v239, v235, v219\n"
  "s_mov_b32 s60, 0x3f80\n"
  "s_mov_b32 s61, 0x3f800000\n"
  "s_waitcnt lgkmcnt(0)\n"
  "s_cmp_ge_u32 s35, 2\n"
  "s_cbranch_scc1 L_finH_%=\n"
  "v_mov_b32 v236, v208\n"
  "v_mov_b32 v237, v212\n"
  "v_cvt_pk_bf16_f32 v64, v64, v65\n"
  "v_cvt_pk_bf16_f32 v65, v66, v67\n"
  "v_cvt_pk_bf16_f32 v66, v68, v69\n"
  "v_cvt_pk_bf16_f32 v67, v70, v71\n"
  "ds_write_b128 v234, v[64:67] offset:0\n"
  "v_cvt_pk_bf16_f32 v68, v72, v73\n"
  "v_cvt_pk_bf16_f32 v69, v74, v75\n"
  "v_cvt_pk_bf16_f32 v70, v76, v77\n"
  "v_cvt_pk_bf16_f32 v71, v78, v79\n"
  "ds_write_b128 v234, v[68:71] offset:1024\n"
  "v_cvt_pk_bf16_f32 v72, v80, v81\n"
  "v_cvt_pk_bf16_f32 v73, v82, v83\n"
  "v_cvt_pk_bf16_f32 v74, v84, v85\n"
  "v_cvt_pk_bf16_f32 v75, v86, v87\n"
  "ds_write_b128 v234, v[72:75] offset:2048\n"
  "v_cvt_pk_bf16_f32 v76, v88, v89\n"
  "v_cvt_pk_bf16_f32 v77, v90, v91\n"
  "v_cvt_pk_bf16_f32 v78, v92, v93\n"
  "v_cvt_pk_bf16_f32 v79, v94, v95\n"
  "ds_write_b128 v234, v[76:79] offset:3072\n"
  "v_cvt_pk_bf16_f32 v80, v96, v97\n"
  "v_cvt_pk_bf16_f32 v81, v98, v99\n"
  "v_cvt_pk_bf16_f32 v82, v100, v101\n"
  "v_cvt_pk_bf16_f32 v83, v102, v103\n"
  "ds_write_b128 v234, v[80:83] offset:4096\n"
  "v_cvt_pk_bf16_f32 v84, v104, v105\n"
  "v_cvt_pk_bf16_f32 v85, v106, v107\n"
  "v_cvt_pk_bf16_f32 v86, v108, v109\n"
  "v_cvt_pk_bf16_f32 v87, v110, v111\n"
  "ds_write_b128 v234, v[84:87] offset:5120\n"
  "v_cvt_pk_bf16_f32 v88, v112, v113\n"
  "v_cvt_pk_bf16_f32 v89, v114, v115\n"
  "v_cvt_pk_bf16_f32 v90, v116, v117\n"
  "v_cvt_pk_bf16_f32 v91, v118, v119\n"
  "ds_write_b128 v234, v[88:91] offset:6144\n"
  "v_cvt_pk_bf16_f32 v92, v120, v121\n"
  "v_cvt_pk_bf16_f32 v93, v122, v123\n"
  "v_cvt_pk_bf16_f32 v94, v124, v125\n"
  "v_cvt_pk_bf16_f32 v95, v126, v127\n"
  "ds_write_b128 v234, v[92:95] offset:7168\n"
  "ds_write_b64 v238, v[236:237] offset:8192\n"
  "s_waitcnt lgkmcnt(0)\n"
  "s_barrier\n"
  "ds_read_b64 v[236:237], v239 offset:8192\n"
  "ds_read_b128 v[96:99], v235 offset:0\n"
  "ds_read_b128 v[100:103], v235 offset:1024\n"
  "ds_read_b128 v[104:107], v235 offset:2048\n"
  "ds_read_b128 v[108:111], v235 offset:3072\n"
  "ds_read_b128 v[112:115], v235 offset:4096\n"
  "ds_read_b128 v[116:119], v235 offset:5120\n"
  "ds_read_b128 v[120:123], v235 offset:6144\n"
  "ds_read_b128 v[124:127], v235 offset:7168\n"
  "s_waitcnt lgkmcnt(8)\n"
  "v_add_f32 v200, v200, v236\n"
  "v_add_f32 v204, v204, v237\n"
  "s_waitcnt lgkmcnt(7)\n"
  "v_dot2c_f32_bf16 v0, s60, v96\n"
  "v_dot2c_f32_bf16 v1, s61, v96\n"
  "v_dot2c_f32_bf16 v2, s60, v97\n"
  "v_dot2c_f32_bf16 v3, s61, v97\n"
  "v_dot2c_f32_bf16 v4, s60, v98\n"
  "v_dot2c_f32_bf16 v5, s61, v98\n"
  "v_dot2c_f32_bf16 v6, s60, v99\n"
  "v_dot2c_f32_bf16 v7, s61, v99\n"
  "s_waitcnt lgkmcnt(6)\n"
  "v_dot2c_f32_bf16 v8, s60, v100\n"
  "v_dot2c_f32_bf16 v9, s61, v100\n"
  "v_dot2c_f32_bf16 v10, s60, v101\n"
  "v_dot2c_f32_bf16 v11, s61, v101\n"
  "v_dot2c_f32_bf16 v12, s60, v102\n"
  "v_dot2c_f32_bf16 v13, s61, v102\n"
  "v_dot2c_f32_bf16 v14, s60, v103\n"
  "v_dot2c_f32_bf16 v15, s61, v103\n"
  "s_waitcnt lgkmcnt(5)\n"
  "v_dot2c_f32_bf16 v16, s60, v104\n"
  "v_dot2c_f32_bf16 v17, s61, v104\n"
  "v_dot2c_f32_bf16 v18, s60, v105\n"
  "v_dot2c_f32_bf16 v19, s61, v105\n"
  "v_dot2c_f32_bf16 v20, s60, v106\n"
  "v_dot2c_f32_bf16 v21, s61, v106\n"
  "v_dot2c_f32_bf16 v22, s60, v107\n"
  "v_dot2c_f32_bf16 v23, s61, v107\n"
  "s_waitcnt lgkmcnt(4)\n"
  "v_dot2c_f32_bf16 v24, s60, v108\n"
  "v_dot2c_f32_bf16 v25, s61, v108\n"
  "v_dot2c_f32_bf16 v26, s60, v109\n"
  "v_dot2c_f32_bf16 v27, s61, v109\n"
  "v_dot2c_f32_bf16 v28, s60, v110\n"
  "v_dot2c_f32_bf16 v29, s61, v110\n"
  "v_dot2c_f32_bf16 v30, s60, v111\n"
  "v_dot2c_f32_bf16 v31, s61, v111\n"
  "s_waitcnt lgkmcnt(3)\n"
  "v_dot2c_f32_bf16 v32, s60, v112\n"
  "v_dot2c_f32_bf16 v33, s61, v112\n"
  "v_dot2c_f32_bf16 v34, s60, v113\n"
  "v_dot2c_f32_bf16 v35, s61, v113\n"
  "v_dot2c_f32_bf16 v36, s60, v114\n"
  "v_dot2c_f32_bf16 v37, s61, v114\n"
  "v_dot2c_f32_bf16 v38, s60, v115\n"
  "v_dot2c_f32_bf16 v39, s61, v115\n"
  "s_waitcnt lgkmcnt(2)\n"
  "v_dot2c_f32_bf16 v40, s60, v116\n"
  "v_dot2c_f32_bf16 v41, s61, v116\n"
  "v_dot2c_f32_bf16 v42, s60, v117\n"
  "v_dot2c_f32_bf16 v43, s61, v117\n"
  "v_dot2c_f32_bf16 v44, s60, v118\n"
  "v_dot2c_f32_bf16 v45, s61, v118\n"
  "v_dot2c_f32_bf16 v46, s60, v119\n"
  "v_dot2c_f32_bf16 v47, s61, v119\n"
  "s_waitcnt lgkmcnt(1)\n"
  "v_dot2c_f32_bf16 v48, s60, v120\n"
  "v_dot2c_f32_bf16 v49, s61, v120\n"
  "v_dot2c_f32_bf16 v50, s60, v121\n"
  "v_dot2c_f32_bf16 v51, s61, v121\n"
  "v_dot2c_f32_bf16 v52, s60, v122\n"
  "v_dot2c_f32_bf16 v53, s61, v122\n"
  "v_dot2c_f32_bf16 v54, s60, v123\n"
  "v_dot2c_f32_bf16 v55, s61, v123\n"
  "s_waitcnt lgkmcnt(0)\n"
  "v_dot2c_f32_bf16 v56, s60, v124\n"
  "v_dot2c_f32_bf16 v57, s61, v124\n"
  "v_dot2c_f32_bf16 v58, s60, v125\n"
  "v_dot2c_f32_bf16 v59, s61, v125\n"
  "v_dot2c_f32_bf16 v60, s60, v126\n"
  "v_dot2c_f32_bf16 v61, s61, v126\n"
  "v_dot2c_f32_bf16 v62, s60, v127\n"
  "v_dot2c_f32_bf16 v63, s61, v127\n"
  "s_nop 2\n"
  "s_mov_b32 s36, 0x1f800000\n"
  "s_mov_b32 s37, 0x5f800000\n"
  "s_mov_b64 s[60:61], 0\n"
  "v_cmp_nle_f32 vcc, s36, v200\n"
  "v_cmp_nge_f32 s[96:97], s37, v200\n"
  "s_or_b64 s[60:61], s[60:61], vcc\n"
  "s_or_b64 s[60:61], s[60:61], s[96:97]\n"
  "v_cmp_nle_f32 vcc, s36, v204\n"
  "v_cmp_nge_f32 s[96:97], s37, v204\n"
  "s_or_b64 s[60:61], s[60:61], vcc\n"
  "s_or_b64 s[60:61], s[60:61], s[96:97]\n"
  "s_cbranch_scc0 L_finM2_%=\n"
  "s_call_b64 s[18:19], L_slow0_%=\n"
  "s_call_b64 s[18:19], L_slow1_%=\n"
  "L_finM2_%=:\n"
  "s_lshl_b32 s36, s39, 12\n"
  "v_add_u32 v219, s36, v232\n"
  "v_rcp_f32 v188, v200\n"
  "v_rcp_f32 v190, v204\n"
  "v_pk_mul_f32 v[0:1], v[0:1], v[188:189] op_sel_hi:[1,0]\n"
  "v_pk_mul_f32 v[2:3], v[2:3], v[188:189] op_sel_hi:[1,0]\n"
  "v_pk_mul_f32 v[4:5], v[4:5], v[188:189] op_sel_hi:[1,0]\n"
  "v_pk_mul_f32 v[6:7], v[6:7], v[188:189] op_sel_hi:[1,0]\n"
  "v_cvt_pk_bf16_f32 v236, v0, v1\n"
  "v_cvt_pk_bf16_f32 v237, v2, v3\n"
  "v_cvt_pk_bf16_f32 v238, v4, v5\n"
  "v_cvt_pk_bf16_f32 v239, v6, v7\n"
  "global_store_dwordx4 v219, v[236:239], s[40:41] offset:0 sc1\n"
  "v_pk_mul_f32 v[8:9], v[8:9], v[188:189] op_sel_hi:[1,0]\n"
  "v_pk_mul_f32 v[10:11], v[10:11], v[188:189] op_sel_hi:[1,0]\n"
  "v_pk_mul_f32 v[12:13], v[12:13], v[188:189] op_sel_hi:[1,0]\n"
  "v_pk_mul_f32 v[14:15], v[14:15], v[188:189] op_sel_hi:[1,0]\n"
  "v_cvt_pk_bf16_f32 v240, v8, v9\n"
  "v_cvt_pk_bf16_f32 v241, v10, v11\n"
  "v_cvt_pk_bf16_f32 v242, v12, v13\n"
  "v_cvt_pk_bf16_f32 v243, v14, v15\n"
  "global_store_dwordx4 v219, v[240:243], s[40:41] offset:1024 sc1\n"
  "v_pk_mul_f32 v[16:17], v[16:17], v[188:189] op_sel_hi:[1,0]\n"
  "v_pk_mul_f32 v[18:19], v[18:19], v[188:189] op_sel_hi:[1,0]\n"
  "v_pk_mul_f32 v[20:21], v[20:21], v[188:189] op_sel_hi:[1,0]\n"
  "v_pk_mul_f32 v[22:23], v[22:23], v[188:189] op_sel_hi:[1,0]\n"
  "v_cvt_pk_bf16_f32 v244, v16, v17\n"
  "v_cvt_pk_bf16_f32 v245, v18, v19\n"
  "v_cvt_pk_bf16_f32 v246, v20, v21\n"
  "v_cvt_pk_bf16_f32 v247, v22, v23\n"
  "global_store_dwordx4 v219, v[244:247], s[40:41] offset:2048 sc1\n"
  "v_pk_mul_f32 v[24:25], v[24:25], v[188:189] op_sel_hi:[1,0]\n"
  "v_pk_mul_f32 v[26:27], v[26:27], v[188:189] op_sel_hi:[1,0]\n"
  "v_pk_mul_f32 v[28:29], v[28:29], v[188:189] op_sel_hi:[1,0]\n"
  "v_pk_mul_f32 v[30:31], v[30:31], v[188:189] op_sel_hi:[1,0]\n"
  "v_cvt_pk_bf16_f32 v248, v24, v25\n"
  "v_cvt_pk_bf16_f32 v249, v26, v27\n"
  "v_cvt_pk_bf16_f32 v250, v28, v29\n"
  "v_cvt_pk_bf16_f32 v251, v30, v31\n"
  "global_store_dwordx4 v219, v[248:251], s[40:41] offset:3072 sc1\n"
  "v_pk_mul_f32 v[32:33], v[32:33], v[190:191] op_sel_hi:[1,0]\n"
  "v_pk_mul_f32 v[34:35], v[34:35], v[190:191] op_sel_hi:[1,0]\n"
  "v_pk_mul_f32 v[36:37], v[36:37], v[190:191] op_sel_hi:[1,0]\n"
  "v_pk_mul_f32 v[38:39], v[38:39], v[190:191] op_sel_hi:[1,0]\n"
  "v_cvt_pk_bf16_f32 v236, v32, v33\n"
  "v_cvt_pk_bf16_f32 v237, v34, v35\n"
  "v_cvt_pk_bf16_f32 v238, v36, v37\n"
  "v_cvt_pk_bf16_f32 v239, v38, v39\n"
  "global_store_dwordx4 v219, v[236:239], s[42:43] offset:0 sc1\n"
  "v_pk_mul_f32 v[40:41], v[40:41], v[190:191] op_sel_hi:[1,0]\n"
  "v_pk_mul_f32 v[42:43], v[42:43], v[190:191] op_sel_hi:[1,0]\n"
  "v_pk_mul_f32 v[44:45], v[44:45], v[190:191] op_sel_hi:[1,0]\n"
  "v_pk_mul_f32 v[46:47], v[46:47], v[190:191] op_sel_hi:[1,0]\n"
  "v_cvt_pk_bf16_f32 v240, v40, v41\n"
  "v_cvt_pk_bf16_f32 v241, v42, v43\n"
  "v_cvt_pk_bf16_f32 v242, v44, v45\n"
  "v_cvt_pk_bf16_f32 v243, v46, v47\n"
  "global_store_dwordx4 v219, v[240:243], s[42:43] offset:1024 sc1\n"
  "v_pk_mul_f32 v[48:49], v[48:49], v[190:191] op_sel_hi:[1,0]\n"
  "v_pk_mul_f32 v[50:51], v[50:51], v[190:191] op_sel_hi:[1,0]\n"
  "v_pk_mul_f32 v[52:53], v[52:53], v[190:191] op_sel_hi:[1,0]\n"
  "v_pk_mul_f32 v[54:55], v[54:55], v[190:191] op_sel_hi:[1,0]\n"
  "v_cvt_pk_bf16_f32 v244, v48, v49\n"
  "v_cvt_pk_bf16_f32 v245, v50, v51\n"
  "v_cvt_pk_bf16_f32 v246, v52, v53\n"
  "v_cvt_pk_bf16_f32 v247, v54, v55\n"
  "global_store_dwordx4 v219, v[244:247], s[42:43] offset:2048 sc1\n"
  "v_pk_mul_f32 v[56:57], v[56:57], v[190:191] op_sel_hi:[1,0]\n"
  "v_pk_mul_f32 v[58:59], v[58:59], v[190:191] op_sel_hi:[1,0]\n"
  "v_pk_mul_f32 v[60:61], v[60:61], v[190:191] op_sel_hi:[1,0]\n"
  "v_pk_mul_f32 v[62:63], v[62:63], v[190:191] op_sel_hi:[1,0]\n"
  "v_cvt_pk_bf16_f32 v248, v56, v57\n"
  "v_cvt_pk_bf16_f32 v249, v58, v59\n"
  "v_cvt_pk_bf16_f32 v250, v60, v61\n"
  "v_cvt_pk_bf16_f32 v251, v62, v63\n"
  "global_store_dwordx4 v219, v[248:251], s[42:43] offset:3072 sc1\n"
  "s_branch L_c0_%=\n"
  "L_finH_%=:\n"
  "v_mov_b32 v236, v200\n"
  "v_mov_b32 v237, v204\n"
  "v_cvt_pk_bf16_f32 v0, v0, v1\n"
  "v_cvt_pk_bf16_f32 v1, v2, v3\n"
  "v_cvt_pk_bf16_f32 v2, v4, v5\n"
  "v_cvt_pk_bf16_f32 v3, v6, v7\n"
  "ds_write_b128 v234, v[0:3] offset:0\n"
  "v_cvt_pk_bf16_f32 v4, v8, v9\n"
  "v_cvt_pk_bf16_f32 v5, v10, v11\n"
  "v_cvt_pk_bf16_f32 v6, v12, v13\n"
  "v_cvt_pk_bf16_f32 v7, v14, v15\n"
  "ds_write_b128 v234, v[4:7] offset:1024\n"
  "v_cvt_pk_bf16_f32 v8, v16, v17\n"
  "v_cvt_pk_bf16_f32 v9, v18, v19\n"
  "v_cvt_pk_bf16_f32 v10, v20, v21\n"
  "v_cvt_pk_bf16_f32 v11, v22, v23\n"
  "ds_write_b128 v234, v[8:11] offset:2048\n"
  "v_cvt_pk_bf16_f32 v12, v24, v25\n"
  "v_cvt_pk_bf16_f32 v13, v26, v27\n"
  "v_cvt_pk_bf16_f32 v14, v28, v29\n"
  "v_cvt_pk_bf16_f32 v15, v30, v31\n"
  "ds_write_b128 v234, v[12:15] offset:3072\n"
  "v_cvt_pk_bf16_f32 v16, v32, v33\n"
  "v_cvt_pk_bf16_f32 v17, v34, v35\n"
  "v_cvt_pk_bf16_f32 v18, v36, v37\n"
  "v_cvt_pk_bf16_f32 v19, v38, v39\n"
  "ds_write_b128 v234, v[16:19] offset:4096\n"
  "v_cvt_pk_bf16_f32 v20, v40, v41\n"
  "v_cvt_pk_bf16_f32 v21, v42, v43\n"
  "v_cvt_pk_bf16_f32 v22, v44, v45\n"
  "v_cvt_pk_bf16_f32 v23, v46, v47\n"
  "ds_write_b128 v234, v[20:23] offset:5120\n"
  "v_cvt_pk_bf16_f32 v24, v48, v49\n"
  "v_cvt_pk_bf16_f32 v25, v50, v51\n"
  "v_cvt_pk_bf16_f32 v26, v52, v53\n"
  "v_cvt_pk_bf16_f32 v27, v54, v55\n"
  "ds_write_b128 v234, v[24:27] offset:6144\n"
  "v_cvt_pk_bf16_f32 v28, v56, v57\n"
  "v_cvt_pk_bf16_f32 v29, v58, v59\n"
  "v_cvt_pk_bf16_f32 v30, v60, v61\n"
  "v_cvt_pk_bf16_f32 v31, v62, v63\n"
  "ds_write_b128 v234, v[28:31] offset:7168\n"
  "ds_write_b64 v238, v[236:237] offset:8192\n"
  "s_waitcnt lgkmcnt(0)\n"
  "s_barrier\n"
  "ds_read_b64 v[236:237], v239 offset:8192\n"
  "ds_read_b128 v[32:35], v235 offset:0\n"
  "ds_read_b128 v[36:39], v235 offset:1024\n"
  "ds_read_b128 v[40:43], v235 offset:2048\n"
  "ds_read_b128 v[44:47], v235 offset:3072\n"
  "ds_read_b128 v[48:51], v235 offset:4096\n"
  "ds_read_b128 v[52:55], v235 offset:5120\n"
  "ds_read_b128 v[56:59], v235 offset:6144\n"
  "ds_read_b128 v[60:63], v235 offset:7168\n"
  "s_waitcnt lgkmcnt(8)\n"
  "v_add_f32 v208, v208, v236\n"
  "v_add_f32 v212, v212, v237\n"
  "s_waitcnt lgkmcnt(7)\n"
  "v_dot2c_f32_bf16 v64, s60, v32\n"
  "v_dot2c_f32_bf16 v65, s61, v32\n"
  "v_dot2c_f32_bf16 v66, s60, v33\n"
  "v_dot2c_f32_bf16 v67, s61, v33\n"
  "v_dot2c_f32_bf16 v68, s60, v34\n"
  "v_dot2c_f32_bf16 v69, s61, v34\n"
  "v_dot2c_f32_bf16 v70, s60, v35\n"
  "v_dot2c_f32_bf16 v71, s61, v35\n"
  "s_waitcnt lgkmcnt(6)\n"
  "v_dot2c_f32_bf16 v72, s60, v36\n"
  "v_dot2c_f32_bf16 v73, s61, v36\n"
  "v_dot2c_f32_bf16 v74, s60, v37\n"
  "v_dot2c_f32_bf16 v75, s61, v37\n"
  "v_dot2c_f32_bf16 v76, s60, v38\n"
  "v_dot2c_f32_bf16 v77, s61, v38\n"
  "v_dot2c_f32_bf16 v78, s60, v39\n"
  "v_dot2c_f32_bf16 v79, s61, v39\n"
  "s_waitcnt lgkmcnt(5)\n"
  "v_dot2c_f32_bf16 v80, s60, v40\n"
  "v_dot2c_f32_bf16 v81, s61, v40\n"
  "v_dot2c_f32_bf16 v82, s60, v41\n"
  "v_dot2c_f32_bf16 v83, s61, v41\n"
  "v_dot2c_f32_bf16 v84, s60, v42\n"
  "v_dot2c_f32_bf16 v85, s61, v42\n"
  "v_dot2c_f32_bf16 v86, s60, v43\n"
  "v_dot2c_f32_bf16 v87, s61, v43\n"
  "s_waitcnt lgkmcnt(4)\n"
  "v_dot2c_f32_bf16 v88, s60, v44\n"
  "v_dot2c_f32_bf16 v89, s61, v44\n"
  "v_dot2c_f32_bf16 v90, s60, v45\n"
  "v_dot2c_f32_bf16 v91, s61, v45\n"
  "v_dot2c_f32_bf16 v92, s60, v46\n"
  "v_dot2c_f32_bf16 v93, s61, v46\n"
  "v_dot2c_f32_bf16 v94, s60, v47\n"
  "v_dot2c_f32_bf16 v95, s61, v47\n"
  "s_waitcnt lgkmcnt(3)\n"
  "v_dot2c_f32_bf16 v96, s60, v48\n"
  "v_dot2c_f32_bf16 v97, s61, v48\n"
  "v_dot2c_f32_bf16 v98, s60, v49\n"
  "v_dot2c_f32_bf16 v99, s61, v49\n"
  "v_dot2c_f32_bf16 v100, s60, v50\n"
  "v_dot2c_f32_bf16 v101, s61, v50\n"
  "v_dot2c_f32_bf16 v102, s60, v51\n"
  "v_dot2c_f32_bf16 v103, s61, v51\n"
  "s_waitcnt lgkmcnt(2)\n"
  "v_dot2c_f32_bf16 v104, s60, v52\n"
  "v_dot2c_f32_bf16 v105, s61, v52\n"
  "v_dot2c_f32_bf16 v106, s60, v53\n"
  "v_dot2c_f32_bf16 v107, s61, v53\n"
  "v_dot2c_f32_bf16 v108, s60, v54\n"
  "v_dot2c_f32_bf16 v109, s61, v54\n"
  "v_dot2c_f32_bf16 v110, s60, v55\n"
  "v_dot2c_f32_bf16 v111, s61, v55\n"
  "s_waitcnt lgkmcnt(1)\n"
  "v_dot2c_f32_bf16 v112, s60, v56\n"
  "v_dot2c_f32_bf16 v113, s61, v56\n"
  "v_dot2c_f32_bf16 v114, s60, v57\n"
  "v_dot2c_f32_bf16 v115, s61, v57\n"
  "v_dot2c_f32_bf16 v116, s60, v58\n"
  "v_dot2c_f32_bf16 v117, s61, v58\n"
  "v_dot2c_f32_bf16 v118, s60, v59\n"
  "v_dot2c_f32_bf16 v119, s61, v59\n"
  "s_waitcnt lgkmcnt(0)\n"
  "v_dot2c_f32_bf16 v120, s60, v60\n"
  "v_dot2c_f32_bf16 v121, s61, v60\n"
  "v_dot2c_f32_bf16 v122, s60, v61\n"
  "v_dot2c_f32_bf16 v123, s61, v61\n"
  "v_dot2c_f32_bf16 v124, s60, v62\n"
  "v_dot2c_f32_bf16 v125, s61, v62\n"
  "v_dot2c_f32_bf16 v126, s60, v63\n"
  "v_dot2c_f32_bf16 v127, s61, v63\n"
  "s_nop 2\n"
  "s_mov_b32 s36, 0x1f800000\n"
  "s_mov_b32 s37, 0x5f800000\n"
  "s_mov_b64 s[60:61], 0\n"
  "v_cmp_nle_f32 vcc, s36, v208\n"
  "v_cmp_nge_f32 s[96:97], s37, v208\n"
  "s_or_b64 s[60:61], s[60:61], vcc\n"
  "s_or_b64 s[60:61], s[60:61], s[96:97]\n"
  "v_cmp_nle_f32 vcc, s36, v212\n"
  "v_cmp_nge_f32 s[96:97], s37, v212\n"
  "s_or_b64 s[60:61], s[60:61], vcc\n"
  "s_or_b64 s[60:61], s[60:61], s[96:97]\n"
  "s_cbranch_scc0 L_finH2_%=\n"
  "s_call_b64 s[18:19], L_slow2_%=\n"
  "s_call_b64 s[18:19], L_slow3_%=\n"
  "L_finH2_%=:\n"
  "s_lshl_b32 s36, s39, 12\n"
  "v_add_u32 v219, s36, v232\n"
  "v_rcp_f32 v188, v208\n"
  "v_rcp_f32 v190, v212\n"
  "v_pk_mul_f32 v[64:65], v[64:65], v[188:189] op_sel_hi:[1,0]\n"
  "v_pk_mul_f32 v[66:67], v[66:67], v[188:189] op_sel_hi:[1,0]\n"
  "v_pk_mul_f32 v[68:69], v[68:69], v[188:189] op_sel_hi:[1,0]\n"
  "v_pk_mul_f32 v[70:71], v[70:71], v[188:189] op_sel_hi:[1,0]\n"
  "v_cvt_pk_bf16_f32 v236, v64, v65\n"
  "v_cvt_pk_bf16_f32 v237, v66, v67\n"
  "v_cvt_pk_bf16_f32 v238, v68, v69\n"
  "v_cvt_pk_bf16_f32 v239, v70, v71\n"
  "global_store_dwordx4 v219, v[236:239], s[44:45] offset:0 sc1\n"
  "v_pk_mul_f32 v[72:73], v[72:73], v[188:189] op_sel_hi:[1,0]\n"
  "v_pk_mul_f32 v[74:75], v[74:75], v[188:189] op_sel_hi:[1,0]\n"
  "v_pk_mul_f32 v[76:77], v[76:77], v[188:189] op_sel_hi:[1,0]\n"
  "v_pk_mul_f32 v[78:79], v[78:79], v[188:189] op_sel_hi:[1,0]\n"
  "v_cvt_pk_bf16_f32 v240, v72, v73\n"
  "v_cvt_pk_bf16_f32 v241, v74, v75\n"
  "v_cvt_pk_bf16_f32 v242, v76, v77\n"
  "v_cvt_pk_bf16_f32 v243, v78, v79\n"
  "global_store_dwordx4 v219, v[240:243], s[44:45] offset:1024 sc1\n"
  "v_pk_mul_f32 v[80:81], v[80:81], v[188:189] op_sel_hi:[1,0]\n"
  "v_pk_mul_f32 v[82:83], v[82:83], v[188:189] op_sel_hi:[1,0]\n"
  "v_pk_mul_f32 v[84:85], v[84:85], v[188:189] op_sel_hi:[1,0]\n"
  "v_pk_mul_f32 v[86:87], v[86:87], v[188:189] op_sel_hi:[1,0]\n"
  "v_cvt_pk_bf16_f32 v244, v80, v81\n"
  "v_cvt_pk_bf16_f32 v245, v82, v83\n"
  "v_cvt_pk_bf16_f32 v246, v84, v85\n"
  "v_cvt_pk_bf16_f32 v247, v86, v87\n"
  "global_store_dwordx4 v219, v[244:247], s[44:45] offset:2048 sc1\n"
  "v_pk_mul_f32 v[88:89], v[88:89], v[188:189] op_sel_hi:[1,0]\n"
  "v_pk_mul_f32 v[90:91], v[90:91], v[188:189] op_sel_hi:[1,0]\n"
  "v_pk_mul_f32 v[92:93], v[92:93], v[188:189] op_sel_hi:[1,0]\n"
  "v_pk_mul_f32 v[94:95], v[94:95], v[188:189] op_sel_hi:[1,0]\n"
  "v_cvt_pk_bf16_f32 v248, v88, v89\n"
  "v_cvt_pk_bf16_f32 v249, v90, v91\n"
  "v_cvt_pk_bf16_f32 v250, v92, v93\n"
  "v_cvt_pk_bf16_f32 v251, v94, v95\n"
  "global_store_dwordx4 v219, v[248:251], s[44:45] offset:3072 sc1\n"
  "v_pk_mul_f32 v[96:97], v[96:97], v[190:191] op_sel_hi:[1,0]\n"
  "v_pk_mul_f32 v[98:99], v[98:99], v[190:191] op_sel_hi:[1,0]\n"
  "v_pk_mul_f32 v[100:101], v[100:101], v[190:191] op_sel_hi:[1,0]\n"
  "v_pk_mul_f32 v[102:103], v[102:103], v[190:191] op_sel_hi:[1,0]\n"
  "v_cvt_pk_bf16_f32 v236, v96, v97\n"
  "v_cvt_pk_bf16_f32 v237, v98, v99\n"
  "v_cvt_pk_bf16_f32 v238, v100, v101\n"
  "v_cvt_pk_bf16_f32 v239, v102, v103\n"
  "global_store_dwordx4 v219, v[236:239], s[46:47] offset:0 sc1\n"
  "v_pk_mul_f32 v[104:105], v[104:105], v[190:191] op_sel_hi:[1,0]\n"
  "v_pk_mul_f32 v[106:107], v[106:107], v[190:191] op_sel_hi:[1,0]\n"
  "v_pk_mul_f32 v[108:109], v[108:109], v[190:191] op_sel_hi:[1,0]\n"
  "v_pk_mul_f32 v[110:111], v[110:111], v[190:191] op_sel_hi:[1,0]\n"
  "v_cvt_pk_bf16_f32 v240, v104, v105\n"
  "v_cvt_pk_bf16_f32 v241, v106, v107\n"
  "v_cvt_pk_bf16_f32 v242, v108, v109\n"
  "v_cvt_pk_bf16_f32 v243, v110, v111\n"
  "global_store_dwordx4 v219, v[240:243], s[46:47] offset:1024 sc1\n"
  "v_pk_mul_f32 v[112:113], v[112:113], v[190:191] op_sel_hi:[1,0]\n"
  "v_pk_mul_f32 v[114:115], v[114:115], v[190:191] op_sel_hi:[1,0]\n"
  "v_pk_mul_f32 v[116:117], v[116:117], v[190:191] op_sel_hi:[1,0]\n"
  "v_pk_mul_f32 v[118:119], v[118:119], v[190:191] op_sel_hi:[1,0]\n"
  "v_cvt_pk_bf16_f32 v244, v112, v113\n"
  "v_cvt_pk_bf16_f32 v245, v114, v115\n"
  "v_cvt_pk_bf16_f32 v246, v116, v117\n"
  "v_cvt_pk_bf16_f32 v247, v118, v119\n"
  "global_store_dwordx4 v219, v[244:247], s[46:47] offset:2048 sc1\n"
  "v_pk_mul_f32 v[120:121], v[120:121], v[190:191] op_sel_hi:[1,0]\n"
  "v_pk_mul_f32 v[122:123], v[122:123], v[190:191] op_sel_hi:[1,0]\n"
  "v_pk_mul_f32 v[124:125], v[124:125], v[190:191] op_sel_hi:[1,0]\n"
  "v_pk_mul_f32 v[126:127], v[126:127], v[190:191] op_sel_hi:[1,0]\n"
  "v_cvt_pk_bf16_f32 v248, v120, v121\n"
  "v_cvt_pk_bf16_f32 v249, v122, v123\n"
  "v_cvt_pk_bf16_f32 v250, v124, v125\n"
  "v_cvt_pk_bf16_f32 v251, v126, v127\n"
  "global_store_dwordx4 v219, v[248:251], s[46:47] offset:3072 sc1\n"
  "L_c0_%=:\n"
  "s_lshr_b32 s36, s35, 1\n"
  "s_lshl_b32 s36, s36, 13\n"
  "s_add_u32 s36, s36, s38\n"
  "v_add_u32 v180, s36, v232\n"
  "v_bfe_u32 v234, v233, 2, 3\n"
  "v_lshrrev_b32 v235, 5, v233\n"
  "v_lshlrev_b32 v235, 12, v235\n"
  "v_lshl_add_u32 v235, v234, 8, v235\n"
  "v_lshl_add_u32 v235, v234, 5, v235\n"
  "v_and_b32 v219, 3, v233\n"
  "v_lshl_add_u32 v235, v219, 3, v235\n"
  "s_add_u32 s36, s38, 2048\n"
  "v_add_u32 v235, s36, v235\n"
  "s_and_b32 s36, s35, 1\n"
  "s_lshl_b32 s36, s36, 7\n"
  "v_xor_b32 v176, s36, v235\n"
  "v_xor_b32 v177, 32, v176\n"
  "v_xor_b32 v178, 64, v176\n"
  "v_xor_b32 v179, 96, v176\n"
  "s_waitcnt vmcnt(36)\n"
  "s_barrier\n"
  "ds_read_b128 v[128:131], v180 offset:0\n"
  "ds_read_b128 v[132:135], v180 offset:1024\n"
  "ds_read_b128 v[136:139], v180 offset:4096\n"
  "ds_read_b128 v[140:143], v180 offset:5120\n"
  "ds_read_b64_tr_b16 v[160:161], v176 offset:0\n"
  "ds_read_b64_tr_b16 v[162:163], v176 offset:8192\n"
  "ds_read_b64_tr_b16 v[164:165], v177 offset:0\n"
  "ds_read_b64_tr_b16 v[166:167], v177 offset:8192\n"
  "ds_read_b64_tr_b16 v[168:169], v178 offset:0\n"
  "ds_read_b64_tr_b16 v[170:171], v178 offset:8192\n"
  "s_cmp_eq_u32 s56, 3\n"
  "s_cbranch_scc1 L_k0z_%=\n"
  "s_waitcnt lgkmcnt(4)\n"
  "v_mfma_f32_16x16x32_bf16 v[0:3], v[160:163], v[128:131], 0\n"
  "ds_read_b64_tr_b16 v[172:173], v179 offset:0\n"
  "ds_read_b64_tr_b16 v[174:175], v179 offset:8192\n"
  "v_mfma_f32_16x16x32_bf16 v[4:7], v[160:163], v[132:135], 0\n"
  "s_waitcnt vmcnt(32)\n"
  "s_barrier\n"
  "v_mfma_f32_16x16x32_bf16 v[8:11], v[160:163], v[136:139], 0\n"
  "v_mfma_f32_16x16x32_bf16 v[12:15], v[160:163], v[140:143], 0\n"
  "ds_read_b128 v[144:147], v180 offset:16384\n"
  "s_waitcnt lgkmcnt(5)\n"
  "v_mfma_f32_16x16x32_bf16 v[28:31], v[164:167], v[140:143], 0\n"
  "s_add_u32 m0, s48, 114688\n"
  "ds_read_b64_tr_b16 v[160:161], v176 offset:16384\n"
  "ds_read_b64_tr_b16 v[162:163], v176 offset:24576\n"
  "v_mfma_f32_16x16x32_bf16 v[24:27], v[164:167], v[136:139], 0\n"
  "global_load_lds_dwordx4 v232, s[50:51] sc1\n"
  "v_mfma_f32_16x16x32_bf16 v[20:23], v[164:167], v[132:135], 0\n"
  "ds_read_b128 v[148:151], v180 offset:17408\n"
  "v_mfma_f32_16x16x32_bf16 v[16:19], v[164:167], v[128:131], 0\n"
  "global_load_lds_dwordx4 v228, s[50:51] offset:1024 sc1\n"
  "s_waitcnt lgkmcnt(6)\n"
  "v_mfma_f32_16x16x32_bf16 v[32:35], v[168:171], v[128:131], 0\n"
  "ds_read_b64_tr_b16 v[164:165], v177 offset:16384\n"
  "ds_read_b64_tr_b16 v[166:167], v177 offset:24576\n"
  "v_mfma_f32_16x16x32_bf16 v[36:39], v[168:171], v[132:135], 0\n"
  "ds_read_b128 v[152:155], v180 offset:20480\n"
  "global_load_lds_dwordx4 v229, s[52:53] offset:2048\n"
  "v_mfma_f32_16x16x32_bf16 v[40:43], v[168:171], v[136:139], 0\n"
  "global_load_lds_dwordx4 v230, s[52:53] offset:3072\n"
  "v_mfma_f32_16x16x32_bf16 v[44:47], v[168:171], v[140:143], 0\n"
  "s_add_u32 s50, s50, 1024\n"
  "s_addc_u32 s51, s51, 0\n"
  "s_waitcnt lgkmcnt(7)\n"
  "v_mfma_f32_16x16x32_bf16 v[60:63], v[172:175], v[140:143], 0\n"
  "ds_read_b64_tr_b16 v[168:169], v178 offset:16384\n"
  "ds_read_b64_tr_b16 v[170:171], v178 offset:24576\n"
  "v_mfma_f32_16x16x32_bf16 v[56:59], v[172:175], v[136:139], 0\n"
  "s_add_u32 s52, s52, 32768\n"
  "s_addc_u32 s53, s53, 0\n"
  "ds_read_b128 v[156:159], v180 offset:21504\n"
  "v_mfma_f32_16x16x32_bf16 v[52:55], v[172:175], v[132:135], 0\n"
  "v_mfma_f32_16x16x32_bf16 v[48:51], v[172:175], v[128:131], 0\n"
  "s_branch L_k1_%=\n"
  "L_k0z_%=:\n"
  "s_waitcnt lgkmcnt(4)\n"
  "v_mfma_f32_16x16x32_bf16 v[0:3], v[160:163], v[224:227], 0\n"
  "ds_read_b64_tr_b16 v[172:173], v179 offset:0\n"
  "ds_read_b64_tr_b16 v[174:175], v179 offset:8192\n"
  "v_mfma_f32_16x16x32_bf16 v[4:7], v[160:163], v[224:227], 0\n"
  "s_waitcnt vmcnt(33)\n"
  "s_barrier\n"
  "v_mfma_f32_16x16x32_bf16 v[8:11], v[160:163], v[224:227], 0\n"
  "v_mfma_f32_16x16x32_bf16 v[12:15], v[160:163], v[224:227], 0\n"
  "ds_read_b128 v[144:147], v180 offset:16384\n"
  "s_waitcnt lgkmcnt(5)\n"
  "v_mfma_f32_16x16x32_bf16 v[28:31], v[164:167], v[224:227], 0\n"
  "s_add_u32 m0, s48, 114688\n"
  "ds_read_b64_tr_b16 v[160:161], v176 offset:16384\n"
  "ds_read_b64_tr_b16 v[162:163], v176 offset:24576\n"
  "v_mfma_f32_16x16x32_bf16 v[24:27], v[164:167], v[224:227], 0\n"
  "global_load_lds_dwordx4 v232, s[50:51] sc1\n"
  "v_mfma_f32_16x16x32_bf16 v[20:23], v[164:167], v[224:227], 0\n"
  "ds_read_b128 v[148:151], v180 offset:17408\n"
  "v_mfma_f32_16x16x32_bf16 v[16:19], v[164:167], v[224:227], 0\n"
  "global_load_lds_dwordx4 v228, s[50:51] offset:1024 sc1\n"
  "s_waitcnt lgkmcnt(6)\n"
  "v_mfma_f32_16x16x32_bf16 v[32:35], v[168:171], v[224:227], 0\n"
  "ds_read_b64_tr_b16 v[164:165], v177 offset:16384\n"
  "ds_read_b64_tr_b16 v[166:167], v177 offset:24576\n"
  "v_mfma_f32_16x16x32_bf16 v[36:39], v[168:171], v[224:227], 0\n"
  "ds_read_b128 v[152:155], v180 offset:20480\n"
  "global_load_lds_dwordx4 v229, s[52:53] offset:2048\n"
  "v_mfma_f32_16x16x32_bf16 v[40:43], v[168:171], v[224:227], 0\n"
  "global_load_lds_dwordx4 v230, s[52:53] offset:3072\n"
  "v_mfma_f32_16x16x32_bf16 v[44:47], v[168:171], v[224:227], 0\n"
  "s_add_u32 s50, s50, 1024\n"
  "s_addc_u32 s51, s51, 0\n"
  "s_waitcnt lgkmcnt(7)\n"
  "v_mfma_f32_16x16x32_bf16 v[60:63], v[172:175], v[224:227], 0\n"
  "ds_read_b64_tr_b16 v[168:169], v178 offset:16384\n"
  "ds_read_b64_tr_b16 v[170:171], v178 offset:24576\n"
  "v_mfma_f32_16x16x32_bf16 v[56:59], v[172:175], v[224:227], 0\n"
  "s_add_u32 s52, s52, 32768\n"
  "s_addc_u32 s53, s53, 0\n"
  "ds_read_b128 v[156:159], v180 offset:21504\n"
  "v_mfma_f32_16x16x32_bf16 v[52:55], v[172:175], v[224:227], 0\n"
  "v_mfma_f32_16x16x32_bf16 v[48:51], v[172:175], v[224:227], 0\n"
  "L_k1_%=:\n"
  "s_waitcnt lgkmcnt(7)\n"
  "v_mfma_f32_16x16x32_bf16 v[0:3], v[160:163], v[144:147], v[0:3]\n"
  "ds_read_b64_tr_b16 v[172:173], v179 offset:16384\n"
  "ds_read_b64_tr_b16 v[174:175], v179 offset:24576\n"
  "s_waitcnt lgkmcnt(8)\n"
  "v_mfma_f32_16x16x32_bf16 v[4:7], v[160:163], v[148:151], v[4:7]\n"
  "s_waitcnt vmcnt(32)\n"
  "s_barrier\n"
  "s_waitcnt lgkmcnt(5)\n"
  "v_mfma_f32_16x16x32_bf16 v[8:11], v[160:163], v[152:155], v[8:11]\n"
  "s_waitcnt lgkmcnt(2)\n"
  "v_mfma_f32_16x16x32_bf16 v[12:15], v[160:163], v[156:159], v[12:15]\n"
  "ds_read_b128 v[128:131], v180 offset:32768\n"
  "v_mfma_f32_16x16x32_bf16 v[28:31], v[164:167], v[156:159], v[28:31]\n"
  "s_add_u32 m0, s48, 0\n"
  "ds_read_b64_tr_b16 v[160:161], v176 offset:32768\n"
  "ds_read_b64_tr_b16 v[162:163], v176 offset:40960\n"
  "v_mfma_f32_16x16x32_bf16 v[24:27], v[164:167], v[152:155], v[24:27]\n"
  "global_load_lds_dwordx4 v232, s[50:51] sc1\n"
  "v_mfma_f32_16x16x32_bf16 v[20:23], v[164:167], v[148:151], v[20:23]\n"
  "ds_read_b128 v[132:135], v180 offset:33792\n"
  "v_mfma_f32_16x16x32_bf16 v[16:19], v[164:167], v[144:147], v[16:19]\n"
  "global_load_lds_dwordx4 v228, s[50:51] offset:1024 sc1\n"
  "v_mfma_f32_16x16x32_bf16 v[32:35], v[168:171], v[144:147], v[32:35]\n"
  "ds_read_b64_tr_b16 v[164:165], v177 offset:32768\n"
  "ds_read_b64_tr_b16 v[166:167], v177 offset:40960\n"
  "v_mfma_f32_16x16x32_bf16 v[36:39], v[168:171], v[148:151], v[36:39]\n"
  "ds_read_b128 v[136:139], v180 offset:36864\n"
  "global_load_lds_dwordx4 v229, s[52:53] offset:2048\n"
  "v_mfma_f32_16x16x32_bf16 v[40:43], v[168:171], v[152:155], v[40:43]\n"
  "global_load_lds_dwordx4 v230, s[52:53] offset:3072\n"
  "v_mfma_f32_16x16x32_bf16 v[44:47], v[168:171], v[156:159], v[44:47]\n"
  "s_add_u32 s50, s50, 1024\n"
  "s_addc_u32 s51, s51, 0\n"
  "s_waitcnt lgkmcnt(7)\n"
  "v_mfma_f32_16x16x32_bf16 v[60:63], v[172:175], v[156:159], v[60:63]\n"
  "ds_read_b64_tr_b16 v[168:169], v178 offset:32768\n"
  "ds_read_b64_tr_b16 v[170:171], v178 offset:40960\n"
  "v_mfma_f32_16x16x32_bf16 v[56:59], v[172:175], v[152:155], v[56:59]\n"
  "s_add_u32 s52, s52, 32768\n"
  "s_addc_u32 s53, s53, 0\n"
  "ds_read_b128 v[140:143], v180 offset:37888\n"
  "v_mfma_f32_16x16x32_bf16 v[52:55], v[172:175], v[148:151], v[52:55]\n"
  "v_mfma_f32_16x16x32_bf16 v[48:51], v[172:175], v[144:147], v[48:51]\n"
  "s_waitcnt lgkmcnt(7)\n"
  "v_mfma_f32_16x16x32_bf16 v[0:3], v[160:163], v[128:131], v[0:3]\n"
  "ds_read_b64_tr_b16 v[172:173], v179 offset:32768\n"
  "ds_read_b64_tr_b16 v[174:175], v179 offset:40960\n"
  "s_waitcnt lgkmcnt(8)\n"
  "v_mfma_f32_16x16x32_bf16 v[4:7], v[160:163], v[132:135], v[4:7]\n"
  "s_waitcnt vmcnt(31)\n"
  "s_waitcnt vmcnt(8)\n"
  "s_barrier\n"
  "s_waitcnt lgkmcnt(5)\n"
  "v_mfma_f32_16x16x32_bf16 v[8:11], v[160:163], v[136:139], v[8:11]\n"
  "s_waitcnt lgkmcnt(2)\n"
  "v_mfma_f32_16x16x32_bf16 v[12:15], v[160:163], v[140:143], v[12:15]\n"
  "ds_read_b128 v[144:147], v180 offset:49152\n"
  "v_mfma_f32_16x16x32_bf16 v[28:31], v[164:167], v[140:143], v[28:31]\n"
  "ds_read_b64_tr_b16 v[160:161], v176 offset:49152\n"
  "ds_read_b64_tr_b16 v[162:163], v176 offset:57344\n"
  "v_mfma_f32_16x16x32_bf16 v[24:27], v[164:167], v[136:139], v[24:27]\n"
  "v_mov_b32 v234, s16\n"
  "v_mov_b32 v235, s17\n"
  "v_mfma_f32_16x16x32_bf16 v[20:23], v[164:167], v[132:135], v[20:23]\n"
  "s_lshl_b32 s36, s86, 9\n"
  "ds_read_b128 v[148:151], v180 offset:50176\n"
  "v_mov_b32 v219, s36\n"
  "v_mfma_f32_16x16x32_bf16 v[16:19], v[164:167], v[128:131], v[16:19]\n"
  "v_mfma_f32_16x16x32_bf16 v[32:35], v[168:171], v[128:131], v[32:35]\n"
  "ds_read_b64_tr_b16 v[164:165], v177 offset:49152\n"
  "ds_read_b64_tr_b16 v[166:167], v177 offset:57344\n"
  "v_mfma_f32_16x16x32_bf16 v[36:39], v[168:171], v[132:135], v[36:39]\n"
  "ds_read_b128 v[152:155], v180 offset:53248\n"
  "v_mfma_f32_16x16x32_bf16 v[40:43], v[168:171], v[136:139], v[40:43]\n"
  "s_mov_b64 exec, 1\n"
  "global_store_dwordx2 v219, v[234:235], s[62:63] offset:192 sc1\n"
  "s_mov_b64 exec, -1\n"
  "v_mfma_f32_16x16x32_bf16 v[44:47], v[168:171], v[140:143], v[44:47]\n"
  "s_add_u32 m0, s48, 16384\n"
  "s_nop 0\n"
  "global_load_lds_dwordx4 v232, s[50:51] sc1\n"
  "s_waitcnt lgkmcnt(7)\n"
  "v_mfma_f32_16x16x32_bf16 v[60:63], v[172:175], v[140:143], v[60:63]\n"
  "ds_read_b64_tr_b16 v[168:169], v178 offset:49152\n"
  "ds_read_b64_tr_b16 v[170:171], v178 offset:57344\n"
  "v_mfma_f32_16x16x32_bf16 v[56:59], v[172:175], v[136:139], v[56:59]\n"
  "ds_read_b128 v[156:159], v180 offset:54272\n"
  "global_load_lds_dwordx4 v228, s[50:51] offset:1024 sc1\n"
  "v_mfma_f32_16x16x32_bf16 v[52:55], v[172:175], v[132:135], v[52:55]\n"
  "global_load_lds_dwordx4 v229, s[52:53] offset:2048\n"
  "global_load_lds_dwordx4 v230, s[52:53] offset:3072\n"
  "v_mfma_f32_16x16x32_bf16 v[48:51], v[172:175], v[128:131], v[48:51]\n"
  "s_add_u32 s50, s50, 1024\n"
  "s_addc_u32 s51, s51, 0\n"
  "s_add_u32 s52, s52, 32768\n"
  "s_addc_u32 s53, s53, 0\n"
  "s_waitcnt lgkmcnt(7)\n"
  "v_mfma_f32_16x16x32_bf16 v[0:3], v[160:163], v[144:147], v[0:3]\n"
  "ds_read_b64_tr_b16 v[172:173], v179 offset:49152\n"
  "ds_read_b64_tr_b16 v[174:175], v179 offset:57344\n"
  "s_waitcnt lgkmcnt(8)\n"
  "v_mfma_f32_16x16x32_bf16 v[4:7], v[160:163], v[148:151], v[4:7]\n"
  "s_barrier\n"
  "s_waitcnt lgkmcnt(5)\n"
  "v_mfma_f32_16x16x32_bf16 v[8:11], v[160:163], v[152:155], v[8:11]\n"
  "s_waitcnt lgkmcnt(2)\n"
  "v_mfma_f32_16x16x32_bf16 v[12:15], v[160:163], v[156:159], v[12:15]\n"
  "v_add_u32 v180, 0x10000, v180\n"
  "v_mfma_f32_16x16x32_bf16 v[28:31], v[164:167], v[156:159], v[28:31]\n"
  "s_add_u32 m0, s48, 32768\n"
  "ds_read_b128 v[128:131], v180 offset:0\n"
  "v_add_u32 v176, 0x10000, v176\n"
  "v_mfma_f32_16x16x32_bf16 v[24:27], v[164:167], v[152:155], v[24:27]\n"
  "ds_read_b64_tr_b16 v[160:161], v176 offset:0\n"
  "ds_read_b64_tr_b16 v[162:163], v176 offset:8192\n"
  "v_mfma_f32_16x16x32_bf16 v[20:23], v[164:167], v[148:151], v[20:23]\n"
  "ds_read_b128 v[132:135], v180 offset:1024\n"
  "global_load_lds_dwordx4 v232, s[50:51] sc1\n"
  "v_mfma_f32_16x16x32_bf16 v[16:19], v[164:167], v[144:147], v[16:19]\n"
  "global_load_lds_dwordx4 v228, s[50:51] offset:1024 sc1\n"
  "v_mfma_f32_16x16x32_bf16 v[32:35], v[168:171], v[144:147], v[32:35]\n"
  "global_load_lds_dwordx4 v229, s[52:53] offset:2048\n"
  "v_add_u32 v177, 0x10000, v177\n"
  "v_mfma_f32_16x16x32_bf16 v[36:39], v[168:171], v[148:151], v[36:39]\n"
  "ds_read_b64_tr_b16 v[164:165], v177 offset:0\n"
  "ds_read_b64_tr_b16 v[166:167], v177 offset:8192\n"
  "v_mfma_f32_16x16x32_bf16 v[40:43], v[168:171], v[152:155], v[40:43]\n"
  "ds_read_b128 v[136:139], v180 offset:4096\n"
  "global_load_lds_dwordx4 v230, s[52:53] offset:3072\n"
  "v_mfma_f32_16x16x32_bf16 v[44:47], v[168:171], v[156:159], v[44:47]\n"
  "s_add_u32 s50, s50, 1024\n"
  "s_addc_u32 s51, s51, 0\n"
  "s_waitcnt lgkmcnt(7)\n"
  "v_mfma_f32_16x16x32_bf16 v[60:63], v[172:175], v[156:159], v[60:63]\n"
  "ds_read_b128 v[140:143], v180 offset:5120\n"
  "v_add_u32 v178, 0x10000, v178\n"
  "v_mfma_f32_16x16x32_bf16 v[56:59], v[172:175], v[152:155], v[56:59]\n"
  "ds_read_b64_tr_b16 v[168:169], v178 offset:0\n"
  "ds_read_b64_tr_b16 v[170:171], v178 offset:8192\n"
  "v_mfma_f32_16x16x32_bf16 v[52:55], v[172:175], v[148:151], v[52:55]\n"
  "s_add_u32 s52, s52, 32768\n"
  "s_addc_u32 s53, s53, 0\n"
  "v_mfma_f32_16x16x32_bf16 v[48:51], v[172:175], v[144:147], v[48:51]\n"
  "s_waitcnt lgkmcnt(7)\n"
  "v_mfma_f32_16x16x32_bf16 v[0:3], v[160:163], v[128:131], v[0:3]\n"
  "v_add_u32 v179, 0x10000, v179\n"
  "s_waitcnt lgkmcnt(6)\n"
  "v_mfma_f32_16x16x32_bf16 v[4:7], v[160:163], v[132:135], v[4:7]\n"
  "ds_read_b64_tr_b16 v[172:173], v179 offset:0\n"
  "ds_read_b64_tr_b16 v[174:175], v179 offset:8192\n"
  "s_waitcnt lgkmcnt(5)\n"
  "v_mfma_f32_16x16x32_bf16 v[8:11], v[160:163], v[136:139], v[8:11]\n"
  "s_barrier\n"
  "s_waitcnt lgkmcnt(4)\n"
  "v_mfma_f32_16x16x32_bf16 v[12:15], v[160:163], v[140:143], v[12:15]\n"
  "ds_read_b128 v[144:147], v180 offset:16384\n"
  "v_mfma_f32_16x16x32_bf16 v[28:31], v[164:167], v[140:143], v[28:31]\n"
  "s_sub_u32 s36, 15, s57\n"
  "ds_read_b64_tr_b16 v[160:161], v176 offset:16384\n"
  "ds_read_b64_tr_b16 v[162:163], v176 offset:24576\n"
  "v_mfma_f32_16x16x32_bf16 v[24:27], v[164:167], v[136:139], v[24:27]\n"
  "s_min_u32 s36, s36, s57\n"
  "v_mfma_f32_16x16x32_bf16 v[20:23], v[164:167], v[132:135], v[20:23]\n"
  "s_lshl_b32 s36, s36, 5\n"
  "ds_read_b128 v[148:151], v180 offset:17408\n"
  "v_mfma_f32_16x16x32_bf16 v[16:19], v[164:167], v[128:131], v[16:19]\n"
  "s_lshl_b32 s37, s56, 1\n"
  "s_waitcnt lgkmcnt(6)\n"
  "v_mfma_f32_16x16x32_bf16 v[32:35], v[168:171], v[128:131], v[32:35]\n"
  "s_add_u32 s36, s36, s37\n"
  "ds_read_b64_tr_b16 v[164:165], v177 offset:16384\n"
  "ds_read_b64_tr_b16 v[166:167], v177 offset:24576\n"
  "v_mfma_f32_16x16x32_bf16 v[36:39], v[168:171], v[132:135], v[36:39]\n"
  "ds_read_b128 v[152:155], v180 offset:20480\n"
  "v_and_b32 v189, 3, v233\n"
  "v_mfma_f32_16x16x32_bf16 v[40:43], v[168:171], v[136:139], v[40:43]\n"
  "v_lshlrev_b32 v189, 3, v189\n"
  "v_mfma_f32_16x16x32_bf16 v[44:47], v[168:171], v[140:143], v[44:47]\n"
  "v_bfe_u32 v190, v233, 2, 1\n"
  "s_waitcnt lgkmcnt(7)\n"
  "v_mfma_f32_16x16x32_bf16 v[60:63], v[172:175], v[140:143], v[60:63]\n"
  "ds_read_b64_tr_b16 v[168:169], v178 offset:16384\n"
  "ds_read_b64_tr_b16 v[170:171], v178 offset:24576\n"
  "v_mfma_f32_16x16x32_bf16 v[56:59], v[172:175], v[136:139], v[56:59]\n"
  "ds_read_b128 v[156:159], v180 offset:21504\n"
  "v_add3_u32 v189, v189, v190, s36\n"
  "v_mfma_f32_16x16x32_bf16 v[52:55], v[172:175], v[132:135], v[52:55]\n"
  "v_lshlrev_b32 v189, 9, v189\n"
  "v_mfma_f32_16x16x32_bf16 v[48:51], v[172:175], v[128:131], v[48:51]\n"
  "s_waitcnt lgkmcnt(7)\n"
  "v_mfma_f32_16x16x32_bf16 v[0:3], v[160:163], v[144:147], v[0:3]\n"
  "ds_read_b64_tr_b16 v[172:173], v179 offset:16384\n"
  "ds_read_b64_tr_b16 v[174:175], v179 offset:24576\n"
  "s_waitcnt lgkmcnt(8)\n"
  "v_mfma_f32_16x16x32_bf16 v[4:7], v[160:163], v[148:151], v[4:7]\n"
  "s_barrier\n"
  "s_waitcnt lgkmcnt(5)\n"
  "v_mfma_f32_16x16x32_bf16 v[8:11], v[160:163], v[152:155], v[8:11]\n"
  "s_waitcnt lgkmcnt(2)\n"
  "v_mfma_f32_16x16x32_bf16 v[12:15], v[160:163], v[156:159], v[12:15]\n"
  "ds_read_b128 v[128:131], v180 offset:32768\n"
  "v_mfma_f32_16x16x32_bf16 v[28:31], v[164:167], v[156:159], v[28:31]\n"
  "s_lshl_b32 s36, s57, 2\n"
  "ds_read_b64_tr_b16 v[160:161], v176 offset:32768\n"
  "ds_read_b64_tr_b16 v[162:163], v176 offset:40960\n"
  "v_mfma_f32_16x16x32_bf16 v[24:27], v[164:167], v[152:155], v[24:27]\n"
  "s_add_u32 s36, s36, s35\n"
  "s_mul_i32 s36, s36, 342\n"
  "v_mfma_f32_16x16x32_bf16 v[20:23], v[164:167], v[148:151], v[20:23]\n"
  "s_lshl_b32 s37, s56, 5\n"
  "s_add_u32 s36, s36, s37\n"
  "ds_read_b128 v[132:135], v180 offset:33792\n"
  "v_mfma_f32_16x16x32_bf16 v[16:19], v[164:167], v[144:147], v[16:19]\n"
  "s_lshl_b32 s36, s36, 10\n"
  "v_mfma_f32_16x16x32_bf16 v[32:35], v[168:171], v[144:147], v[32:35]\n"
  "s_add_u32 s36, s36, 35651584\n"
  "ds_read_b64_tr_b16 v[164:165], v177 offset:32768\n"
  "ds_read_b64_tr_b16 v[166:167], v177 offset:40960\n"
  "v_mfma_f32_16x16x32_bf16 v[36:39], v[168:171], v[148:151], v[36:39]\n"
  "s_add_u32 s50, s84, s36\n"
  "s_addc_u32 s51, s85, 0\n"
  "ds_read_b128 v[136:139], v180 offset:36864\n"
  "v_mfma_f32_16x16x32_bf16 v[40:43], v[168:171], v[152:155], v[40:43]\n"
  "s_lshl_b32 s36, s56, 20\n"
  "s_lshl_b32 s37, s35, 13\n"
  "v_mfma_f32_16x16x32_bf16 v[44:47], v[168:171], v[156:159], v[44:47]\n"
  "s_add_u32 s36, s36, s37\n"
  "s_waitcnt lgkmcnt(7)\n"
  "v_mfma_f32_16x16x32_bf16 v[60:63], v[172:175], v[156:159], v[60:63]\n"
  "s_lshl_b32 s37, s58, 8\n"
  "ds_read_b64_tr_b16 v[168:169], v178 offset:32768\n"
  "ds_read_b64_tr_b16 v[170:171], v178 offset:40960\n"
  "v_mfma_f32_16x16x32_bf16 v[56:59], v[172:175], v[152:155], v[56:59]\n"
  "s_add_u32 s36, s36, s37\n"
  "ds_read_b128 v[140:143], v180 offset:37888\n"
  "v_mfma_f32_16x16x32_bf16 v[52:55], v[172:175], v[148:151], v[52:55]\n"
  "s_add_u32 s52, s74, s36\n"
  "s_addc_u32 s53, s75, 0\n"
  "v_mfma_f32_16x16x32_bf16 v[48:51], v[172:175], v[144:147], v[48:51]\n"
  "s_sub_u32 s52, s52, 4096\n"
  "s_subb_u32 s53, s53, 0\n"
  "s_waitcnt lgkmcnt(7)\n"
  "v_mfma_f32_16x16x32_bf16 v[0:3], v[160:163], v[128:131], v[0:3]\n"
  "ds_read_b64_tr_b16 v[172:173], v179 offset:32768\n"
  "ds_read_b64_tr_b16 v[174:175], v179 offset:40960\n"
  "s_waitcnt lgkmcnt(8)\n"
  "v_mfma_f32_16x16x32_bf16 v[4:7], v[160:163], v[132:135], v[4:7]\n"
  "s_waitcnt vmcnt(13)\n"
  "s_barrier\n"
  "s_waitcnt lgkmcnt(5)\n"
  "v_mfma_f32_16x16x32_bf16 v[8:11], v[160:163], v[136:139], v[8:11]\n"
  "s_waitcnt lgkmcnt(2)\n"
  "v_mfma_f32_16x16x32_bf16 v[12:15], v[160:163], v[140:143], v[12:15]\n"
  "ds_read_b128 v[144:147], v180 offset:49152\n"
  "v_mfma_f32_16x16x32_bf16 v[28:31], v[164:167], v[140:143], v[28:31]\n"
  "ds_read_b64_tr_b16 v[160:161], v176 offset:49152\n"
  "ds_read_b64_tr_b16 v[162:163], v176 offset:57344\n"
  "v_mfma_f32_16x16x32_bf16 v[24:27], v[164:167], v[136:139], v[24:27]\n"
  "global_load_dwordx2 v[190:191], v189, s[62:63] offset:192 sc1\n"
  "v_mfma_f32_16x16x32_bf16 v[20:23], v[164:167], v[132:135], v[20:23]\n"
  "ds_read_b128 v[148:151], v180 offset:50176\n"
  "v_mfma_f32_16x16x32_bf16 v[16:19], v[164:167], v[128:131], v[16:19]\n"
  "v_mfma_f32_16x16x32_bf16 v[32:35], v[168:171], v[128:131], v[32:35]\n"
  "ds_read_b64_tr_b16 v[164:165], v177 offset:49152\n"
  "ds_read_b64_tr_b16 v[166:167], v177 offset:57344\n"
  "v_mfma_f32_16x16x32_bf16 v[36:39], v[168:171], v[132:135], v[36:39]\n"
  "ds_read_b128 v[152:155], v180 offset:53248\n"
  "v_mfma_f32_16x16x32_bf16 v[40:43], v[168:171], v[136:139], v[40:43]\n"
  "v_mfma_f32_16x16x32_bf16 v[44:47], v[168:171], v[140:143], v[44:47]\n"
  "s_waitcnt lgkmcnt(7)\n"
  "v_mfma_f32_16x16x32_bf16 v[60:63], v[172:175], v[140:143], v[60:63]\n"
  "ds_read_b64_tr_b16 v[168:169], v178 offset:49152\n"
  "ds_read_b64_tr_b16 v[170:171], v178 offset:57344\n"
  "v_mfma_f32_16x16x32_bf16 v[56:59], v[172:175], v[136:139], v[56:59]\n"
  "ds_read_b128 v[156:159], v180 offset:54272\n"
  "v_mfma_f32_16x16x32_bf16 v[52:55], v[172:175], v[132:135], v[52:55]\n"
  "v_mfma_f32_16x16x32_bf16 v[48:51], v[172:175], v[128:131], v[48:51]\n"
  "s_waitcnt lgkmcnt(7)\n"
  "v_mfma_f32_16x16x32_bf16 v[0:3], v[160:163], v[144:147], v[0:3]\n"
  "ds_read_b64_tr_b16 v[172:173], v179 offset:49152\n"
  "ds_read_b64_tr_b16 v[174:175], v179 offset:57344\n"
  "s_waitcnt lgkmcnt(8)\n"
  "v_mfma_f32_16x16x32_bf16 v[4:7], v[160:163], v[148:151], v[4:7]\n"
  "s_waitcnt vmcnt(10)\n"
  "s_barrier\n"
  "s_waitcnt lgkmcnt(5)\n"
  "v_mfma_f32_16x16x32_bf16 v[8:11], v[160:163], v[152:155], v[8:11]\n"
  "s_waitcnt lgkmcnt(2)\n"
  "v_mfma_f32_16x16x32_bf16 v[12:15], v[160:163], v[156:159], v[12:15]\n"
  "v_add_u32 v180, 0xffff0000, v180\n"
  "v_mfma_f32_16x16x32_bf16 v[28:31], v[164:167], v[156:159], v[28:31]\n"
  "ds_read_b128 v[128:131], v180 offset:0\n"
  "v_add_u32 v176, 0xffff0000, v176\n"
  "v_mfma_f32_16x16x32_bf16 v[24:27], v[164:167], v[152:155], v[24:27]\n"
  "ds_read_b64_tr_b16 v[160:161], v176 offset:0\n"
  "ds_read_b64_tr_b16 v[162:163], v176 offset:8192\n"
  "v_mfma_f32_16x16x32_bf16 v[20:23], v[164:167], v[148:151], v[20:23]\n"
  "ds_read_b128 v[132:135], v180 offset:1024\n"
  "v_mfma_f32_16x16x32_bf16 v[16:19], v[164:167], v[144:147], v[16:19]\n"
  "v_mfma_f32_16x16x32_bf16 v[32:35], v[168:171], v[144:147], v[32:35]\n"
  "v_add_u32 v177, 0xffff0000, v177\n"
  "v_mfma_f32_16x16x32_bf16 v[36:39], v[168:171], v[148:151], v[36:39]\n"
  "ds_read_b64_tr_b16 v[164:165], v177 offset:0\n"
  "ds_read_b64_tr_b16 v[166:167], v177 offset:8192\n"
  "v_mfma_f32_16x16x32_bf16 v[40:43], v[168:171], v[152:155], v[40:43]\n"
  "ds_read_b128 v[136:139], v180 offset:4096\n"
  "v_mfma_f32_16x16x32_bf16 v[44:47], v[168:171], v[156:159], v[44:47]\n"
  "s_waitcnt lgkmcnt(7)\n"
  "v_mfma_f32_16x16x32_bf16 v[60:63], v[172:175], v[156:159], v[60:63]\n"
  "ds_read_b128 v[140:143], v180 offset:5120\n"
  "v_add_u32 v178, 0xffff0000, v178\n"
  "v_mfma_f32_16x16x32_bf16 v[56:59], v[172:175], v[152:155], v[56:59]\n"
  "ds_read_b64_tr_b16 v[168:169], v178 offset:0\n"
  "ds_read_b64_tr_b16 v[170:171], v178 offset:8192\n"
  "v_mfma_f32_16x16x32_bf16 v[52:55], v[172:175], v[148:151], v[52:55]\n"
  "v_mfma_f32_16x16x32_bf16 v[48:51], v[172:175], v[144:147], v[48:51]\n"
  "s_waitcnt vmcnt(0)\n"
  "v_cmp_ne_u64 s[96:97], s[16:17], v[190:191]\n"
  "s_nop 0\n"
  "s_cmp_lg_u64 s[96:97], 0\n"
  "s_cbranch_scc1 L_late_%=\n"
  "s_waitcnt lgkmcnt(7)\n"
  "v_mfma_f32_16x16x32_bf16 v[0:3], v[160:163], v[128:131], v[0:3]\n"
  "v_add_u32 v179, 0xffff0000, v179\n"
  "s_waitcnt lgkmcnt(6)\n"
  "v_mfma_f32_16x16x32_bf16 v[4:7], v[160:163], v[132:135], v[4:7]\n"
  "ds_read_b64_tr_b16 v[172:173], v179 offset:0\n"
  "ds_read_b64_tr_b16 v[174:175], v179 offset:8192\n"
  "s_waitcnt lgkmcnt(5)\n"
  "v_mfma_f32_16x16x32_bf16 v[8:11], v[160:163], v[136:139], v[8:11]\n"
  "s_barrier\n"
  "s_waitcnt lgkmcnt(4)\n"
  "v_mfma_f32_16x16x32_bf16 v[12:15], v[160:163], v[140:143], v[12:15]\n"
  "ds_read_b128 v[144:147], v180 offset:16384\n"
  "v_mfma_f32_16x16x32_bf16 v[28:31], v[164:167], v[140:143], v[28:31]\n"
  "s_add_u32 m0, s48, 49152\n"
  "ds_read_b64_tr_b16 v[160:161], v176 offset:16384\n"
  "ds_read_b64_tr_b16 v[162:163], v176 offset:24576\n"
  "v_mfma_f32_16x16x32_bf16 v[24:27], v[164:167], v[136:139], v[24:27]\n"
  "global_load_lds_dwordx4 v232, s[50:51] sc1\n"
  "global_load_lds_dwordx4 v228, s[50:51] offset:1024 sc1\n"
  "v_mfma_f32_16x16x32_bf16 v[20:23], v[164:167], v[132:135], v[20:23]\n"
  "ds_read_b128 v[148:151], v180 offset:17408\n"
  "global_load_lds_dwordx4 v229, s[52:53] offset:2048\n"
  "v_mfma_f32_16x16x32_bf16 v[16:19], v[164:167], v[128:131], v[16:19]\n"
  "global_load_lds_dwordx4 v230, s[52:53] offset:3072\n"
  "s_waitcnt lgkmcnt(6)\n"
  "v_mfma_f32_16x16x32_bf16 v[32:35], v[168:171], v[128:131], v[32:35]\n"
  "ds_read_b64_tr_b16 v[164:165], v177 offset:16384\n"
  "ds_read_b64_tr_b16 v[166:167], v177 offset:24576\n"
  "v_mfma_f32_16x16x32_bf16 v[36:39], v[168:171], v[132:135], v[36:39]\n"
  "s_add_u32 s50, s50, 1024\n"
  "s_addc_u32 s51, s51, 0\n"
  "ds_read_b128 v[152:155], v180 offset:20480\n"
  "v_mfma_f32_16x16x32_bf16 v[40:43], v[168:171], v[136:139], v[40:43]\n"
  "s_add_u32 s52, s52, 32768\n"
  "s_addc_u32 s53, s53, 0\n"
  "s_add_u32 m0, s48, 65536\n"
  "v_mfma_f32_16x16x32_bf16 v[44:47], v[168:171], v[140:143], v[44:47]\n"
  "global_load_lds_dwordx4 v232, s[50:51] sc1\n"
  "global_load_lds_dwordx4 v228, s[50:51] offset:1024 sc1\n"
  "s_waitcnt lgkmcnt(7)\n"
  "v_mfma_f32_16x16x32_bf16 v[60:63], v[172:175], v[140:143], v[60:63]\n"
  "ds_read_b64_tr_b16 v[168:169], v178 offset:16384\n"
  "ds_read_b64_tr_b16 v[170:171], v178 offset:24576\n"
  "v_mfma_f32_16x16x32_bf16 v[56:59], v[172:175], v[136:139], v[56:59]\n"
  "ds_read_b128 v[156:159], v180 offset:21504\n"
  "global_load_lds_dwordx4 v229, s[52:53] offset:2048\n"
  "v_mfma_f32_16x16x32_bf16 v[52:55], v[172:175], v[132:135], v[52:55]\n"
  "global_load_lds_dwordx4 v230, s[52:53] offset:3072\n"
  "s_add_u32 s50, s50, 1024\n"
  "s_addc_u32 s51, s51, 0\n"
  "v_mfma_f32_16x16x32_bf16 v[48:51], v[172:175], v[128:131], v[48:51]\n"
  "s_add_u32 s52, s52, 32768\n"
  "s_addc_u32 s53, s53, 0\n"
  "s_add_u32 m0, s48, 81920\n"
  "s_nop 0\n"
  "global_load_lds_dwordx4 v232, s[50:51] sc1\n"
  "global_load_lds_dwordx4 v228, s[50:51] offset:1024 sc1\n"
  "global_load_lds_dwordx4 v229, s[52:53] offset:2048\n"
  "global_load_lds_dwordx4 v230, s[52:53] offset:3072\n"
  "s_add_u32 s50, s50, 1024\n"
  "s_addc_u32 s51, s51, 0\n"
  "s_add_u32 s52, s52, 32768\n"
  "s_addc_u32 s53, s53, 0\n"
  "s_add_u32 m0, s48, 98304\n"
  "s_nop 0\n"
  "global_load_lds_dwordx4 v232, s[50:51] sc1\n"
  "global_load_lds_dwordx4 v228, s[50:51] offset:1024 sc1\n"
  "global_load_lds_dwordx4 v229, s[52:53] offset:2048\n"
  "global_load_lds_dwordx4 v230, s[52:53] offset:3072\n"
  "s_add_u32 s50, s50, 1024\n"
  "s_addc_u32 s51, s51, 0\n"
  "s_add_u32 s52, s52, 32768\n"
  "s_addc_u32 s53, s53, 0\n"
  "s_add_u32 m0, s48, 114688\n"
  "s_nop 0\n"
  "global_load_lds_dwordx4 v232, s[50:51] sc1\n"
  "global_load_lds_dwordx4 v228, s[50:51] offset:1024 sc1\n"
  "global_load_lds_dwordx4 v229, s[52:53] offset:2048\n"
  "global_load_lds_dwordx4 v230, s[52:53] offset:3072\n"
  "s_add_u32 s50, s50, 1024\n"
  "s_addc_u32 s51, s51, 0\n"
  "s_add_u32 s52, s52, 32768\n"
  "s_addc_u32 s53, s53, 0\n"
  "s_waitcnt lgkmcnt(7)\n"
  "v_mfma_f32_16x16x32_bf16 v[0:3], v[160:163], v[144:147], v[0:3]\n"
  "ds_read_b64_tr_b16 v[172:173], v179 offset:16384\n"
  "ds_read_b64_tr_b16 v[174:175], v179 offset:24576\n"
  "s_waitcnt lgkmcnt(8)\n"
  "v_mfma_f32_16x16x32_bf16 v[4:7], v[160:163], v[148:151], v[4:7]\n"
  "s_barrier\n"
  "s_waitcnt lgkmcnt(5)\n"
  "v_mfma_f32_16x16x32_bf16 v[8:11], v[160:163], v[152:155], v[8:11]\n"
  "s_waitcnt lgkmcnt(2)\n"
  "v_mfma_f32_16x16x32_bf16 v[12:15], v[160:163], v[156:159], v[12:15]\n"
  "ds_read_b128 v[128:131], v180 offset:32768\n"
  "v_mfma_f32_16x16x32_bf16 v[28:31], v[164:167], v[156:159], v[28:31]\n"
  "s_add_u32 m0, s48, 0\n"
  "ds_read_b64_tr_b16 v[160:161], v176 offset:32768\n"
  "ds_read_b64_tr_b16 v[162:163], v176 offset:40960\n"
  "v_mfma_f32_16x16x32_bf16 v[24:27], v[164:167], v[152:155], v[24:27]\n"
  "global_load_lds_dwordx4 v232, s[50:51] sc1\n"
  "v_mfma_f32_16x16x32_bf16 v[20:23], v[164:167], v[148:151], v[20:23]\n"
  "ds_read_b128 v[132:135], v180 offset:33792\n"
  "v_mfma_f32_16x16x32_bf16 v[16:19], v[164:167], v[144:147], v[16:19]\n"
  "global_load_lds_dwordx4 v228, s[50:51] offset:1024 sc1\n"
  "v_mfma_f32_16x16x32_bf16 v[32:35], v[168:171], v[144:147], v[32:35]\n"
  "ds_read_b64_tr_b16 v[164:165], v177 offset:32768\n"
  "ds_read_b64_tr_b16 v[166:167], v177 offset:40960\n"
  "v_mfma_f32_16x16x32_bf16 v[36:39], v[168:171], v[148:151], v[36:39]\n"
  "ds_read_b128 v[136:139], v180 offset:36864\n"
  "global_load_lds_dwordx4 v229, s[52:53] offset:2048\n"
  "v_mfma_f32_16x16x32_bf16 v[40:43], v[168:171], v[152:155], v[40:43]\n"
  "global_load_lds_dwordx4 v230, s[52:53] offset:3072\n"
  "v_mfma_f32_16x16x32_bf16 v[44:47], v[168:171], v[156:159], v[44:47]\n"
  "s_add_u32 s50, s50, 1024\n"
  "s_addc_u32 s51, s51, 0\n"
  "s_waitcnt lgkmcnt(7)\n"
  "v_mfma_f32_16x16x32_bf16 v[60:63], v[172:175], v[156:159], v[60:63]\n"
  "ds_read_b64_tr_b16 v[168:169], v178 offset:32768\n"
  "ds_read_b64_tr_b16 v[170:171], v178 offset:40960\n"
  "v_mfma_f32_16x16x32_bf16 v[56:59], v[172:175], v[152:155], v[56:59]\n"
  "s_add_u32 s52, s52, 32768\n"
  "s_addc_u32 s53, s53, 0\n"
  "ds_read_b128 v[140:143], v180 offset:37888\n"
  "v_mfma_f32_16x16x32_bf16 v[52:55], v[172:175], v[148:151], v[52:55]\n"
  "v_mfma_f32_16x16x32_bf16 v[48:51], v[172:175], v[144:147], v[48:51]\n"
  "s_waitcnt lgkmcnt(7)\n"
  "v_mfma_f32_16x16x32_bf16 v[0:3], v[160:163], v[128:131], v[0:3]\n"
  "ds_read_b64_tr_b16 v[172:173], v179 offset:32768\n"
  "ds_read_b64_tr_b16 v[174:175], v179 offset:40960\n"
  "s_waitcnt lgkmcnt(8)\n"
  "v_mfma_f32_16x16x32_bf16 v[4:7], v[160:163], v[132:135], v[4:7]\n"
  "s_waitcnt vmcnt(20)\n"
  "s_barrier\n"
  "s_waitcnt lgkmcnt(5)\n"
  "v_mfma_f32_16x16x32_bf16 v[8:11], v[160:163], v[136:139], v[8:11]\n"
  "s_waitcnt lgkmcnt(2)\n"
  "v_mfma_f32_16x16x32_bf16 v[12:15], v[160:163], v[140:143], v[12:15]\n"
  "ds_read_b128 v[144:147], v180 offset:49152\n"
  "v_mfma_f32_16x16x32_bf16 v[28:31], v[164:167], v[140:143], v[28:31]\n"
  "s_add_u32 m0, s48, 16384\n"
  "ds_read_b64_tr_b16 v[160:161], v176 offset:49152\n"
  "ds_read_b64_tr_b16 v[162:163], v176 offset:57344\n"
  "v_mfma_f32_16x16x32_bf16 v[24:27], v[164:167], v[136:139], v[24:27]\n"
  "global_load_lds_dwordx4 v232, s[50:51] sc1\n"
  "v_mfma_f32_16x16x32_bf16 v[20:23], v[164:167], v[132:135], v[20:23]\n"
  "ds_read_b128 v[148:151], v180 offset:50176\n"
  "v_mfma_f32_16x16x32_bf16 v[16:19], v[164:167], v[128:131], v[16:19]\n"
  "global_load_lds_dwordx4 v228, s[50:51] offset:1024 sc1\n"
  "v_mfma_f32_16x16x32_bf16 v[32:35], v[168:171], v[128:131], v[32:35]\n"
  "ds_read_b64_tr_b16 v[164:165], v177 offset:49152\n"
  "ds_read_b64_tr_b16 v[166:167], v177 offset:57344\n"
  "v_mfma_f32_16x16x32_bf16 v[36:39], v[168:171], v[132:135], v[36:39]\n"
  "ds_read_b128 v[152:155], v180 offset:53248\n"
  "global_load_lds_dwordx4 v229, s[52:53] offset:2048\n"
  "v_mfma_f32_16x16x32_bf16 v[40:43], v[168:171], v[136:139], v[40:43]\n"
  "global_load_lds_dwordx4 v230, s[52:53] offset:3072\n"
  "v_mfma_f32_16x16x32_bf16 v[44:47], v[168:171], v[140:143], v[44:47]\n"
  "s_add_u32 s50, s50, 1024\n"
  "s_addc_u32 s51, s51, 0\n"
  "s_waitcnt lgkmcnt(7)\n"
  "v_mfma_f32_16x16x32_bf16 v[60:63], v[172:175], v[140:143], v[60:63]\n"
  "ds_read_b64_tr_b16 v[168:169], v178 offset:49152\n"
  "ds_read_b64_tr_b16 v[170:171], v178 offset:57344\n"
  "v_mfma_f32_16x16x32_bf16 v[56:59], v[172:175], v[136:139], v[56:59]\n"
  "s_add_u32 s52, s52, 32768\n"
  "s_addc_u32 s53, s53, 0\n"
  "ds_read_b128 v[156:159], v180 offset:54272\n"
  "v_mfma_f32_16x16x32_bf16 v[52:55], v[172:175], v[132:135], v[52:55]\n"
  "v_mfma_f32_16x16x32_bf16 v[48:51], v[172:175], v[128:131], v[48:51]\n"
  "s_branch L_k11_%=\n"
  "L_late_%=:\n"
  "s_waitcnt lgkmcnt(7)\n"
  "v_mfma_f32_16x16x32_bf16 v[0:3], v[160:163], v[128:131], v[0:3]\n"
  "v_add_u32 v179, 0xffff0000, v179\n"
  "s_waitcnt lgkmcnt(6)\n"
  "v_mfma_f32_16x16x32_bf16 v[4:7], v[160:163], v[132:135], v[4:7]\n"
  "ds_read_b64_tr_b16 v[172:173], v179 offset:0\n"
  "ds_read_b64_tr_b16 v[174:175], v179 offset:8192\n"
  "s_waitcnt lgkmcnt(5)\n"
  "v_mfma_f32_16x16x32_bf16 v[8:11], v[160:163], v[136:139], v[8:11]\n"
  "s_barrier\n"
  "s_waitcnt lgkmcnt(4)\n"
  "v_mfma_f32_16x16x32_bf16 v[12:15], v[160:163], v[140:143], v[12:15]\n"
  "ds_read_b128 v[144:147], v180 offset:16384\n"
  "v_mfma_f32_16x16x32_bf16 v[28:31], v[164:167], v[140:143], v[28:31]\n"
  "ds_read_b64_tr_b16 v[160:161], v176 offset:16384\n"
  "ds_read_b64_tr_b16 v[162:163], v176 offset:24576\n"
  "v_mfma_f32_16x16x32_bf16 v[24:27], v[164:167], v[136:139], v[24:27]\n"
  "v_mfma_f32_16x16x32_bf16 v[20:23], v[164:167], v[132:135], v[20:23]\n"
  "ds_read_b128 v[148:151], v180 offset:17408\n"
  "v_mfma_f32_16x16x32_bf16 v[16:19], v[164:167], v[128:131], v[16:19]\n"
  "s_waitcnt lgkmcnt(6)\n"
  "v_mfma_f32_16x16x32_bf16 v[32:35], v[168:171], v[128:131], v[32:35]\n"
  "ds_read_b64_tr_b16 v[164:165], v177 offset:16384\n"
  "ds_read_b64_tr_b16 v[166:167], v177 offset:24576\n"
  "v_mfma_f32_16x16x32_bf16 v[36:39], v[168:171], v[132:135], v[36:39]\n"
  "ds_read_b128 v[152:155], v180 offset:20480\n"
  "v_mfma_f32_16x16x32_bf16 v[40:43], v[168:171], v[136:139], v[40:43]\n"
  "v_mfma_f32_16x16x32_bf16 v[44:47], v[168:171], v[140:143], v[44:47]\n"
  "s_waitcnt lgkmcnt(7)\n"
  "v_mfma_f32_16x16x32_bf16 v[60:63], v[172:175], v[140:143], v[60:63]\n"
  "ds_read_b64_tr_b16 v[168:169], v178 offset:16384\n"
  "ds_read_b64_tr_b16 v[170:171], v178 offset:24576\n"
  "v_mfma_f32_16x16x32_bf16 v[56:59], v[172:175], v[136:139], v[56:59]\n"
  "ds_read_b128 v[156:159], v180 offset:21504\n"
  "v_mfma_f32_16x16x32_bf16 v[52:55], v[172:175], v[132:135], v[52:55]\n"
  "v_mfma_f32_16x16x32_bf16 v[48:51], v[172:175], v[128:131], v[48:51]\n"
  "s_waitcnt lgkmcnt(7)\n"
  "v_mfma_f32_16x16x32_bf16 v[0:3], v[160:163], v[144:147], v[0:3]\n"
  "ds_read_b64_tr_b16 v[172:173], v179 offset:16384\n"
  "ds_read_b64_tr_b16 v[174:175], v179 offset:24576\n"
  "s_waitcnt lgkmcnt(8)\n"
  "v_mfma_f32_16x16x32_bf16 v[4:7], v[160:163], v[148:151], v[4:7]\n"
  "s_barrier\n"
  "s_waitcnt lgkmcnt(5)\n"
  "v_mfma_f32_16x16x32_bf16 v[8:11], v[160:163], v[152:155], v[8:11]\n"
  "s_waitcnt lgkmcnt(2)\n"
  "v_mfma_f32_16x16x32_bf16 v[12:15], v[160:163], v[156:159], v[12:15]\n"
  "ds_read_b128 v[128:131], v180 offset:32768\n"
  "v_mfma_f32_16x16x32_bf16 v[28:31], v[164:167], v[156:159], v[28:31]\n"
  "ds_read_b64_tr_b16 v[160:161], v176 offset:32768\n"
  "ds_read_b64_tr_b16 v[162:163], v176 offset:40960\n"
  "v_mfma_f32_16x16x32_bf16 v[24:27], v[164:167], v[152:155], v[24:27]\n"
  "v_mfma_f32_16x16x32_bf16 v[20:23], v[164:167], v[148:151], v[20:23]\n"
  "ds_read_b128 v[132:135], v180 offset:33792\n"
  "v_mfma_f32_16x16x32_bf16 v[16:19], v[164:167], v[144:147], v[16:19]\n"
  "v_mfma_f32_16x16x32_bf16 v[32:35], v[168:171], v[144:147], v[32:35]\n"
  "ds_read_b64_tr_b16 v[164:165], v177 offset:32768\n"
  "ds_read_b64_tr_b16 v[166:167], v177 offset:40960\n"
  "v_mfma_f32_16x16x32_bf16 v[36:39], v[168:171], v[148:151], v[36:39]\n"
  "ds_read_b128 v[136:139], v180 offset:36864\n"
  "v_mfma_f32_16x16x32_bf16 v[40:43], v[168:171], v[152:155], v[40:43]\n"
  "v_mfma_f32_16x16x32_bf16 v[44:47], v[168:171], v[156:159], v[44:47]\n"
  "s_waitcnt lgkmcnt(7)\n"
  "v_mfma_f32_16x16x32_bf16 v[60:63], v[172:175], v[156:159], v[60:63]\n"
  "ds_read_b64_tr_b16 v[168:169], v178 offset:32768\n"
  "ds_read_b64_tr_b16 v[170:171], v178 offset:40960\n"
  "v_mfma_f32_16x16x32_bf16 v[56:59], v[172:175], v[152:155], v[56:59]\n"
  "ds_read_b128 v[140:143], v180 offset:37888\n"
  "v_mfma_f32_16x16x32_bf16 v[52:55], v[172:175], v[148:151], v[52:55]\n"
  "v_mfma_f32_16x16x32_bf16 v[48:51], v[172:175], v[144:147], v[48:51]\n"
  "s_waitcnt lgkmcnt(7)\n"
  "v_mfma_f32_16x16x32_bf16 v[0:3], v[160:163], v[128:131], v[0:3]\n"
  "ds_read_b64_tr_b16 v[172:173], v179 offset:32768\n"
  "ds_read_b64_tr_b16 v[174:175], v179 offset:40960\n"
  "s_waitcnt lgkmcnt(8)\n"
  "v_mfma_f32_16x16x32_bf16 v[4:7], v[160:163], v[132:135], v[4:7]\n"
  "s_waitcnt lgkmcnt(5)\n"
  "v_mfma_f32_16x16x32_bf16 v[8:11], v[160:163], v[136:139], v[8:11]\n"
  "s_waitcnt lgkmcnt(2)\n"
  "v_mfma_f32_16x16x32_bf16 v[12:15], v[160:163], v[140:143], v[12:15]\n"
  "v_mfma_f32_16x16x32_bf16 v[28:31], v[164:167], v[140:143], v[28:31]\n"
  "v_mfma_f32_16x16x32_bf16 v[24:27], v[164:167], v[136:139], v[24:27]\n"
  "v_mfma_f32_16x16x32_bf16 v[20:23], v[164:167], v[132:135], v[20:23]\n"
  "v_mfma_f32_16x16x32_bf16 v[16:19], v[164:167], v[128:131], v[16:19]\n"
  "v_mfma_f32_16x16x32_bf16 v[32:35], v[168:171], v[128:131], v[32:35]\n"
  "v_mfma_f32_16x16x32_bf16 v[36:39], v[168:171], v[132:135], v[36:39]\n"
  "v_mfma_f32_16x16x32_bf16 v[40:43], v[168:171], v[136:139], v[40:43]\n"
  "v_mfma_f32_16x16x32_bf16 v[44:47], v[168:171], v[140:143], v[44:47]\n"
  "s_waitcnt lgkmcnt(0)\n"
  "v_mfma_f32_16x16x32_bf16 v[60:63], v[172:175], v[140:143], v[60:63]\n"
  "v_mfma_f32_16x16x32_bf16 v[56:59], v[172:175], v[136:139], v[56:59]\n"
  "v_mfma_f32_16x16x32_bf16 v[52:55], v[172:175], v[132:135], v[52:55]\n"
  "v_mfma_f32_16x16x32_bf16 v[48:51], v[172:175], v[128:131], v[48:51]\n"
  "global_load_dwordx2 v[190:191], v189, s[62:63] offset:192 sc1\n"
  "s_waitcnt vmcnt(0)\n"
  "v_cmp_ne_u64 s[96:97], s[16:17], v[190:191]\n"
  "s_nop 0\n"
  "s_cmp_lg_u64 s[96:97], 0\n"
  "s_cbranch_scc1 L_o_retry_%=\n"
  "L_o_ok_%=:\n"
  "s_add_u32 m0, s48, 49152\n"
  "s_nop 0\n"
  "global_load_lds_dwordx4 v232, s[50:51] sc1\n"
  "global_load_lds_dwordx4 v228, s[50:51] offset:1024 sc1\n"
  "global_load_lds_dwordx4 v229, s[52:53] offset:2048\n"
  "global_load_lds_dwordx4 v230, s[52:53] offset:3072\n"
  "s_add_u32 s50, s50, 1024\n"
  "s_addc_u32 s51, s51, 0\n"
  "s_add_u32 s52, s52, 32768\n"
  "s_addc_u32 s53, s53, 0\n"
  "s_add_u32 m0, s48, 65536\n"
  "s_nop 0\n"
  "global_load_lds_dwordx4 v232, s[50:51] sc1\n"
  "global_load_lds_dwordx4 v228, s[50:51] offset:1024 sc1\n"
  "global_load_lds_dwordx4 v229, s[52:53] offset:2048\n"
  "global_load_lds_dwordx4 v230, s[52:53] offset:3072\n"
  "s_add_u32 s50, s50, 1024\n"
  "s_addc_u32 s51, s51, 0\n"
  "s_add_u32 s52, s52, 32768\n"
  "s_addc_u32 s53, s53, 0\n"
  "s_add_u32 m0, s48, 81920\n"
  "s_nop 0\n"
  "global_load_lds_dwordx4 v232, s[50:51] sc1\n"
  "global_load_lds_dwordx4 v228, s[50:51] offset:1024 sc1\n"
  "global_load_lds_dwordx4 v229, s[52:53] offset:2048\n"
  "global_load_lds_dwordx4 v230, s[52:53] offset:3072\n"
  "s_add_u32 s50, s50, 1024\n"
  "s_addc_u32 s51, s51, 0\n"
  "s_add_u32 s52, s52, 32768\n"
  "s_addc_u32 s53, s53, 0\n"
  "s_add_u32 m0, s48, 98304\n"
  "s_nop 0\n"
  "global_load_lds_dwordx4 v232, s[50:51] sc1\n"
  "global_load_lds_dwordx4 v228, s[50:51] offset:1024 sc1\n"
  "global_load_lds_dwordx4 v229, s[52:53] offset:2048\n"
  "global_load_lds_dwordx4 v230, s[52:53] offset:3072\n"
  "s_add_u32 s50, s50, 1024\n"
  "s_addc_u32 s51, s51, 0\n"
  "s_add_u32 s52, s52, 32768\n"
  "s_addc_u32 s53, s53, 0\n"
  "s_add_u32 m0, s48, 114688\n"
  "s_nop 0\n"
  "global_load_lds_dwordx4 v232, s[50:51] sc1\n"
  "global_load_lds_dwordx4 v228, s[50:51] offset:1024 sc1\n"
  "global_load_lds_dwordx4 v229, s[52:53] offset:2048\n"
  "global_load_lds_dwordx4 v230, s[52:53] offset:3072\n"
  "s_add_u32 s50, s50, 1024\n"
  "s_addc_u32 s51, s51, 0\n"
  "s_add_u32 s52, s52, 32768\n"
  "s_addc_u32 s53, s53, 0\n"
  "s_add_u32 m0, s48, 0\n"
  "s_nop 0\n"
  "global_load_lds_dwordx4 v232, s[50:51] sc1\n"
  "global_load_lds_dwordx4 v228, s[50:51] offset:1024 sc1\n"
  "global_load_lds_dwordx4 v229, s[52:53] offset:2048\n"
  "global_load_lds_dwordx4 v230, s[52:53] offset:3072\n"
  "s_add_u32 s50, s50, 1024\n"
  "s_addc_u32 s51, s51, 0\n"
  "s_add_u32 s52, s52, 32768\n"
  "s_addc_u32 s53, s53, 0\n"
  "s_waitcnt vmcnt(20)\n"
  "s_barrier\n"
  "s_add_u32 m0, s48, 16384\n"
  "s_nop 0\n"
  "global_load_lds_dwordx4 v232, s[50:51] sc1\n"
  "global_load_lds_dwordx4 v228, s[50:51] offset:1024 sc1\n"
  "global_load_lds_dwordx4 v229, s[52:53] offset:2048\n"
  "global_load_lds_dwordx4 v230, s[52:53] offset:3072\n"
  "s_add_u32 s50, s50, 1024\n"
  "s_addc_u32 s51, s51, 0\n"
  "s_add_u32 s52, s52, 32768\n"
  "s_addc_u32 s53, s53, 0\n"
  "ds_read_b64_tr_b16 v[160:161], v176 offset:49152\n"
  "ds_read_b64_tr_b16 v[162:163], v176 offset:57344\n"
  "ds_read_b64_tr_b16 v[164:165], v177 offset:49152\n"
  "ds_read_b64_tr_b16 v[166:167], v177 offset:57344\n"
  "ds_read_b64_tr_b16 v[168:169], v178 offset:49152\n"
  "ds_read_b64_tr_b16 v[170:171], v178 offset:57344\n"
  "ds_read_b128 v[144:147], v180 offset:49152\n"
  "ds_read_b128 v[148:151], v180 offset:50176\n"
  "ds_read_b128 v[152:155], v180 offset:53248\n"
  "ds_read_b128 v[156:159], v180 offset:54272\n"
  "L_k11_%=:\n"
  "s_waitcnt lgkmcnt(3)\n"
  "v_mfma_f32_16x16x32_bf16 v[0:3], v[160:163], v[144:147], v[0:3]\n"
  "ds_read_b64_tr_b16 v[172:173], v179 offset:49152\n"
  "ds_read_b64_tr_b16 v[174:175], v179 offset:57344\n"
  "s_waitcnt lgkmcnt(4)\n"
  "v_mfma_f32_16x16x32_bf16 v[4:7], v[160:163], v[148:151], v[4:7]\n"
  "s_waitcnt vmcnt(20)\n"
  "s_barrier\n"
  "s_waitcnt lgkmcnt(3)\n"
  "v_mfma_f32_16x16x32_bf16 v[8:11], v[160:163], v[152:155], v[8:11]\n"
  "s_waitcnt lgkmcnt(2)\n"
  "v_mfma_f32_16x16x32_bf16 v[12:15], v[160:163], v[156:159], v[12:15]\n"
  "v_add_u32 v180, 0x10000, v180\n"
  "v_mfma_f32_16x16x32_bf16 v[28:31], v[164:167], v[156:159], v[28:31]\n"
  "s_add_u32 m0, s48, 32768\n"
  "ds_read_b128 v[128:131], v180 offset:0\n"
  "v_add_u32 v176, 0x10000, v176\n"
  "v_mfma_f32_16x16x32_bf16 v[24:27], v[164:167], v[152:155], v[24:27]\n"
  "ds_read_b64_tr_b16 v[160:161], v176 offset:0\n"
  "ds_read_b64_tr_b16 v[162:163], v176 offset:8192\n"
  "v_mfma_f32_16x16x32_bf16 v[20:23], v[164:167], v[148:151], v[20:23]\n"
  "ds_read_b128 v[132:135], v180 offset:1024\n"
  "global_load_lds_dwordx4 v232, s[50:51] sc1\n"
  "v_mfma_f32_16x16x32_bf16 v[16:19], v[164:167], v[144:147], v[16:19]\n"
  "global_load_lds_dwordx4 v228, s[50:51] offset:1024 sc1\n"
  "v_mfma_f32_16x16x32_bf16 v[32:35], v[168:171], v[144:147], v[32:35]\n"
  "global_load_lds_dwordx4 v229, s[52:53] offset:2048\n"
  "v_add_u32 v177, 0x10000, v177\n"
  "v_mfma_f32_16x16x32_bf16 v[36:39], v[168:171], v[148:151], v[36:39]\n"
  "ds_read_b64_tr_b16 v[164:165], v177 offset:0\n"
  "ds_read_b64_tr_b16 v[166:167], v177 offset:8192\n"
  "v_mfma_f32_16x16x32_bf16 v[40:43], v[168:171], v[152:155], v[40:43]\n"
  "ds_read_b128 v[136:139], v180 offset:4096\n"
  "global_load_lds_dwordx4 v230, s[52:53] offset:3072\n"
  "v_mfma_f32_16x16x32_bf16 v[44:47], v[168:171], v[156:159], v[44:47]\n"
  "s_add_u32 s50, s50, 1024\n"
  "s_addc_u32 s51, s51, 0\n"
  "s_waitcnt lgkmcnt(7)\n"
  "v_mfma_f32_16x16x32_bf16 v[60:63], v[172:175], v[156:159], v[60:63]\n"
  "ds_read_b128 v[140:143], v180 offset:5120\n"
  "v_add_u32 v178, 0x10000, v178\n"
  "v_mfma_f32_16x16x32_bf16 v[56:59], v[172:175], v[152:155], v[56:59]\n"
  "ds_read_b64_tr_b16 v[168:169], v178 offset:0\n"
  "ds_read_b64_tr_b16 v[170:171], v178 offset:8192\n"
  "v_mfma_f32_16x16x32_bf16 v[52:55], v[172:175], v[148:151], v[52:55]\n"
  "s_add_u32 s52, s52, 32768\n"
  "s_addc_u32 s53, s53, 0\n"
  "v_mfma_f32_16x16x32_bf16 v[48:51], v[172:175], v[144:147], v[48:51]\n"
  "s_waitcnt lgkmcnt(7)\n"
  "v_mfma_f32_16x16x32_bf16 v[0:3], v[160:163], v[128:131], v[0:3]\n"
  "v_add_u32 v179, 0x10000, v179\n"
  "s_waitcnt lgkmcnt(6)\n"
  "v_mfma_f32_16x16x32_bf16 v[4:7], v[160:163], v[132:135], v[4:7]\n"
  "ds_read_b64_tr_b16 v[172:173], v179 offset:0\n"
  "ds_read_b64_tr_b16 v[174:175], v179 offset:8192\n"
  "s_waitcnt vmcnt(20)\n"
  "s_waitcnt lgkmcnt(5)\n"
  "v_mfma_f32_16x16x32_bf16 v[8:11], v[160:163], v[136:139], v[8:11]\n"
  "s_barrier\n"
  "s_waitcnt lgkmcnt(4)\n"
  "v_mfma_f32_16x16x32_bf16 v[12:15], v[160:163], v[140:143], v[12:15]\n"
  "ds_read_b128 v[144:147], v180 offset:16384\n"
  "v_mfma_f32_16x16x32_bf16 v[28:31], v[164:167], v[140:143], v[28:31]\n"
  "s_add_u32 m0, s48, 49152\n"
  "ds_read_b64_tr_b16 v[160:161], v176 offset:16384\n"
  "ds_read_b64_tr_b16 v[162:163], v176 offset:24576\n"
  "v_mfma_f32_16x16x32_bf16 v[24:27], v[164:167], v[136:139], v[24:27]\n"
  "global_load_lds_dwordx4 v232, s[50:51] sc1\n"
  "v_mfma_f32_16x16x32_bf16 v[20:23], v[164:167], v[132:135], v[20:23]\n"
  "ds_read_b128 v[148:151], v180 offset:17408\n"
  "v_mfma_f32_16x16x32_bf16 v[16:19], v[164:167], v[128:131], v[16:19]\n"
  "global_load_lds_dwordx4 v228, s[50:51] offset:1024 sc1\n"
  "s_waitcnt lgkmcnt(6)\n"
  "v_mfma_f32_16x16x32_bf16 v[32:35], v[168:171], v[128:131], v[32:35]\n"
  "ds_read_b64_tr_b16 v[164:165], v177 offset:16384\n"
  "ds_read_b64_tr_b16 v[166:167], v177 offset:24576\n"
  "v_mfma_f32_16x16x32_bf16 v[36:39], v[168:171], v[132:135], v[36:39]\n"
  "ds_read_b128 v[152:155], v180 offset:20480\n"
  "global_load_lds_dwordx4 v229, s[52:53] offset:2048\n"
  "v_mfma_f32_16x16x32_bf16 v[40:43], v[168:171], v[136:139], v[40:43]\n"
  "global_load_lds_dwordx4 v230, s[52:53] offset:3072\n"
  "v_mfma_f32_16x16x32_bf16 v[44:47], v[168:171], v[140:143], v[44:47]\n"
  "s_add_u32 s50, s50, 1024\n"
  "s_addc_u32 s51, s51, 0\n"
  "s_waitcnt lgkmcnt(7)\n"
  "v_mfma_f32_16x16x32_bf16 v[60:63], v[172:175], v[140:143], v[60:63]\n"
  "ds_read_b64_tr_b16 v[168:169], v178 offset:16384\n"
  "ds_read_b64_tr_b16 v[170:171], v178 offset:24576\n"
  "v_mfma_f32_16x16x32_bf16 v[56:59], v[172:175], v[136:139], v[56:59]\n"
  "s_add_u32 s52, s52, 32768\n"
  "s_addc_u32 s53, s53, 0\n"
  "ds_read_b128 v[156:159], v180 offset:21504\n"
  "v_mfma_f32_16x16x32_bf16 v[52:55], v[172:175], v[132:135], v[52:55]\n"
  "v_mfma_f32_16x16x32_bf16 v[48:51], v[172:175], v[128:131], v[48:51]\n"
  "s_waitcnt lgkmcnt(7)\n"
  "v_mfma_f32_16x16x32_bf16 v[0:3], v[160:163], v[144:147], v[0:3]\n"
  "ds_read_b64_tr_b16 v[172:173], v179 offset:16384\n"
  "ds_read_b64_tr_b16 v[174:175], v179 offset:24576\n"
  "s_waitcnt lgkmcnt(8)\n"
  "v_mfma_f32_16x16x32_bf16 v[4:7], v[160:163], v[148:151], v[4:7]\n"
  "s_waitcnt vmcnt(20)\n"
  "s_barrier\n"
  "s_waitcnt lgkmcnt(5)\n"
  "v_mfma_f32_16x16x32_bf16 v[8:11], v[160:163], v[152:155], v[8:11]\n"
  "s_waitcnt lgkmcnt(2)\n"
  "v_mfma_f32_16x16x32_bf16 v[12:15], v[160:163], v[156:159], v[12:15]\n"
  "ds_read_b128 v[128:131], v180 offset:32768\n"
  "v_mfma_f32_16x16x32_bf16 v[28:31], v[164:167], v[156:159], v[28:31]\n"
  "s_add_u32 m0, s48, 65536\n"
  "ds_read_b64_tr_b16 v[160:161], v176 offset:32768\n"
  "ds_read_b64_tr_b16 v[162:163], v176 offset:40960\n"
  "v_mfma_f32_16x16x32_bf16 v[24:27], v[164:167], v[152:155], v[24:27]\n"
  "global_load_lds_dwordx4 v232, s[50:51] sc1\n"
  "v_mfma_f32_16x16x32_bf16 v[20:23], v[164:167], v[148:151], v[20:23]\n"
  "ds_read_b128 v[132:135], v180 offset:33792\n"
  "v_mfma_f32_16x16x32_bf16 v[16:19], v[164:167], v[144:147], v[16:19]\n"
  "global_load_lds_dwordx4 v228, s[50:51] offset:1024 sc1\n"
  "v_mfma_f32_16x16x32_bf16 v[32:35], v[168:171], v[144:147], v[32:35]\n"
  "ds_read_b64_tr_b16 v[164:165], v177 offset:32768\n"
  "ds_read_b64_tr_b16 v[166:167], v177 offset:40960\n"
  "v_mfma_f32_16x16x32_bf16 v[36:39], v[168:171], v[148:151], v[36:39]\n"
  "ds_read_b128 v[136:139], v180 offset:36864\n"
  "global_load_lds_dwordx4 v229, s[52:53] offset:2048\n"
  "v_mfma_f32_16x16x32_bf16 v[40:43], v[168:171], v[152:155], v[40:43]\n"
  "global_load_lds_dwordx4 v230, s[52:53] offset:3072\n"
  "v_mfma_f32_16x16x32_bf16 v[44:47], v[168:171], v[156:159], v[44:47]\n"
  "s_add_u32 s50, s50, 1024\n"
  "s_addc_u32 s51, s51, 0\n"
  "s_waitcnt lgkmcnt(7)\n"
  "v_mfma_f32_16x16x32_bf16 v[60:63], v[172:175], v[156:159], v[60:63]\n"
  "ds_read_b64_tr_b16 v[168:169], v178 offset:32768\n"
  "ds_read_b64_tr_b16 v[170:171], v178 offset:40960\n"
  "v_mfma_f32_16x16x32_bf16 v[56:59], v[172:175], v[152:155], v[56:59]\n"
  "s_add_u32 s52, s52, 32768\n"
  "s_addc_u32 s53, s53, 0\n"
  "ds_read_b128 v[140:143], v180 offset:37888\n"
  "v_mfma_f32_16x16x32_bf16 v[52:55], v[172:175], v[148:151], v[52:55]\n"
  "v_mfma_f32_16x16x32_bf16 v[48:51], v[172:175], v[144:147], v[48:51]\n"
  "s_waitcnt lgkmcnt(7)\n"
  "v_mfma_f32_16x16x32_bf16 v[0:3], v[160:163], v[128:131], v[0:3]\n"
  "ds_read_b64_tr_b16 v[172:173], v179 offset:32768\n"
  "ds_read_b64_tr_b16 v[174:175], v179 offset:40960\n"
  "s_waitcnt lgkmcnt(8)\n"
  "v_mfma_f32_16x16x32_bf16 v[4:7], v[160:163], v[132:135], v[4:7]\n"
  "s_waitcnt vmcnt(20)\n"
  "s_barrier\n"
  "s_waitcnt lgkmcnt(5)\n"
  "v_mfma_f32_16x16x32_bf16 v[8:11], v[160:163], v[136:139], v[8:11]\n"
  "s_waitcnt lgkmcnt(2)\n"
  "v_mfma_f32_16x16x32_bf16 v[12:15], v[160:163], v[140:143], v[12:15]\n"
  "ds_read_b128 v[144:147], v180 offset:49152\n"
  "v_mfma_f32_16x16x32_bf16 v[28:31], v[164:167], v[140:143], v[28:31]\n"
  "s_add_u32 m0, s48, 81920\n"
  "ds_read_b64_tr_b16 v[160:161], v176 offset:49152\n"
  "ds_read_b64_tr_b16 v[162:163], v176 offset:57344\n"
  "v_mfma_f32_16x16x32_bf16 v[24:27], v[164:167], v[136:139], v[24:27]\n"
  "global_load_lds_dwordx4 v232, s[50:51] sc1\n"
  "v_mfma_f32_16x16x32_bf16 v[20:23], v[164:167], v[132:135], v[20:23]\n"
  "ds_read_b128 v[148:151], v180 offset:50176\n"
  "v_mfma_f32_16x16x32_bf16 v[16:19], v[164:167], v[128:131], v[16:19]\n"
  "global_load_lds_dwordx4 v228, s[50:51] offset:1024 sc1\n"
  "v_mfma_f32_16x16x32_bf16 v[32:35], v[168:171], v[128:131], v[32:35]\n"
  "ds_read_b64_tr_b16 v[164:165], v177 offset:49152\n"
  "ds_read_b64_tr_b16 v[166:167], v177 offset:57344\n"
  "v_mfma_f32_16x16x32_bf16 v[36:39], v[168:171], v[132:135], v[36:39]\n"
  "ds_read_b128 v[152:155], v180 offset:53248\n"
  "global_load_lds_dwordx4 v229, s[52:53] offset:2048\n"
  "v_mfma_f32_16x16x32_bf16 v[40:43], v[168:171], v[136:139], v[40:43]\n"
  "global_load_lds_dwordx4 v230, s[52:53] offset:3072\n"
  "v_mfma_f32_16x16x32_bf16 v[44:47], v[168:171], v[140:143], v[44:47]\n"
  "s_add_u32 s50, s50, 1024\n"
  "s_addc_u32 s51, s51, 0\n"
  "s_waitcnt lgkmcnt(7)\n"
  "v_mfma_f32_16x16x32_bf16 v[60:63], v[172:175], v[140:143], v[60:63]\n"
  "ds_read_b64_tr_b16 v[168:169], v178 offset:49152\n"
  "ds_read_b64_tr_b16 v[170:171], v178 offset:57344\n"
  "v_mfma_f32_16x16x32_bf16 v[56:59], v[172:175], v[136:139], v[56:59]\n"
  "s_add_u32 s52, s52, 32768\n"
  "s_addc_u32 s53, s53, 0\n"
  "ds_read_b128 v[156:159], v180 offset:54272\n"
  "v_mfma_f32_16x16x32_bf16 v[52:55], v[172:175], v[132:135], v[52:55]\n"
  "v_mfma_f32_16x16x32_bf16 v[48:51], v[172:175], v[128:131], v[48:51]\n"
  "s_waitcnt lgkmcnt(7)\n"
  "v_mfma_f32_16x16x32_bf16 v[0:3], v[160:163], v[144:147], v[0:3]\n"
  "ds_read_b64_tr_b16 v[172:173], v179 offset:49152\n"
  "ds_read_b64_tr_b16 v[174:175], v179 offset:57344\n"
  "s_waitcnt lgkmcnt(8)\n"
  "v_mfma_f32_16x16x32_bf16 v[4:7], v[160:163], v[148:151], v[4:7]\n"
  "s_waitcnt vmcnt(20)\n"
  "s_barrier\n"
  "s_waitcnt lgkmcnt(5)\n"
  "v_mfma_f32_16x16x32_bf16 v[8:11], v[160:163], v[152:155], v[8:11]\n"
  "s_waitcnt lgkmcnt(2)\n"
  "v_mfma_f32_16x16x32_bf16 v[12:15], v[160:163], v[156:159], v[12:15]\n"
  "v_add_u32 v180, 0xffff0000, v180\n"
  "v_mfma_f32_16x16x32_bf16 v[28:31], v[164:167], v[156:159], v[28:31]\n"
  "s_add_u32 m0, s48, 98304\n"
  "ds_read_b128 v[128:131], v180 offset:0\n"
  "v_add_u32 v176, 0xffff0000, v176\n"
  "v_mfma_f32_16x16x32_bf16 v[24:27], v[164:167], v[152:155], v[24:27]\n"
  "ds_read_b64_tr_b16 v[160:161], v176 offset:0\n"
  "ds_read_b64_tr_b16 v[162:163], v176 offset:8192\n"
  "v_mfma_f32_16x16x32_bf16 v[20:23], v[164:167], v[148:151], v[20:23]\n"
  "ds_read_b128 v[132:135], v180 offset:1024\n"
  "global_load_lds_dwordx4 v232, s[50:51] sc1\n"
  "v_mfma_f32_16x16x32_bf16 v[16:19], v[164:167], v[144:147], v[16:19]\n"
  "global_load_lds_dwordx4 v228, s[50:51] offset:1024 sc1\n"
  "v_mfma_f32_16x16x32_bf16 v[32:35], v[168:171], v[144:147], v[32:35]\n"
  "global_load_lds_dwordx4 v229, s[52:53] offset:2048\n"
  "v_add_u32 v177, 0xffff0000, v177\n"
  "v_mfma_f32_16x16x32_bf16 v[36:39], v[168:171], v[148:151], v[36:39]\n"
  "ds_read_b64_tr_b16 v[164:165], v177 offset:0\n"
  "ds_read_b64_tr_b16 v[166:167], v177 offset:8192\n"
  "v_mfma_f32_16x16x32_bf16 v[40:43], v[168:171], v[152:155], v[40:43]\n"
  "ds_read_b128 v[136:139], v180 offset:4096\n"
  "global_load_lds_dwordx4 v230, s[52:53] offset:3072\n"
  "v_mfma_f32_16x16x32_bf16 v[44:47], v[168:171], v[156:159], v[44:47]\n"
  "s_add_u32 s50, s50, 1024\n"
  "s_addc_u32 s51, s51, 0\n"
  "s_waitcnt lgkmcnt(7)\n"
  "v_mfma_f32_16x16x32_bf16 v[60:63], v[172:175], v[156:159], v[60:63]\n"
  "ds_read_b128 v[140:143], v180 offset:5120\n"
  "v_add_u32 v178, 0xffff0000, v178\n"
  "v_mfma_f32_16x16x32_bf16 v[56:59], v[172:175], v[152:155], v[56:59]\n"
  "ds_read_b64_tr_b16 v[168:169], v178 offset:0\n"
  "ds_read_b64_tr_b16 v[170:171], v178 offset:8192\n"
  "v_mfma_f32_16x16x32_bf16 v[52:55], v[172:175], v[148:151], v[52:55]\n"
  "s_add_u32 s52, s52, 32768\n"
  "s_addc_u32 s53, s53, 0\n"
  "v_mfma_f32_16x16x32_bf16 v[48:51], v[172:175], v[144:147], v[48:51]\n"
  "s_waitcnt lgkmcnt(7)\n"
  "v_mfma_f32_16x16x32_bf16 v[0:3], v[160:163], v[128:131], v[0:3]\n"
  "v_add_u32 v179, 0xffff0000, v179\n"
  "s_waitcnt lgkmcnt(6)\n"
  "v_mfma_f32_16x16x32_bf16 v[4:7], v[160:163], v[132:135], v[4:7]\n"
  "ds_read_b64_tr_b16 v[172:173], v179 offset:0\n"
  "ds_read_b64_tr_b16 v[174:175], v179 offset:8192\n"
  "s_waitcnt vmcnt(20)\n"
  "s_waitcnt lgkmcnt(5)\n"
  "v_mfma_f32_16x16x32_bf16 v[8:11], v[160:163], v[136:139], v[8:11]\n"
  "s_barrier\n"
  "s_waitcnt lgkmcnt(4)\n"
  "v_mfma_f32_16x16x32_bf16 v[12:15], v[160:163], v[140:143], v[12:15]\n"
  "ds_read_b128 v[144:147], v180 offset:16384\n"
  "v_mfma_f32_16x16x32_bf16 v[28:31], v[164:167], v[140:143], v[28:31]\n"
  "s_add_u32 m0, s48, 114688\n"
  "ds_read_b64_tr_b16 v[160:161], v176 offset:16384\n"
  "ds_read_b64_tr_b16 v[162:163], v176 offset:24576\n"
  "v_mfma_f32_16x16x32_bf16 v[24:27], v[164:167], v[136:139], v[24:27]\n"
  "global_load_lds_dwordx4 v232, s[50:51] sc1\n"
  "v_mfma_f32_16x16x32_bf16 v[20:23], v[164:167], v[132:135], v[20:23]\n"
  "ds_read_b128 v[148:151], v180 offset:17408\n"
  "v_mfma_f32_16x16x32_bf16 v[16:19], v[164:167], v[128:131], v[16:19]\n"
  "global_load_lds_dwordx4 v228, s[50:51] offset:1024 sc1\n"
  "s_waitcnt lgkmcnt(6)\n"
  "v_mfma_f32_16x16x32_bf16 v[32:35], v[168:171], v[128:131], v[32:35]\n"
  "ds_read_b64_tr_b16 v[164:165], v177 offset:16384\n"
  "ds_read_b64_tr_b16 v[166:167], v177 offset:24576\n"
  "v_mfma_f32_16x16x32_bf16 v[36:39], v[168:171], v[132:135], v[36:39]\n"
  "ds_read_b128 v[152:155], v180 offset:20480\n"
  "global_load_lds_dwordx4 v229, s[52:53] offset:2048\n"
  "v_mfma_f32_16x16x32_bf16 v[40:43], v[168:171], v[136:139], v[40:43]\n"
  "global_load_lds_dwordx4 v230, s[52:53] offset:3072\n"
  "v_mfma_f32_16x16x32_bf16 v[44:47], v[168:171], v[140:143], v[44:47]\n"
  "s_add_u32 s50, s50, 1024\n"
  "s_addc_u32 s51, s51, 0\n"
  "s_waitcnt lgkmcnt(7)\n"
  "v_mfma_f32_16x16x32_bf16 v[60:63], v[172:175], v[140:143], v[60:63]\n"
  "ds_read_b64_tr_b16 v[168:169], v178 offset:16384\n"
  "ds_read_b64_tr_b16 v[170:171], v178 offset:24576\n"
  "v_mfma_f32_16x16x32_bf16 v[56:59], v[172:175], v[136:139], v[56:59]\n"
  "s_add_u32 s52, s52, 32768\n"
  "s_addc_u32 s53, s53, 0\n"
  "ds_read_b128 v[156:159], v180 offset:21504\n"
  "v_mfma_f32_16x16x32_bf16 v[52:55], v[172:175], v[132:135], v[52:55]\n"
  "v_mfma_f32_16x16x32_bf16 v[48:51], v[172:175], v[128:131], v[48:51]\n"
  "s_waitcnt lgkmcnt(7)\n"
  "v_mfma_f32_16x16x32_bf16 v[0:3], v[160:163], v[144:147], v[0:3]\n"
  "ds_read_b64_tr_b16 v[172:173], v179 offset:16384\n"
  "ds_read_b64_tr_b16 v[174:175], v179 offset:24576\n"
  "s_waitcnt lgkmcnt(8)\n"
  "v_mfma_f32_16x16x32_bf16 v[4:7], v[160:163], v[148:151], v[4:7]\n"
  "s_waitcnt vmcnt(20)\n"
  "s_barrier\n"
  "s_waitcnt lgkmcnt(5)\n"
  "v_mfma_f32_16x16x32_bf16 v[8:11], v[160:163], v[152:155], v[8:11]\n"
  "s_waitcnt lgkmcnt(2)\n"
  "v_mfma_f32_16x16x32_bf16 v[12:15], v[160:163], v[156:159], v[12:15]\n"
  "ds_read_b128 v[128:131], v180 offset:32768\n"
  "v_mfma_f32_16x16x32_bf16 v[28:31], v[164:167], v[156:159], v[28:31]\n"
  "s_add_u32 m0, s48, 0\n"
  "ds_read_b64_tr_b16 v[160:161], v176 offset:32768\n"
  "ds_read_b64_tr_b16 v[162:163], v176 offset:40960\n"
  "v_mfma_f32_16x16x32_bf16 v[24:27], v[164:167], v[152:155], v[24:27]\n"
  "global_load_lds_dwordx4 v232, s[50:51] sc1\n"
  "v_mfma_f32_16x16x32_bf16 v[20:23], v[164:167], v[148:151], v[20:23]\n"
  "ds_read_b128 v[132:135], v180 offset:33792\n"
  "v_mfma_f32_16x16x32_bf16 v[16:19], v[164:167], v[144:147], v[16:19]\n"
  "global_load_lds_dwordx4 v228, s[50:51] offset:1024 sc1\n"
  "v_mfma_f32_16x16x32_bf16 v[32:35], v[168:171], v[144:147], v[32:35]\n"
  "ds_read_b64_tr_b16 v[164:165], v177 offset:32768\n"
  "ds_read_b64_tr_b16 v[166:167], v177 offset:40960\n"
  "v_mfma_f32_16x16x32_bf16 v[36:39], v[168:171], v[148:151], v[36:39]\n"
  "ds_read_b128 v[136:139], v180 offset:36864\n"
  "global_load_lds_dwordx4 v229, s[52:53] offset:2048\n"
  "v_mfma_f32_16x16x32_bf16 v[40:43], v[168:171], v[152:155], v[40:43]\n"
  "global_load_lds_dwordx4 v230, s[52:53] offset:3072\n"
  "v_mfma_f32_16x16x32_bf16 v[44:47], v[168:171], v[156:159], v[44:47]\n"
  "s_add_u32 s50, s50, 1024\n"
  "s_addc_u32 s51, s51, 0\n"
  "s_waitcnt lgkmcnt(7)\n"
  "v_mfma_f32_16x16x32_bf16 v[60:63], v[172:175], v[156:159], v[60:63]\n"
  "ds_read_b64_tr_b16 v[168:169], v178 offset:32768\n"
  "ds_read_b64_tr_b16 v[170:171], v178 offset:40960\n"
  "v_mfma_f32_16x16x32_bf16 v[56:59], v[172:175], v[152:155], v[56:59]\n"
  "s_add_u32 s52, s52, 32768\n"
  "s_addc_u32 s53, s53, 0\n"
  "ds_read_b128 v[140:143], v180 offset:37888\n"
  "v_mfma_f32_16x16x32_bf16 v[52:55], v[172:175], v[148:151], v[52:55]\n"
  "v_mfma_f32_16x16x32_bf16 v[48:51], v[172:175], v[144:147], v[48:51]\n"
  "s_waitcnt lgkmcnt(7)\n"
  "v_mfma_f32_16x16x32_bf16 v[0:3], v[160:163], v[128:131], v[0:3]\n"
  "ds_read_b64_tr_b16 v[172:173], v179 offset:32768\n"
  "ds_read_b64_tr_b16 v[174:175], v179 offset:40960\n"
  "s_waitcnt lgkmcnt(8)\n"
  "v_mfma_f32_16x16x32_bf16 v[4:7], v[160:163], v[132:135], v[4:7]\n"
  "s_waitcnt vmcnt(20)\n"
  "s_barrier\n"
  "s_waitcnt lgkmcnt(5)\n"
  "v_mfma_f32_16x16x32_bf16 v[8:11], v[160:163], v[136:139], v[8:11]\n"
  "s_waitcnt lgkmcnt(2)\n"
  "v_mfma_f32_16x16x32_bf16 v[12:15], v[160:163], v[140:143], v[12:15]\n"
  "ds_read_b128 v[144:147], v180 offset:49152\n"
  "v_mfma_f32_16x16x32_bf16 v[28:31], v[164:167], v[140:143], v[28:31]\n"
  "s_add_u32 m0, s48, 16384\n"
  "ds_read_b64_tr_b16 v[160:161], v176 offset:49152\n"
  "ds_read_b64_tr_b16 v[162:163], v176 offset:57344\n"
  "v_mfma_f32_16x16x32_bf16 v[24:27], v[164:167], v[136:139], v[24:27]\n"
  "global_load_lds_dwordx4 v232, s[50:51] sc1\n"
  "v_mfma_f32_16x16x32_bf16 v[20:23], v[164:167], v[132:135], v[20:23]\n"
  "ds_read_b128 v[148:151], v180 offset:50176\n"
  "v_mfma_f32_16x16x32_bf16 v[16:19], v[164:167], v[128:131], v[16:19]\n"
  "global_load_lds_dwordx4 v228, s[50:51] offset:1024 sc1\n"
  "v_mfma_f32_16x16x32_bf16 v[32:35], v[168:171], v[128:131], v[32:35]\n"
  "ds_read_b64_tr_b16 v[164:165], v177 offset:49152\n"
  "ds_read_b64_tr_b16 v[166:167], v177 offset:57344\n"
  "v_mfma_f32_16x16x32_bf16 v[36:39], v[168:171], v[132:135], v[36:39]\n"
  "ds_read_b128 v[152:155], v180 offset:53248\n"
  "global_load_lds_dwordx4 v229, s[52:53] offset:2048\n"
  "v_mfma_f32_16x16x32_bf16 v[40:43], v[168:171], v[136:139], v[40:43]\n"
  "global_load_lds_dwordx4 v230, s[52:53] offset:3072\n"
  "v_mfma_f32_16x16x32_bf16 v[44:47], v[168:171], v[140:143], v[44:47]\n"
  "s_add_u32 s50, s50, 1024\n"
  "s_addc_u32 s51, s51, 0\n"
  "s_waitcnt lgkmcnt(7)\n"
  "v_mfma_f32_16x16x32_bf16 v[60:63], v[172:175], v[140:143], v[60:63]\n"
  "ds_read_b64_tr_b16 v[168:169], v178 offset:49152\n"
  "ds_read_b64_tr_b16 v[170:171], v178 offset:57344\n"
  "v_mfma_f32_16x16x32_bf16 v[56:59], v[172:175], v[136:139], v[56:59]\n"
  "s_add_u32 s52, s52, 32768\n"
  "s_addc_u32 s53, s53, 0\n"
  "ds_read_b128 v[156:159], v180 offset:54272\n"
  "v_mfma_f32_16x16x32_bf16 v[52:55], v[172:175], v[132:135], v[52:55]\n"
  "v_mfma_f32_16x16x32_bf16 v[48:51], v[172:175], v[128:131], v[48:51]\n"
  "s_waitcnt lgkmcnt(7)\n"
  "v_mfma_f32_16x16x32_bf16 v[0:3], v[160:163], v[144:147], v[0:3]\n"
  "ds_read_b64_tr_b16 v[172:173], v179 offset:49152\n"
  "ds_read_b64_tr_b16 v[174:175], v179 offset:57344\n"
  "s_waitcnt lgkmcnt(8)\n"
  "v_mfma_f32_16x16x32_bf16 v[4:7], v[160:163], v[148:151], v[4:7]\n"
  "s_waitcnt vmcnt(20)\n"
  "s_barrier\n"
  "s_waitcnt lgkmcnt(5)\n"
  "v_mfma_f32_16x16x32_bf16 v[8:11], v[160:163], v[152:155], v[8:11]\n"
  "s_waitcnt lgkmcnt(2)\n"
  "v_mfma_f32_16x16x32_bf16 v[12:15], v[160:163], v[156:159], v[12:15]\n"
  "v_add_u32 v180, 0x10000, v180\n"
  "v_mfma_f32_16x16x32_bf16 v[28:31], v[164:167], v[156:159], v[28:31]\n"
  "s_add_u32 m0, s48, 32768\n"
  "ds_read_b128 v[128:131], v180 offset:0\n"
  "v_add_u32 v176, 0x10000, v176\n"
  "v_mfma_f32_16x16x32_bf16 v[24:27], v[164:167], v[152:155], v[24:27]\n"
  "ds_read_b64_tr_b16 v[160:161], v176 offset:0\n"
  "ds_read_b64_tr_b16 v[162:163], v176 offset:8192\n"
  "v_mfma_f32_16x16x32_bf16 v[20:23], v[164:167], v[148:151], v[20:23]\n"
  "ds_read_b128 v[132:135], v180 offset:1024\n"
  "global_load_lds_dwordx4 v232, s[50:51] sc1\n"
  "v_mfma_f32_16x16x32_bf16 v[16:19], v[164:167], v[144:147], v[16:19]\n"
  "global_load_lds_dwordx4 v228, s[50:51] offset:1024 sc1\n"
  "v_mfma_f32_16x16x32_bf16 v[32:35], v[168:171], v[144:147], v[32:35]\n"
  "global_load_lds_dwordx4 v229, s[52:53] offset:2048\n"
  "v_add_u32 v177, 0x10000, v177\n"
  "v_mfma_f32_16x16x32_bf16 v[36:39], v[168:171], v[148:151], v[36:39]\n"
  "ds_read_b64_tr_b16 v[164:165], v177 offset:0\n"
  "ds_read_b64_tr_b16 v[166:167], v177 offset:8192\n"
  "v_mfma_f32_16x16x32_bf16 v[40:43], v[168:171], v[152:155], v[40:43]\n"
  "ds_read_b128 v[136:139], v180 offset:4096\n"
  "global_load_lds_dwordx4 v230, s[52:53] offset:3072\n"
  "v_mfma_f32_16x16x32_bf16 v[44:47], v[168:171], v[156:159], v[44:47]\n"
  "s_add_u32 s50, s50, 1024\n"
  "s_addc_u32 s51, s51, 0\n"
  "s_waitcnt lgkmcnt(7)\n"
  "v_mfma_f32_16x16x32_bf16 v[60:63], v[172:175], v[156:159], v[60:63]\n"
  "ds_read_b128 v[140:143], v180 offset:5120\n"
  "v_add_u32 v178, 0x10000, v178\n"
  "v_mfma_f32_16x16x32_bf16 v[56:59], v[172:175], v[152:155], v[56:59]\n"
  "ds_read_b64_tr_b16 v[168:169], v178 offset:0\n"
  "ds_read_b64_tr_b16 v[170:171], v178 offset:8192\n"
  "v_mfma_f32_16x16x32_bf16 v[52:55], v[172:175], v[148:151], v[52:55]\n"
  "s_add_u32 s52, s52, 32768\n"
  "s_addc_u32 s53, s53, 0\n"
  "v_mfma_f32_16x16x32_bf16 v[48:51], v[172:175], v[144:147], v[48:51]\n"
  "s_waitcnt lgkmcnt(7)\n"
  "v_mfma_f32_16x16x32_bf16 v[0:3], v[160:163], v[128:131], v[0:3]\n"
  "v_add_u32 v179, 0x10000, v179\n"
  "s_waitcnt lgkmcnt(6)\n"
  "v_mfma_f32_16x16x32_bf16 v[4:7], v[160:163], v[132:135], v[4:7]\n"
  "ds_read_b64_tr_b16 v[172:173], v179 offset:0\n"
  "ds_read_b64_tr_b16 v[174:175], v179 offset:8192\n"
  "s_waitcnt vmcnt(20)\n"
  "s_waitcnt lgkmcnt(5)\n"
  "v_mfma_f32_16x16x32_bf16 v[8:11], v[160:163], v[136:139], v[8:11]\n"
  "s_barrier\n"
  "s_waitcnt lgkmcnt(4)\n"
  "v_mfma_f32_16x16x32_bf16 v[12:15], v[160:163], v[140:143], v[12:15]\n"
  "ds_read_b128 v[144:147], v180 offset:16384\n"
  "v_mfma_f32_16x16x32_bf16 v[28:31], v[164:167], v[140:143], v[28:31]\n"
  "s_add_u32 m0, s48, 49152\n"
  "ds_read_b64_tr_b16 v[160:161], v176 offset:16384\n"
  "ds_read_b64_tr_b16 v[162:163], v176 offset:24576\n"
  "v_mfma_f32_16x16x32_bf16 v[24:27], v[164:167], v[136:139], v[24:27]\n"
  "global_load_lds_dwordx4 v232, s[50:51] sc1\n"
  "v_mfma_f32_16x16x32_bf16 v[20:23], v[164:167], v[132:135], v[20:23]\n"
  "ds_read_b128 v[148:151], v180 offset:17408\n"
  "v_mfma_f32_16x16x32_bf16 v[16:19], v[164:167], v[128:131], v[16:19]\n"
  "global_load_lds_dwordx4 v228, s[50:51] offset:1024 sc1\n"
  "s_waitcnt lgkmcnt(6)\n"
  "v_mfma_f32_16x16x32_bf16 v[32:35], v[168:171], v[128:131], v[32:35]\n"
  "ds_read_b64_tr_b16 v[164:165], v177 offset:16384\n"
  "ds_read_b64_tr_b16 v[166:167], v177 offset:24576\n"
  "v_mfma_f32_16x16x32_bf16 v[36:39], v[168:171], v[132:135], v[36:39]\n"
  "ds_read_b128 v[152:155], v180 offset:20480\n"
  "global_load_lds_dwordx4 v229, s[52:53] offset:2048\n"
  "v_mfma_f32_16x16x32_bf16 v[40:43], v[168:171], v[136:139], v[40:43]\n"
  "global_load_lds_dwordx4 v230, s[52:53] offset:3072\n"
  "v_mfma_f32_16x16x32_bf16 v[44:47], v[168:171], v[140:143], v[44:47]\n"
  "s_add_u32 s50, s50, 1024\n"
  "s_addc_u32 s51, s51, 0\n"
  "s_waitcnt lgkmcnt(7)\n"
  "v_mfma_f32_16x16x32_bf16 v[60:63], v[172:175], v[140:143], v[60:63]\n"
  "ds_read_b64_tr_b16 v[168:169], v178 offset:16384\n"
  "ds_read_b64_tr_b16 v[170:171], v178 offset:24576\n"
  "v_mfma_f32_16x16x32_bf16 v[56:59], v[172:175], v[136:139], v[56:59]\n"
  "s_add_u32 s52, s52, 32768\n"
  "s_addc_u32 s53, s53, 0\n"
  "ds_read_b128 v[156:159], v180 offset:21504\n"
  "v_mfma_f32_16x16x32_bf16 v[52:55], v[172:175], v[132:135], v[52:55]\n"
  "v_mfma_f32_16x16x32_bf16 v[48:51], v[172:175], v[128:131], v[48:51]\n"
  "s_waitcnt lgkmcnt(7)\n"
  "v_mfma_f32_16x16x32_bf16 v[0:3], v[160:163], v[144:147], v[0:3]\n"
  "ds_read_b64_tr_b16 v[172:173], v179 offset:16384\n"
  "ds_read_b64_tr_b16 v[174:175], v179 offset:24576\n"
  "s_waitcnt lgkmcnt(8)\n"
  "v_mfma_f32_16x16x32_bf16 v[4:7], v[160:163], v[148:151], v[4:7]\n"
  "s_waitcnt vmcnt(20)\n"
  "s_barrier\n"
  "s_waitcnt lgkmcnt(5)\n"
  "v_mfma_f32_16x16x32_bf16 v[8:11], v[160:163], v[152:155], v[8:11]\n"
  "s_waitcnt lgkmcnt(2)\n"
  "v_mfma_f32_16x16x32_bf16 v[12:15], v[160:163], v[156:159], v[12:15]\n"
  "ds_read_b128 v[128:131], v180 offset:32768\n"
  "v_mfma_f32_16x16x32_bf16 v[28:31], v[164:167], v[156:159], v[28:31]\n"
  "s_add_u32 m0, s48, 65536\n"
  "ds_read_b64_tr_b16 v[160:161], v176 offset:32768\n"
  "ds_read_b64_tr_b16 v[162:163], v176 offset:40960\n"
  "v_mfma_f32_16x16x32_bf16 v[24:27], v[164:167], v[152:155], v[24:27]\n"
  "global_load_lds_dwordx4 v232, s[50:51] sc1\n"
  "v_mfma_f32_16x16x32_bf16 v[20:23], v[164:167], v[148:151], v[20:23]\n"
  "ds_read_b128 v[132:135], v180 offset:33792\n"
  "v_mfma_f32_16x16x32_bf16 v[16:19], v[164:167], v[144:147], v[16:19]\n"
  "global_load_lds_dwordx4 v228, s[50:51] offset:1024 sc1\n"
  "v_mfma_f32_16x16x32_bf16 v[32:35], v[168:171], v[144:147], v[32:35]\n"
  "ds_read_b64_tr_b16 v[164:165], v177 offset:32768\n"
  "ds_read_b64_tr_b16 v[166:167], v177 offset:40960\n"
  "v_mfma_f32_16x16x32_bf16 v[36:39], v[168:171], v[148:151], v[36:39]\n"
  "ds_read_b128 v[136:139], v180 offset:36864\n"
  "global_load_lds_dwordx4 v229, s[52:53] offset:2048\n"
  "v_mfma_f32_16x16x32_bf16 v[40:43], v[168:171], v[152:155], v[40:43]\n"
  "global_load_lds_dwordx4 v230, s[52:53] offset:3072\n"
  "v_mfma_f32_16x16x32_bf16 v[44:47], v[168:171], v[156:159], v[44:47]\n"
  "s_add_u32 s50, s50, 1024\n"
  "s_addc_u32 s51, s51, 0\n"
  "s_waitcnt lgkmcnt(7)\n"
  "v_mfma_f32_16x16x32_bf16 v[60:63], v[172:175], v[156:159], v[60:63]\n"
  "ds_read_b64_tr_b16 v[168:169], v178 offset:32768\n"
  "ds_read_b64_tr_b16 v[170:171], v178 offset:40960\n"
  "v_mfma_f32_16x16x32_bf16 v[56:59], v[172:175], v[152:155], v[56:59]\n"
  "s_add_u32 s52, s52, 32768\n"
  "s_addc_u32 s53, s53, 0\n"
  "ds_read_b128 v[140:143], v180 offset:37888\n"
  "v_mfma_f32_16x16x32_bf16 v[52:55], v[172:175], v[148:151], v[52:55]\n"
  "v_mfma_f32_16x16x32_bf16 v[48:51], v[172:175], v[144:147], v[48:51]\n"
  "s_waitcnt lgkmcnt(7)\n"
  "v_mfma_f32_16x16x32_bf16 v[0:3], v[160:163], v[128:131], v[0:3]\n"
  "ds_read_b64_tr_b16 v[172:173], v179 offset:32768\n"
  "ds_read_b64_tr_b16 v[174:175], v179 offset:40960\n"
  "s_waitcnt lgkmcnt(8)\n"
  "v_mfma_f32_16x16x32_bf16 v[4:7], v[160:163], v[132:135], v[4:7]\n"
  "s_waitcnt vmcnt(20)\n"
  "s_barrier\n"
  "s_waitcnt lgkmcnt(5)\n"
  "v_mfma_f32_16x16x32_bf16 v[8:11], v[160:163], v[136:139], v[8:11]\n"
  "s_waitcnt lgkmcnt(2)\n"
  "v_mfma_f32_16x16x32_bf16 v[12:15], v[160:163], v[140:143], v[12:15]\n"
  "ds_read_b128 v[144:147], v180 offset:49152\n"
  "v_mfma_f32_16x16x32_bf16 v[28:31], v[164:167], v[140:143], v[28:31]\n"
  "s_add_u32 m0, s48, 81920\n"
  "ds_read_b64_tr_b16 v[160:161], v176 offset:49152\n"
  "ds_read_b64_tr_b16 v[162:163], v176 offset:57344\n"
  "v_mfma_f32_16x16x32_bf16 v[24:27], v[164:167], v[136:139], v[24:27]\n"
  "global_load_lds_dwordx4 v232, s[50:51] sc1\n"
  "v_mfma_f32_16x16x32_bf16 v[20:23], v[164:167], v[132:135], v[20:23]\n"
  "ds_read_b128 v[148:151], v180 offset:50176\n"
  "v_mfma_f32_16x16x32_bf16 v[16:19], v[164:167], v[128:131], v[16:19]\n"
  "global_load_lds_dwordx4 v228, s[50:51] offset:1024 sc1\n"
  "v_mfma_f32_16x16x32_bf16 v[32:35], v[168:171], v[128:131], v[32:35]\n"
  "ds_read_b64_tr_b16 v[164:165], v177 offset:49152\n"
  "ds_read_b64_tr_b16 v[166:167], v177 offset:57344\n"
  "v_mfma_f32_16x16x32_bf16 v[36:39], v[168:171], v[132:135], v[36:39]\n"
  "ds_read_b128 v[152:155], v180 offset:53248\n"
  "global_load_lds_dwordx4 v229, s[52:53] offset:2048\n"
  "v_mfma_f32_16x16x32_bf16 v[40:43], v[168:171], v[136:139], v[40:43]\n"
  "global_load_lds_dwordx4 v230, s[52:53] offset:3072\n"
  "v_mfma_f32_16x16x32_bf16 v[44:47], v[168:171], v[140:143], v[44:47]\n"
  "s_add_u32 s50, s50, 1024\n"
  "s_addc_u32 s51, s51, 0\n"
  "s_waitcnt lgkmcnt(7)\n"
  "v_mfma_f32_16x16x32_bf16 v[60:63], v[172:175], v[140:143], v[60:63]\n"
  "ds_read_b64_tr_b16 v[168:169], v178 offset:49152\n"
  "ds_read_b64_tr_b16 v[170:171], v178 offset:57344\n"
  "v_mfma_f32_16x16x32_bf16 v[56:59], v[172:175], v[136:139], v[56:59]\n"
  "s_add_u32 s52, s52, 32768\n"
  "s_addc_u32 s53, s53, 0\n"
  "ds_read_b128 v[156:159], v180 offset:54272\n"
  "v_mfma_f32_16x16x32_bf16 v[52:55], v[172:175], v[132:135], v[52:55]\n"
  "v_mfma_f32_16x16x32_bf16 v[48:51], v[172:175], v[128:131], v[48:51]\n"
  "s_waitcnt lgkmcnt(7)\n"
  "v_mfma_f32_16x16x32_bf16 v[0:3], v[160:163], v[144:147], v[0:3]\n"
  "ds_read_b64_tr_b16 v[172:173], v179 offset:49152\n"
  "ds_read_b64_tr_b16 v[174:175], v179 offset:57344\n"
  "s_waitcnt lgkmcnt(8)\n"
  "v_mfma_f32_16x16x32_bf16 v[4:7], v[160:163], v[148:151], v[4:7]\n"
  "s_waitcnt vmcnt(20)\n"
  "s_barrier\n"
  "s_waitcnt lgkmcnt(5)\n"
  "v_mfma_f32_16x16x32_bf16 v[8:11], v[160:163], v[152:155], v[8:11]\n"
  "s_waitcnt lgkmcnt(2)\n"
  "v_mfma_f32_16x16x32_bf16 v[12:15], v[160:163], v[156:159], v[12:15]\n"
  "v_add_u32 v180, 0xffff0000, v180\n"
  "v_mfma_f32_16x16x32_bf16 v[28:31], v[164:167], v[156:159], v[28:31]\n"
  "s_add_u32 m0, s48, 98304\n"
  "ds_read_b128 v[128:131], v180 offset:0\n"
  "v_add_u32 v176, 0xffff0000, v176\n"
  "v_mfma_f32_16x16x32_bf16 v[24:27], v[164:167], v[152:155], v[24:27]\n"
  "ds_read_b64_tr_b16 v[160:161], v176 offset:0\n"
  "ds_read_b64_tr_b16 v[162:163], v176 offset:8192\n"
  "v_mfma_f32_16x16x32_bf16 v[20:23], v[164:167], v[148:151], v[20:23]\n"
  "ds_read_b128 v[132:135], v180 offset:1024\n"
  "global_load_lds_dwordx4 v232, s[50:51] sc1\n"
  "v_mfma_f32_16x16x32_bf16 v[16:19], v[164:167], v[144:147], v[16:19]\n"
  "global_load_lds_dwordx4 v228, s[50:51] offset:1024 sc1\n"
  "v_mfma_f32_16x16x32_bf16 v[32:35], v[168:171], v[144:147], v[32:35]\n"
  "global_load_lds_dwordx4 v229, s[52:53] offset:2048\n"
  "v_add_u32 v177, 0xffff0000, v177\n"
  "v_mfma_f32_16x16x32_bf16 v[36:39], v[168:171], v[148:151], v[36:39]\n"
  "ds_read_b64_tr_b16 v[164:165], v177 offset:0\n"
  "ds_read_b64_tr_b16 v[166:167], v177 offset:8192\n"
  "v_mfma_f32_16x16x32_bf16 v[40:43], v[168:171], v[152:155], v[40:43]\n"
  "ds_read_b128 v[136:139], v180 offset:4096\n"
  "global_load_lds_dwordx4 v230, s[52:53] offset:3072\n"
  "v_mfma_f32_16x16x32_bf16 v[44:47], v[168:171], v[156:159], v[44:47]\n"
  "s_add_u32 s50, s50, 1024\n"
  "s_addc_u32 s51, s51, 0\n"
  "s_waitcnt lgkmcnt(7)\n"
  "v_mfma_f32_16x16x32_bf16 v[60:63], v[172:175], v[156:159], v[60:63]\n"
  "ds_read_b128 v[140:143], v180 offset:5120\n"
  "v_add_u32 v178, 0xffff0000, v178\n"
  "v_mfma_f32_16x16x32_bf16 v[56:59], v[172:175], v[152:155], v[56:59]\n"
  "ds_read_b64_tr_b16 v[168:169], v178 offset:0\n"
  "ds_read_b64_tr_b16 v[170:171], v178 offset:8192\n"
  "v_mfma_f32_16x16x32_bf16 v[52:55], v[172:175], v[148:151], v[52:55]\n"
  "s_add_u32 s52, s52, 32768\n"
  "s_addc_u32 s53, s53, 0\n"
  "v_mfma_f32_16x16x32_bf16 v[48:51], v[172:175], v[144:147], v[48:51]\n"
  "s_waitcnt lgkmcnt(7)\n"
  "v_mfma_f32_16x16x32_bf16 v[0:3], v[160:163], v[128:131], v[0:3]\n"
  "v_add_u32 v179, 0xffff0000, v179\n"
  "s_waitcnt lgkmcnt(6)\n"
  "v_mfma_f32_16x16x32_bf16 v[4:7], v[160:163], v[132:135], v[4:7]\n"
  "ds_read_b64_tr_b16 v[172:173], v179 offset:0\n"
  "ds_read_b64_tr_b16 v[174:175], v179 offset:8192\n"
  "s_waitcnt vmcnt(20)\n"
  "s_waitcnt lgkmcnt(5)\n"
  "v_mfma_f32_16x16x32_bf16 v[8:11], v[160:163], v[136:139], v[8:11]\n"
  "s_barrier\n"
  "s_waitcnt lgkmcnt(4)\n"
  "v_mfma_f32_16x16x32_bf16 v[12:15], v[160:163], v[140:143], v[12:15]\n"
  "ds_read_b128 v[144:147], v180 offset:16384\n"
  "v_mfma_f32_16x16x32_bf16 v[28:31], v[164:167], v[140:143], v[28:31]\n"
  "s_add_u32 m0, s48, 114688\n"
  "ds_read_b64_tr_b16 v[160:161], v176 offset:16384\n"
  "ds_read_b64_tr_b16 v[162:163], v176 offset:24576\n"
  "v_mfma_f32_16x16x32_bf16 v[24:27], v[164:167], v[136:139], v[24:27]\n"
  "global_load_lds_dwordx4 v232, s[50:51] sc1\n"
  "v_mfma_f32_16x16x32_bf16 v[20:23], v[164:167], v[132:135], v[20:23]\n"
  "ds_read_b128 v[148:151], v180 offset:17408\n"
  "v_mfma_f32_16x16x32_bf16 v[16:19], v[164:167], v[128:131], v[16:19]\n"
  "global_load_lds_dwordx4 v228, s[50:51] offset:1024 sc1\n"
  "s_waitcnt lgkmcnt(6)\n"
  "v_mfma_f32_16x16x32_bf16 v[32:35], v[168:171], v[128:131], v[32:35]\n"
  "ds_read_b64_tr_b16 v[164:165], v177 offset:16384\n"
  "ds_read_b64_tr_b16 v[166:167], v177 offset:24576\n"
  "v_mfma_f32_16x16x32_bf16 v[36:39], v[168:171], v[132:135], v[36:39]\n"
  "ds_read_b128 v[152:155], v180 offset:20480\n"
  "global_load_lds_dwordx4 v229, s[52:53] offset:2048\n"
  "v_mfma_f32_16x16x32_bf16 v[40:43], v[168:171], v[136:139], v[40:43]\n"
  "global_load_lds_dwordx4 v230, s[52:53] offset:3072\n"
  "v_mfma_f32_16x16x32_bf16 v[44:47], v[168:171], v[140:143], v[44:47]\n"
  "s_add_u32 s50, s50, 1024\n"
  "s_addc_u32 s51, s51, 0\n"
  "s_waitcnt lgkmcnt(7)\n"
  "v_mfma_f32_16x16x32_bf16 v[60:63], v[172:175], v[140:143], v[60:63]\n"
  "ds_read_b64_tr_b16 v[168:169], v178 offset:16384\n"
  "ds_read_b64_tr_b16 v[170:171], v178 offset:24576\n"
  "v_mfma_f32_16x16x32_bf16 v[56:59], v[172:175], v[136:139], v[56:59]\n"
  "s_add_u32 s52, s52, 32768\n"
  "s_addc_u32 s53, s53, 0\n"
  "ds_read_b128 v[156:159], v180 offset:21504\n"
  "v_mfma_f32_16x16x32_bf16 v[52:55], v[172:175], v[132:135], v[52:55]\n"
  "v_mfma_f32_16x16x32_bf16 v[48:51], v[172:175], v[128:131], v[48:51]\n"
  "s_waitcnt lgkmcnt(7)\n"
  "v_mfma_f32_16x16x32_bf16 v[0:3], v[160:163], v[144:147], v[0:3]\n"
  "ds_read_b64_tr_b16 v[172:173], v179 offset:16384\n"
  "ds_read_b64_tr_b16 v[174:175], v179 offset:24576\n"
  "s_waitcnt lgkmcnt(8)\n"
  "v_mfma_f32_16x16x32_bf16 v[4:7], v[160:163], v[148:151], v[4:7]\n"
  "s_waitcnt vmcnt(20)\n"
  "s_barrier\n"
  "s_waitcnt lgkmcnt(5)\n"
  "v_mfma_f32_16x16x32_bf16 v[8:11], v[160:163], v[152:155], v[8:11]\n"
  "s_waitcnt lgkmcnt(2)\n"
  "v_mfma_f32_16x16x32_bf16 v[12:15], v[160:163], v[156:159], v[12:15]\n"
  "ds_read_b128 v[128:131], v180 offset:32768\n"
  "v_mfma_f32_16x16x32_bf16 v[28:31], v[164:167], v[156:159], v[28:31]\n"
  "s_add_u32 m0, s48, 0\n"
  "ds_read_b64_tr_b16 v[160:161], v176 offset:32768\n"
  "ds_read_b64_tr_b16 v[162:163], v176 offset:40960\n"
  "v_mfma_f32_16x16x32_bf16 v[24:27], v[164:167], v[152:155], v[24:27]\n"
  "global_load_lds_dwordx4 v232, s[50:51] sc1\n"
  "v_mfma_f32_16x16x32_bf16 v[20:23], v[164:167], v[148:151], v[20:23]\n"
  "ds_read_b128 v[132:135], v180 offset:33792\n"
  "v_mfma_f32_16x16x32_bf16 v[16:19], v[164:167], v[144:147], v[16:19]\n"
  "global_load_lds_dwordx4 v228, s[50:51] offset:1024 sc1\n"
  "v_mfma_f32_16x16x32_bf16 v[32:35], v[168:171], v[144:147], v[32:35]\n"
  "ds_read_b64_tr_b16 v[164:165], v177 offset:32768\n"
  "ds_read_b64_tr_b16 v[166:167], v177 offset:40960\n"
  "v_mfma_f32_16x16x32_bf16 v[36:39], v[168:171], v[148:151], v[36:39]\n"
  "ds_read_b128 v[136:139], v180 offset:36864\n"
  "global_load_lds_dwordx4 v229, s[52:53] offset:2048\n"
  "v_mfma_f32_16x16x32_bf16 v[40:43], v[168:171], v[152:155], v[40:43]\n"
  "global_load_lds_dwordx4 v230, s[52:53] offset:3072\n"
  "v_mfma_f32_16x16x32_bf16 v[44:47], v[168:171], v[156:159], v[44:47]\n"
  "s_add_u32 s50, s50, 1024\n"
  "s_addc_u32 s51, s51, 0\n"
  "s_waitcnt lgkmcnt(7)\n"
  "v_mfma_f32_16x16x32_bf16 v[60:63], v[172:175], v[156:159], v[60:63]\n"
  "ds_read_b64_tr_b16 v[168:169], v178 offset:32768\n"
  "ds_read_b64_tr_b16 v[170:171], v178 offset:40960\n"
  "v_mfma_f32_16x16x32_bf16 v[56:59], v[172:175], v[152:155], v[56:59]\n"
  "s_add_u32 s52, s52, 32768\n"
  "s_addc_u32 s53, s53, 0\n"
  "ds_read_b128 v[140:143], v180 offset:37888\n"
  "v_mfma_f32_16x16x32_bf16 v[52:55], v[172:175], v[148:151], v[52:55]\n"
  "v_mfma_f32_16x16x32_bf16 v[48:51], v[172:175], v[144:147], v[48:51]\n"
  "s_waitcnt lgkmcnt(7)\n"
  "v_mfma_f32_16x16x32_bf16 v[0:3], v[160:163], v[128:131], v[0:3]\n"
  "ds_read_b64_tr_b16 v[172:173], v179 offset:32768\n"
  "ds_read_b64_tr_b16 v[174:175], v179 offset:40960\n"
  "s_waitcnt lgkmcnt(8)\n"
  "v_mfma_f32_16x16x32_bf16 v[4:7], v[160:163], v[132:135], v[4:7]\n"
  "s_waitcnt vmcnt(20)\n"
  "s_barrier\n"
  "s_waitcnt lgkmcnt(5)\n"
  "v_mfma_f32_16x16x32_bf16 v[8:11], v[160:163], v[136:139], v[8:11]\n"
  "s_waitcnt lgkmcnt(2)\n"
  "v_mfma_f32_16x16x32_bf16 v[12:15], v[160:163], v[140:143], v[12:15]\n"
  "ds_read_b128 v[144:147], v180 offset:49152\n"
  "v_mfma_f32_16x16x32_bf16 v[28:31], v[164:167], v[140:143], v[28:31]\n"
  "s_add_u32 m0, s48, 16384\n"
  "ds_read_b64_tr_b16 v[160:161], v176 offset:49152\n"
  "ds_read_b64_tr_b16 v[162:163], v176 offset:57344\n"
  "v_mfma_f32_16x16x32_bf16 v[24:27], v[164:167], v[136:139], v[24:27]\n"
  "global_load_lds_dwordx4 v232, s[50:51] sc1\n"
  "v_mfma_f32_16x16x32_bf16 v[20:23], v[164:167], v[132:135], v[20:23]\n"
  "ds_read_b128 v[148:151], v180 offset:50176\n"
  "v_mfma_f32_16x16x32_bf16 v[16:19], v[164:167], v[128:131], v[16:19]\n"
  "global_load_lds_dwordx4 v228, s[50:51] offset:1024 sc1\n"
  "v_mfma_f32_16x16x32_bf16 v[32:35], v[168:171], v[128:131], v[32:35]\n"
  "ds_read_b64_tr_b16 v[164:165], v177 offset:49152\n"
  "ds_read_b64_tr_b16 v[166:167], v177 offset:57344\n"
  "v_mfma_f32_16x16x32_bf16 v[36:39], v[168:171], v[132:135], v[36:39]\n"
  "ds_read_b128 v[152:155], v180 offset:53248\n"
  "global_load_lds_dwordx4 v229, s[52:53] offset:2048\n"
  "v_mfma_f32_16x16x32_bf16 v[40:43], v[168:171], v[136:139], v[40:43]\n"
  "global_load_lds_dwordx4 v230, s[52:53] offset:3072\n"
  "v_mfma_f32_16x16x32_bf16 v[44:47], v[168:171], v[140:143], v[44:47]\n"
  "s_add_u32 s50, s50, 1024\n"
  "s_addc_u32 s51, s51, 0\n"
  "s_waitcnt lgkmcnt(7)\n"
  "v_mfma_f32_16x16x32_bf16 v[60:63], v[172:175], v[140:143], v[60:63]\n"
  "ds_read_b64_tr_b16 v[168:169], v178 offset:49152\n"
  "ds_read_b64_tr_b16 v[170:171], v178 offset:57344\n"
  "v_mfma_f32_16x16x32_bf16 v[56:59], v[172:175], v[136:139], v[56:59]\n"
  "s_add_u32 s52, s52, 32768\n"
  "s_addc_u32 s53, s53, 0\n"
  "ds_read_b128 v[156:159], v180 offset:54272\n"
  "v_mfma_f32_16x16x32_bf16 v[52:55], v[172:175], v[132:135], v[52:55]\n"
  "v_mfma_f32_16x16x32_bf16 v[48:51], v[172:175], v[128:131], v[48:51]\n"
  "s_waitcnt lgkmcnt(7)\n"
  "v_mfma_f32_16x16x32_bf16 v[0:3], v[160:163], v[144:147], v[0:3]\n"
  "ds_read_b64_tr_b16 v[172:173], v179 offset:49152\n"
  "ds_read_b64_tr_b16 v[174:175], v179 offset:57344\n"
  "s_waitcnt lgkmcnt(8)\n"
  "v_mfma_f32_16x16x32_bf16 v[4:7], v[160:163], v[148:151], v[4:7]\n"
  "s_waitcnt vmcnt(20)\n"
  "s_barrier\n"
  "s_waitcnt lgkmcnt(5)\n"
  "v_mfma_f32_16x16x32_bf16 v[8:11], v[160:163], v[152:155], v[8:11]\n"
  "s_waitcnt lgkmcnt(2)\n"
  "v_mfma_f32_16x16x32_bf16 v[12:15], v[160:163], v[156:159], v[12:15]\n"
  "v_add_u32 v180, 0x10000, v180\n"
  "v_mfma_f32_16x16x32_bf16 v[28:31], v[164:167], v[156:159], v[28:31]\n"
  "s_add_u32 m0, s48, 32768\n"
  "ds_read_b128 v[128:131], v180 offset:0\n"
  "v_add_u32 v176, 0x10000, v176\n"
  "v_mfma_f32_16x16x32_bf16 v[24:27], v[164:167], v[152:155], v[24:27]\n"
  "ds_read_b64_tr_b16 v[160:161], v176 offset:0\n"
  "ds_read_b64_tr_b16 v[162:163], v176 offset:8192\n"
  "v_mfma_f32_16x16x32_bf16 v[20:23], v[164:167], v[148:151], v[20:23]\n"
  "ds_read_b128 v[132:135], v180 offset:1024\n"
  "global_load_lds_dwordx4 v232, s[50:51] sc1\n"
  "v_mfma_f32_16x16x32_bf16 v[16:19], v[164:167], v[144:147], v[16:19]\n"
  "global_load_lds_dwordx4 v228, s[50:51] offset:1024 sc1\n"
  "v_mfma_f32_16x16x32_bf16 v[32:35], v[168:171], v[144:147], v[32:35]\n"
  "global_load_lds_dwordx4 v229, s[52:53] offset:2048\n"
  "v_add_u32 v177, 0x10000, v177\n"
  "v_mfma_f32_16x16x32_bf16 v[36:39], v[168:171], v[148:151], v[36:39]\n"
  "ds_read_b64_tr_b16 v[164:165], v177 offset:0\n"
  "ds_read_b64_tr_b16 v[166:167], v177 offset:8192\n"
  "v_mfma_f32_16x16x32_bf16 v[40:43], v[168:171], v[152:155], v[40:43]\n"
  "ds_read_b128 v[136:139], v180 offset:4096\n"
  "global_load_lds_dwordx4 v230, s[52:53] offset:3072\n"
  "v_mfma_f32_16x16x32_bf16 v[44:47], v[168:171], v[156:159], v[44:47]\n"
  "s_add_u32 s50, s50, 1024\n"
  "s_addc_u32 s51, s51, 0\n"
  "s_waitcnt lgkmcnt(7)\n"
  "v_mfma_f32_16x16x32_bf16 v[60:63], v[172:175], v[156:159], v[60:63]\n"
  "ds_read_b128 v[140:143], v180 offset:5120\n"
  "v_add_u32 v178, 0x10000, v178\n"
  "v_mfma_f32_16x16x32_bf16 v[56:59], v[172:175], v[152:155], v[56:59]\n"
  "ds_read_b64_tr_b16 v[168:169], v178 offset:0\n"
  "ds_read_b64_tr_b16 v[170:171], v178 offset:8192\n"
  "v_mfma_f32_16x16x32_bf16 v[52:55], v[172:175], v[148:151], v[52:55]\n"
  "s_add_u32 s52, s52, 32768\n"
  "s_addc_u32 s53, s53, 0\n"
  "v_mfma_f32_16x16x32_bf16 v[48:51], v[172:175], v[144:147], v[48:51]\n"
  "s_waitcnt lgkmcnt(7)\n"
  "v_mfma_f32_16x16x32_bf16 v[0:3], v[160:163], v[128:131], v[0:3]\n"
  "v_add_u32 v179, 0x10000, v179\n"
  "s_waitcnt lgkmcnt(6)\n"
  "v_mfma_f32_16x16x32_bf16 v[4:7], v[160:163], v[132:135], v[4:7]\n"
  "ds_read_b64_tr_b16 v[172:173], v179 offset:0\n"
  "ds_read_b64_tr_b16 v[174:175], v179 offset:8192\n"
  "s_waitcnt vmcnt(20)\n"
  "s_waitcnt lgkmcnt(5)\n"
  "v_mfma_f32_16x16x32_bf16 v[8:11], v[160:163], v[136:139], v[8:11]\n"
  "s_barrier\n"
  "s_waitcnt lgkmcnt(4)\n"
  "v_mfma_f32_16x16x32_bf16 v[12:15], v[160:163], v[140:143], v[12:15]\n"
  "ds_read_b128 v[144:147], v180 offset:16384\n"
  "v_mfma_f32_16x16x32_bf16 v[28:31], v[164:167], v[140:143], v[28:31]\n"
  "s_add_u32 m0, s48, 49152\n"
  "ds_read_b64_tr_b16 v[160:161], v176 offset:16384\n"
  "ds_read_b64_tr_b16 v[162:163], v176 offset:24576\n"
  "v_mfma_f32_16x16x32_bf16 v[24:27], v[164:167], v[136:139], v[24:27]\n"
  "global_load_lds_dwordx4 v232, s[50:51] sc1\n"
  "v_mfma_f32_16x16x32_bf16 v[20:23], v[164:167], v[132:135], v[20:23]\n"
  "ds_read_b128 v[148:151], v180 offset:17408\n"
  "v_mfma_f32_16x16x32_bf16 v[16:19], v[164:167], v[128:131], v[16:19]\n"
  "global_load_lds_dwordx4 v228, s[50:51] offset:1024 sc1\n"
  "s_waitcnt lgkmcnt(6)\n"
  "v_mfma_f32_16x16x32_bf16 v[32:35], v[168:171], v[128:131], v[32:35]\n"
  "ds_read_b64_tr_b16 v[164:165], v177 offset:16384\n"
  "ds_read_b64_tr_b16 v[166:167], v177 offset:24576\n"
  "v_mfma_f32_16x16x32_bf16 v[36:39], v[168:171], v[132:135], v[36:39]\n"
  "ds_read_b128 v[152:155], v180 offset:20480\n"
  "global_load_lds_dwordx4 v229, s[52:53] offset:2048\n"
  "v_mfma_f32_16x16x32_bf16 v[40:43], v[168:171], v[136:139], v[40:43]\n"
  "global_load_lds_dwordx4 v230, s[52:53] offset:3072\n"
  "v_mfma_f32_16x16x32_bf16 v[44:47], v[168:171], v[140:143], v[44:47]\n"
  "s_add_u32 s50, s50, 1024\n"
  "s_addc_u32 s51, s51, 0\n"
  "s_waitcnt lgkmcnt(7)\n"
  "v_mfma_f32_16x16x32_bf16 v[60:63], v[172:175], v[140:143], v[60:63]\n"
  "ds_read_b64_tr_b16 v[168:169], v178 offset:16384\n"
  "ds_read_b64_tr_b16 v[170:171], v178 offset:24576\n"
  "v_mfma_f32_16x16x32_bf16 v[56:59], v[172:175], v[136:139], v[56:59]\n"
  "s_add_u32 s52, s52, 32768\n"
  "s_addc_u32 s53, s53, 0\n"
  "ds_read_b128 v[156:159], v180 offset:21504\n"
  "v_mfma_f32_16x16x32_bf16 v[52:55], v[172:175], v[132:135], v[52:55]\n"
  "v_mfma_f32_16x16x32_bf16 v[48:51], v[172:175], v[128:131], v[48:51]\n"
  "s_waitcnt lgkmcnt(7)\n"
  "v_mfma_f32_16x16x32_bf16 v[0:3], v[160:163], v[144:147], v[0:3]\n"
  "ds_read_b64_tr_b16 v[172:173], v179 offset:16384\n"
  "ds_read_b64_tr_b16 v[174:175], v179 offset:24576\n"
  "s_waitcnt lgkmcnt(8)\n"
  "v_mfma_f32_16x16x32_bf16 v[4:7], v[160:163], v[148:151], v[4:7]\n"
  "s_waitcnt vmcnt(20)\n"
  "s_barrier\n"
  "s_waitcnt lgkmcnt(5)\n"
  "v_mfma_f32_16x16x32_bf16 v[8:11], v[160:163], v[152:155], v[8:11]\n"
  "s_waitcnt lgkmcnt(2)\n"
  "v_mfma_f32_16x16x32_bf16 v[12:15], v[160:163], v[156:159], v[12:15]\n"
  "ds_read_b128 v[128:131], v180 offset:32768\n"
  "v_mfma_f32_16x16x32_bf16 v[28:31], v[164:167], v[156:159], v[28:31]\n"
  "s_add_u32 m0, s48, 65536\n"
  "ds_read_b64_tr_b16 v[160:161], v176 offset:32768\n"
  "ds_read_b64_tr_b16 v[162:163], v176 offset:40960\n"
  "v_mfma_f32_16x16x32_bf16 v[24:27], v[164:167], v[152:155], v[24:27]\n"
  "global_load_lds_dwordx4 v232, s[50:51] sc1\n"
  "v_mfma_f32_16x16x32_bf16 v[20:23], v[164:167], v[148:151], v[20:23]\n"
  "ds_read_b128 v[132:135], v180 offset:33792\n"
  "v_mfma_f32_16x16x32_bf16 v[16:19], v[164:167], v[144:147], v[16:19]\n"
  "global_load_lds_dwordx4 v228, s[50:51] offset:1024 sc1\n"
  "v_mfma_f32_16x16x32_bf16 v[32:35], v[168:171], v[144:147], v[32:35]\n"
  "ds_read_b64_tr_b16 v[164:165], v177 offset:32768\n"
  "ds_read_b64_tr_b16 v[166:167], v177 offset:40960\n"
  "v_mfma_f32_16x16x32_bf16 v[36:39], v[168:171], v[148:151], v[36:39]\n"
  "ds_read_b128 v[136:139], v180 offset:36864\n"
  "global_load_lds_dwordx4 v229, s[52:53] offset:2048\n"
  "v_mfma_f32_16x16x32_bf16 v[40:43], v[168:171], v[152:155], v[40:43]\n"
  "global_load_lds_dwordx4 v230, s[52:53] offset:3072\n"
  "v_mfma_f32_16x16x32_bf16 v[44:47], v[168:171], v[156:159], v[44:47]\n"
  "s_add_u32 s50, s50, 1024\n"
  "s_addc_u32 s51, s51, 0\n"
  "s_waitcnt lgkmcnt(7)\n"
  "v_mfma_f32_16x16x32_bf16 v[60:63], v[172:175], v[156:159], v[60:63]\n"
  "ds_read_b64_tr_b16 v[168:169], v178 offset:32768\n"
  "ds_read_b64_tr_b16 v[170:171], v178 offset:40960\n"
  "v_mfma_f32_16x16x32_bf16 v[56:59], v[172:175], v[152:155], v[56:59]\n"
  "s_add_u32 s52, s52, 32768\n"
  "s_addc_u32 s53, s53, 0\n"
  "ds_read_b128 v[140:143], v180 offset:37888\n"
  "v_mfma_f32_16x16x32_bf16 v[52:55], v[172:175], v[148:151], v[52:55]\n"
  "v_mfma_f32_16x16x32_bf16 v[48:51], v[172:175], v[144:147], v[48:51]\n"
  "s_waitcnt lgkmcnt(7)\n"
  "v_mfma_f32_16x16x32_bf16 v[0:3], v[160:163], v[128:131], v[0:3]\n"
  "ds_read_b64_tr_b16 v[172:173], v179 offset:32768\n"
  "ds_read_b64_tr_b16 v[174:175], v179 offset:40960\n"
  "s_waitcnt lgkmcnt(8)\n"
  "v_mfma_f32_16x16x32_bf16 v[4:7], v[160:163], v[132:135], v[4:7]\n"
  "s_waitcnt vmcnt(20)\n"
  "s_barrier\n"
  "s_waitcnt lgkmcnt(5)\n"
  "v_mfma_f32_16x16x32_bf16 v[8:11], v[160:163], v[136:139], v[8:11]\n"
  "s_waitcnt lgkmcnt(2)\n"
  "v_mfma_f32_16x16x32_bf16 v[12:15], v[160:163], v[140:143], v[12:15]\n"
  "ds_read_b128 v[144:147], v180 offset:49152\n"
  "v_mfma_f32_16x16x32_bf16 v[28:31], v[164:167], v[140:143], v[28:31]\n"
  "s_add_u32 m0, s48, 81920\n"
  "ds_read_b64_tr_b16 v[160:161], v176 offset:49152\n"
  "ds_read_b64_tr_b16 v[162:163], v176 offset:57344\n"
  "v_mfma_f32_16x16x32_bf16 v[24:27], v[164:167], v[136:139], v[24:27]\n"
  "global_load_lds_dwordx4 v232, s[50:51] sc1\n"
  "v_mfma_f32_16x16x32_bf16 v[20:23], v[164:167], v[132:135], v[20:23]\n"
  "ds_read_b128 v[148:151], v180 offset:50176\n"
  "v_mfma_f32_16x16x32_bf16 v[16:19], v[164:167], v[128:131], v[16:19]\n"
  "global_load_lds_dwordx4 v228, s[50:51] offset:1024 sc1\n"
  "v_mfma_f32_16x16x32_bf16 v[32:35], v[168:171], v[128:131], v[32:35]\n"
  "ds_read_b64_tr_b16 v[164:165], v177 offset:49152\n"
  "ds_read_b64_tr_b16 v[166:167], v177 offset:57344\n"
  "v_mfma_f32_16x16x32_bf16 v[36:39], v[168:171], v[132:135], v[36:39]\n"
  "ds_read_b128 v[152:155], v180 offset:53248\n"
  "global_load_lds_dwordx4 v229, s[52:53] offset:2048\n"
  "v_mfma_f32_16x16x32_bf16 v[40:43], v[168:171], v[136:139], v[40:43]\n"
  "global_load_lds_dwordx4 v230, s[52:53] offset:3072\n"
  "v_mfma_f32_16x16x32_bf16 v[44:47], v[168:171], v[140:143], v[44:47]\n"
  "s_add_u32 s50, s50, 1024\n"
  "s_addc_u32 s51, s51, 0\n"
  "s_waitcnt lgkmcnt(7)\n"
  "v_mfma_f32_16x16x32_bf16 v[60:63], v[172:175], v[140:143], v[60:63]\n"
  "ds_read_b64_tr_b16 v[168:169], v178 offset:49152\n"
  "ds_read_b64_tr_b16 v[170:171], v178 offset:57344\n"
  "v_mfma_f32_16x16x32_bf16 v[56:59], v[172:175], v[136:139], v[56:59]\n"
  "s_add_u32 s52, s52, 32768\n"
  "s_addc_u32 s53, s53, 0\n"
  "ds_read_b128 v[156:159], v180 offset:54272\n"
  "v_mfma_f32_16x16x32_bf16 v[52:55], v[172:175], v[132:135], v[52:55]\n"
  "v_mfma_f32_16x16x32_bf16 v[48:51], v[172:175], v[128:131], v[48:51]\n"
  "s_waitcnt lgkmcnt(7)\n"
  "v_mfma_f32_16x16x32_bf16 v[0:3], v[160:163], v[144:147], v[0:3]\n"
  "ds_read_b64_tr_b16 v[172:173], v179 offset:49152\n"
  "ds_read_b64_tr_b16 v[174:175], v179 offset:57344\n"
  "s_waitcnt lgkmcnt(8)\n"
  "v_mfma_f32_16x16x32_bf16 v[4:7], v[160:163], v[148:151], v[4:7]\n"
  "s_waitcnt vmcnt(20)\n"
  "s_barrier\n"
  "s_waitcnt lgkmcnt(5)\n"
  "v_mfma_f32_16x16x32_bf16 v[8:11], v[160:163], v[152:155], v[8:11]\n"
  "s_waitcnt lgkmcnt(2)\n"
  "v_mfma_f32_16x16x32_bf16 v[12:15], v[160:163], v[156:159], v[12:15]\n"
  "v_add_u32 v180, 0xffff0000, v180\n"
  "v_mfma_f32_16x16x32_bf16 v[28:31], v[164:167], v[156:159], v[28:31]\n"
  "s_add_u32 m0, s48, 98304\n"
  "ds_read_b128 v[128:131], v180 offset:0\n"
  "v_add_u32 v176, 0xffff0000, v176\n"
  "v_mfma_f32_16x16x32_bf16 v[24:27], v[164:167], v[152:155], v[24:27]\n"
  "ds_read_b64_tr_b16 v[160:161], v176 offset:0\n"
  "ds_read_b64_tr_b16 v[162:163], v176 offset:8192\n"
  "v_mfma_f32_16x16x32_bf16 v[20:23], v[164:167], v[148:151], v[20:23]\n"
  "ds_read_b128 v[132:135], v180 offset:1024\n"
  "global_load_lds_dwordx4 v232, s[50:51] sc1\n"
  "v_mfma_f32_16x16x32_bf16 v[16:19], v[164:167], v[144:147], v[16:19]\n"
  "global_load_lds_dwordx4 v228, s[50:51] offset:1024 sc1\n"
  "v_mfma_f32_16x16x32_bf16 v[32:35], v[168:171], v[144:147], v[32:35]\n"
  "global_load_lds_dwordx4 v229, s[52:53] offset:2048\n"
  "v_add_u32 v177, 0xffff0000, v177\n"
  "v_mfma_f32_16x16x32_bf16 v[36:39], v[168:171], v[148:151], v[36:39]\n"
  "ds_read_b64_tr_b16 v[164:165], v177 offset:0\n"
  "ds_read_b64_tr_b16 v[166:167], v177 offset:8192\n"
  "v_mfma_f32_16x16x32_bf16 v[40:43], v[168:171], v[152:155], v[40:43]\n"
  "ds_read_b128 v[136:139], v180 offset:4096\n"
  "global_load_lds_dwordx4 v230, s[52:53] offset:3072\n"
  "v_mfma_f32_16x16x32_bf16 v[44:47], v[168:171], v[156:159], v[44:47]\n"
  "s_add_u32 s50, s50, 1024\n"
  "s_addc_u32 s51, s51, 0\n"
  "s_waitcnt lgkmcnt(7)\n"
  "v_mfma_f32_16x16x32_bf16 v[60:63], v[172:175], v[156:159], v[60:63]\n"
  "ds_read_b128 v[140:143], v180 offset:5120\n"
  "v_add_u32 v178, 0xffff0000, v178\n"
  "v_mfma_f32_16x16x32_bf16 v[56:59], v[172:175], v[152:155], v[56:59]\n"
  "ds_read_b64_tr_b16 v[168:169], v178 offset:0\n"
  "ds_read_b64_tr_b16 v[170:171], v178 offset:8192\n"
  "v_mfma_f32_16x16x32_bf16 v[52:55], v[172:175], v[148:151], v[52:55]\n"
  "s_add_u32 s52, s52, 32768\n"
  "s_addc_u32 s53, s53, 0\n"
  "v_mfma_f32_16x16x32_bf16 v[48:51], v[172:175], v[144:147], v[48:51]\n"
  "s_waitcnt lgkmcnt(7)\n"
  "v_mfma_f32_16x16x32_bf16 v[0:3], v[160:163], v[128:131], v[0:3]\n"
  "v_add_u32 v179, 0xffff0000, v179\n"
  "s_waitcnt lgkmcnt(6)\n"
  "v_mfma_f32_16x16x32_bf16 v[4:7], v[160:163], v[132:135], v[4:7]\n"
  "ds_read_b64_tr_b16 v[172:173], v179 offset:0\n"
  "ds_read_b64_tr_b16 v[174:175], v179 offset:8192\n"
  "s_waitcnt vmcnt(20)\n"
  "s_waitcnt lgkmcnt(5)\n"
  "v_mfma_f32_16x16x32_bf16 v[8:11], v[160:163], v[136:139], v[8:11]\n"
  "s_barrier\n"
  "s_waitcnt lgkmcnt(4)\n"
  "v_mfma_f32_16x16x32_bf16 v[12:15], v[160:163], v[140:143], v[12:15]\n"
  "ds_read_b128 v[144:147], v180 offset:16384\n"
  "v_mfma_f32_16x16x32_bf16 v[28:31], v[164:167], v[140:143], v[28:31]\n"
  "s_add_u32 m0, s48, 114688\n"
  "ds_read_b64_tr_b16 v[160:161], v176 offset:16384\n"
  "ds_read_b64_tr_b16 v[162:163], v176 offset:24576\n"
  "v_mfma_f32_16x16x32_bf16 v[24:27], v[164:167], v[136:139], v[24:27]\n"
  "global_load_lds_dwordx4 v232, s[50:51] sc1\n"
  "v_mfma_f32_16x16x32_bf16 v[20:23], v[164:167], v[132:135], v[20:23]\n"
  "ds_read_b128 v[148:151], v180 offset:17408\n"
  "v_mfma_f32_16x16x32_bf16 v[16:19], v[164:167], v[128:131], v[16:19]\n"
  "global_load_lds_dwordx4 v228, s[50:51] offset:1024 sc1\n"
  "s_waitcnt lgkmcnt(6)\n"
  "v_mfma_f32_16x16x32_bf16 v[32:35], v[168:171], v[128:131], v[32:35]\n"
  "ds_read_b64_tr_b16 v[164:165], v177 offset:16384\n"
  "ds_read_b64_tr_b16 v[166:167], v177 offset:24576\n"
  "v_mfma_f32_16x16x32_bf16 v[36:39], v[168:171], v[132:135], v[36:39]\n"
  "ds_read_b128 v[152:155], v180 offset:20480\n"
  "global_load_lds_dwordx4 v229, s[52:53] offset:2048\n"
  "v_mfma_f32_16x16x32_bf16 v[40:43], v[168:171], v[136:139], v[40:43]\n"
  "global_load_lds_dwordx4 v230, s[52:53] offset:3072\n"
  "v_mfma_f32_16x16x32_bf16 v[44:47], v[168:171], v[140:143], v[44:47]\n"
  "s_add_u32 s50, s50, 1024\n"
  "s_addc_u32 s51, s51, 0\n"
  "s_waitcnt lgkmcnt(7)\n"
  "v_mfma_f32_16x16x32_bf16 v[60:63], v[172:175], v[140:143], v[60:63]\n"
  "ds_read_b64_tr_b16 v[168:169], v178 offset:16384\n"
  "ds_read_b64_tr_b16 v[170:171], v178 offset:24576\n"
  "v_mfma_f32_16x16x32_bf16 v[56:59], v[172:175], v[136:139], v[56:59]\n"
  "s_add_u32 s52, s52, 32768\n"
  "s_addc_u32 s53, s53, 0\n"
  "ds_read_b128 v[156:159], v180 offset:21504\n"
  "v_mfma_f32_16x16x32_bf16 v[52:55], v[172:175], v[132:135], v[52:55]\n"
  "v_mfma_f32_16x16x32_bf16 v[48:51], v[172:175], v[128:131], v[48:51]\n"
  "s_waitcnt lgkmcnt(7)\n"
  "v_mfma_f32_16x16x32_bf16 v[0:3], v[160:163], v[144:147], v[0:3]\n"
  "ds_read_b64_tr_b16 v[172:173], v179 offset:16384\n"
  "ds_read_b64_tr_b16 v[174:175], v179 offset:24576\n"
  "s_waitcnt lgkmcnt(8)\n"
  "v_mfma_f32_16x16x32_bf16 v[4:7], v[160:163], v[148:151], v[4:7]\n"
  "s_waitcnt vmcnt(20)\n"
  "s_barrier\n"
  "s_waitcnt lgkmcnt(5)\n"
  "v_mfma_f32_16x16x32_bf16 v[8:11], v[160:163], v[152:155], v[8:11]\n"
  "s_waitcnt lgkmcnt(2)\n"
  "v_mfma_f32_16x16x32_bf16 v[12:15], v[160:163], v[156:159], v[12:15]\n"
  "ds_read_b128 v[128:131], v180 offset:32768\n"
  "v_mfma_f32_16x16x32_bf16 v[28:31], v[164:167], v[156:159], v[28:31]\n"
  "s_add_u32 m0, s48, 0\n"
  "ds_read_b64_tr_b16 v[160:161], v176 offset:32768\n"
  "ds_read_b64_tr_b16 v[162:163], v176 offset:40960\n"
  "v_mfma_f32_16x16x32_bf16 v[24:27], v[164:167], v[152:155], v[24:27]\n"
  "global_load_lds_dwordx4 v232, s[50:51] sc1\n"
  "global_load_lds_dwordx4 v228, s[50:51] offset:1024 sc1\n"
  "v_mfma_f32_16x16x32_bf16 v[20:23], v[164:167], v[148:151], v[20:23]\n"
  "ds_read_b128 v[132:135], v180 offset:33792\n"
  "global_load_lds_dwordx4 v229, s[52:53] offset:2048\n"
  "v_mfma_f32_16x16x32_bf16 v[16:19], v[164:167], v[144:147], v[16:19]\n"
  "global_load_lds_dwordx4 v230, s[52:53] offset:3072\n"
  "v_mfma_f32_16x16x32_bf16 v[32:35], v[168:171], v[144:147], v[32:35]\n"
  "ds_read_b64_tr_b16 v[164:165], v177 offset:32768\n"
  "ds_read_b64_tr_b16 v[166:167], v177 offset:40960\n"
  "v_mfma_f32_16x16x32_bf16 v[36:39], v[168:171], v[148:151], v[36:39]\n"
  "s_add_u32 s50, s50, 1024\n"
  "s_addc_u32 s51, s51, 0\n"
  "ds_read_b128 v[136:139], v180 offset:36864\n"
  "v_mfma_f32_16x16x32_bf16 v[40:43], v[168:171], v[152:155], v[40:43]\n"
  "s_add_u32 s52, s52, 32768\n"
  "s_addc_u32 s53, s53, 0\n"
  "s_lshl_b32 s36, s57, 2\n"
  "v_mfma_f32_16x16x32_bf16 v[44:47], v[168:171], v[156:159], v[44:47]\n"
  "s_add_u32 s36, s36, s58\n"
  "s_lshl_b32 s46, s36, 4\n"
  "s_waitcnt lgkmcnt(7)\n"
  "v_mfma_f32_16x16x32_bf16 v[60:63], v[172:175], v[156:159], v[60:63]\n"
  "s_sub_u32 s36, s56, s35\n"
  "ds_read_b64_tr_b16 v[168:169], v178 offset:32768\n"
  "ds_read_b64_tr_b16 v[170:171], v178 offset:40960\n"
  "v_mfma_f32_16x16x32_bf16 v[56:59], v[172:175], v[152:155], v[56:59]\n"
  "s_sub_u32 s36, s36, 1\n"
  "s_and_b32 s36, s36, 3\n"
  "ds_read_b128 v[140:143], v180 offset:37888\n"
  "v_mfma_f32_16x16x32_bf16 v[52:55], v[172:175], v[148:151], v[52:55]\n"
  "s_lshl_b32 s37, s35, 2\n"
  "s_add_u32 s37, s37, s36\n"
  "s_add_u32 s37, s37, s46\n"
  "v_mfma_f32_16x16x32_bf16 v[48:51], v[172:175], v[144:147], v[48:51]\n"
  "s_lshl_b32 s37, s37, 13\n"
  "s_add_u32 s37, s37, 75501568\n"
  "s_waitcnt lgkmcnt(7)\n"
  "v_mfma_f32_16x16x32_bf16 v[0:3], v[160:163], v[128:131], v[0:3]\n"
  "ds_read_b64_tr_b16 v[172:173], v179 offset:32768\n"
  "ds_read_b64_tr_b16 v[174:175], v179 offset:40960\n"
  "s_waitcnt lgkmcnt(8)\n"
  "v_mfma_f32_16x16x32_bf16 v[4:7], v[160:163], v[132:135], v[4:7]\n"
  "s_waitcnt vmcnt(20)\n"
  "s_barrier\n"
  "s_waitcnt lgkmcnt(5)\n"
  "v_mfma_f32_16x16x32_bf16 v[8:11], v[160:163], v[136:139], v[8:11]\n"
  "s_waitcnt lgkmcnt(2)\n"
  "v_mfma_f32_16x16x32_bf16 v[12:15], v[160:163], v[140:143], v[12:15]\n"
  "ds_read_b128 v[144:147], v180 offset:49152\n"
  "v_mfma_f32_16x16x32_bf16 v[28:31], v[164:167], v[140:143], v[28:31]\n"
  "s_add_u32 m0, s48, 16384\n"
  "ds_read_b64_tr_b16 v[160:161], v176 offset:49152\n"
  "ds_read_b64_tr_b16 v[162:163], v176 offset:57344\n"
  "v_mfma_f32_16x16x32_bf16 v[24:27], v[164:167], v[136:139], v[24:27]\n"
  "global_load_lds_dwordx4 v232, s[50:51] sc1\n"
  "global_load_lds_dwordx4 v228, s[50:51] offset:1024 sc1\n"
  "v_mfma_f32_16x16x32_bf16 v[20:23], v[164:167], v[132:135], v[20:23]\n"
  "ds_read_b128 v[148:151], v180 offset:50176\n"
  "global_load_lds_dwordx4 v229, s[52:53] offset:2048\n"
  "v_mfma_f32_16x16x32_bf16 v[16:19], v[164:167], v[128:131], v[16:19]\n"
  "global_load_lds_dwordx4 v230, s[52:53] offset:3072\n"
  "v_mfma_f32_16x16x32_bf16 v[32:35], v[168:171], v[128:131], v[32:35]\n"
  "ds_read_b64_tr_b16 v[164:165], v177 offset:49152\n"
  "ds_read_b64_tr_b16 v[166:167], v177 offset:57344\n"
  "v_mfma_f32_16x16x32_bf16 v[36:39], v[168:171], v[132:135], v[36:39]\n"
  "s_add_u32 s50, s50, 1024\n"
  "s_addc_u32 s51, s51, 0\n"
  "ds_read_b128 v[152:155], v180 offset:53248\n"
  "v_mfma_f32_16x16x32_bf16 v[40:43], v[168:171], v[136:139], v[40:43]\n"
  "s_add_u32 s52, s52, 32768\n"
  "s_addc_u32 s53, s53, 0\n"
  "v_mfma_f32_16x16x32_bf16 v[44:47], v[168:171], v[140:143], v[44:47]\n"
  "s_add_u32 s24, s84, s37\n"
  "s_addc_u32 s25, s85, 0\n"
  "s_waitcnt lgkmcnt(7)\n"
  "v_mfma_f32_16x16x32_bf16 v[60:63], v[172:175], v[140:143], v[60:63]\n"
  "s_lshl_b32 s37, s56, 2\n"
  "ds_read_b64_tr_b16 v[168:169], v178 offset:49152\n"
  "ds_read_b64_tr_b16 v[170:171], v178 offset:57344\n"
  "v_mfma_f32_16x16x32_bf16 v[56:59], v[172:175], v[136:139], v[56:59]\n"
  "s_add_u32 s46, s46, s37\n"
  "s_add_u32 s36, s46, 0\n"
  "ds_read_b128 v[156:159], v180 offset:54272\n"
  "v_mfma_f32_16x16x32_bf16 v[52:55], v[172:175], v[132:135], v[52:55]\n"
  "s_lshl_b32 s36, s36, 2\n"
  "s_add_u32 s36, s36, s35\n"
  "s_lshl_b32 s36, s36, 11\n"
  "v_mfma_f32_16x16x32_bf16 v[48:51], v[172:175], v[128:131], v[48:51]\n"
  "s_add_u32 s36, s36, 75497472\n"
  "s_add_u32 s26, s84, s36\n"
  "s_addc_u32 s27, s85, 0\n"
  "s_add_u32 s36, s46, 1\n"
  "s_lshl_b32 s36, s36, 2\n"
  "s_add_u32 s36, s36, s35\n"
  "s_waitcnt lgkmcnt(7)\n"
  "v_mfma_f32_16x16x32_bf16 v[0:3], v[160:163], v[144:147], v[0:3]\n"
  "ds_read_b64_tr_b16 v[172:173], v179 offset:49152\n"
  "ds_read_b64_tr_b16 v[174:175], v179 offset:57344\n"
  "s_waitcnt lgkmcnt(8)\n"
  "v_mfma_f32_16x16x32_bf16 v[4:7], v[160:163], v[148:151], v[4:7]\n"
  "s_waitcnt vmcnt(20)\n"
  "s_barrier\n"
  "s_waitcnt lgkmcnt(5)\n"
  "v_mfma_f32_16x16x32_bf16 v[8:11], v[160:163], v[152:155], v[8:11]\n"
  "s_waitcnt lgkmcnt(2)\n"
  "v_mfma_f32_16x16x32_bf16 v[12:15], v[160:163], v[156:159], v[12:15]\n"
  "v_add_u32 v180, 0x10000, v180\n"
  "v_mfma_f32_16x16x32_bf16 v[28:31], v[164:167], v[156:159], v[28:31]\n"
  "s_add_u32 m0, s48, 32768\n"
  "ds_read_b128 v[128:131], v180 offset:0\n"
  "v_add_u32 v176, 0x10000, v176\n"
  "v_mfma_f32_16x16x32_bf16 v[24:27], v[164:167], v[152:155], v[24:27]\n"
  "ds_read_b64_tr_b16 v[160:161], v176 offset:0\n"
  "ds_read_b64_tr_b16 v[162:163], v176 offset:8192\n"
  "v_mfma_f32_16x16x32_bf16 v[20:23], v[164:167], v[148:151], v[20:23]\n"
  "ds_read_b128 v[132:135], v180 offset:1024\n"
  "global_load_lds_dwordx4 v232, s[50:51] sc1\n"
  "v_mfma_f32_16x16x32_bf16 v[16:19], v[164:167], v[144:147], v[16:19]\n"
  "global_load_lds_dwordx4 v228, s[50:51] offset:1024 sc1\n"
  "global_load_lds_dwordx4 v229, s[52:53] offset:2048\n"
  "v_mfma_f32_16x16x32_bf16 v[32:35], v[168:171], v[144:147], v[32:35]\n"
  "global_load_lds_dwordx4 v230, s[52:53] offset:3072\n"
  "v_add_u32 v177, 0x10000, v177\n"
  "v_mfma_f32_16x16x32_bf16 v[36:39], v[168:171], v[148:151], v[36:39]\n"
  "ds_read_b64_tr_b16 v[164:165], v177 offset:0\n"
  "ds_read_b64_tr_b16 v[166:167], v177 offset:8192\n"
  "v_mfma_f32_16x16x32_bf16 v[40:43], v[168:171], v[152:155], v[40:43]\n"
  "s_add_u32 s50, s50, 1024\n"
  "s_addc_u32 s51, s51, 0\n"
  "ds_read_b128 v[136:139], v180 offset:4096\n"
  "v_mfma_f32_16x16x32_bf16 v[44:47], v[168:171], v[156:159], v[44:47]\n"
  "s_add_u32 s52, s52, 32768\n"
  "s_addc_u32 s53, s53, 0\n"
  "s_waitcnt lgkmcnt(7)\n"
  "v_mfma_f32_16x16x32_bf16 v[60:63], v[172:175], v[156:159], v[60:63]\n"
  "s_lshl_b32 s36, s36, 11\n"
  "ds_read_b128 v[140:143], v180 offset:5120\n"
  "v_add_u32 v178, 0x10000, v178\n"
  "v_mfma_f32_16x16x32_bf16 v[56:59], v[172:175], v[152:155], v[56:59]\n"
  "s_add_u32 s36, s36, 75497472\n"
  "ds_read_b64_tr_b16 v[168:169], v178 offset:0\n"
  "ds_read_b64_tr_b16 v[170:171], v178 offset:8192\n"
  "v_mfma_f32_16x16x32_bf16 v[52:55], v[172:175], v[148:151], v[52:55]\n"
  "s_add_u32 s40, s84, s36\n"
  "s_addc_u32 s41, s85, 0\n"
  "s_add_u32 s36, s46, 2\n"
  "v_mfma_f32_16x16x32_bf16 v[48:51], v[172:175], v[144:147], v[48:51]\n"
  "s_lshl_b32 s36, s36, 2\n"
  "s_add_u32 s36, s36, s35\n"
  "s_lshl_b32 s36, s36, 11\n"
  "s_add_u32 s36, s36, 75497472\n"
  "s_add_u32 s42, s84, s36\n"
  "s_addc_u32 s43, s85, 0\n"
  "s_lshl_b32 s36, s57, 3\n"
  "s_lshr_b32 s37, s56, 1\n"
  "s_lshl_b32 s37, s37, 2\n"
  "s_waitcnt lgkmcnt(7)\n"
  "v_mfma_f32_16x16x32_bf16 v[0:3], v[160:163], v[128:131], v[0:3]\n"
  "v_add_u32 v179, 0x10000, v179\n"
  "s_waitcnt lgkmcnt(6)\n"
  "v_mfma_f32_16x16x32_bf16 v[4:7], v[160:163], v[132:135], v[4:7]\n"
  "ds_read_b64_tr_b16 v[172:173], v179 offset:0\n"
  "ds_read_b64_tr_b16 v[174:175], v179 offset:8192\n"
  "s_waitcnt vmcnt(20)\n"
  "s_waitcnt lgkmcnt(5)\n"
  "v_mfma_f32_16x16x32_bf16 v[8:11], v[160:163], v[136:139], v[8:11]\n"
  "s_barrier\n"
  "s_waitcnt lgkmcnt(4)\n"
  "v_mfma_f32_16x16x32_bf16 v[12:15], v[160:163], v[140:143], v[12:15]\n"
  "ds_read_b128 v[144:147], v180 offset:16384\n"
  "v_mfma_f32_16x16x32_bf16 v[28:31], v[164:167], v[140:143], v[28:31]\n"
  "s_add_u32 s36, s36, s37\n"
  "ds_read_b64_tr_b16 v[160:161], v176 offset:16384\n"
  "ds_read_b64_tr_b16 v[162:163], v176 offset:24576\n"
  "v_mfma_f32_16x16x32_bf16 v[24:27], v[164:167], v[136:139], v[24:27]\n"
  "s_add_u32 s36, s36, s35\n"
  "s_lshl_b32 s36, s36, 14\n"
  "v_mfma_f32_16x16x32_bf16 v[20:23], v[164:167], v[132:135], v[20:23]\n"
  "s_lshl_b32 s37, s58, 8\n"
  "ds_read_b128 v[148:151], v180 offset:17408\n"
  "v_mfma_f32_16x16x32_bf16 v[16:19], v[164:167], v[128:131], v[16:19]\n"
  "s_add_u32 s36, s36, s37\n"
  "s_waitcnt lgkmcnt(6)\n"
  "v_mfma_f32_16x16x32_bf16 v[32:35], v[168:171], v[128:131], v[32:35]\n"
  "s_and_b32 s37, s56, 1\n"
  "ds_read_b64_tr_b16 v[164:165], v177 offset:16384\n"
  "ds_read_b64_tr_b16 v[166:167], v177 offset:24576\n"
  "v_mfma_f32_16x16x32_bf16 v[36:39], v[168:171], v[132:135], v[36:39]\n"
  "s_lshl_b32 s37, s37, 7\n"
  "ds_read_b128 v[152:155], v180 offset:20480\n"
  "v_mfma_f32_16x16x32_bf16 v[40:43], v[168:171], v[136:139], v[40:43]\n"
  "s_add_u32 s36, s36, s37\n"
  "v_mfma_f32_16x16x32_bf16 v[44:47], v[168:171], v[140:143], v[44:47]\n"
  "s_add_u32 s28, s64, s36\n"
  "s_addc_u32 s29, s65, 0\n"
  "s_waitcnt lgkmcnt(7)\n"
  "v_mfma_f32_16x16x32_bf16 v[60:63], v[172:175], v[140:143], v[60:63]\n"
  "ds_read_b64_tr_b16 v[168:169], v178 offset:16384\n"
  "ds_read_b64_tr_b16 v[170:171], v178 offset:24576\n"
  "v_mfma_f32_16x16x32_bf16 v[56:59], v[172:175], v[136:139], v[56:59]\n"
  "s_add_u32 s30, s82, s36\n"
  "s_addc_u32 s31, s83, 0\n"
  "ds_read_b128 v[156:159], v180 offset:21504\n"
  "v_mfma_f32_16x16x32_bf16 v[52:55], v[172:175], v[132:135], v[52:55]\n"
  "v_and_b32 v186, 15, v233\n"
  "v_lshlrev_b32 v186, 10, v186\n"
  "v_mfma_f32_16x16x32_bf16 v[48:51], v[172:175], v[128:131], v[48:51]\n"
  "s_waitcnt lgkmcnt(7)\n"
  "v_mfma_f32_16x16x32_bf16 v[0:3], v[160:163], v[144:147], v[0:3]\n"
  "ds_read_b64_tr_b16 v[172:173], v179 offset:16384\n"
  "ds_read_b64_tr_b16 v[174:175], v179 offset:24576\n"
  "s_waitcnt lgkmcnt(8)\n"
  "v_mfma_f32_16x16x32_bf16 v[4:7], v[160:163], v[148:151], v[4:7]\n"
  "s_waitcnt vmcnt(16)\n"
  "s_barrier\n"
  "s_waitcnt lgkmcnt(5)\n"
  "v_mfma_f32_16x16x32_bf16 v[8:11], v[160:163], v[152:155], v[8:11]\n"
  "s_waitcnt lgkmcnt(2)\n"
  "v_mfma_f32_16x16x32_bf16 v[12:15], v[160:163], v[156:159], v[12:15]\n"
  "ds_read_b128 v[128:131], v180 offset:32768\n"
  "v_mfma_f32_16x16x32_bf16 v[28:31], v[164:167], v[156:159], v[28:31]\n"
  "ds_read_b64_tr_b16 v[160:161], v176 offset:32768\n"
  "ds_read_b64_tr_b16 v[162:163], v176 offset:40960\n"
  "v_mfma_f32_16x16x32_bf16 v[24:27], v[164:167], v[152:155], v[24:27]\n"
  "v_lshrrev_b32 v234, 4, v233\n"
  "v_lshl_add_u32 v187, v234, 3, v186\n"
  "v_mfma_f32_16x16x32_bf16 v[20:23], v[164:167], v[148:151], v[20:23]\n"
  "ds_read_b128 v[132:135], v180 offset:33792\n"
  "global_load_dwordx2 v[240:241], v187, s[28:29] offset:0\n"
  "v_mfma_f32_16x16x32_bf16 v[16:19], v[164:167], v[144:147], v[16:19]\n"
  "global_load_dwordx2 v[242:243], v187, s[28:29] offset:32\n"
  "global_load_dwordx2 v[244:245], v187, s[28:29] offset:64\n"
  "v_mfma_f32_16x16x32_bf16 v[32:35], v[168:171], v[144:147], v[32:35]\n"
  "ds_read_b64_tr_b16 v[164:165], v177 offset:32768\n"
  "ds_read_b64_tr_b16 v[166:167], v177 offset:40960\n"
  "v_mfma_f32_16x16x32_bf16 v[36:39], v[168:171], v[148:151], v[36:39]\n"
  "ds_read_b128 v[136:139], v180 offset:36864\n"
  "global_load_dwordx2 v[246:247], v187, s[28:29] offset:96\n"
  "v_mfma_f32_16x16x32_bf16 v[40:43], v[168:171], v[152:155], v[40:43]\n"
  "v_and_b32 v235, 1, v234\n"
  "v_lshl_add_u32 v186, v235, 5, v186\n"
  "v_mfma_f32_16x16x32_bf16 v[44:47], v[168:171], v[156:159], v[44:47]\n"
  "v_lshrrev_b32 v234, 1, v234\n"
  "s_waitcnt lgkmcnt(7)\n"
  "v_mfma_f32_16x16x32_bf16 v[60:63], v[172:175], v[156:159], v[60:63]\n"
  "ds_read_b64_tr_b16 v[168:169], v178 offset:32768\n"
  "ds_read_b64_tr_b16 v[170:171], v178 offset:40960\n"
  "v_mfma_f32_16x16x32_bf16 v[56:59], v[172:175], v[152:155], v[56:59]\n"
  "ds_read_b128 v[140:143], v180 offset:37888\n"
  "v_lshl_add_u32 v186, v234, 4, v186\n"
  "v_mfma_f32_16x16x32_bf16 v[52:55], v[172:175], v[148:151], v[52:55]\n"
  "v_and_b32 v189, 3, v233\n"
  "v_min_u32 v189, 2, v189\n"
  "v_mfma_f32_16x16x32_bf16 v[48:51], v[172:175], v[144:147], v[48:51]\n"
  "s_waitcnt lgkmcnt(7)\n"
  "v_mfma_f32_16x16x32_bf16 v[0:3], v[160:163], v[128:131], v[0:3]\n"
  "ds_read_b64_tr_b16 v[172:173], v179 offset:32768\n"
  "ds_read_b64_tr_b16 v[174:175], v179 offset:40960\n"
  "s_waitcnt lgkmcnt(8)\n"
  "v_mfma_f32_16x16x32_bf16 v[4:7], v[160:163], v[132:135], v[4:7]\n"
  "s_waitcnt vmcnt(16)\n"
  "s_barrier\n"
  "s_waitcnt lgkmcnt(5)\n"
  "v_mfma_f32_16x16x32_bf16 v[8:11], v[160:163], v[136:139], v[8:11]\n"
  "s_waitcnt lgkmcnt(2)\n"
  "v_mfma_f32_16x16x32_bf16 v[12:15], v[160:163], v[140:143], v[12:15]\n"
  "ds_read_b128 v[144:147], v180 offset:49152\n"
  "v_mfma_f32_16x16x32_bf16 v[28:31], v[164:167], v[140:143], v[28:31]\n"
  "s_add_u32 s36, s56, 1\n"
  "ds_read_b64_tr_b16 v[160:161], v176 offset:49152\n"
  "ds_read_b64_tr_b16 v[162:163], v176 offset:57344\n"
  "v_mfma_f32_16x16x32_bf16 v[24:27], v[164:167], v[136:139], v[24:27]\n"
  "v_add_u32 v189, s36, v189\n"
  "v_and_b32 v189, 3, v189\n"
  "v_mfma_f32_16x16x32_bf16 v[20:23], v[164:167], v[132:135], v[20:23]\n"
  "s_andn2_b32 s36, s86, 3\n"
  "ds_read_b128 v[148:151], v180 offset:50176\n"
  "v_mfma_f32_16x16x32_bf16 v[16:19], v[164:167], v[128:131], v[16:19]\n"
  "v_add_u32 v189, s36, v189\n"
  "v_mfma_f32_16x16x32_bf16 v[32:35], v[168:171], v[128:131], v[32:35]\n"
  "ds_read_b64_tr_b16 v[164:165], v177 offset:49152\n"
  "ds_read_b64_tr_b16 v[166:167], v177 offset:57344\n"
  "v_mfma_f32_16x16x32_bf16 v[36:39], v[168:171], v[132:135], v[36:39]\n"
  "ds_read_b128 v[152:155], v180 offset:53248\n"
  "v_lshlrev_b32 v189, 9, v189\n"
  "v_mfma_f32_16x16x32_bf16 v[40:43], v[168:171], v[136:139], v[40:43]\n"
  "s_add_u32 s36, s38, 131072\n"
  "v_add_u32 v181, s36, v232\n"
  "v_mfma_f32_16x16x32_bf16 v[44:47], v[168:171], v[140:143], v[44:47]\n"
  "s_lshl_b32 s37, s35, 12\n"
  "s_waitcnt lgkmcnt(7)\n"
  "v_mfma_f32_16x16x32_bf16 v[60:63], v[172:175], v[140:143], v[60:63]\n"
  "ds_read_b64_tr_b16 v[168:169], v178 offset:49152\n"
  "ds_read_b64_tr_b16 v[170:171], v178 offset:57344\n"
  "v_mfma_f32_16x16x32_bf16 v[56:59], v[172:175], v[136:139], v[56:59]\n"
  "ds_read_b128 v[156:159], v180 offset:54272\n"
  "v_add_u32 v182, s37, v181\n"
  "v_mfma_f32_16x16x32_bf16 v[52:55], v[172:175], v[132:135], v[52:55]\n"
  "s_mov_b32 s44, 0x3f80\n"
  "s_mov_b32 s45, 0x3f800000\n"
  "v_mfma_f32_16x16x32_bf16 v[48:51], v[172:175], v[128:131], v[48:51]\n"
  "s_waitcnt lgkmcnt(7)\n"
  "v_mfma_f32_16x16x32_bf16 v[0:3], v[160:163], v[144:147], v[0:3]\n"
  "ds_read_b64_tr_b16 v[172:173], v179 offset:49152\n"
  "ds_read_b64_tr_b16 v[174:175], v179 offset:57344\n"
  "s_waitcnt lgkmcnt(8)\n"
  "v_mfma_f32_16x16x32_bf16 v[4:7], v[160:163], v[148:151], v[4:7]\n"
  "s_waitcnt vmcnt(12)\n"
  "s_barrier\n"
  "s_waitcnt lgkmcnt(5)\n"
  "v_mfma_f32_16x16x32_bf16 v[8:11], v[160:163], v[152:155], v[8:11]\n"
  "s_waitcnt lgkmcnt(2)\n"
  "v_mfma_f32_16x16x32_bf16 v[12:15], v[160:163], v[156:159], v[12:15]\n"
  "v_add_u32 v180, 0xffff0000, v180\n"
  "v_mfma_f32_16x16x32_bf16 v[28:31], v[164:167], v[156:159], v[28:31]\n"
  "ds_read_b128 v[128:131], v180 offset:0\n"
  "v_add_u32 v176, 0xffff0000, v176\n"
  "v_mfma_f32_16x16x32_bf16 v[24:27], v[164:167], v[152:155], v[24:27]\n"
  "ds_read_b64_tr_b16 v[160:161], v176 offset:0\n"
  "ds_read_b64_tr_b16 v[162:163], v176 offset:8192\n"
  "v_mfma_f32_16x16x32_bf16 v[20:23], v[164:167], v[148:151], v[20:23]\n"
  "ds_read_b128 v[132:135], v180 offset:1024\n"
  "global_load_dwordx2 v[190:191], v189, s[62:63] offset:192 sc1\n"
  "v_mfma_f32_16x16x32_bf16 v[16:19], v[164:167], v[144:147], v[16:19]\n"
  "v_mfma_f32_16x16x32_bf16 v[32:35], v[168:171], v[144:147], v[32:35]\n"
  "v_add_u32 v177, 0xffff0000, v177\n"
  "v_mfma_f32_16x16x32_bf16 v[36:39], v[168:171], v[148:151], v[36:39]\n"
  "ds_read_b64_tr_b16 v[164:165], v177 offset:0\n"
  "ds_read_b64_tr_b16 v[166:167], v177 offset:8192\n"
  "v_mfma_f32_16x16x32_bf16 v[40:43], v[168:171], v[152:155], v[40:43]\n"
  "ds_read_b128 v[136:139], v180 offset:4096\n"
  "v_mfma_f32_16x16x32_bf16 v[44:47], v[168:171], v[156:159], v[44:47]\n"
  "s_waitcnt lgkmcnt(7)\n"
  "v_mfma_f32_16x16x32_bf16 v[60:63], v[172:175], v[156:159], v[60:63]\n"
  "ds_read_b128 v[140:143], v180 offset:5120\n"
  "v_add_u32 v178, 0xffff0000, v178\n"
  "v_mfma_f32_16x16x32_bf16 v[56:59], v[172:175], v[152:155], v[56:59]\n"
  "ds_read_b64_tr_b16 v[168:169], v178 offset:0\n"
  "ds_read_b64_tr_b16 v[170:171], v178 offset:8192\n"
  "v_mfma_f32_16x16x32_bf16 v[52:55], v[172:175], v[148:151], v[52:55]\n"
  "v_mfma_f32_16x16x32_bf16 v[48:51], v[172:175], v[144:147], v[48:51]\n"
  "s_waitcnt lgkmcnt(7)\n"
  "v_mfma_f32_16x16x32_bf16 v[0:3], v[160:163], v[128:131], v[0:3]\n"
  "v_add_u32 v179, 0xffff0000, v179\n"
  "s_waitcnt lgkmcnt(6)\n"
  "v_mfma_f32_16x16x32_bf16 v[4:7], v[160:163], v[132:135], v[4:7]\n"
  "ds_read_b64_tr_b16 v[172:173], v179 offset:0\n"
  "ds_read_b64_tr_b16 v[174:175], v179 offset:8192\n"
  "s_waitcnt vmcnt(9)\n"
  "s_waitcnt lgkmcnt(5)\n"
  "v_mfma_f32_16x16x32_bf16 v[8:11], v[160:163], v[136:139], v[8:11]\n"
  "s_barrier\n"
  "s_waitcnt lgkmcnt(4)\n"
  "v_mfma_f32_16x16x32_bf16 v[12:15], v[160:163], v[140:143], v[12:15]\n"
  "ds_read_b128 v[144:147], v180 offset:16384\n"
  "v_mfma_f32_16x16x32_bf16 v[28:31], v[164:167], v[140:143], v[28:31]\n"
  "ds_read_b64_tr_b16 v[160:161], v176 offset:16384\n"
  "ds_read_b64_tr_b16 v[162:163], v176 offset:24576\n"
  "v_mfma_f32_16x16x32_bf16 v[24:27], v[164:167], v[136:139], v[24:27]\n"
  "v_mfma_f32_16x16x32_bf16 v[20:23], v[164:167], v[132:135], v[20:23]\n"
  "ds_read_b128 v[148:151], v180 offset:17408\n"
  "v_mfma_f32_16x16x32_bf16 v[16:19], v[164:167], v[128:131], v[16:19]\n"
  "s_waitcnt lgkmcnt(6)\n"
  "v_mfma_f32_16x16x32_bf16 v[32:35], v[168:171], v[128:131], v[32:35]\n"
  "ds_read_b64_tr_b16 v[164:165], v177 offset:16384\n"
  "ds_read_b64_tr_b16 v[166:167], v177 offset:24576\n"
  "v_mfma_f32_16x16x32_bf16 v[36:39], v[168:171], v[132:135], v[36:39]\n"
  "ds_read_b128 v[152:155], v180 offset:20480\n"
  "v_mfma_f32_16x16x32_bf16 v[40:43], v[168:171], v[136:139], v[40:43]\n"
  "v_mfma_f32_16x16x32_bf16 v[44:47], v[168:171], v[140:143], v[44:47]\n"
  "s_waitcnt lgkmcnt(7)\n"
  "v_mfma_f32_16x16x32_bf16 v[60:63], v[172:175], v[140:143], v[60:63]\n"
  "ds_read_b64_tr_b16 v[168:169], v178 offset:16384\n"
  "ds_read_b64_tr_b16 v[170:171], v178 offset:24576\n"
  "v_mfma_f32_16x16x32_bf16 v[56:59], v[172:175], v[136:139], v[56:59]\n"
  "ds_read_b128 v[156:159], v180 offset:21504\n"
  "v_mfma_f32_16x16x32_bf16 v[52:55], v[172:175], v[132:135], v[52:55]\n"
  "v_mfma_f32_16x16x32_bf16 v[48:51], v[172:175], v[128:131], v[48:51]\n"
  "s_waitcnt lgkmcnt(7)\n"
  "v_mfma_f32_16x16x32_bf16 v[0:3], v[160:163], v[144:147], v[0:3]\n"
  "ds_read_b64_tr_b16 v[172:173], v179 offset:16384\n"
  "ds_read_b64_tr_b16 v[174:175], v179 offset:24576\n"
  "s_waitcnt lgkmcnt(8)\n"
  "v_mfma_f32_16x16x32_bf16 v[4:7], v[160:163], v[148:151], v[4:7]\n"
  "s_waitcnt vmcnt(5)\n"
  "s_barrier\n"
  "s_waitcnt lgkmcnt(5)\n"
  "v_mfma_f32_16x16x32_bf16 v[8:11], v[160:163], v[152:155], v[8:11]\n"
  "s_waitcnt lgkmcnt(2)\n"
  "v_mfma_f32_16x16x32_bf16 v[12:15], v[160:163], v[156:159], v[12:15]\n"
  "ds_read_b128 v[128:131], v180 offset:32768\n"
  "v_mfma_f32_16x16x32_bf16 v[28:31], v[164:167], v[156:159], v[28:31]\n"
  "ds_read_b64_tr_b16 v[160:161], v176 offset:32768\n"
  "ds_read_b64_tr_b16 v[162:163], v176 offset:40960\n"
  "v_mfma_f32_16x16x32_bf16 v[24:27], v[164:167], v[152:155], v[24:27]\n"
  "v_mfma_f32_16x16x32_bf16 v[20:23], v[164:167], v[148:151], v[20:23]\n"
  "ds_read_b128 v[132:135], v180 offset:33792\n"
  "v_mfma_f32_16x16x32_bf16 v[16:19], v[164:167], v[144:147], v[16:19]\n"
  "v_mfma_f32_16x16x32_bf16 v[32:35], v[168:171], v[144:147], v[32:35]\n"
  "ds_read_b64_tr_b16 v[164:165], v177 offset:32768\n"
  "ds_read_b64_tr_b16 v[166:167], v177 offset:40960\n"
  "v_mfma_f32_16x16x32_bf16 v[36:39], v[168:171], v[148:151], v[36:39]\n"
  "ds_read_b128 v[136:139], v180 offset:36864\n"
  "v_mfma_f32_16x16x32_bf16 v[40:43], v[168:171], v[152:155], v[40:43]\n"
  "v_mfma_f32_16x16x32_bf16 v[44:47], v[168:171], v[156:159], v[44:47]\n"
  "s_waitcnt lgkmcnt(7)\n"
  "v_mfma_f32_16x16x32_bf16 v[60:63], v[172:175], v[156:159], v[60:63]\n"
  "ds_read_b64_tr_b16 v[168:169], v178 offset:32768\n"
  "ds_read_b64_tr_b16 v[170:171], v178 offset:40960\n"
  "v_mfma_f32_16x16x32_bf16 v[56:59], v[172:175], v[152:155], v[56:59]\n"
  "ds_read_b128 v[140:143], v180 offset:37888\n"
  "v_mfma_f32_16x16x32_bf16 v[52:55], v[172:175], v[148:151], v[52:55]\n"
  "v_mfma_f32_16x16x32_bf16 v[48:51], v[172:175], v[144:147], v[48:51]\n"
  "s_waitcnt lgkmcnt(7)\n"
  "v_mfma_f32_16x16x32_bf16 v[0:3], v[160:163], v[128:131], v[0:3]\n"
  "ds_read_b64_tr_b16 v[172:173], v179 offset:32768\n"
  "ds_read_b64_tr_b16 v[174:175], v179 offset:40960\n"
  "s_waitcnt lgkmcnt(8)\n"
  "v_mfma_f32_16x16x32_bf16 v[4:7], v[160:163], v[132:135], v[4:7]\n"
  "s_waitcnt lgkmcnt(5)\n"
  "v_mfma_f32_16x16x32_bf16 v[8:11], v[160:163], v[136:139], v[8:11]\n"
  "s_waitcnt lgkmcnt(2)\n"
  "v_mfma_f32_16x16x32_bf16 v[12:15], v[160:163], v[140:143], v[12:15]\n"
  "v_mfma_f32_16x16x32_bf16 v[28:31], v[164:167], v[140:143], v[28:31]\n"
  "v_mfma_f32_16x16x32_bf16 v[24:27], v[164:167], v[136:139], v[24:27]\n"
  "v_mfma_f32_16x16x32_bf16 v[20:23], v[164:167], v[132:135], v[20:23]\n"
  "v_mfma_f32_16x16x32_bf16 v[16:19], v[164:167], v[128:131], v[16:19]\n"
  "v_mfma_f32_16x16x32_bf16 v[32:35], v[168:171], v[128:131], v[32:35]\n"
  "v_mfma_f32_16x16x32_bf16 v[36:39], v[168:171], v[132:135], v[36:39]\n"
  "v_mfma_f32_16x16x32_bf16 v[40:43], v[168:171], v[136:139], v[40:43]\n"
  "v_mfma_f32_16x16x32_bf16 v[44:47], v[168:171], v[140:143], v[44:47]\n"
  "s_waitcnt lgkmcnt(0)\n"
  "v_mfma_f32_16x16x32_bf16 v[60:63], v[172:175], v[140:143], v[60:63]\n"
  "v_mfma_f32_16x16x32_bf16 v[56:59], v[172:175], v[136:139], v[56:59]\n"
  "v_mfma_f32_16x16x32_bf16 v[52:55], v[172:175], v[132:135], v[52:55]\n"
  "v_mfma_f32_16x16x32_bf16 v[48:51], v[172:175], v[128:131], v[48:51]\n"
  "s_waitcnt vmcnt(0)\n"
  "v_cmp_ne_u64 s[96:97], s[16:17], v[190:191]\n"
  "s_nop 0\n"
  "s_cmp_lg_u64 s[96:97], 0\n"
  "s_cbranch_scc1 L_ib_retry_%=\n"
  "L_ib_ok_%=:\n"
  "s_cmp_eq_u32 s35, s56\n"
  "s_cbranch_scc1 L_own_%=\n"
  "v_cvt_pk_bf16_f32 v128, v0, v1\n"
  "v_cvt_pk_bf16_f32 v129, v2, v3\n"
  "v_cvt_pk_bf16_f32 v130, v16, v17\n"
  "v_cvt_pk_bf16_f32 v131, v18, v19\n"
  "global_store_dwordx4 v232, v[128:131], s[24:25] offset:-4096 sc1\n"
  "v_cvt_pk_bf16_f32 v132, v32, v33\n"
  "v_cvt_pk_bf16_f32 v133, v34, v35\n"
  "v_cvt_pk_bf16_f32 v134, v48, v49\n"
  "v_cvt_pk_bf16_f32 v135, v50, v51\n"
  "global_store_dwordx4 v232, v[132:135], s[24:25] offset:-3072 sc1\n"
  "v_cvt_pk_bf16_f32 v136, v4, v5\n"
  "v_cvt_pk_bf16_f32 v137, v6, v7\n"
  "v_cvt_pk_bf16_f32 v138, v20, v21\n"
  "v_cvt_pk_bf16_f32 v139, v22, v23\n"
  "global_store_dwordx4 v232, v[136:139], s[24:25] offset:-2048 sc1\n"
  "v_cvt_pk_bf16_f32 v140, v36, v37\n"
  "v_cvt_pk_bf16_f32 v141, v38, v39\n"
  "v_cvt_pk_bf16_f32 v142, v52, v53\n"
  "v_cvt_pk_bf16_f32 v143, v54, v55\n"
  "global_store_dwordx4 v232, v[140:143], s[24:25] offset:-1024 sc1\n"
  "v_cvt_pk_bf16_f32 v128, v8, v9\n"
  "v_cvt_pk_bf16_f32 v129, v10, v11\n"
  "v_cvt_pk_bf16_f32 v130, v24, v25\n"
  "v_cvt_pk_bf16_f32 v131, v26, v27\n"
  "global_store_dwordx4 v232, v[128:131], s[24:25] offset:0 sc1\n"
  "v_cvt_pk_bf16_f32 v132, v40, v41\n"
  "v_cvt_pk_bf16_f32 v133, v42, v43\n"
  "v_cvt_pk_bf16_f32 v134, v56, v57\n"
  "v_cvt_pk_bf16_f32 v135, v58, v59\n"
  "global_store_dwordx4 v232, v[132:135], s[24:25] offset:1024 sc1\n"
  "v_cvt_pk_bf16_f32 v136, v12, v13\n"
  "v_cvt_pk_bf16_f32 v137, v14, v15\n"
  "v_cvt_pk_bf16_f32 v138, v28, v29\n"
  "v_cvt_pk_bf16_f32 v139, v30, v31\n"
  "global_store_dwordx4 v232, v[136:139], s[24:25] offset:2048 sc1\n"
  "v_cvt_pk_bf16_f32 v140, v44, v45\n"
  "v_cvt_pk_bf16_f32 v141, v46, v47\n"
  "v_cvt_pk_bf16_f32 v142, v60, v61\n"
  "v_cvt_pk_bf16_f32 v143, v62, v63\n"
  "global_store_dwordx4 v232, v[140:143], s[24:25] offset:3072 sc1\n"
  "s_branch L_pub2_%=\n"
  "L_own_%=:\n"
  "ds_write_b128 v181, v[0:3] offset:0\n"
  "ds_write_b128 v181, v[16:19] offset:1024\n"
  "ds_write_b128 v181, v[32:35] offset:2048\n"
  "ds_write_b128 v181, v[48:51] offset:3072\n"
  "ds_write_b128 v181, v[4:7] offset:4096\n"
  "ds_write_b128 v181, v[20:23] offset:5120\n"
  "ds_write_b128 v181, v[36:39] offset:6144\n"
  "ds_write_b128 v181, v[52:55] offset:7168\n"
  "ds_write_b128 v181, v[8:11] offset:8192\n"
  "ds_write_b128 v181, v[24:27] offset:9216\n"
  "ds_write_b128 v181, v[40:43] offset:10240\n"
  "ds_write_b128 v181, v[56:59] offset:11264\n"
  "ds_write_b128 v181, v[12:15] offset:12288\n"
  "ds_write_b128 v181, v[28:31] offset:13312\n"
  "ds_write_b128 v181, v[44:47] offset:14336\n"
  "ds_write_b128 v181, v[60:63] offset:15360\n"
  "L_pub2_%=:\n"
  "v_lshlrev_b32 v80, 16, v240\n"
  "v_and_b32 v81, 0xffff0000, v240\n"
  "v_lshlrev_b32 v82, 16, v241\n"
  "v_and_b32 v83, 0xffff0000, v241\n"
  "v_lshlrev_b32 v84, 16, v242\n"
  "v_and_b32 v85, 0xffff0000, v242\n"
  "v_lshlrev_b32 v86, 16, v243\n"
  "v_and_b32 v87, 0xffff0000, v243\n"
  "v_lshlrev_b32 v88, 16, v244\n"
  "v_and_b32 v89, 0xffff0000, v244\n"
  "v_lshlrev_b32 v90, 16, v245\n"
  "v_and_b32 v91, 0xffff0000, v245\n"
  "v_lshlrev_b32 v92, 16, v246\n"
  "v_and_b32 v93, 0xffff0000, v246\n"
  "v_lshlrev_b32 v94, 16, v247\n"
  "v_and_b32 v95, 0xffff0000, v247\n"
  "global_load_dwordx4 v[96:99], v232, s[26:27] offset:0 sc1\n"
  "global_load_dwordx4 v[100:103], v232, s[26:27] offset:1024 sc1\n"
  "global_load_dwordx4 v[104:107], v232, s[40:41] offset:0 sc1\n"
  "global_load_dwordx4 v[108:111], v232, s[40:41] offset:1024 sc1\n"
  "global_load_dwordx4 v[112:115], v232, s[42:43] offset:0 sc1\n"
  "global_load_dwordx4 v[116:119], v232, s[42:43] offset:1024 sc1\n"
  "s_waitcnt lgkmcnt(0)\n"
  "s_barrier\n"
  "ds_read_b128 v[64:67], v182 offset:0\n"
  "ds_read_b128 v[68:71], v182 offset:1024\n"
  "ds_read_b128 v[72:75], v182 offset:2048\n"
  "ds_read_b128 v[76:79], v182 offset:3072\n"
  "s_waitcnt lgkmcnt(3)\n"
  "v_pk_add_f32 v[80:81], v[80:81], v[64:65]\n"
  "v_pk_add_f32 v[82:83], v[82:83], v[66:67]\n"
  "s_waitcnt lgkmcnt(2)\n"
  "v_pk_add_f32 v[84:85], v[84:85], v[68:69]\n"
  "v_pk_add_f32 v[86:87], v[86:87], v[70:71]\n"
  "s_waitcnt lgkmcnt(1)\n"
  "v_pk_add_f32 v[88:89], v[88:89], v[72:73]\n"
  "v_pk_add_f32 v[90:91], v[90:91], v[74:75]\n"
  "s_waitcnt lgkmcnt(0)\n"
  "v_pk_add_f32 v[92:93], v[92:93], v[76:77]\n"
  "v_pk_add_f32 v[94:95], v[94:95], v[78:79]\n"
  "s_waitcnt vmcnt(5)\n"
  "v_max3_u32 v234, v96, v97, v98\n"
  "s_waitcnt vmcnt(4)\n"
  "v_max3_u32 v234, v234, v99, v100\n"
  "v_max3_u32 v234, v234, v101, v102\n"
  "s_waitcnt vmcnt(3)\n"
  "v_max3_u32 v234, v234, v103, v104\n"
  "v_max3_u32 v234, v234, v105, v106\n"
  "s_waitcnt vmcnt(2)\n"
  "v_max3_u32 v234, v234, v107, v108\n"
  "v_max3_u32 v234, v234, v109, v110\n"
  "s_waitcnt vmcnt(1)\n"
  "v_max3_u32 v234, v234, v111, v112\n"
  "v_max3_u32 v234, v234, v113, v114\n"
  "s_waitcnt vmcnt(0)\n"
  "v_max3_u32 v234, v234, v115, v116\n"
  "v_max3_u32 v234, v234, v117, v118\n"
  "v_max_u32 v234, v234, v119\n"
  "v_cmp_eq_u32 s[96:97], -1, v234\n"
  "s_nop 0\n"
  "s_cmp_lg_u64 s[96:97], 0\n"
  "s_cbranch_scc1 L_p_retry_%=\n"
  "L_p_ok_%=:\n"
  "v_dot2c_f32_bf16 v80, s44, v96\n"
  "v_dot2c_f32_bf16 v81, s45, v96\n"
  "v_dot2c_f32_bf16 v82, s44, v97\n"
  "v_dot2c_f32_bf16 v83, s45, v97\n"
  "v_dot2c_f32_bf16 v84, s44, v98\n"
  "v_dot2c_f32_bf16 v85, s45, v98\n"
  "v_dot2c_f32_bf16 v86, s44, v99\n"
  "v_dot2c_f32_bf16 v87, s45, v99\n"
  "v_dot2c_f32_bf16 v88, s44, v100\n"
  "v_dot2c_f32_bf16 v89, s45, v100\n"
  "v_dot2c_f32_bf16 v90, s44, v101\n"
  "v_dot2c_f32_bf16 v91, s45, v101\n"
  "v_dot2c_f32_bf16 v92, s44, v102\n"
  "v_dot2c_f32_bf16 v93, s45, v102\n"
  "v_dot2c_f32_bf16 v94, s44, v103\n"
  "v_dot2c_f32_bf16 v95, s45, v103\n"
  "v_dot2c_f32_bf16 v80, s44, v104\n"
  "v_dot2c_f32_bf16 v81, s45, v104\n"
  "v_dot2c_f32_bf16 v82, s44, v105\n"
  "v_dot2c_f32_bf16 v83, s45, v105\n"
  "v_dot2c_f32_bf16 v84, s44, v106\n"
  "v_dot2c_f32_bf16 v85, s45, v106\n"
  "v_dot2c_f32_bf16 v86, s44, v107\n"
  "v_dot2c_f32_bf16 v87, s45, v107\n"
  "v_dot2c_f32_bf16 v88, s44, v108\n"
  "v_dot2c_f32_bf16 v89, s45, v108\n"
  "v_dot2c_f32_bf16 v90, s44, v109\n"
  "v_dot2c_f32_bf16 v91, s45, v109\n"
  "v_dot2c_f32_bf16 v92, s44, v110\n"
  "v_dot2c_f32_bf16 v93, s45, v110\n"
  "v_dot2c_f32_bf16 v94, s44, v111\n"
  "v_dot2c_f32_bf16 v95, s45, v111\n"
  "v_dot2c_f32_bf16 v80, s44, v112\n"
  "v_dot2c_f32_bf16 v81, s45, v112\n"
  "v_dot2c_f32_bf16 v82, s44, v113\n"
  "v_dot2c_f32_bf16 v83, s45, v113\n"
  "v_dot2c_f32_bf16 v84, s44, v114\n"
  "v_dot2c_f32_bf16 v85, s45, v114\n"
  "v_dot2c_f32_bf16 v86, s44, v115\n"
  "v_dot2c_f32_bf16 v87, s45, v115\n"
  "v_dot2c_f32_bf16 v88, s44, v116\n"
  "v_dot2c_f32_bf16 v89, s45, v116\n"
  "v_dot2c_f32_bf16 v90, s44, v117\n"
  "v_dot2c_f32_bf16 v91, s45, v117\n"
  "v_dot2c_f32_bf16 v92, s44, v118\n"
  "v_dot2c_f32_bf16 v93, s45, v118\n"
  "v_dot2c_f32_bf16 v94, s44, v119\n"
  "v_dot2c_f32_bf16 v95, s45, v119\n"
  "s_nop 2\n"
  "v_cvt_pk_bf16_f32 v64, v80, v81\n"
  "v_cvt_pk_bf16_f32 v65, v82, v83\n"
  "v_cvt_pk_bf16_f32 v66, v84, v85\n"
  "v_cvt_pk_bf16_f32 v67, v86, v87\n"
  "v_cvt_pk_bf16_f32 v68, v88, v89\n"
  "v_cvt_pk_bf16_f32 v69, v90, v91\n"
  "v_cvt_pk_bf16_f32 v70, v92, v93\n"
  "v_cvt_pk_bf16_f32 v71, v94, v95\n"
  "v_permlane16_swap_b32 v64, v66\n"
  "v_permlane16_swap_b32 v65, v67\n"
  "global_store_dwordx4 v186, v[64:67], s[30:31] offset:0 sc1\n"
  "v_permlane16_swap_b32 v68, v70\n"
  "v_permlane16_swap_b32 v69, v71\n"
  "global_store_dwordx4 v186, v[68:71], s[30:31] offset:64 sc1\n"
  "s_or_b32 s36, s86, s35\n"
  "s_cmp_lg_u32 s36, 0\n"
  "s_cbranch_scc1 L_end_%=\n"
  "v_lshlrev_b32 v0, 9, v233\n"
  "s_mov_b32 s98, 0x100000\n"
  "v_add_u32 v1, 32768, v0\n"
  "v_add_u32 v2, 65536, v0\n"
  "v_add_u32 v3, 98304, v0\n"
  "L_ep_%=:\n"
  "global_load_dwordx2 v[4:5], v0, s[62:63] offset:0 sc1\n"
  "global_load_dwordx2 v[6:7], v1, s[62:63] offset:0 sc1\n"
  "global_load_dwordx2 v[8:9], v2, s[62:63] offset:0 sc1\n"
  "global_load_dwordx2 v[10:11], v3, s[62:63] offset:0 sc1\n"
  "s_waitcnt vmcnt(3)\n"
  "v_cmp_ne_u64 s[60:61], s[16:17], v[4:5]\n"
  "s_waitcnt vmcnt(2)\n"
  "v_cmp_ne_u64 vcc, s[16:17], v[6:7]\n"
  "s_nop 0\n"
  "s_or_b64 s[60:61], s[60:61], vcc\n"
  "s_waitcnt vmcnt(1)\n"
  "v_cmp_ne_u64 vcc, s[16:17], v[8:9]\n"
  "s_nop 0\n"
  "s_or_b64 s[60:61], s[60:61], vcc\n"
  "s_waitcnt vmcnt(0)\n"
  "v_cmp_ne_u64 vcc, s[16:17], v[10:11]\n"
  "s_nop 0\n"
  "s_or_b64 s[60:61], s[60:61], vcc\n"
  "s_cbranch_scc0 L_epok_%=\n"
  "s_sleep 4\n"
  "s_sub_u32 s98, s98, 1\n"
  "s_cmp_lg_u32 s98, 0\n"
  "s_cbranch_scc1 L_ep_%=\n"
  "L_epok_%=:\n"
  "v_mov_b32 v0, 131072\n"
  "v_mov_b32 v2, s16\n"
  "v_mov_b32 v3, s17\n"
  "s_mov_b64 exec, 1\n"
  "global_store_dwordx2 v0, v[2:3], s[62:63] sc1\n"
  "s_mov_b64 exec, -1\n"
  "L_end_%=:\n"
  "s_branch L_last_%=\n"
  "L_xn_retry_%=:\n"
  "s_mov_b32 s98, 0x100000\n"
  "L_xn_loop_%=:\n"
  "s_sleep 2\n"
  "global_load_dwordx2 v[234:235], v219, s[62:63] offset:0 sc1\n"
  "s_waitcnt vmcnt(0)\n"
  "v_cmp_ne_u64 s[96:97], s[16:17], v[234:235]\n"
  "s_nop 0\n"
  "s_cmp_eq_u64 s[96:97], 0\n"
  "s_cbranch_scc1 L_xn_ok_%=\n"
  "s_sub_u32 s98, s98, 1\n"
  "s_cmp_lg_u32 s98, 0\n"
  "s_cbranch_scc1 L_xn_loop_%=\n"
  "s_branch L_xn_ok_%=\n"
  "L_kv_retry_%=:\n"
  "s_mov_b32 s98, 0x100000\n"
  "L_kv_loop_%=:\n"
  "s_sleep 2\n"
  "global_load_dwordx2 v[234:235], v219, s[62:63] offset:64 sc1\n"
  "s_waitcnt vmcnt(0)\n"
  "v_cmp_ne_u64 s[96:97], s[16:17], v[234:235]\n"
  "s_nop 0\n"
  "s_cmp_eq_u64 s[96:97], 0\n"
  "s_cbranch_scc1 L_kv_ok_%=\n"
  "s_sub_u32 s98, s98, 1\n"
  "s_cmp_lg_u32 s98, 0\n"
  "s_cbranch_scc1 L_kv_loop_%=\n"
  "s_branch L_kv_ok_%=\n"
  "L_o_retry_%=:\n"
  "s_mov_b32 s98, 0x100000\n"
  "L_o_loop_%=:\n"
  "s_sleep 2\n"
  "global_load_dwordx2 v[190:191], v189, s[62:63] offset:192 sc1\n"
  "s_waitcnt vmcnt(0)\n"
  "v_cmp_ne_u64 s[96:97], s[16:17], v[190:191]\n"
  "s_nop 0\n"
  "s_cmp_eq_u64 s[96:97], 0\n"
  "s_cbranch_scc1 L_o_ok_%=\n"
  "s_sub_u32 s98, s98, 1\n"
  "s_cmp_lg_u32 s98, 0\n"
  "s_cbranch_scc1 L_o_loop_%=\n"
  "s_branch L_o_ok_%=\n"
  "L_ib_retry_%=:\n"
  "s_mov_b32 s98, 0x100000\n"
  "L_ib_loop_%=:\n"
  "s_sleep 2\n"
  "global_load_dwordx2 v[190:191], v189, s[62:63] offset:192 sc1\n"
  "s_waitcnt vmcnt(0)\n"
  "v_cmp_ne_u64 s[96:97], s[16:17], v[190:191]\n"
  "s_nop 0\n"
  "s_cmp_eq_u64 s[96:97], 0\n"
  "s_cbranch_scc1 L_ib_ok_%=\n"
  "s_sub_u32 s98, s98, 1\n"
  "s_cmp_lg_u32 s98, 0\n"
  "s_cbranch_scc1 L_ib_loop_%=\n"
  "s_branch L_ib_ok_%=\n"
  "L_p_retry_%=:\n"
  "s_mov_b32 s98, 0x100000\n"
  "L_p_loop_%=:\n"
  "global_load_dwordx4 v[96:99], v232, s[26:27] offset:0 sc1\n"
  "global_load_dwordx4 v[100:103], v232, s[26:27] offset:1024 sc1\n"
  "global_load_dwordx4 v[104:107], v232, s[40:41] offset:0 sc1\n"
  "global_load_dwordx4 v[108:111], v232, s[40:41] offset:1024 sc1\n"
  "global_load_dwordx4 v[112:115], v232, s[42:43] offset:0 sc1\n"
  "global_load_dwordx4 v[116:119], v232, s[42:43] offset:1024 sc1\n"
  "s_waitcnt vmcnt(5)\n"
  "v_max3_u32 v234, v96, v97, v98\n"
  "s_waitcnt vmcnt(4)\n"
  "v_max3_u32 v234, v234, v99, v100\n"
  "v_max3_u32 v234, v234, v101, v102\n"
  "s_waitcnt vmcnt(3)\n"
  "v_max3_u32 v234, v234, v103, v104\n"
  "v_max3_u32 v234, v234, v105, v106\n"
  "s_waitcnt vmcnt(2)\n"
  "v_max3_u32 v234, v234, v107, v108\n"
  "v_max3_u32 v234, v234, v109, v110\n"
  "s_waitcnt vmcnt(1)\n"
  "v_max3_u32 v234, v234, v111, v112\n"
  "v_max3_u32 v234, v234, v113, v114\n"
  "s_waitcnt vmcnt(0)\n"
  "v_max3_u32 v234, v234, v115, v116\n"
  "v_max3_u32 v234, v234, v117, v118\n"
  "v_max_u32 v234, v234, v119\n"
  "v_cmp_eq_u32 s[96:97], -1, v234\n"
  "s_nop 0\n"
  "s_cmp_lg_u64 s[96:97], 0\n"
  "s_cbranch_scc0 L_p_ok_%=\n"
  "s_sub_u32 s98, s98, 1\n"
  "s_cmp_lg_u32 s98, 0\n"
  "s_cbranch_scc1 L_p_loop_%=\n"
  "s_branch L_p_ok_%=\n"
  "L_hAFF_retry_%=:\n"
  "s_mov_b32 s98, 0x100000\n"
  "L_hAFF_loop_%=:\n"
  "s_sleep 2\n"
  "global_load_dwordx2 v[234:235], v253, s[62:63] offset:128 sc1\n"
  "s_waitcnt vmcnt(0)\n"
  "v_cmp_ne_u64 s[96:97], s[16:17], v[234:235]\n"
  "s_nop 0\n"
  "s_cmp_eq_u64 s[96:97], 0\n"
  "s_cbranch_scc1 L_hAFF_ok_%=\n"
  "s_sub_u32 s98, s98, 1\n"
  "s_cmp_lg_u32 s98, 0\n"
  "s_cbranch_scc1 L_hAFF_loop_%=\n"
  "s_branch L_hAFF_ok_%=\n"
  "L_hAFD_retry_%=:\n"
  "s_mov_b32 s98, 0x100000\n"
  "L_hAFD_loop_%=:\n"
  "s_sleep 2\n"
  "global_load_dwordx2 v[234:235], v253, s[62:63] offset:128 sc1\n"
  "s_waitcnt vmcnt(0)\n"
  "v_cmp_ne_u64 s[96:97], s[16:17], v[234:235]\n"
  "s_nop 0\n"
  "s_cmp_eq_u64 s[96:97], 0\n"
  "s_cbranch_scc1 L_hAFD_ok_%=\n"
  "s_sub_u32 s98, s98, 1\n"
  "s_cmp_lg_u32 s98, 0\n"
  "s_cbranch_scc1 L_hAFD_loop_%=\n"
  "s_branch L_hAFD_ok_%=\n"
  "L_hADX_retry_%=:\n"
  "s_mov_b32 s98, 0x100000\n"
  "L_hADX_loop_%=:\n"
  "s_sleep 2\n"
  "global_load_dwordx2 v[234:235], v253, s[62:63] offset:128 sc1\n"
  "s_waitcnt vmcnt(0)\n"
  "v_cmp_ne_u64 s[96:97], s[16:17], v[234:235]\n"
  "s_nop 0\n"
  "s_cmp_eq_u64 s[96:97], 0\n"
  "s_cbranch_scc1 L_hADX_ok_%=\n"
  "s_sub_u32 s98, s98, 1\n"
  "s_cmp_lg_u32 s98, 0\n"
  "s_cbranch_scc1 L_hADX_loop_%=\n"
  "s_branch L_hADX_ok_%=\n"
  "L_hIA_retry_%=:\n"
  "s_mov_b32 s98, 0x100000\n"
  "L_hIA_loop_%=:\n"
  "s_sleep 2\n"
  "global_load_dwordx2 v[234:235], v253, s[62:63] offset:128 sc1\n"
  "s_waitcnt vmcnt(0)\n"
  "v_cmp_ne_u64 s[96:97], s[16:17], v[234:235]\n"
  "s_nop 0\n"
  "s_cmp_eq_u64 s[96:97], 0\n"
  "s_cbranch_scc1 L_hIA_ok_%=\n"
  "s_sub_u32 s98, s98, 1\n"
  "s_cmp_lg_u32 s98, 0\n"
  "s_cbranch_scc1 L_hIA_loop_%=\n"
  "s_branch L_hIA_ok_%=\n"
  "L_slow0_%=:\n"
  "s_add_u32 s36, s54, 0\n"
  "s_lshr_b32 s59, s36, 1\n"
  "s_lshl_b32 s36, s36, 4\n"
  "v_and_b32 v239, 15, v233\n"
  "v_add_u32 v239, s36, v239\n"
  "v_lshrrev_b32 v240, 4, v233\n"
  "v_lshlrev_b32 v240, 2, v240\n"
  "v_sub_u32 v239, v239, v240\n"
  "s_lshl_b32 s36, s39, 20\n"
  "s_add_u32 s36, s36, 2101248\n"
  "s_add_u32 s60, s84, s36\n"
  "s_addc_u32 s61, s85, 0\n"
  "v_add_u32 v240, 0x2000, v232\n"
  "v_mov_b32 v236, 0xf149f2ca\n"
  "v_mov_b32 v0, 0\n"
  "v_mov_b32 v1, 0\n"
  "v_mov_b32 v2, 0\n"
  "v_mov_b32 v3, 0\n"
  "v_mov_b32 v4, 0\n"
  "v_mov_b32 v5, 0\n"
  "v_mov_b32 v6, 0\n"
  "v_mov_b32 v7, 0\n"
  "v_mov_b32 v8, 0\n"
  "v_mov_b32 v9, 0\n"
  "v_mov_b32 v10, 0\n"
  "v_mov_b32 v11, 0\n"
  "v_mov_b32 v12, 0\n"
  "v_mov_b32 v13, 0\n"
  "v_mov_b32 v14, 0\n"
  "v_mov_b32 v15, 0\n"
  "v_mov_b32 v16, 0\n"
  "v_mov_b32 v17, 0\n"
  "v_mov_b32 v18, 0\n"
  "v_mov_b32 v19, 0\n"
  "v_mov_b32 v20, 0\n"
  "v_mov_b32 v21, 0\n"
  "v_mov_b32 v22, 0\n"
  "v_mov_b32 v23, 0\n"
  "v_mov_b32 v24, 0\n"
  "v_mov_b32 v25, 0\n"
  "v_mov_b32 v26, 0\n"
  "v_mov_b32 v27, 0\n"
  "v_mov_b32 v28, 0\n"
  "v_mov_b32 v29, 0\n"
  "v_mov_b32 v30, 0\n"
  "v_mov_b32 v31, 0\n"
  "v_mov_b32 v200, 0\n"
  "v_mov_b32 v201, 0\n"
  "v_mov_b32 v202, 0\n"
  "v_mov_b32 v203, 0\n"
  "L_slow0_loop_%=:\n"
  "global_load_dwordx4 a[128:131], v232, s[60:61] offset:-4096 sc1\n"
  "global_load_dwordx4 a[132:135], v232, s[60:61] offset:-3072 sc1\n"
  "global_load_dwordx4 a[136:139], v232, s[60:61] offset:-2048 sc1\n"
  "global_load_dwordx4 a[140:143], v232, s[60:61] offset:-1024 sc1\n"
  "global_load_dwordx4 a[144:147], v232, s[60:61] offset:0 sc1\n"
  "global_load_dwordx4 a[148:151], v232, s[60:61] offset:1024 sc1\n"
  "global_load_dwordx4 a[152:155], v232, s[60:61] offset:2048 sc1\n"
  "global_load_dwordx4 a[156:159], v232, s[60:61] offset:3072 sc1\n"
  "global_load_dwordx4 a[64:67], v240, s[60:61] offset:-4096 sc1\n"
  "global_load_dwordx4 a[68:71], v240, s[60:61] offset:-3072 sc1\n"
  "global_load_dwordx4 a[72:75], v240, s[60:61] offset:-2048 sc1\n"
  "global_load_dwordx4 a[76:79], v240, s[60:61] offset:-1024 sc1\n"
  "global_load_dwordx4 a[80:83], v240, s[60:61] offset:0 sc1\n"
  "global_load_dwordx4 a[84:87], v240, s[60:61] offset:1024 sc1\n"
  "global_load_dwordx4 a[88:91], v240, s[60:61] offset:2048 sc1\n"
  "global_load_dwordx4 a[92:95], v240, s[60:61] offset:3072 sc1\n"
  "s_waitcnt vmcnt(15)\n"
  "v_mfma_f32_16x16x32_bf16 v[128:131], a[128:131], a[0:3], 0\n"
  "s_waitcnt vmcnt(14)\n"
  "v_mfma_f32_16x16x32_bf16 v[128:131], a[132:135], a[4:7], v[128:131]\n"
  "s_waitcnt vmcnt(13)\n"
  "v_mfma_f32_16x16x32_bf16 v[128:131], a[136:139], a[8:11], v[128:131]\n"
  "s_waitcnt vmcnt(12)\n"
  "v_mfma_f32_16x16x32_bf16 v[128:131], a[140:143], a[12:15], v[128:131]\n"
  "s_waitcnt vmcnt(11)\n"
  "v_mfma_f32_16x16x32_bf16 v[132:135], a[144:147], a[0:3], 0\n"
  "s_waitcnt vmcnt(10)\n"
  "v_mfma_f32_16x16x32_bf16 v[132:135], a[148:151], a[4:7], v[132:135]\n"
  "s_waitcnt vmcnt(9)\n"
  "v_mfma_f32_16x16x32_bf16 v[132:135], a[152:155], a[8:11], v[132:135]\n"
  "s_waitcnt vmcnt(8)\n"
  "v_mfma_f32_16x16x32_bf16 v[132:135], a[156:159], a[12:15], v[132:135]\n"
  "v_cmp_gt_i32 vcc, 0, v239\n"
  "s_nop 1\n"
  "v_cndmask_b32 v128, v128, v218, vcc\n"
  "v_cmp_gt_i32 vcc, 1, v239\n"
  "s_nop 1\n"
  "v_cndmask_b32 v129, v129, v218, vcc\n"
  "v_cmp_gt_i32 vcc, 2, v239\n"
  "s_nop 1\n"
  "v_cndmask_b32 v130, v130, v218, vcc\n"
  "v_cmp_gt_i32 vcc, 3, v239\n"
  "s_nop 1\n"
  "v_cndmask_b32 v131, v131, v218, vcc\n"
  "v_cmp_gt_i32 vcc, 16, v239\n"
  "s_nop 1\n"
  "v_cndmask_b32 v132, v132, v218, vcc\n"
  "v_cmp_gt_i32 vcc, 17, v239\n"
  "s_nop 1\n"
  "v_cndmask_b32 v133, v133, v218, vcc\n"
  "v_cmp_gt_i32 vcc, 18, v239\n"
  "s_nop 1\n"
  "v_cndmask_b32 v134, v134, v218, vcc\n"
  "v_cmp_gt_i32 vcc, 19, v239\n"
  "s_nop 1\n"
  "v_cndmask_b32 v135, v135, v218, vcc\n"
  "v_max3_f32 v237, v128, v129, v236\n"
  "v_max3_f32 v237, v130, v131, v237\n"
  "v_max3_f32 v237, v132, v133, v237\n"
  "v_max3_f32 v237, v134, v135, v237\n"
  "v_mov_b32 v238, v237\n"
  "s_nop 1\n"
  "v_permlane16_swap_b32 v237, v238\n"
  "v_max_f32 v237, v237, v238\n"
  "v_mov_b32 v238, v237\n"
  "s_nop 1\n"
  "v_permlane32_swap_b32 v237, v238\n"
  "v_max_f32 v237, v237, v238\n"
  "v_sub_f32 v238, v236, v237\n"
  "v_exp_f32 v238, v238\n"
  "v_mov_b32 v236, v237\n"
  "v_sub_f32 v128, v128, v237\n"
  "v_exp_f32 v128, v128\n"
  "v_sub_f32 v129, v129, v237\n"
  "v_exp_f32 v129, v129\n"
  "v_sub_f32 v130, v130, v237\n"
  "v_exp_f32 v130, v130\n"
  "v_sub_f32 v131, v131, v237\n"
  "v_exp_f32 v131, v131\n"
  "v_sub_f32 v132, v132, v237\n"
  "v_exp_f32 v132, v132\n"
  "v_sub_f32 v133, v133, v237\n"
  "v_exp_f32 v133, v133\n"
  "v_sub_f32 v134, v134, v237\n"
  "v_exp_f32 v134, v134\n"
  "v_sub_f32 v135, v135, v237\n"
  "v_exp_f32 v135, v135\n"
  "v_mul_f32 v0, v0, v238\n"
  "v_mul_f32 v1, v1, v238\n"
  "v_mul_f32 v2, v2, v238\n"
  "v_mul_f32 v3, v3, v238\n"
  "v_mul_f32 v4, v4, v238\n"
  "v_mul_f32 v5, v5, v238\n"
  "v_mul_f32 v6, v6, v238\n"
  "v_mul_f32 v7, v7, v238\n"
  "v_mul_f32 v8, v8, v238\n"
  "v_mul_f32 v9, v9, v238\n"
  "v_mul_f32 v10, v10, v238\n"
  "v_mul_f32 v11, v11, v238\n"
  "v_mul_f32 v12, v12, v238\n"
  "v_mul_f32 v13, v13, v238\n"
  "v_mul_f32 v14, v14, v238\n"
  "v_mul_f32 v15, v15, v238\n"
  "v_mul_f32 v16, v16, v238\n"
  "v_mul_f32 v17, v17, v238\n"
  "v_mul_f32 v18, v18, v238\n"
  "v_mul_f32 v19, v19, v238\n"
  "v_mul_f32 v20, v20, v238\n"
  "v_mul_f32 v21, v21, v238\n"
  "v_mul_f32 v22, v22, v238\n"
  "v_mul_f32 v23, v23, v238\n"
  "v_mul_f32 v24, v24, v238\n"
  "v_mul_f32 v25, v25, v238\n"
  "v_mul_f32 v26, v26, v238\n"
  "v_mul_f32 v27, v27, v238\n"
  "v_mul_f32 v28, v28, v238\n"
  "v_mul_f32 v29, v29, v238\n"
  "v_mul_f32 v30, v30, v238\n"
  "v_mul_f32 v31, v31, v238\n"
  "v_mul_f32 v200, v200, v238\n"
  "v_mul_f32 v201, v201, v238\n"
  "v_mul_f32 v202, v202, v238\n"
  "v_mul_f32 v203, v203, v238\n"
  "v_cvt_pk_bf16_f32 v144, v128, v129\n"
  "v_cvt_pk_bf16_f32 v145, v130, v131\n"
  "v_cvt_pk_bf16_f32 v146, v132, v133\n"
  "v_cvt_pk_bf16_f32 v147, v134, v135\n"
  "s_waitcnt vmcnt(7)\n"
  "s_nop 0\n"
  "v_mfma_f32_16x16x32_bf16 v[0:3], a[64:67], v[144:147], v[0:3]\n"
  "s_waitcnt vmcnt(6)\n"
  "v_mfma_f32_16x16x32_bf16 v[4:7], a[68:71], v[144:147], v[4:7]\n"
  "s_waitcnt vmcnt(5)\n"
  "v_mfma_f32_16x16x32_bf16 v[8:11], a[72:75], v[144:147], v[8:11]\n"
  "s_waitcnt vmcnt(4)\n"
  "v_mfma_f32_16x16x32_bf16 v[12:15], a[76:79], v[144:147], v[12:15]\n"
  "s_waitcnt vmcnt(3)\n"
  "v_mfma_f32_16x16x32_bf16 v[16:19], a[80:83], v[144:147], v[16:19]\n"
  "s_waitcnt vmcnt(2)\n"
  "v_mfma_f32_16x16x32_bf16 v[20:23], a[84:87], v[144:147], v[20:23]\n"
  "s_waitcnt vmcnt(1)\n"
  "v_mfma_f32_16x16x32_bf16 v[24:27], a[88:91], v[144:147], v[24:27]\n"
  "s_waitcnt vmcnt(0)\n"
  "v_mfma_f32_16x16x32_bf16 v[28:31], a[92:95], v[144:147], v[28:31]\n"
  "v_mfma_f32_16x16x32_bf16 v[200:203], a[192:195], v[144:147], v[200:203]\n"
  "v_subrev_u32 v239, 32, v239\n"
  "s_add_u32 s60, s60, 16384\n"
  "s_addc_u32 s61, s61, 0\n"
  "s_sub_u32 s59, s59, 1\n"
  "s_cmp_ge_i32 s59, 0\n"
  "s_cbranch_scc1 L_slow0_loop_%=\n"
  "s_nop 7\n"
  "s_setpc_b64 s[18:19]\n"
  "L_slow1_%=:\n"
  "s_add_u32 s36, s54, 1\n"
  "s_lshr_b32 s59, s36, 1\n"
  "s_lshl_b32 s36, s36, 4\n"
  "v_and_b32 v239, 15, v233\n"
  "v_add_u32 v239, s36, v239\n"
  "v_lshrrev_b32 v240, 4, v233\n"
  "v_lshlrev_b32 v240, 2, v240\n"
  "v_sub_u32 v239, v239, v240\n"
  "s_lshl_b32 s36, s39, 20\n"
  "s_add_u32 s36, s36, 2101248\n"
  "s_add_u32 s60, s84, s36\n"
  "s_addc_u32 s61, s85, 0\n"
  "v_add_u32 v240, 0x2000, v232\n"
  "v_mov_b32 v236, 0xf149f2ca\n"
  "v_mov_b32 v32, 0\n"
  "v_mov_b32 v33, 0\n"
  "v_mov_b32 v34, 0\n"
  "v_mov_b32 v35, 0\n"
  "v_mov_b32 v36, 0\n"
  "v_mov_b32 v37, 0\n"
  "v_mov_b32 v38, 0\n"
  "v_mov_b32 v39, 0\n"
  "v_mov_b32 v40, 0\n"
  "v_mov_b32 v41, 0\n"
  "v_mov_b32 v42, 0\n"
  "v_mov_b32 v43, 0\n"
  "v_mov_b32 v44, 0\n"
  "v_mov_b32 v45, 0\n"
  "v_mov_b32 v46, 0\n"
  "v_mov_b32 v47, 0\n"
  "v_mov_b32 v48, 0\n"
  "v_mov_b32 v49, 0\n"
  "v_mov_b32 v50, 0\n"
  "v_mov_b32 v51, 0\n"
  "v_mov_b32 v52, 0\n"
  "v_mov_b32 v53, 0\n"
  "v_mov_b32 v54, 0\n"
  "v_mov_b32 v55, 0\n"
  "v_mov_b32 v56, 0\n"
  "v_mov_b32 v57, 0\n"
  "v_mov_b32 v58, 0\n"
  "v_mov_b32 v59, 0\n"
  "v_mov_b32 v60, 0\n"
  "v_mov_b32 v61, 0\n"
  "v_mov_b32 v62, 0\n"
  "v_mov_b32 v63, 0\n"
  "v_mov_b32 v204, 0\n"
  "v_mov_b32 v205, 0\n"
  "v_mov_b32 v206, 0\n"
  "v_mov_b32 v207, 0\n"
  "L_slow1_loop_%=:\n"
  "global_load_dwordx4 a[128:131], v232, s[60:61] offset:-4096 sc1\n"
  "global_load_dwordx4 a[132:135], v232, s[60:61] offset:-3072 sc1\n"
  "global_load_dwordx4 a[136:139], v232, s[60:61] offset:-2048 sc1\n"
  "global_load_dwordx4 a[140:143], v232, s[60:61] offset:-1024 sc1\n"
  "global_load_dwordx4 a[144:147], v232, s[60:61] offset:0 sc1\n"
  "global_load_dwordx4 a[148:151], v232, s[60:61] offset:1024 sc1\n"
  "global_load_dwordx4 a[152:155], v232, s[60:61] offset:2048 sc1\n"
  "global_load_dwordx4 a[156:159], v232, s[60:61] offset:3072 sc1\n"
  "global_load_dwordx4 a[64:67], v240, s[60:61] offset:-4096 sc1\n"
  "global_load_dwordx4 a[68:71], v240, s[60:61] offset:-3072 sc1\n"
  "global_load_dwordx4 a[72:75], v240, s[60:61] offset:-2048 sc1\n"
  "global_load_dwordx4 a[76:79], v240, s[60:61] offset:-1024 sc1\n"
  "global_load_dwordx4 a[80:83], v240, s[60:61] offset:0 sc1\n"
  "global_load_dwordx4 a[84:87], v240, s[60:61] offset:1024 sc1\n"
  "global_load_dwordx4 a[88:91], v240, s[60:61] offset:2048 sc1\n"
  "global_load_dwordx4 a[92:95], v240, s[60:61] offset:3072 sc1\n"
  "s_waitcnt vmcnt(15)\n"
  "v_mfma_f32_16x16x32_bf16 v[128:131], a[128:131], a[16:19], 0\n"
  "s_waitcnt vmcnt(14)\n"
  "v_mfma_f32_16x16x32_bf16 v[128:131], a[132:135], a[20:23], v[128:131]\n"
  "s_waitcnt vmcnt(13)\n"
  "v_mfma_f32_16x16x32_bf16 v[128:131], a[136:139], a[24:27], v[128:131]\n"
  "s_waitcnt vmcnt(12)\n"
  "v_mfma_f32_16x16x32_bf16 v[128:131], a[140:143], a[28:31], v[128:131]\n"
  "s_waitcnt vmcnt(11)\n"
  "v_mfma_f32_16x16x32_bf16 v[132:135], a[144:147], a[16:19], 0\n"
  "s_waitcnt vmcnt(10)\n"
  "v_mfma_f32_16x16x32_bf16 v[132:135], a[148:151], a[20:23], v[132:135]\n"
  "s_waitcnt vmcnt(9)\n"
  "v_mfma_f32_16x16x32_bf16 v[132:135], a[152:155], a[24:27], v[132:135]\n"
  "s_waitcnt vmcnt(8)\n"
  "v_mfma_f32_16x16x32_bf16 v[132:135], a[156:159], a[28:31], v[132:135]\n"
  "v_cmp_gt_i32 vcc, 0, v239\n"
  "s_nop 1\n"
  "v_cndmask_b32 v128, v128, v218, vcc\n"
  "v_cmp_gt_i32 vcc, 1, v239\n"
  "s_nop 1\n"
  "v_cndmask_b32 v129, v129, v218, vcc\n"
  "v_cmp_gt_i32 vcc, 2, v239\n"
  "s_nop 1\n"
  "v_cndmask_b32 v130, v130, v218, vcc\n"
  "v_cmp_gt_i32 vcc, 3, v239\n"
  "s_nop 1\n"
  "v_cndmask_b32 v131, v131, v218, vcc\n"
  "v_cmp_gt_i32 vcc, 16, v239\n"
  "s_nop 1\n"
  "v_cndmask_b32 v132, v132, v218, vcc\n"
  "v_cmp_gt_i32 vcc, 17, v239\n"
  "s_nop 1\n"
  "v_cndmask_b32 v133, v133, v218, vcc\n"
  "v_cmp_gt_i32 vcc, 18, v239\n"
  "s_nop 1\n"
  "v_cndmask_b32 v134, v134, v218, vcc\n"
  "v_cmp_gt_i32 vcc, 19, v239\n"
  "s_nop 1\n"
  "v_cndmask_b32 v135, v135, v218, vcc\n"
  "v_max3_f32 v237, v128, v129, v236\n"
  "v_max3_f32 v237, v130, v131, v237\n"
  "v_max3_f32 v237, v132, v133, v237\n"
  "v_max3_f32 v237, v134, v135, v237\n"
  "v_mov_b32 v238, v237\n"
  "s_nop 1\n"
  "v_permlane16_swap_b32 v237, v238\n"
  "v_max_f32 v237, v237, v238\n"
  "v_mov_b32 v238, v237\n"
  "s_nop 1\n"
  "v_permlane32_swap_b32 v237, v238\n"
  "v_max_f32 v237, v237, v238\n"
  "v_sub_f32 v238, v236, v237\n"
  "v_exp_f32 v238, v238\n"
  "v_mov_b32 v236, v237\n"
  "v_sub_f32 v128, v128, v237\n"
  "v_exp_f32 v128, v128\n"
  "v_sub_f32 v129, v129, v237\n"
  "v_exp_f32 v129, v129\n"
  "v_sub_f32 v130, v130, v237\n"
  "v_exp_f32 v130, v130\n"
  "v_sub_f32 v131, v131, v237\n"
  "v_exp_f32 v131, v131\n"
  "v_sub_f32 v132, v132, v237\n"
  "v_exp_f32 v132, v132\n"
  "v_sub_f32 v133, v133, v237\n"
  "v_exp_f32 v133, v133\n"
  "v_sub_f32 v134, v134, v237\n"
  "v_exp_f32 v134, v134\n"
  "v_sub_f32 v135, v135, v237\n"
  "v_exp_f32 v135, v135\n"
  "v_mul_f32 v32, v32, v238\n"
  "v_mul_f32 v33, v33, v238\n"
  "v_mul_f32 v34, v34, v238\n"
  "v_mul_f32 v35, v35, v238\n"
  "v_mul_f32 v36, v36, v238\n"
  "v_mul_f32 v37, v37, v238\n"
  "v_mul_f32 v38, v38, v238\n"
  "v_mul_f32 v39, v39, v238\n"
  "v_mul_f32 v40, v40, v238\n"
  "v_mul_f32 v41, v41, v238\n"
  "v_mul_f32 v42, v42, v238\n"
  "v_mul_f32 v43, v43, v238\n"
  "v_mul_f32 v44, v44, v238\n"
  "v_mul_f32 v45, v45, v238\n"
  "v_mul_f32 v46, v46, v238\n"
  "v_mul_f32 v47, v47, v238\n"
  "v_mul_f32 v48, v48, v238\n"
  "v_mul_f32 v49, v49, v238\n"
  "v_mul_f32 v50, v50, v238\n"
  "v_mul_f32 v51, v51, v238\n"
  "v_mul_f32 v52, v52, v238\n"
  "v_mul_f32 v53, v53, v238\n"
  "v_mul_f32 v54, v54, v238\n"
  "v_mul_f32 v55, v55, v238\n"
  "v_mul_f32 v56, v56, v238\n"
  "v_mul_f32 v57, v57, v238\n"
  "v_mul_f32 v58, v58, v238\n"
  "v_mul_f32 v59, v59, v238\n"
  "v_mul_f32 v60, v60, v238\n"
  "v_mul_f32 v61, v61, v238\n"
  "v_mul_f32 v62, v62, v238\n"
  "v_mul_f32 v63, v63, v238\n"
  "v_mul_f32 v204, v204, v238\n"
  "v_mul_f32 v205, v205, v238\n"
  "v_mul_f32 v206, v206, v238\n"
  "v_mul_f32 v207, v207, v238\n"
  "v_cvt_pk_bf16_f32 v144, v128, v129\n"
  "v_cvt_pk_bf16_f32 v145, v130, v131\n"
  "v_cvt_pk_bf16_f32 v146, v132, v133\n"
  "v_cvt_pk_bf16_f32 v147, v134, v135\n"
  "s_waitcnt vmcnt(7)\n"
  "s_nop 0\n"
  "v_mfma_f32_16x16x32_bf16 v[32:35], a[64:67], v[144:147], v[32:35]\n"
  "s_waitcnt vmcnt(6)\n"
  "v_mfma_f32_16x16x32_bf16 v[36:39], a[68:71], v[144:147], v[36:39]\n"
  "s_waitcnt vmcnt(5)\n"
  "v_mfma_f32_16x16x32_bf16 v[40:43], a[72:75], v[144:147], v[40:43]\n"
  "s_waitcnt vmcnt(4)\n"
  "v_mfma_f32_16x16x32_bf16 v[44:47], a[76:79], v[144:147], v[44:47]\n"
  "s_waitcnt vmcnt(3)\n"
  "v_mfma_f32_16x16x32_bf16 v[48:51], a[80:83], v[144:147], v[48:51]\n"
  "s_waitcnt vmcnt(2)\n"
  "v_mfma_f32_16x16x32_bf16 v[52:55], a[84:87], v[144:147], v[52:55]\n"
  "s_waitcnt vmcnt(1)\n"
  "v_mfma_f32_16x16x32_bf16 v[56:59], a[88:91], v[144:147], v[56:59]\n"
  "s_waitcnt vmcnt(0)\n"
  "v_mfma_f32_16x16x32_bf16 v[60:63], a[92:95], v[144:147], v[60:63]\n"
  "v_mfma_f32_16x16x32_bf16 v[204:207], a[192:195], v[144:147], v[204:207]\n"
  "v_subrev_u32 v239, 32, v239\n"
  "s_add_u32 s60, s60, 16384\n"
  "s_addc_u32 s61, s61, 0\n"
  "s_sub_u32 s59, s59, 1\n"
  "s_cmp_ge_i32 s59, 0\n"
  "s_cbranch_scc1 L_slow1_loop_%=\n"
  "s_nop 7\n"
  "s_setpc_b64 s[18:19]\n"
  "L_slow2_%=:\n"
  "s_add_u32 s36, s54, 2\n"
  "s_lshr_b32 s59, s36, 1\n"
  "s_lshl_b32 s36, s36, 4\n"
  "v_and_b32 v239, 15, v233\n"
  "v_add_u32 v239, s36, v239\n"
  "v_lshrrev_b32 v240, 4, v233\n"
  "v_lshlrev_b32 v240, 2, v240\n"
  "v_sub_u32 v239, v239, v240\n"
  "s_lshl_b32 s36, s39, 20\n"
  "s_add_u32 s36, s36, 2101248\n"
  "s_add_u32 s60, s84, s36\n"
  "s_addc_u32 s61, s85, 0\n"
  "v_add_u32 v240, 0x2000, v232\n"
  "v_mov_b32 v236, 0xf149f2ca\n"
  "v_mov_b32 v64, 0\n"
  "v_mov_b32 v65, 0\n"
  "v_mov_b32 v66, 0\n"
  "v_mov_b32 v67, 0\n"
  "v_mov_b32 v68, 0\n"
  "v_mov_b32 v69, 0\n"
  "v_mov_b32 v70, 0\n"
  "v_mov_b32 v71, 0\n"
  "v_mov_b32 v72, 0\n"
  "v_mov_b32 v73, 0\n"
  "v_mov_b32 v74, 0\n"
  "v_mov_b32 v75, 0\n"
  "v_mov_b32 v76, 0\n"
  "v_mov_b32 v77, 0\n"
  "v_mov_b32 v78, 0\n"
  "v_mov_b32 v79, 0\n"
  "v_mov_b32 v80, 0\n"
  "v_mov_b32 v81, 0\n"
  "v_mov_b32 v82, 0\n"
  "v_mov_b32 v83, 0\n"
  "v_mov_b32 v84, 0\n"
  "v_mov_b32 v85, 0\n"
  "v_mov_b32 v86, 0\n"
  "v_mov_b32 v87, 0\n"
  "v_mov_b32 v88, 0\n"
  "v_mov_b32 v89, 0\n"
  "v_mov_b32 v90, 0\n"
  "v_mov_b32 v91, 0\n"
  "v_mov_b32 v92, 0\n"
  "v_mov_b32 v93, 0\n"
  "v_mov_b32 v94, 0\n"
  "v_mov_b32 v95, 0\n"
  "v_mov_b32 v208, 0\n"
  "v_mov_b32 v209, 0\n"
  "v_mov_b32 v210, 0\n"
  "v_mov_b32 v211, 0\n"
  "L_slow2_loop_%=:\n"
  "global_load_dwordx4 a[128:131], v232, s[60:61] offset:-4096 sc1\n"
  "global_load_dwordx4 a[132:135], v232, s[60:61] offset:-3072 sc1\n"
  "global_load_dwordx4 a[136:139], v232, s[60:61] offset:-2048 sc1\n"
  "global_load_dwordx4 a[140:143], v232, s[60:61] offset:-1024 sc1\n"
  "global_load_dwordx4 a[144:147], v232, s[60:61] offset:0 sc1\n"
  "global_load_dwordx4 a[148:151], v232, s[60:61] offset:1024 sc1\n"
  "global_load_dwordx4 a[152:155], v232, s[60:61] offset:2048 sc1\n"
  "global_load_dwordx4 a[156:159], v232, s[60:61] offset:3072 sc1\n"
  "global_load_dwordx4 a[64:67], v240, s[60:61] offset:-4096 sc1\n"
  "global_load_dwordx4 a[68:71], v240, s[60:61] offset:-3072 sc1\n"
  "global_load_dwordx4 a[72:75], v240, s[60:61] offset:-2048 sc1\n"
  "global_load_dwordx4 a[76:79], v240, s[60:61] offset:-1024 sc1\n"
  "global_load_dwordx4 a[80:83], v240, s[60:61] offset:0 sc1\n"
  "global_load_dwordx4 a[84:87], v240, s[60:61] offset:1024 sc1\n"
  "global_load_dwordx4 a[88:91], v240, s[60:61] offset:2048 sc1\n"
  "s_waitcnt vmcnt(58)\n"
  "global_load_dwordx4 a[92:95], v240, s[60:61] offset:3072 sc1\n"
  "s_waitcnt vmcnt(15)\n"
  "v_mfma_f32_16x16x32_bf16 v[128:131], a[128:131], a[32:35], 0\n"
  "s_waitcnt vmcnt(14)\n"
  "v_mfma_f32_16x16x32_bf16 v[128:131], a[132:135], a[36:39], v[128:131]\n"
  "s_waitcnt vmcnt(13)\n"
  "v_mfma_f32_16x16x32_bf16 v[128:131], a[136:139], a[40:43], v[128:131]\n"
  "s_waitcnt vmcnt(12)\n"
  "v_mfma_f32_16x16x32_bf16 v[128:131], a[140:143], a[44:47], v[128:131]\n"
  "s_waitcnt vmcnt(11)\n"
  "v_mfma_f32_16x16x32_bf16 v[132:135], a[144:147], a[32:35], 0\n"
  "s_waitcnt vmcnt(10)\n"
  "v_mfma_f32_16x16x32_bf16 v[132:135], a[148:151], a[36:39], v[132:135]\n"
  "s_waitcnt vmcnt(9)\n"
  "v_mfma_f32_16x16x32_bf16 v[132:135], a[152:155], a[40:43], v[132:135]\n"
  "s_waitcnt vmcnt(8)\n"
  "v_mfma_f32_16x16x32_bf16 v[132:135], a[156:159], a[44:47], v[132:135]\n"
  "v_cmp_gt_i32 vcc, 0, v239\n"
  "s_nop 1\n"
  "v_cndmask_b32 v128, v128, v218, vcc\n"
  "v_cmp_gt_i32 vcc, 1, v239\n"
  "s_nop 1\n"
  "v_cndmask_b32 v129, v129, v218, vcc\n"
  "v_cmp_gt_i32 vcc, 2, v239\n"
  "s_nop 1\n"
  "v_cndmask_b32 v130, v130, v218, vcc\n"
  "v_cmp_gt_i32 vcc, 3, v239\n"
  "s_nop 1\n"
  "v_cndmask_b32 v131, v131, v218, vcc\n"
  "v_cmp_gt_i32 vcc, 16, v239\n"
  "s_nop 1\n"
  "v_cndmask_b32 v132, v132, v218, vcc\n"
  "v_cmp_gt_i32 vcc, 17, v239\n"
  "s_nop 1\n"
  "v_cndmask_b32 v133, v133, v218, vcc\n"
  "v_cmp_gt_i32 vcc, 18, v239\n"
  "s_nop 1\n"
  "v_cndmask_b32 v134, v134, v218, vcc\n"
  "v_cmp_gt_i32 vcc, 19, v239\n"
  "s_nop 1\n"
  "v_cndmask_b32 v135, v135, v218, vcc\n"
  "v_max3_f32 v237, v128, v129, v236\n"
  "v_max3_f32 v237, v130, v131, v237\n"
  "v_max3_f32 v237, v132, v133, v237\n"
  "v_max3_f32 v237, v134, v135, v237\n"
  "v_mov_b32 v238, v237\n"
  "s_nop 1\n"
  "v_permlane16_swap_b32 v237, v238\n"
  "v_max_f32 v237, v237, v238\n"
  "v_mov_b32 v238, v237\n"
  "s_nop 1\n"
  "v_permlane32_swap_b32 v237, v238\n"
  "v_max_f32 v237, v237, v238\n"
  "v_sub_f32 v238, v236, v237\n"
  "v_exp_f32 v238, v238\n"
  "v_mov_b32 v236, v237\n"
  "v_sub_f32 v128, v128, v237\n"
  "v_exp_f32 v128, v128\n"
  "v_sub_f32 v129, v129, v237\n"
  "v_exp_f32 v129, v129\n"
  "v_sub_f32 v130, v130, v237\n"
  "v_exp_f32 v130, v130\n"
  "v_sub_f32 v131, v131, v237\n"
  "v_exp_f32 v131, v131\n"
  "v_sub_f32 v132, v132, v237\n"
  "v_exp_f32 v132, v132\n"
  "v_sub_f32 v133, v133, v237\n"
  "v_exp_f32 v133, v133\n"
  "v_sub_f32 v134, v134, v237\n"
  "v_exp_f32 v134, v134\n"
  "v_sub_f32 v135, v135, v237\n"
  "v_exp_f32 v135, v135\n"
  "v_mul_f32 v64, v64, v238\n"
  "v_mul_f32 v65, v65, v238\n"
  "v_mul_f32 v66, v66, v238\n"
  "v_mul_f32 v67, v67, v238\n"
  "v_mul_f32 v68, v68, v238\n"
  "v_mul_f32 v69, v69, v238\n"
  "v_mul_f32 v70, v70, v238\n"
  "v_mul_f32 v71, v71, v238\n"
  "v_mul_f32 v72, v72, v238\n"
  "v_mul_f32 v73, v73, v238\n"
  "v_mul_f32 v74, v74, v238\n"
  "v_mul_f32 v75, v75, v238\n"
  "v_mul_f32 v76, v76, v238\n"
  "v_mul_f32 v77, v77, v238\n"
  "v_mul_f32 v78, v78, v238\n"
  "v_mul_f32 v79, v79, v238\n"
  "v_mul_f32 v80, v80, v238\n"
  "v_mul_f32 v81, v81, v238\n"
  "v_mul_f32 v82, v82, v238\n"
  "v_mul_f32 v83, v83, v238\n"
  "v_mul_f32 v84, v84, v238\n"
  "v_mul_f32 v85, v85, v238\n"
  "v_mul_f32 v86, v86, v238\n"
  "v_mul_f32 v87, v87, v238\n"
  "v_mul_f32 v88, v88, v238\n"
  "v_mul_f32 v89, v89, v238\n"
  "v_mul_f32 v90, v90, v238\n"
  "v_mul_f32 v91, v91, v238\n"
  "v_mul_f32 v92, v92, v238\n"
  "v_mul_f32 v93, v93, v238\n"
  "v_mul_f32 v94, v94, v238\n"
  "v_mul_f32 v95, v95, v238\n"
  "v_mul_f32 v208, v208, v238\n"
  "v_mul_f32 v209, v209, v238\n"
  "v_mul_f32 v210, v210, v238\n"
  "v_mul_f32 v211, v211, v238\n"
  "v_cvt_pk_bf16_f32 v144, v128, v129\n"
  "v_cvt_pk_bf16_f32 v145, v130, v131\n"
  "v_cvt_pk_bf16_f32 v146, v132, v133\n"
  "v_cvt_pk_bf16_f32 v147, v134, v135\n"
  "s_waitcnt vmcnt(7)\n"
  "s_nop 0\n"
  "v_mfma_f32_16x16x32_bf16 v[64:67], a[64:67], v[144:147], v[64:67]\n"
  "s_waitcnt vmcnt(6)\n"
  "v_mfma_f32_16x16x32_bf16 v[68:71], a[68:71], v[144:147], v[68:71]\n"
  "s_waitcnt vmcnt(5)\n"
  "v_mfma_f32_16x16x32_bf16 v[72:75], a[72:75], v[144:147], v[72:75]\n"
  "s_waitcnt vmcnt(4)\n"
  "v_mfma_f32_16x16x32_bf16 v[76:79], a[76:79], v[144:147], v[76:79]\n"
  "s_waitcnt vmcnt(3)\n"
  "v_mfma_f32_16x16x32_bf16 v[80:83], a[80:83], v[144:147], v[80:83]\n"
  "s_waitcnt vmcnt(2)\n"
  "v_mfma_f32_16x16x32_bf16 v[84:87], a[84:87], v[144:147], v[84:87]\n"
  "s_waitcnt vmcnt(1)\n"
  "v_mfma_f32_16x16x32_bf16 v[88:91], a[88:91], v[144:147], v[88:91]\n"
  "s_waitcnt vmcnt(0)\n"
  "v_mfma_f32_16x16x32_bf16 v[92:95], a[92:95], v[144:147], v[92:95]\n"
  "v_mfma_f32_16x16x32_bf16 v[208:211], a[192:195], v[144:147], v[208:211]\n"
  "v_subrev_u32 v239, 32, v239\n"
  "s_add_u32 s60, s60, 16384\n"
  "s_addc_u32 s61, s61, 0\n"
  "s_sub_u32 s59, s59, 1\n"
  "s_cmp_ge_i32 s59, 0\n"
  "s_cbranch_scc1 L_slow2_loop_%=\n"
  "s_nop 7\n"
  "s_setpc_b64 s[18:19]\n"
  "L_slow3_%=:\n"
  "s_add_u32 s36, s54, 3\n"
  "s_lshr_b32 s59, s36, 1\n"
  "s_lshl_b32 s36, s36, 4\n"
  "v_and_b32 v239, 15, v233\n"
  "v_add_u32 v239, s36, v239\n"
  "v_lshrrev_b32 v240, 4, v233\n"
  "v_lshlrev_b32 v240, 2, v240\n"
  "v_sub_u32 v239, v239, v240\n"
  "s_lshl_b32 s36, s39, 20\n"
  "s_add_u32 s36, s36, 2101248\n"
  "s_add_u32 s60, s84, s36\n"
  "s_addc_u32 s61, s85, 0\n"
  "v_add_u32 v240, 0x2000, v232\n"
  "v_mov_b32 v236, 0xf149f2ca\n"
  "v_mov_b32 v96, 0\n"
  "v_mov_b32 v97, 0\n"
  "v_mov_b32 v98, 0\n"
  "v_mov_b32 v99, 0\n"
  "v_mov_b32 v100, 0\n"
  "v_mov_b32 v101, 0\n"
  "v_mov_b32 v102, 0\n"
  "v_mov_b32 v103, 0\n"
  "v_mov_b32 v104, 0\n"
  "v_mov_b32 v105, 0\n"
  "v_mov_b32 v106, 0\n"
  "v_mov_b32 v107, 0\n"
  "v_mov_b32 v108, 0\n"
  "v_mov_b32 v109, 0\n"
  "v_mov_b32 v110, 0\n"
  "v_mov_b32 v111, 0\n"
  "v_mov_b32 v112, 0\n"
  "v_mov_b32 v113, 0\n"
  "v_mov_b32 v114, 0\n"
  "v_mov_b32 v115, 0\n"
  "v_mov_b32 v116, 0\n"
  "v_mov_b32 v117, 0\n"
  "v_mov_b32 v118, 0\n"
  "v_mov_b32 v119, 0\n"
  "v_mov_b32 v120, 0\n"
  "v_mov_b32 v121, 0\n"
  "v_mov_b32 v122, 0\n"
  "v_mov_b32 v123, 0\n"
  "v_mov_b32 v124, 0\n"
  "v_mov_b32 v125, 0\n"
  "v_mov_b32 v126, 0\n"
  "v_mov_b32 v127, 0\n"
  "v_mov_b32 v212, 0\n"
  "v_mov_b32 v213, 0\n"
  "v_mov_b32 v214, 0\n"
  "v_mov_b32 v215, 0\n"
  "L_slow3_loop_%=:\n"
  "global_load_dwordx4 a[128:131], v232, s[60:61] offset:-4096 sc1\n"
  "global_load_dwordx4 a[132:135], v232, s[60:61] offset:-3072 sc1\n"
  "global_load_dwordx4 a[136:139], v232, s[60:61] offset:-2048 sc1\n"
  "global_load_dwordx4 a[140:143], v232, s[60:61] offset:-1024 sc1\n"
  "global_load_dwordx4 a[144:147], v232, s[60:61] offset:0 sc1\n"
  "global_load_dwordx4 a[148:151], v232, s[60:61] offset:1024 sc1\n"
  "global_load_dwordx4 a[152:155], v232, s[60:61] offset:2048 sc1\n"
  "global_load_dwordx4 a[156:159], v232, s[60:61] offset:3072 sc1\n"
  "global_load_dwordx4 a[64:67], v240, s[60:61] offset:-4096 sc1\n"
  "global_load_dwordx4 a[68:71], v240, s[60:61] offset:-3072 sc1\n"
  "global_load_dwordx4 a[72:75], v240, s[60:61] offset:-2048 sc1\n"
  "global_load_dwordx4 a[76:79], v240, s[60:61] offset:-1024 sc1\n"
  "global_load_dwordx4 a[80:83], v240, s[60:61] offset:0 sc1\n"
  "global_load_dwordx4 a[84:87], v240, s[60:61] offset:1024 sc1\n"
  "global_load_dwordx4 a[88:91], v240, s[60:61] offset:2048 sc1\n"
  "global_load_dwordx4 a[92:95], v240, s[60:61] offset:3072 sc1\n"
  "s_waitcnt vmcnt(15)\n"
  "v_mfma_f32_16x16x32_bf16 v[128:131], a[128:131], a[48:51], 0\n"
  "s_waitcnt vmcnt(14)\n"
  "v_mfma_f32_16x16x32_bf16 v[128:131], a[132:135], a[52:55], v[128:131]\n"
  "s_waitcnt vmcnt(13)\n"
  "v_mfma_f32_16x16x32_bf16 v[128:131], a[136:139], a[56:59], v[128:131]\n"
  "s_waitcnt vmcnt(12)\n"
  "v_mfma_f32_16x16x32_bf16 v[128:131], a[140:143], a[60:63], v[128:131]\n"
  "s_waitcnt vmcnt(11)\n"
  "v_mfma_f32_16x16x32_bf16 v[132:135], a[144:147], a[48:51], 0\n"
  "s_waitcnt vmcnt(10)\n"
  "v_mfma_f32_16x16x32_bf16 v[132:135], a[148:151], a[52:55], v[132:135]\n"
  "s_waitcnt vmcnt(9)\n"
  "v_mfma_f32_16x16x32_bf16 v[132:135], a[152:155], a[56:59], v[132:135]\n"
  "s_waitcnt vmcnt(8)\n"
  "v_mfma_f32_16x16x32_bf16 v[132:135], a[156:159], a[60:63], v[132:135]\n"
  "v_cmp_gt_i32 vcc, 0, v239\n"
  "s_nop 1\n"
  "v_cndmask_b32 v128, v128, v218, vcc\n"
  "v_cmp_gt_i32 vcc, 1, v239\n"
  "s_nop 1\n"
  "v_cndmask_b32 v129, v129, v218, vcc\n"
  "v_cmp_gt_i32 vcc, 2, v239\n"
  "s_nop 1\n"
  "v_cndmask_b32 v130, v130, v218, vcc\n"
  "v_cmp_gt_i32 vcc, 3, v239\n"
  "s_nop 1\n"
  "v_cndmask_b32 v131, v131, v218, vcc\n"
  "v_cmp_gt_i32 vcc, 16, v239\n"
  "s_nop 1\n"
  "v_cndmask_b32 v132, v132, v218, vcc\n"
  "v_cmp_gt_i32 vcc, 17, v239\n"
  "s_nop 1\n"
  "v_cndmask_b32 v133, v133, v218, vcc\n"
  "v_cmp_gt_i32 vcc, 18, v239\n"
  "s_nop 1\n"
  "v_cndmask_b32 v134, v134, v218, vcc\n"
  "v_cmp_gt_i32 vcc, 19, v239\n"
  "s_nop 1\n"
  "v_cndmask_b32 v135, v135, v218, vcc\n"
  "v_max3_f32 v237, v128, v129, v236\n"
  "v_max3_f32 v237, v130, v131, v237\n"
  "v_max3_f32 v237, v132, v133, v237\n"
  "v_max3_f32 v237, v134, v135, v237\n"
  "v_mov_b32 v238, v237\n"
  "s_nop 1\n"
  "v_permlane16_swap_b32 v237, v238\n"
  "v_max_f32 v237, v237, v238\n"
  "v_mov_b32 v238, v237\n"
  "s_nop 1\n"
  "v_permlane32_swap_b32 v237, v238\n"
  "v_max_f32 v237, v237, v238\n"
  "v_sub_f32 v238, v236, v237\n"
  "v_exp_f32 v238, v238\n"
  "v_mov_b32 v236, v237\n"
  "v_sub_f32 v128, v128, v237\n"
  "v_exp_f32 v128, v128\n"
  "v_sub_f32 v129, v129, v237\n"
  "v_exp_f32 v129, v129\n"
  "v_sub_f32 v130, v130, v237\n"
  "v_exp_f32 v130, v130\n"
  "v_sub_f32 v131, v131, v237\n"
  "v_exp_f32 v131, v131\n"
  "v_sub_f32 v132, v132, v237\n"
  "v_exp_f32 v132, v132\n"
  "v_sub_f32 v133, v133, v237\n"
  "v_exp_f32 v133, v133\n"
  "v_sub_f32 v134, v134, v237\n"
  "v_exp_f32 v134, v134\n"
  "v_sub_f32 v135, v135, v237\n"
  "v_exp_f32 v135, v135\n"
  "v_mul_f32 v96, v96, v238\n"
  "v_mul_f32 v97, v97, v238\n"
  "v_mul_f32 v98, v98, v238\n"
  "v_mul_f32 v99, v99, v238\n"
  "v_mul_f32 v100, v100, v238\n"
  "v_mul_f32 v101, v101, v238\n"
  "v_mul_f32 v102, v102, v238\n"
  "v_mul_f32 v103, v103, v238\n"
  "v_mul_f32 v104, v104, v238\n"
  "v_mul_f32 v105, v105, v238\n"
  "v_mul_f32 v106, v106, v238\n"
  "v_mul_f32 v107, v107, v238\n"
  "v_mul_f32 v108, v108, v238\n"
  "v_mul_f32 v109, v109, v238\n"
  "v_mul_f32 v110, v110, v238\n"
  "v_mul_f32 v111, v111, v238\n"
  "v_mul_f32 v112, v112, v238\n"
  "v_mul_f32 v113, v113, v238\n"
  "v_mul_f32 v114, v114, v238\n"
  "v_mul_f32 v115, v115, v238\n"
  "v_mul_f32 v116, v116, v238\n"
  "v_mul_f32 v117, v117, v238\n"
  "v_mul_f32 v118, v118, v238\n"
  "v_mul_f32 v119, v119, v238\n"
  "v_mul_f32 v120, v120, v238\n"
  "v_mul_f32 v121, v121, v238\n"
  "v_mul_f32 v122, v122, v238\n"
  "v_mul_f32 v123, v123, v238\n"
  "v_mul_f32 v124, v124, v238\n"
  "v_mul_f32 v125, v125, v238\n"
  "v_mul_f32 v126, v126, v238\n"
  "v_mul_f32 v127, v127, v238\n"
  "v_mul_f32 v212, v212, v238\n"
  "v_mul_f32 v213, v213, v238\n"
  "v_mul_f32 v214, v214, v238\n"
  "v_mul_f32 v215, v215, v238\n"
  "v_cvt_pk_bf16_f32 v144, v128, v129\n"
  "v_cvt_pk_bf16_f32 v145, v130, v131\n"
  "v_cvt_pk_bf16_f32 v146, v132, v133\n"
  "v_cvt_pk_bf16_f32 v147, v134, v135\n"
  "s_waitcnt vmcnt(7)\n"
  "s_nop 0\n"
  "v_mfma_f32_16x16x32_bf16 v[96:99], a[64:67], v[144:147], v[96:99]\n"
  "s_waitcnt vmcnt(6)\n"
  "v_mfma_f32_16x16x32_bf16 v[100:103], a[68:71], v[144:147], v[100:103]\n"
  "s_waitcnt vmcnt(5)\n"
  "v_mfma_f32_16x16x32_bf16 v[104:107], a[72:75], v[144:147], v[104:107]\n"
  "s_waitcnt vmcnt(4)\n"
  "v_mfma_f32_16x16x32_bf16 v[108:111], a[76:79], v[144:147], v[108:111]\n"
  "s_waitcnt vmcnt(3)\n"
  "v_mfma_f32_16x16x32_bf16 v[112:115], a[80:83], v[144:147], v[112:115]\n"
  "s_waitcnt vmcnt(2)\n"
  "v_mfma_f32_16x16x32_bf16 v[116:119], a[84:87], v[144:147], v[116:119]\n"
  "s_waitcnt vmcnt(1)\n"
  "v_mfma_f32_16x16x32_bf16 v[120:123], a[88:91], v[144:147], v[120:123]\n"
  "s_waitcnt vmcnt(0)\n"
  "v_mfma_f32_16x16x32_bf16 v[124:127], a[92:95], v[144:147], v[124:127]\n"
  "v_mfma_f32_16x16x32_bf16 v[212:215], a[192:195], v[144:147], v[212:215]\n"
  "v_subrev_u32 v239, 32, v239\n"
  "s_add_u32 s60, s60, 16384\n"
  "s_addc_u32 s61, s61, 0\n"
  "s_sub_u32 s59, s59, 1\n"
  "s_cmp_ge_i32 s59, 0\n"
  "s_cbranch_scc1 L_slow3_loop_%=\n"
  "s_nop 7\n"
  "s_setpc_b64 s[18:19]\n"
  "L_last_%=:\n"
  :  : "s"(__builtin_amdgcn_kernarg_segment_ptr()), "s"(bid), "s"(wid), "s"(ldsb) : CLOB);

}

extern "C" void kernel_launch(void* const* d_in, const int* in_sizes, int n_in, void* d_out, int out_size, void* d_ws, size_t ws_size, hipStream_t stream) {
  const u16 *x = (const u16*)d_in[0], *ckv = (const u16*)d_in[1], *wq = (const u16*)d_in[2], *wuk = (const u16*)d_in[3], *wuv = (const u16*)d_in[4],
            *wo = (const u16*)d_in[5], *w1 = (const u16*)d_in[7], *b1 = (const u16*)d_in[8], *w2 = (const u16*)d_in[9];
  char* ws = (char*)d_ws + 0; u16* out = (u16*)d_out;
  if (n_in != 10 || in_sizes[0] != 2048 * 512 || in_sizes[2] != 512 * 4096 || in_sizes[7] != 512 * 1376 || out_size != 2048 * 512 || ws_size < ((size_t)104 << 20)) return;
  (void)hipFuncSetAttribute((const void*)kf_block, hipFuncAttributeMaxDynamicSharedMemorySize, 163840);
  kf_block<<<256, 256, 163840, stream>>>(x, ckv, wq, wuk, wuv, wo, w1, b1, w2, out, ws);
}
```
